```python
import torch

def _softmax(x, axis=-1):
    return torch.softmax(x, dim=axis)

def _gelu(x):
    return torch.nn.functional.gelu(x, approximate='tanh')

def _silu(x):
    return torch.nn.functional.silu(x)

def _sigmoid(x):
    return torch.sigmoid(x)

def _layernorm(x, axis=-1, eps=1e-05):
    m = x.mean(dim=axis, keepdim=True)
    v = x.var(dim=axis, keepdim=True, unbiased=False)
    return (x - m) / torch.sqrt(v + eps)

def _scatter_add(tgt, idx, src, axis=0):
    return tgt.clone().index_add_(axis, idx, src)

def _segment_sum(src, seg, n):
    out = torch.zeros((n,) + src.shape[1:], dtype=src.dtype)
    return out.index_add_(0, seg, src)

def _pad(x, pw):
    flat = tuple((v for pair in reversed(pw) for v in pair))
    return torch.nn.functional.pad(x, flat)
kernel_name = 'adaLN_cond__spectral_fnet__moe_arch_mod__prompt'

def setup_inputs():

    _w = lambda a: (lambda t: t.to(torch.bfloat16) if t.is_floating_point() else t)(torch.as_tensor(a))

    def _A_setup():
        B, S, D = (1, 2048, 1024)
        ns = torch.arange(S)[:, None] * torch.arange(S)[None, :]
        nd = torch.arange(D)[:, None] * torch.arange(D)[None, :]
        Ws_re = torch.cos(-2.0 * torch.pi * ns / S).to(float)
        Ws_im = torch.sin(-2.0 * torch.pi * ns / S).to(float)
        Wd_re = torch.cos(-2.0 * torch.pi * nd / D).to(float)
        Wd_im = torch.sin(-2.0 * torch.pi * nd / D).to(float)
        return {'x': _w((lambda *s: torch.randn(*s, dtype=torch.bfloat16) * 0.1)(B, S, D)), 'dft_seq_re': _w(Ws_re), 'dft_seq_im': _w(Ws_im), 'dft_hid_re': _w(Wd_re), 'dft_hid_im': _w(Wd_im)}
    _A_in = _A_setup()

    def _B_setup():
        B, S, D, F, C = (1, 2048, 1024, 3584, 2)
        return {'x': _w((lambda *s: torch.randn(*s, dtype=torch.bfloat16) * 0.1)(B, S, D)), 'w_r': _w((lambda *s: torch.randn(*s, dtype=torch.bfloat16) * 1.6)(D)), 'w_gate': _w((lambda *s: torch.randn(*s, dtype=torch.bfloat16) * 0.8)(D, F)), 'w_up': _w((lambda *s: torch.randn(*s, dtype=torch.bfloat16) * 0.8)(D, F)), 'w_down': _w((lambda *s: torch.randn(*s, dtype=torch.bfloat16) * 0.8)(F, D))}
    _B_in = _B_setup()
    return {'A_x': _A_in['x'], 'A_dft_seq_re': _A_in['dft_seq_re'], 'A_dft_seq_im': _A_in['dft_seq_im'], 'A_dft_hid_re': _A_in['dft_hid_re'], 'A_dft_hid_im': _A_in['dft_hid_im'], 'B_x': _B_in['x'], 'B_w_r': _B_in['w_r'], 'B_w_gate': _B_in['w_gate'], 'B_w_up': _B_in['w_up'], 'B_w_down': _B_in['w_down'], 'c': _w((lambda *s: torch.randn(*s, dtype=torch.bfloat16) * 0.1)(1, 1024) * 8.0), 'w_mod': _w((lambda *s: torch.randn(*s, dtype=torch.bfloat16) * 0.1)(1024, 6 * 1024) * 1024 ** (-0.5) * 8.0)}

def reference(A_x, A_dft_seq_re, A_dft_seq_im, A_dft_hid_re, A_dft_hid_im, B_x, B_w_r, B_w_gate, B_w_up, B_w_down, c, w_mod):

    def _A_ref(x, dft_seq_re, dft_seq_im, dft_hid_re, dft_hid_im):
        a = torch.einsum('bsd,ed->bse', x, dft_hid_re)
        b = torch.einsum('bsd,ed->bse', x, dft_hid_im)
        y = torch.einsum('ts,bse->bte', dft_seq_re, a) - torch.einsum('ts,bse->bte', dft_seq_im, b)
        return y

    def _B_ref(x, w_r, w_gate, w_up, w_down):
        B, S, D = x.shape
        C = 2
        r = x @ w_r
        idx = torch.argsort(-r, axis=-1)[:, :C]
        idx_e = (lambda x, n, axis: torch.repeat_interleave(x, n, dim=axis))(idx.reshape(B, C, 1), D, axis=2)
        x_sel = (lambda x, i, axis: torch.take_along_dim(x, i, dim=axis))(x, idx_e, axis=1)
        g = _silu(x_sel @ w_gate)
        u = x_sel @ w_up
        y = g * u @ w_down
        r_sel = _sigmoid((lambda x, i, axis: torch.take_along_dim(x, i, dim=axis))(r, idx, axis=1))
        y = y * r_sel.reshape(B, C, 1)
        b_off = (torch.arange(B) * S).reshape(B, 1)
        flat_idx = (idx + b_off).reshape(B * C)
        zero = (x * 0.0).reshape(B * S, D)
        upd = _scatter_add(zero, flat_idx, y.reshape(B * C, D), axis=0)
        return x + upd.reshape(B, S, D)
    x = A_x
    mod = (_silu(c) @ w_mod).reshape(c.shape[0], 6, 1, -1)
    sa, ca, ga, sb, cb, gb = (mod[:, 0].clone(), mod[:, 1].clone(), mod[:, 2].clone(), mod[:, 3].clone(), mod[:, 4].clone(), mod[:, 5].clone())
    h = _layernorm(x, axis=-1) * (1 + ca) + sa
    x = x + ga * _A_ref(h, A_dft_seq_re, A_dft_seq_im, A_dft_hid_re, A_dft_hid_im)
    h = _layernorm(x, axis=-1) * (1 + cb) + sb
    x = x + gb * _B_ref(h, B_w_r, B_w_gate, B_w_up, B_w_down)
    return x
```

```cpp
#include <hip/hip_runtime.h>
#include <hip/hip_bf16.h>
#include <stdint.h>
#include <utility>

typedef unsigned short u16;
typedef unsigned long long u64;
using bf16x8 = __attribute__((ext_vector_type(8))) short;
using bf16v8 = __attribute__((ext_vector_type(8))) __bf16;
using f32x4 = __attribute__((ext_vector_type(4))) float;
using f32x2 = __attribute__((ext_vector_type(2))) float;
using u32x4 = __attribute__((ext_vector_type(4))) unsigned;
using u32x2 = __attribute__((ext_vector_type(2))) unsigned;

constexpr int S = 2048, D = 1024, F = 3584;

__device__ __forceinline__ float bf2f(unsigned v) { return __uint_as_float(v << 16); }
__device__ __forceinline__ float bflo(unsigned v) { return __uint_as_float(v << 16); }
__device__ __forceinline__ float bfhi(unsigned v) { return __uint_as_float(v & 0xffff0000u); }
__device__ __forceinline__ unsigned f2bf(float f) {
  __hip_bfloat16 b = __float2bfloat16(f);
  return (unsigned)*reinterpret_cast<u16*>(&b);
}
__device__ __forceinline__ float rbf(float f) { return bf2f(f2bf(f)); }
using bf16v2 = __attribute__((ext_vector_type(2))) __bf16;
__device__ __forceinline__ unsigned pack2(float lo, float hi) {
  return __builtin_bit_cast(unsigned, __builtin_convertvector(f32x2{lo, hi}, bf16v2));
}

template <int CTRL>
__device__ __forceinline__ float dpp_f(float v) {
  return __int_as_float(__builtin_amdgcn_mov_dpp(__float_as_int(v), CTRL, 0xf, 0xf, true));
}
template <int CTRL>
__device__ __forceinline__ float dpp_add(float v) { return v + dpp_f<CTRL>(v); }
__device__ __forceinline__ float row_sum(float v) { return dpp_add<0x140>(dpp_add<0x141>(dpp_add<0x4E>(dpp_add<0xB1>(v)))); }
template <int CTRL>
__device__ __forceinline__ unsigned dpp_u(unsigned v) { return (unsigned)__builtin_amdgcn_mov_dpp((int)v, CTRL, 0xf, 0xf, true); }
__device__ __forceinline__ unsigned wave_max_u(unsigned v) {
  v = max(v, dpp_u<0xB1>(v));
  v = max(v, dpp_u<0x4E>(v));
  v = max(v, dpp_u<0x141>(v));
  v = max(v, dpp_u<0x140>(v));
  const unsigned a = (unsigned)__builtin_amdgcn_readlane((int)v, 0), b = (unsigned)__builtin_amdgcn_readlane((int)v, 16);
  const unsigned c = (unsigned)__builtin_amdgcn_readlane((int)v, 32), d = (unsigned)__builtin_amdgcn_readlane((int)v, 48);
  return max(max(a, b), max(c, d));
}
__device__ __forceinline__ u64 mk64(unsigned hi, unsigned lo) { return ((u64)hi << 32) | lo; }
__device__ __forceinline__ float swap32_add(float a, float b) {
  auto r = __builtin_amdgcn_permlane32_swap(__float_as_uint(a), __float_as_uint(b), false, false);
  return __uint_as_float(r[0]) + __uint_as_float(r[1]);
}
__device__ __forceinline__ float swap16_add(float a, float b) {
  auto r = __builtin_amdgcn_permlane16_swap(__float_as_uint(a), __float_as_uint(b), false, false);
  return __uint_as_float(r[0]) + __uint_as_float(r[1]);
}
__device__ __forceinline__ float ar4(float v) {
  const float t = swap32_add(v, v);
  return swap16_add(t, t);
}
__device__ __forceinline__ float wave_sum_s(float v) {
  v = row_sum(v);
  v += __int_as_float(__builtin_amdgcn_update_dpp(0, __float_as_int(v), 0x142, 0xa, 0xf, false));
  v += __int_as_float(__builtin_amdgcn_update_dpp(0, __float_as_int(v), 0x143, 0xc, 0xf, false));
  return __int_as_float(__builtin_amdgcn_readlane(__float_as_int(v), 63));
}
__device__ __forceinline__ float silu_bf(float c) { return rbf(c * __builtin_amdgcn_rcpf(1.0f + __expf(-c))); }
__device__ __forceinline__ float dsum(unsigned v, float a) {
  return __builtin_amdgcn_fdot2_f32_bf16(__builtin_bit_cast(bf16v2, v), __builtin_bit_cast(bf16v2, 0x3f803f80u), a, false);
}
template <class Fn, int... I>
__device__ __forceinline__ void sfor_(Fn&& f, std::integer_sequence<int, I...>) { (f(std::integral_constant<int, I>{}), ...); }
template <int N, class Fn>
__device__ __forceinline__ void sfor(Fn&& f) { sfor_(f, std::make_integer_sequence<int, N>{}); }
constexpr unsigned SENT = 0xFFFFFFFFu;
__device__ __forceinline__ f32x2 unpk(unsigned v) { return f32x2{bflo(v), bfhi(v)}; }

constexpr size_t WS_EPO = 0;
constexpr size_t WS_XFL = 4096;
constexpr size_t WS_HFL = 40960;
constexpr size_t WS_SFL = WS_HFL + 8192;
constexpr size_t WS_MOD = 65536;
constexpr size_t WS_Q = WS_MOD + 32768;
constexpr size_t WS_ARM = WS_Q + 8192;
constexpr size_t WS_RS = WS_ARM;
constexpr size_t WS_KEY = WS_RS + 16384;
constexpr size_t WS_HDR = WS_KEY + 16384;
constexpr size_t WS_UFL = WS_HDR + 4096;
constexpr size_t WS_DON = WS_UFL + 98304;
constexpr size_t WS_STAT = WS_DON + 12288;
constexpr size_t WS_CAND = WS_STAT + 262144;
constexpr size_t WS_PART = WS_CAND + 524288;
constexpr size_t WS_XAF = WS_PART + 458752;
constexpr size_t WS_WAK = WS_XAF + 24576;
static_assert(WS_WAK + 8192 <= WS_ARM + 448 * 3328, "armed region");
constexpr size_t WS_XCH = 2u << 20;
constexpr size_t WS_H = 8u << 20;
constexpr size_t WS_WH = 12u << 20;
constexpr size_t WS_U = 16u << 20;
constexpr size_t WS_DBG = 64u << 20;
#define NEWDESIGN 1

#define PCAP (1 << 20)
#define PDBG(it)
#define TS(i)
#define TSCAP()
#define TSC0()
#define TSC1(i)
#define TSD(i)
#define TSE()
__device__ __forceinline__ void st16_wt(void* p, u32x4 v) {
  asm volatile("global_store_dwordx4 %0, %1, off sc1\n\ts_nop 1" ::"v"(p), "v"(v) : "memory");
}
#define ST16(voff, v, base, off) asm volatile("global_store_dwordx4 %0, %1, %2 offset:%3 sc1\n\ts_nop 1" ::"v"(voff), "v"(v), "s"(base), "n"(off) : "memory")
#define ST4(p, v) __hip_atomic_store(p, v, __ATOMIC_RELAXED, __HIP_MEMORY_SCOPE_AGENT)
#define WAIT_V(n) asm volatile("s_waitcnt vmcnt(%0)" ::"n"(n) : "memory")
#define WAIT_L(n) asm volatile("s_waitcnt lgkmcnt(%0)" ::"n"(n) : "memory")
#define BARRIER() asm volatile("s_barrier" ::: "memory")
#define LBAR() asm volatile("s_waitcnt lgkmcnt(0)\n\ts_barrier" ::: "memory")
#define DSR(dst, addr, off) asm volatile("ds_read_b128 %0, %1 offset:%2" : "=v"(dst) : "v"(addr), "n"(off) : "memory")
#define MFMA(c, a, b) asm volatile("v_mfma_f32_16x16x32_bf16 %0, %1, %2, %0" : "+v"(c) : "v"(a), "v"(b))
#define MFMA0(c, a, b) asm volatile("v_mfma_f32_16x16x32_bf16 %0, %1, %2, 0" : "=&v"(c) : "v"(a), "v"(b))
#define GLDSP(m0v, voff, gbase, pol) \
  asm volatile("s_mov_b32 m0, %0\n\ts_nop 0\n\tglobal_load_lds_dwordx4 %1, %2" pol ::"s"(m0v), "v"(voff), "s"(gbase) : "memory")
#define GLDS(m0v, voff, gbase) GLDSP(m0v, voff, gbase, "")
#define WPOL " nt"
#define LDX(dst, voff, base, off) asm volatile("global_load_dwordx4 %0, %1, %2 offset:%3" : "=v"(dst) : "v"(voff), "s"(base), "n"(off) : "memory")
#define LDXS(dst, voff, base, off) asm volatile("global_load_dwordx4 %0, %1, %2 offset:%3 sc1" : "=v"(dst) : "v"(voff), "s"(base), "n"(off) : "memory")
#define BF8(v) __builtin_bit_cast(bf16v8, v)
#define MM(a, b, c) __builtin_amdgcn_mfma_f32_16x16x32_bf16(BF8(a), BF8(b), c, 0, 0, 0)
#define LSPIN(v, p, cond)                                   \
  _Pragma("unroll 1") for (int it_ = 0; it_ < (1 << 26); ++it_) { \
    v = __atomic_load_n(p, __ATOMIC_RELAXED);               \
    if (cond) break;                                        \
    __builtin_amdgcn_s_sleep(1);                            \
  }
#define LSPIN0(v, p, cond)                                  \
  _Pragma("unroll 1") for (int it_ = 0; it_ < (1 << 28); ++it_) { \
    v = __atomic_load_n(p, __ATOMIC_RELAXED);               \
    if (cond) break;                                        \
  }
#define W6(a) "+v"(a[0]), "+v"(a[1]), "+v"(a[2]), "+v"(a[3]), "+v"(a[4]), "+v"(a[5])
#define W8(a) W6(a), "+v"(a[6]), "+v"(a[7])
#define PFL(ptr) asm volatile("s_mov_b32 m0, %0\n\ts_nop 0\n\tglobal_load_lds_dword %1, off" ::"s"(pfl), "v"(ptr) : "memory")
using s16x4 = __attribute__((ext_vector_type(4))) short;
__device__ __forceinline__ u32x4 trd(unsigned a) {
  const u32x2 lo = __builtin_bit_cast(u32x2, __builtin_amdgcn_ds_read_tr16_b64_v4i16((__attribute__((address_space(3))) s16x4*)(size_t)a));
  const u32x2 hi = __builtin_bit_cast(u32x2, __builtin_amdgcn_ds_read_tr16_b64_v4i16((__attribute__((address_space(3))) s16x4*)(size_t)(a + 512)));
  return u32x4{lo[0], lo[1], hi[0], hi[1]};
}
__device__ __forceinline__ int key_row(unsigned k) { return (int)((0xffffu - (k & 0xffffu)) & 2047u); }
__device__ __forceinline__ float key_val(unsigned k) {
  const unsigned ord = k >> 16;
  return bf2f(ord ^ ((ord & 0x8000u) ? 0x8000u : 0xffffu));
}
__device__ __forceinline__ unsigned nsent(float v) { return min(__float_as_uint(v), SENT - 1u); }
__device__ __forceinline__ f32x4 lo4(u32x4 q) { return f32x4{bflo(q[0]), bfhi(q[0]), bflo(q[1]), bfhi(q[1])}; }
__device__ __forceinline__ f32x4 hi4(u32x4 q) { return f32x4{bflo(q[2]), bfhi(q[2]), bflo(q[3]), bfhi(q[3])}; }
__device__ __forceinline__ u32x4 pk8(f32x4 a, f32x4 b) { return u32x4{pack2(a[0], a[1]), pack2(a[2], a[3]), pack2(b[0], b[1]), pack2(b[2], b[3])}; }
__device__ __forceinline__ u32x4 nsent4(u32x4 v) { return u32x4{min(v[0], SENT - 1u), min(v[1], SENT - 1u), min(v[2], SENT - 1u), min(v[3], SENT - 1u)}; }
__device__ __forceinline__ unsigned max4(u32x4 v) { return max(max(v[0], v[1]), max(v[2], v[3])); }
__device__ __forceinline__ unsigned max2(u32x2 v) { return max(v[0], v[1]); }
__device__ __forceinline__ unsigned hi2(u32x2 v) { return (v[0] >> 16) | (v[1] & 0xffff0000u); }
#define TOP2(CTRL)                                                  \
  do {                                                              \
    const unsigned o1 = dpp_u<CTRL>(t1), o2 = dpp_u<CTRL>(t2);      \
    t2 = max(min(t1, o1), max(t2, o2));                             \
    t1 = max(t1, o1);                                               \
  } while (0)
#define POLL _Pragma("unroll 1") for (int it = 0; it < PCAP; ++it)
#define OPT_RD 10
#define OPT_RR 2
#define OPT_SD 18
#define OPT_KS 2
#define P_D3 16
#define P_D4 2
#define U_D 32
#define XJ0 2
#define XJ1 5
#define XJ2 9
#define XJ3 10
#define TP_P0 309
#define TP_XL 346
#define CPF_K 24
#define CPF_N 10
#define HP4 0
#define HP5 1
#define HP6 2
#define HP7 3
#define CPF_B 10
#define NOEXP 0
#define NOARM 0
#define HST 32
#define WFO 2048
#define XST 32
#define UST 32
#define OPT_KD 80
#define KORD 1
#define UF_AT 11
#define P_NT 5
#define P_D5 4
#define PF23 1
#define MQ_AT 31
#define MP_AT 16
#define P_AT 6
#define H_AT 24
#define W_NQ 0
#define XQ_AT 24
#define U_R 1
#define P_E 0
#define NORS 0
#define LSLP 0
#define Q_AT 11
#define B_VAT 41
constexpr int SL = 22528, NS = 6, MISC = NS * SL;
constexpr int ah(int e) { return e < 7 ? 4 : e < 28 ? 5 + (e - 7) % 3 : 4 + (e - 28) % 4; }
constexpr int ap(int e) { return e < 7 ? e : e < 28 ? (e - 7) / 3 : 7 + (e - 28) / 4; }
constexpr int E1 = 286, E3 = E1 + 66, NE = E3 + 6;
constexpr int eh(int e) { return e < E1 ? e / 11 : 26 + (e - E1) % 6; }
constexpr int ep(int e) { return e < E1 ? e % 11 : e < E3 ? (e - E1) / 6 : 5 + (e - E3); }

__global__ __launch_bounds__(512, 2) void k_main(const u16* __restrict__ x, char* ws, const u16* __restrict__ Y0, const u16* __restrict__ Y1,
                                              const u16* __restrict__ cvec, const u16* __restrict__ wmod, const u16* __restrict__ X0,
                                              const u16* __restrict__ X1, const u16* __restrict__ wr_, const u16* __restrict__ wg,
                                              const u16* __restrict__ wu, const u16* __restrict__ wd, u16* __restrict__ out) {
  constexpr int STG = 114688;
  __shared__ __attribute__((aligned(1024))) char shm[MISC + 16384];
  const int b = blockIdx.x, xcd = b & 7, j8 = b >> 3;
  const int cc = xcd >> 2, qx = xcd & 3;
  const int tid = threadIdx.x, lane = tid & 63;
  const int wid = __builtin_amdgcn_readfirstlane(tid >> 6);
  const unsigned lds0 = (unsigned)(size_t)(__attribute__((address_space(3))) char*)shm;
  const char* cpf;
  {
    unsigned long long pc;
    unsigned len;
    asm volatile("s_getpc_b64 %0\n.Lcpf_here:\n\ts_mov_b32 %1, .Lcpf_e1-.Lcpf_here\n\ts_max_u32 %1, %1, .Lcpf_e2-.Lcpf_here" : "=s"(pc), "=s"(len)::"scc");
    cpf = reinterpret_cast<const char*>(pc) + min((unsigned)((lane * 32 + j8) * 128), len);
  }
  const unsigned pfl = lds0 + MISC + 16128;
  if (wid == 4 && lane < CPF_N) PFL(cpf);
  u32x4 xv[2];
  {
    const unsigned xo = (unsigned)((b * 8 + wid) * D + lane * 8) * 2;
    asm volatile("global_load_dwordx4 %0, %2, %3\n\tglobal_load_dwordx4 %1, %2, %3 offset:1024" : "=&v"(xv[0]), "=&v"(xv[1]) : "v"(xo), "s"(x) : "memory");
  }
  asm volatile("" ::"s"(ws), "s"(Y0), "s"(Y1), "s"(X0), "s"(X1));
  unsigned* xfl = reinterpret_cast<unsigned*>(ws + WS_XFL);
  unsigned* hfl = reinterpret_cast<unsigned*>(ws + WS_HFL);
  unsigned* modv = reinterpret_cast<unsigned*>(ws + WS_MOD);
#define SKEWPT()
  const int g4 = lane >> 4, l16 = lane & 15;
  u64 tag0, tag;
  asm volatile("global_load_dwordx2 %0, %1, %2 sc1" : "=v"(tag0) : "v"(0u), "s"(ws) : "memory");
  const char* Yx = reinterpret_cast<const char*>(cc ? Y1 : Y0) + (size_t)qx * 2097152;
#define PFG(r16, g) (Yx + (size_t)(((r16) * 16 + l16) * 4096 + g4 * 128 + ((((g) + qx) & 7) << 9)))
  unsigned ta = lds0 + STG + ((lane >> 2) & 3) * 2368 + (wid * 128 + (lane >> 5) * 32 + (lane & 3) * 8 + (g4 & 1) * 4) * 2;
  unsigned oo = (unsigned)((wid * 4 + (lane >> 5)) * 131072 + (b >> 2) * 2048 + (g4 & 1) * 1024 + ((b & 3) * 16 + l16) * 16), oo2 = oo + 262144;
  unsigned wa = lds0 + STG + wid * 2368 + lane * 16;
  asm volatile("" : "+v"(ta), "+v"(oo), "+v"(oo2), "+v"(wa));
  const int hsl = (b & 32) + xcd * 4 + (j8 & 3);
  const char* hp = reinterpret_cast<const char*>((b & 32) ? X1 : X0) + (size_t)(hsl & 31) * 65536 + wid * 8192;
#define WHLD(i0)                                                                                                                  \
  _Pragma("unroll") for (int i = i0; i < i0 + 4; ++i)                                                                             \
      GLDS(lds0 + wid * 8192 + i * 1024, (unsigned)((lane ^ ((i >> 1) | ((wid & 2) << 1))) * 16), hp + i * 1024)
  if (wid >= 6) PFL(PFG(j8, wid & 1));
  if (j8 >= 24) {
    WHLD(0);
    WAIT_V(5);
  } else if (wid >= 6) {
    WAIT_V(2);
  } else {
    WAIT_V(1);
  }
  {
    asm volatile("; AUDIT_OK %0 %1" : "+v"(xv[0]), "+v"(xv[1])::"memory");
    TS(32);
    const float s0 = dsum(xv[0][3], dsum(xv[0][2], dsum(xv[0][1], dsum(xv[0][0], 0.f))));
    const float s1 = dsum(xv[1][3], dsum(xv[1][2], dsum(xv[1][1], dsum(xv[1][0], 0.f))));
    const float mf = wave_sum_s(s0 + s1) * (1.0f / D), m = rbf(mf);
    const f32x4 d0 = lo4(xv[0]) - m, d1 = hi4(xv[0]) - m, d2 = lo4(xv[1]) - m, d3 = hi4(xv[1]) - m;
    const f32x4 q = (d0 * d0 + d1 * d1) + (d2 * d2 + d3 * d3);
    const float vf = wave_sum_s((q[0] + q[1]) + (q[2] + q[3])) * (1.0f / D) - (mf - m) * (mf - m);
    const float rden = __builtin_amdgcn_rcpf(rbf(__builtin_amdgcn_sqrtf(rbf(rbf(vf) + 1e-5f))));
    asm volatile("ds_write_b128 %0, %1" ::"v"(wa), "v"(pk8(d0 * rden, d1 * rden)) : "memory");
    asm volatile("ds_write_b128 %0, %1 offset:1024" ::"v"(wa), "v"(pk8(d2 * rden, d3 * rden)) : "memory");
    LBAR();
    TS(34);
    u32x2 t[4];
    asm volatile(
        "ds_read_b64_tr_b16 %0, %4\n\tds_read_b64_tr_b16 %1, %4 offset:%5\n\t"
        "ds_read_b64_tr_b16 %2, %4 offset:128\n\tds_read_b64_tr_b16 %3, %4 offset:%6\n\ts_waitcnt lgkmcnt(0)"
        : "=&v"(t[0]), "=&v"(t[1]), "=&v"(t[2]), "=&v"(t[3])
        : "v"(ta), "n"(4 * 2368), "n"(4 * 2368 + 128)
        : "memory");
    asm volatile("global_store_dwordx4 %0, %2, %4 sc1\n\tglobal_store_dwordx4 %1, %3, %4 sc1\n\ts_nop 1" ::"v"(oo), "v"(oo2),
                 "v"(u32x4{t[0][0], t[0][1], t[1][0], t[1][1]}), "v"(u32x4{t[2][0], t[2][1], t[3][0], t[3][1]}), "s"(ws + WS_H)
                 : "memory");
  }
#define PBAR(n)                                                                                                          \
  do {                                                                                                                   \
    asm volatile("s_waitcnt vmcnt(%2)\n\tv_lshl_add_u64 %0, %1, 0, 1" : "=v"(tag) : "v"(tag0), "n"(n) : "memory");       \
    TS(36);                                                                                                              \
    BARRIER();                                                                                                           \
    TS(37);                                                                                                              \
  } while (0)
#define XFLAG() do { if (tid == 384) ST4(reinterpret_cast<u64*>(xfl + b * XST), tag); } while (0)
  if (j8 < 24) {
    const int mt = (j8 * 11) >> 5, nt = j8 - mt * 3, grp = xcd * 3 + nt;
#define FROW(f) (nt * 176 + (f) * 16 - (nt == 2 && (f) > 5 ? 16 : 0))
#define PROW(p) FROW(cc ? 10 - (p) : (p))
    constexpr int SCA = MISC, TGA = MISC + 512, TV = MISC + 1024, TSUM = MISC + 1792, FLG = MISC + 1856, SCW = MISC + 2048, RKA = MISC + 3072,
                  RKB = MISC + 5120, RKZ = MISC + 6656, IDT = MISC + 7168;
    unsigned* flg = reinterpret_cast<unsigned*>(shm + FLG);
    unsigned* ufl = reinterpret_cast<unsigned*>(ws + WS_UFL) + grp * 1024;
    const char* Ug = ws + WS_U + (size_t)grp * (16 * SL);
    const unsigned perm = KORD ? (unsigned)mt | (((0x76543210u & ((1u << (4 * mt)) - 1u)) << 4) | (0x76543210u & (0xfffffff0u << (4 * mt))))
                               : __builtin_rotateright32(0x76543210u, 4 * mt);
#define KT(i) ((perm >> (4 * (i))) & 7u)
    const int kk = wid < 4 ? wid : wid - 2;
    const bool act = (wid < 4 || wid >= 6) && (kk || (cc == 0 && nt < 2));
    const int rg = qx * 32 + (PROW(5 + kk) >> 4);
    u32x4 pv[6];
    f32x4 acc[2][11], ga[2], x5[2];
    bf16x8 idt[2];
    unsigned spo = (unsigned)(((rg * 8 + g4 * 2) * 16 + l16) * 16);
    u32x4 sr[2];
#define STLD() asm volatile("global_load_dwordx4 %0, %2, %3 sc1\n\tglobal_load_dwordx4 %1, %2, %3 offset:256 sc1" : "=&v"(sr[0]), "=&v"(sr[1]) : "v"(spo), "s"(ws + WS_STAT) : "memory")
    const char* xr = ws + WS_XCH + (size_t)(b * 4 + (wid & 3)) * 6144 + 3072;
#define XCHLD()                                                                                                       \
  asm volatile(                                                                                                       \
      "global_load_dwordx4 %0, %6, %7 offset:2048 sc1\n\tglobal_load_dwordx4 %1, %6, %7 offset:1024 sc1\n\t"          \
      "global_load_dwordx4 %2, %6, %7 sc1\n\tglobal_load_dwordx4 %3, %6, %7 offset:-1024 sc1\n\t"                     \
      "global_load_dwordx4 %4, %6, %7 offset:-2048 sc1\n\tglobal_load_dwordx4 %5, %6, %7 offset:-3072 sc1"            \
      : "=&v"(pv[0]), "=&v"(pv[1]), "=&v"(pv[2]), "=&v"(pv[3]), "=&v"(pv[4]), "=&v"(pv[5])                            \
      : "v"(lane * 16), "s"(xr)                                                                                       \
      : "memory")
    if (wid < 4) {
      bf16x8 X[4][2][2], Y[8];
      u32x4 xq[6];
      const unsigned xo0 = lane * 16 + 4096;

#define XLD(dst, XP, XH, i) LDXS(dst, xo0, XP, ((XH)&3) * 2048 + (i)*1024 - 4096)
      int xt = lane < 16 ? (int)WS_H + (mt * 4 + wid) * 131072 + (((qx * 2 + lane) & 15) << 13) : (int)WS_WH + (cc * 32 + mt * 4 + wid) * 65536 + (KT(lane & 7) << 13);
      asm volatile("" : "+v"(xt));
#define XT(j) (ws + (unsigned)__builtin_amdgcn_readlane(xt, j))
#define WP(h) XT(16 + ((h) >> 2))
#define XS(h) X[((h) >> 1) & 3][(h)&1]
      {
        unsigned ye = lds0 + l16 * 128 + (g4 ^ (lane & 7)) * 16, yo = ye ^ 64;
#define HALFA(HB, ACUR, ANXT, XP)                                                                        \
  sfor<11>([&](auto pp) __attribute__((always_inline)) {                                                 \
    constexpr int p = decltype(pp)::value, n = (HB)*11 + p;                                              \
    (void)acc, (void)X, (void)Y, (void)(ACUR), (void)(ANXT), (void)xo0, (void)(XP);                      \
    if constexpr ((n & 1) == 0) WAIT_L(5);                                                               \
    MFMA(acc[0][p], XS(HB)[0], Y[n & 7]);                                                                \
    if constexpr (p < 4) DSR(Y[(n + 7) & 7], ACUR, (p + 7) * 2048);                                      \
    else DSR(Y[(n + 7) & 7], ANXT, (p - 4) * 2048);                                                      \
    MFMA(acc[1][p], XS(HB)[1], Y[n & 7]);                                                                \
    if constexpr (p == (((HB)&1) ? XJ2 : XJ0)) XLD(XS((HB) + 7)[0], XP, (HB) + 7, 0);                    \
    if constexpr (p == (((HB)&1) ? XJ3 : XJ1)) XLD(XS((HB) + 7)[1], XP, (HB) + 7, 1);                    \
  })
        const char* xpa = XT(0);
        const char* xpb = XT(1);
#pragma unroll
        for (int i = 0; i < 22; ++i) {
          acc[i / 11][i % 11] = f32x4{0.f, 0.f, 0.f, 0.f};
          asm volatile("" : "+v"(acc[i / 11][i % 11]));
        }
        __builtin_amdgcn_s_setprio(1);
        PBAR(0);
        if (wid == 3) {
          const unsigned* fp = xfl + (qx * 32 + lane) * XST;
          if (P_D3) __builtin_amdgcn_s_sleep(P_D3);
          POLL { PDBG(it);
            u64 f;
            asm volatile("global_load_dwordx2 %0, %1, off sc1\n\ts_waitcnt vmcnt(0)" : "=&v"(f) : "v"(fp) : "memory");
            if (__all(f == tag)) break;
            __builtin_amdgcn_s_sleep(P_D4);
          }
        }
        TS(38);
        BARRIER();
        TS(39);
#pragma unroll
        for (int h = 0; h < 7; ++h) {
          XLD(XS(h)[0], h < 4 ? xpa : xpb, h, 0);
          XLD(XS(h)[1], h < 4 ? xpa : xpb, h, 1);
        }
#pragma unroll
        for (int j = 0; j < 6; ++j) DSR(xq[j], lds0 + 5 * SL + l16 * 256 + (((wid * 4 + g4) ^ (lane & 7)) << 4), j * 4096);
#pragma unroll
        for (int j = 0; j < 7; ++j) DSR(Y[j], ye, j * 2048);
        unsigned so = 0, a0 = ye, yb[2];
        int u = 2;
#define STEP2(S0)                                                            \
  do {                                                                       \
    xpa = xpb;                                                               \
    xpb = XT(u);                                                             \
    ++u;                                                                     \
    sfor<2>([&](auto ss) __attribute__((always_inline)) {                    \
      constexpr int s = (S0) + decltype(ss)::value;                          \
      const unsigned a1 = yo + so;                                           \
      so = so == (NS - 1) * SL ? 0 : so + SL;                                \
      WAIT_V(12);                                                            \
      HALFA(2 * s, a0, a1, ((s & 1) ? xpb : xpa));                           \
      a0 = ye + so;                                                          \
      BARRIER();                                                             \
      WAIT_V(12);                                                            \
      HALFA(2 * s + 1, a1, a0, xpb);                                         \
    });                                                                      \
  } while (0)
#define YRD(e) DSR(Y[(e)&7], yb[((32 + eh(e) / 2) % NS) / 2 - 1], ((32 + eh(e) / 2) & 1) * SL + ep(e) * 2048 + (eh(e) & 1) * 1024)
        const char* xw = ws + WS_XCH + (size_t)((b ^ 4) * 4 + wid) * 6144 + 3072;
        unsigned rka, rkb;
        u32x4 ov;
        bf16x8 qf[2];
        auto BP = [&](auto nn) __attribute__((always_inline)) {
          constexpr int n = decltype(nn)::value, h = eh(n), p = ep(n), e = n + 7, m = n - E1 - 7;
          (void)ga, (void)qf, (void)idt, (void)rka, (void)rkb, (void)xq, (void)ov, (void)xw, (void)x, (void)pv, (void)xr, (void)ufl;
          (void)acc, (void)X, (void)Y, (void)yb, (void)xo0, (void)xt, (void)ws, (void)lane, (void)mt, (void)nt, (void)cc, (void)lds0, (void)wid, (void)g4, (void)perm;
          TSCAP();
          if constexpr (n == UF_AT) {
            TS(45);
            WAIT_V(n < 9 ? (n ? n - 1 : 0) : 8);
            if (lane == 0) ST4(ufl + (mt * 4 + wid) * UST, 1u);
            TS(46);
          }
          if constexpr (n == 37) TS(48);
          if constexpr (n == 37 || n == 275) BARRIER();
          if constexpr (n == 37) TS(4);
          if constexpr (n == E1) { TS(3); SKEWPT(); }
          if constexpr (n < E3 && p == 0) WAIT_V(n == 0 ? 17 : h <= 25 ? 12 : 2 * (31 - h));
          if constexpr (n == TP_XL) XCHLD();
          if constexpr ((n & 1) == 0) WAIT_L(e < NE ? 5 : NE - 2 - n);
          if constexpr (n < 11) MFMA0(acc[0][p], XS(h)[0], Y[n & 7]);
          else if constexpr (n < E3) MFMA(acc[0][p], XS(h)[0], Y[n & 7]);
          else MFMA(acc[0][p], qf[0], Y[n & 7]);
          if constexpr (e == E3) {
            DSR(qf[0], rka, 0);
            DSR(qf[1], rka, 256);
            DSR(ga[0], lds0 + TGA + wid * 128 + g4 * 32, 0);
            DSR(ga[1], lds0 + TGA + wid * 128 + g4 * 32, 16);
            DSR(idt[0], yb[1], IDT - 4 * SL);
            DSR(idt[1], yb[1], IDT - 4 * SL + 1024);
          }
          if constexpr (e < E3) YRD(e);
          else if constexpr (e < NE) DSR(Y[e & 7], rkb, (e - E3) * 256);
          if constexpr (n < 11) MFMA0(acc[1][p], XS(h)[1], Y[n & 7]);
          else if constexpr (n < E3) MFMA(acc[1][p], XS(h)[1], Y[n & 7]);
          else MFMA(acc[1][p], qf[1], Y[n & 7]);
          if constexpr (n >= 1 && n < 9) XLD(XS(4 + (n - 1) / 2)[(n - 1) & 1], WP(4), (n - 1) / 2, (n - 1) & 1);
          if constexpr (h >= 1 && h <= 24) {
            if constexpr (p == ((h & 1) ? XJ2 : XJ0)) XLD(XS(h + 7)[0], WP(h + 7), h + 7, 0);
            if constexpr (p == ((h & 1) ? XJ3 : XJ1)) XLD(XS(h + 7)[1], WP(h + 7), h + 7, 1);
          }
          if constexpr (m >= 0 && m < 36) {
            constexpr int q = m / 6, r = m % 6;
            if constexpr (r < 2) {
              asm volatile("v_cvt_pk_bf16_f32 %0, %2, %3\n\tv_cvt_pk_bf16_f32 %1, %4, %5" : "=&v"(ov[2 * r]), "=&v"(ov[2 * r + 1])
                           : "v"(acc[r][q][0]), "v"(acc[r][q][1]), "v"(acc[r][q][2]), "v"(acc[r][q][3]));
              if constexpr (r == 1) asm volatile("v_min_u32 %0, -2, %0\n\tv_min_u32 %1, -2, %1" : "+v"(ov[0]), "+v"(ov[1]));
            } else if constexpr (r == 2) {
              asm volatile("v_min_u32 %0, -2, %0\n\tv_min_u32 %1, -2, %1" : "+v"(ov[2]), "+v"(ov[3]));
              ST16(lane * 16, ov, xw, q * 1024 - 3072);
              if constexpr (q == 5) TS(10);
            } else if constexpr (r < 5 && q < 5) {
              constexpr int i = r - 3;
              asm volatile("v_lshlrev_b32 %0, 16, %4\n\tv_and_b32 %1, 0xffff0000, %4\n\tv_lshlrev_b32 %2, 16, %5\n\tv_and_b32 %3, 0xffff0000, %5"
                           : "=&v"(acc[i][q][0]), "=&v"(acc[i][q][1]), "=&v"(acc[i][q][2]), "=&v"(acc[i][q][3])
                           : "v"(xq[q][2 * i]), "v"(xq[q][2 * i + 1]));
            }
          }
        };
#pragma unroll 1
        for (int it = 0; it < 10; ++it) {
          STEP2(0);
          if (__builtin_expect(it == 7, 0)) {
            TS(57);
            f32x4 c0, c1;
            const char* up = Ug + (mt * 2 + (wid >> 1)) * SL + (wid & 1) * 1024 + 4096;
            const unsigned ul = lds0 + (2 + (wid >> 1)) * SL + (wid & 1) * 1024 + lane * 16;
            u32x4 uv;
            DSR(c0, lds0 + SCA + wid * 128 + g4 * 32, 0);
            DSR(c1, lds0 + SCA + wid * 128 + g4 * 32, 16);
#define UMUL(j, k) asm volatile("v_mul_f32 %0, %0, %1" : "+v"(acc[(k) >> 2][j][(k)&3]) : "v"(((k) >> 2 ? c1 : c0)[(k)&3]))
#define UCVT(j, k) asm volatile("v_cvt_pk_bf16_f32 %0, %1, %2" : "=v"(uv[k]) : "v"(acc[(k) >> 1][j][((k)&1) * 2]), "v"(acc[(k) >> 1][j][((k)&1) * 2 + 1]))
#define UEP(j, r)                                                                                                                          \
  do {                                                                                                                                     \
    if constexpr ((r) == 0) { UMUL(j, 0); UMUL(j, 1); }                                                                                    \
    if constexpr ((r) == 1) { UMUL(j, 2); UMUL(j, 3); UMUL(j, 4); }                                                                        \
    if constexpr ((r) == 2) { UMUL(j, 5); UMUL(j, 6); }                                                                                    \
    if constexpr ((r) == 3) { UMUL(j, 7); UCVT(j, 0); UCVT(j, 1); }                                                                        \
    if constexpr ((r) == 4) { UCVT(j, 2); UCVT(j, 3); }                                                                                    \
    if constexpr ((r) == 5)                                                                                                                \
      asm volatile("global_store_dwordx4 %0, %2, %3 offset:%4 sc1\n\tds_write_b128 %1, %2 offset:%5\n\ts_nop 0"                        \
                   ::"v"(lane * 16), "v"(ul), "v"(uv), "s"(up + ((j) >> 2) * 8192), "n"(((j)&3) * 2048 - 4096), "n"((j)*2048) : "memory"); \
  } while (0)
            sfor<44>([&](auto nn) __attribute__((always_inline)) {
              constexpr int n = decltype(nn)::value, h = ah(n), p = ap(n), e = n + 7, j = n < 32 ? (n - 11) / 3 : 7 + (n - 33) / 4, r = n < 32 ? (n - 11) % 3 : (n - 33) % 4;
              constexpr bool ue = n >= 11 && n != 32 && r < 3;
              (void)up, (void)ul, (void)acc, (void)X, (void)Y, (void)ye, (void)yo, (void)c0, (void)c1, (void)xo0, (void)xpb, (void)lane, (void)uv;
              if constexpr (p == 0) WAIT_V(n < 8 ? 12 : 2 * (13 - n));
              if constexpr ((n & 1) == 0) WAIT_L(e < 44 ? 5 : 42 - n);
              MFMA(acc[0][p], XS(h)[0], Y[(n + 4) & 7]);
              if constexpr (e < 44) DSR(Y[(e + 4) & 7], (ah(e) & 1) ? yo : ye, ((ah(e) >> 1) & 1) * SL + ap(e) * 2048);
              if constexpr (ue) UEP(j, 2 * r);
              MFMA(acc[1][p], XS(h)[1], Y[(n + 4) & 7]);
              if constexpr (n < 2) XLD(XS(3)[n], xpb, 3, n);
              if constexpr (ue) UEP(j, 2 * r + 1);
            });
            TS(0);
            SKEWPT();
            asm volatile("s_nop 4");
            UEP(10, 0); UEP(10, 1); UEP(10, 2); UEP(10, 3); UEP(10, 4); UEP(10, 5);
            ye = lds0 + lane * 16;
            yo = ye + 1024;
            so = 4 * SL;
            a0 = ye + 4 * SL;
            yb[0] = ye + 2 * SL;
            yb[1] = a0;
            asm volatile("" : "+v"(yb[0]), "+v"(yb[1]), "+v"(ye), "+v"(yo));
            LBAR();
            TS(44);
            sfor<7>([&](auto ee) __attribute__((always_inline)) { (void)Y, (void)yb; YRD(decltype(ee)::value); });
            sfor<44>(BP);
            u = 19;
            xpb = WP(8);
          }
          STEP2(2);
        }
        TS(2);
        yb[0] = a0;
        yb[1] = ye + 4 * SL;
        asm volatile("" : W6(xq));
        rka = lds0 + (g4 ? RKZ : RKA + wid * 512 + l16 * 16);
        rkb = lds0 + RKB + l16 * 16;
        sfor<NE - 264>([&](auto nn) __attribute__((always_inline)) { BP(std::integral_constant<int, 264 + decltype(nn)::value>{}); });
        __builtin_amdgcn_s_setprio(0);
        TSC1(59);
        TS(13);
        SKEWPT();
        x5[0] = lo4(xq[5]);
        x5[1] = hi4(xq[5]);
        asm volatile("" : "+v"(x5[0]), "+v"(x5[1]), "+v"(spo));
      }
    } else if (wid < 6) {
      const int hw = wid - 4;
      __builtin_amdgcn_s_setprio(2);
      unsigned voy[11];
#pragma unroll
      for (int i = 0; i < 11; ++i) voy[i] = (unsigned)((PROW(i) + hw * 8 + (lane >> 3)) * 4096 + ((lane & 7) ^ (lane >> 3)) * 16);
      const unsigned lbase = lds0 + hw * 1024;
      auto stage = [&](int n, int slot) {
        const char* yp = Yx + (((n + qx * 4) & 31) << 7);
        const unsigned l = lbase + slot * SL;
#pragma unroll
        for (int i = 0; i < 11; ++i) GLDS(l + i * 2048, voy[i], yp);
      };
      const unsigned xo = (unsigned)(((qx * 512 + hw * 4 + g4) * D + mt * 128) * 2 + ((l16 ^ (hw * 4 + g4)) << 4));
      asm volatile("s_waitcnt vmcnt(0)" ::"v"(voy[0]), "v"(voy[10]), "v"(tag0), "v"(xo) : "memory");
      BARRIER();
#pragma unroll
      for (int i = 0; i < 12; ++i) GLDS(lds0 + 5 * SL + i * 2048 + hw * 1024, xo, x + (PROW(5 + i / 2) + (i & 1) * 8) * D);
#pragma unroll 1
      for (int s = 0; s < NS - 1; ++s) stage(s, s);
      WAIT_V(44);
      TS(38);
      BARRIER();
      int slot = NS - 1;
#pragma unroll 1
      for (int k = 1; k <= 27; ++k) {
        WAIT_V(33);
        BARRIER();
        if (hw == 1) TSD(k);
        if (LSLP) __builtin_amdgcn_s_sleep(LSLP);
        stage(k + 4, slot);
        slot = slot == NS - 1 ? 0 : slot + 1;
        if (k == CPF_K && hw == 1 && lane < CPF_N) PFL(cpf);
      }
      WAIT_V(33); BARRIER();
      WAIT_V(22); BARRIER();
      WAIT_V(0); BARRIER();
      const unsigned* ufp = ufl + (KT(min(lane >> 2, 6) + 1) * 4 + (lane & 3)) * UST;
      unsigned miss = ~0u;
      BARRIER();
      TS(47);
      if (U_D) __builtin_amdgcn_s_sleep(U_D);
      slot = 34 % NS;
#pragma unroll 1
      for (int n = 2; n < 16; ++n) {
        if (n == 4) { WAIT_V(11); BARRIER(); }
        if (n == 6) { WAIT_V(22); BARRIER(); }
        POLL { PDBG(it);
          if (((miss >> (2 * n - 4)) & 3u) == 0) break;
          if (it) __builtin_amdgcn_s_sleep(U_R);
          unsigned f;
          asm volatile("global_load_dword %0, %1, off sc1\n\ts_waitcnt vmcnt(0)" : "=&v"(f) : "v"(ufp) : "memory");
          miss = (unsigned)__builtin_amdgcn_ballot_w64(f == SENT);
        }
        if (n == 2) TS(48);
        const char* yp = Ug + (KT(n >> 1) * 2 + (n & 1)) * SL + hw * 11264;
        unsigned l = lds0 + slot * SL + hw * 11264;
        slot = slot == NS - 1 ? 0 : slot + 1;
        if (n == CPF_B && hw == 1 && lane < CPF_N) PFL(cpf);
        asm volatile("" : "+s"(yp), "+s"(l));
        if (n >= 8) {
          WAIT_V(33);
          BARRIER();
          if (hw == 1) TSD(n + 44);
          if (LSLP) __builtin_amdgcn_s_sleep(LSLP);
        }
#define G1(o) "global_load_lds_dwordx4 %0, %1 offset:" #o " sc1\n\t"
        asm volatile("s_mov_b32 m0, %2\n\ts_nop 0\n\t" G1(0) G1(1024) G1(2048) G1(3072) ::"v"(lane * 16), "s"(yp), "s"(l) : "memory");
        asm volatile("s_mov_b32 m0, %2\n\ts_nop 0\n\t" G1(0) G1(1024) G1(2048) G1(3072) ::"v"(lane * 16 + 4096), "s"(yp), "s"(l + 4096) : "memory");
        asm volatile("s_mov_b32 m0, %2\n\ts_nop 0\n\t" G1(0) G1(1024) G1(2048) ::"v"(lane * 16 + 8192), "s"(yp), "s"(l + 8192) : "memory");
      }
      WAIT_V(33);
      BARRIER();
      WAIT_V(0);
      BARRIER();
      if (b < 56 && tid == 256) ST4(reinterpret_cast<unsigned*>(ws + WS_WAK) + b * 32, 1u);
      BARRIER();
      TSE();
      return;
    } else {
      const int aw = wid - 6, rp = min(mt * 2 + aw, 10);
      PBAR(0);
      XFLAG();
      BARRIER();
      int wq = nt * 2 + aw;
#define WPF()                                                                                                                   \
  do {                                                                                                                          \
    if (wq < W_NQ) PFL(ws + WS_WH + (size_t)(cc * 32 + mt * 4 + (wq & 3)) * 65536 + (KT(wq >> 2) << 13) + lane * 128);          \
    wq += 6;                                                                                                                    \
  } while (0)
      float rs[4] = {};
      const unsigned ra = lds0 + rp * 2048 + lane * 16;
      unsigned so = 0;
#define RSUM()                                                                                                                        \
  do {                                                                                                                                \
    u32x4 R_[2];                                                                                                                      \
    if (NORS) break;                                                                                                                  \
    asm volatile("ds_read_b128 %0, %2\n\tds_read_b128 %1, %2 offset:1024\n\ts_waitcnt lgkmcnt(0)" : "=&v"(R_[0]), "=&v"(R_[1]) : "v"(ra + so) : "memory"); \
    _Pragma("unroll") for (int j = 0; j < 8; ++j) {                                                                                   \
      asm volatile("v_add_f32 %0, %0, %1" : "+v"(rs[(j >> 2) * 2]) : "v"(R_[j >> 2][j & 3] << 16));                                   \
      asm volatile("v_add_f32 %0, %0, %1" : "+v"(rs[(j >> 2) * 2 + 1]) : "v"(R_[j >> 2][j & 3] & 0xffff0000u));                       \
    }                                                                                                                                 \
    so = so == (NS - 1) * SL ? 0 : so + SL;                                                                                           \
  } while (0)
      {
        unsigned fo = ((qx * 32 + 64 + aw * 64 + lane) % 192) * XST * 4;
#pragma unroll 1
        for (int rd = 0; rd < 2; ++rd) {
          u64 f;
#define FLD() asm volatile("global_load_dwordx2 %0, %1, %2 sc1" : "=v"(f) : "v"(fo), "s"(xfl) : "memory")
          RSUM();
          FLD();
          BARRIER();
          RSUM();
          BARRIER();
          POLL { PDBG(it);
            asm volatile("s_waitcnt vmcnt(0)" : "+v"(f)::"memory");
            if (__all(f == tag)) break;
            __builtin_amdgcn_s_sleep(1);
            FLD();
          }
          asm volatile("s_waitcnt vmcnt(0)" : "+v"(f)::"memory");
          RSUM();
          BARRIER();
          RSUM();
          BARRIER();
          fo = (192 + aw * 32 + (lane & 31)) * XST * 4;
        }
      }
#pragma unroll 1
      for (int bi = 8; bi < H_AT; ++bi) {
        RSUM();
        BARRIER();
      }
      {
        u64 f;
        const unsigned* fp = hfl + lane * HST;
        asm volatile("global_load_dwordx2 %0, %1, off sc1" : "=v"(f) : "v"(fp) : "memory");
        RSUM();
        BARRIER();
        RSUM();
        BARRIER();
        POLL { PDBG(it);
          asm volatile("s_waitcnt vmcnt(0)" : "+v"(f)::"memory");
          if (__all(f == tag)) break;
          __builtin_amdgcn_s_sleep(1);
          asm volatile("global_load_dwordx2 %0, %1, off sc1" : "=v"(f) : "v"(fp) : "memory");
        }
        asm volatile("s_waitcnt vmcnt(0)" : "+v"(f)::"memory");
        unsigned c;
        const unsigned* cp = modv + 1024 + mt * 128 + aw * 64 + lane;
        asm volatile("global_load_dword %0, %1, off sc1" : "=v"(c) : "v"(cp) : "memory");
        WPF();
        RSUM();
        BARRIER();
        WPF();
        RSUM();
        BARRIER();
        POLL { PDBG(it);
          asm volatile("s_waitcnt vmcnt(0)" : "+v"(c)::"memory");
          if (__all(c != SENT)) break;
          __builtin_amdgcn_s_sleep(1);
          asm volatile("global_load_dword %0, %1, off sc1" : "=v"(c) : "v"(cp) : "memory");
        }
        asm volatile("s_waitcnt vmcnt(0)" : "+v"(c)::"memory");
        *reinterpret_cast<unsigned*>(shm + SCA + (aw * 64 + lane) * 4) = c ^ (cc ? 0x80000000u : 0u);
        flg[0] = 0u;
      }
#pragma unroll 1
      for (int bi = H_AT + 4; bi < 32; ++bi) {
        WPF();
        RSUM();
        if (bi < 30) BARRIER();
      }
      unsigned* rsb = reinterpret_cast<unsigned*>(ws + WS_RS) + qx * 512;
      {
        const float v0 = dpp_add<0x141>(dpp_add<0x4E>(dpp_add<0xB1>(rs[0] + rs[1])));
        const float v1 = dpp_add<0x141>(dpp_add<0x4E>(dpp_add<0xB1>(rs[2] + rs[3])));
        if ((lane & 7) < 2 && mt * 2 + aw < 11) ST4(rsb + cc * S + PROW(rp) + (lane & 1) * 8 + (lane >> 3), nsent((lane & 1) ? v1 : v0));
      }
      BARRIER();
#pragma unroll 1
      for (int k = 34; k <= B_VAT; ++k) BARRIER();
      u32x2 m[6];
      const unsigned lr = min(lane, 47);
      const unsigned o0 = aw ? (unsigned)(WS_Q - WS_MOD) + mt * 512 + lane * 8 : 8192u + mt * 512 + lane * 8;
      const unsigned o1 = aw ? (unsigned)(WS_RS - WS_MOD) + (qx * 512 + PROW(5 + (lr >> 3)) + (lr & 7) * 2) * 4 : o0 + 8192;
      const unsigned o2 = aw ? o1 + 8192 : o0 + 16384, o3 = aw ? o1 : 28672u + min(lane, 23) * 8;
#define VLD()                                                                                                                     \
  asm volatile("global_load_dwordx2 %0, %6, %10 sc1\n\tglobal_load_dwordx2 %1, %6, %11 sc1\n\tglobal_load_dwordx2 %2, %7, %10 sc1\n\t" \
               "global_load_dwordx2 %3, %7, %11 sc1\n\tglobal_load_dwordx2 %4, %8, %10 sc1\n\tglobal_load_dwordx2 %5, %9, %10 sc1"    \
               : "=&v"(m[0]), "=&v"(m[1]), "=&v"(m[2]), "=&v"(m[3]), "=&v"(m[4]), "=&v"(m[5])                                     \
               : "v"(o0), "v"(o1), "v"(o2), "v"(o3), "s"(modv), "s"(modv + 1024)                                                  \
               : "memory")
      VLD();
#pragma unroll
      for (int k = B_VAT + 1; k <= 43; ++k) BARRIER();
      TS(30);
      POLL { PDBG(it);
        asm volatile("s_waitcnt vmcnt(0)" : W6(m)::"memory");
        if (__all(max(max(max(max2(m[0]), max2(m[1])), max(max2(m[2]), aw ? 0u : max2(m[3]))), max(max2(m[4]), max2(m[5]))) != SENT)) break;
        __builtin_amdgcn_s_sleep(2);
        VLD();
      }
      asm volatile("s_waitcnt vmcnt(0)" : W6(m)::"memory");
      BARRIER();
      if (aw == 0) {
        *reinterpret_cast<u32x2*>(shm + TGA + lane * 8) = m[0];
        *reinterpret_cast<unsigned*>(shm + TV + lane * 4) = hi2(m[2]);
        *reinterpret_cast<unsigned*>(shm + TV + 256 + lane * 4) = hi2(m[1]);
        *reinterpret_cast<unsigned*>(shm + TV + 512 + lane * 4) = hi2(m[3]);
        const float p0 = __uint_as_float(m[4][0]), p1 = __uint_as_float(m[4][1]);
        const unsigned hh = pack2(p0, p1);
        *reinterpret_cast<unsigned*>(shm + SCW + lane * 4) = hh;
        *reinterpret_cast<unsigned*>(shm + SCW + 256 + lane * 4) = pack2(p0 - bflo(hh), p1 - bfhi(hh));
        *reinterpret_cast<u32x2*>(shm + SCW + 512 + lane * 8) = u32x2{0x3f803f80u, 0x3f803f80u};
        const float u = __uint_as_float(m[5][0]) + __uint_as_float(m[5][1]), u2 = dpp_add<0xB1>(u), u8 = dpp_add<0x141>(dpp_add<0x4E>(u2));
        if (lane < 17 && !(lane & 1)) *reinterpret_cast<float*>(shm + TSUM + lane * 2) = lane < 16 ? u2 : u8;
      } else {
#pragma unroll
        for (int j = 0; j < 2; ++j) {
          const float r0 = __uint_as_float(m[0][j]), r1 = __uint_as_float(m[1][j]);
          const unsigned a0 = pack2(r0, r0), a1 = pack2(r1, r1);
          *reinterpret_cast<u32x4*>(shm + RKA + ((lane >> 4) * 32 + ((lane >> 1) & 1) * 16 + ((lane >> 2) & 3) * 4 + (lane & 1) * 2 + j) * 16) =
              u32x4{a0, pack2(r0 - bflo(a0), r0 - bflo(a0)), a1, pack2(r1 - bflo(a1), r1 - bflo(a1))};
          const float s0 = __uint_as_float(m[2][j]), s1 = -__uint_as_float(m[4][j]);
          const unsigned b0 = pack2(s0, s0 - rbf(s0)), b1 = pack2(s1, s1 - rbf(s1));
          *reinterpret_cast<u32x4*>(shm + RKB + (lr * 2 + j) * 16) = u32x4{b0, b0, b1, b1};
        }
        *reinterpret_cast<u32x2*>(shm + RKZ + lane * 8) = u32x2{0u, 0u};
        const unsigned one = g4 == (l16 >> 2) ? ((lane & 1) ? 0x3f800000u : 0x3f80u) : 0u, e0 = (lane & 2) ? 0u : one, e1 = (lane & 2) ? one : 0u;
        *reinterpret_cast<u32x4*>(shm + IDT + lane * 16) = u32x4{e0, e1, 0u, 0u};
        *reinterpret_cast<u32x4*>(shm + IDT + 1024 + lane * 16) = u32x4{0u, 0u, e0, e1};
      }
      TS(17);
      LBAR();
    }
    const int row0 = rg * 16 + l16, col1 = mt * 128 + g4 * 8;
    u32x4 xq[4], sa4[4];
#pragma unroll
    for (int q = 0; q < 4; ++q) DSR(sa4[q], lds0 + SCW + (lane & 3) * 256 + g4 * 16, q * 64);
    f32x2 cj = *reinterpret_cast<const f32x2*>(shm + TSUM + g4 * 8);
    float sw = *reinterpret_cast<const float*>(shm + TSUM + 32), ce = *reinterpret_cast<const float*>(shm + TSUM + mt * 4);
    u32x4 dk;
#pragma unroll
    for (int q = 0; q < 4; ++q) dk[q] = l16 == g4 * 4 + q ? ~0u : 0u;
    unsigned xw1 = lds0 + tid * 16;
    unsigned xa = lds0 + kk * 4096 + lane * 16, so = (unsigned)(((rg * 8 + mt) * 16 + lane) * 16);
    asm volatile("" : "+v"(dk), "+v"(cj), "+v"(sw), "+v"(ce), "+v"(xa), "+v"(so), "+v"(xw1));
    if (wid < 4) {
      POLL { PDBG(it);
        asm volatile("s_waitcnt vmcnt(0)" : W6(pv)::"memory");
        if (__all(max(max(max(max4(pv[0]), max4(pv[1])), max(max4(pv[2]), max4(pv[3]))), max(max4(pv[4]), max4(pv[5]))) != SENT)) break;
        __builtin_amdgcn_s_sleep(1);
        XCHLD();
      }
      asm volatile("s_waitcnt vmcnt(0)" : W6(pv)::"memory");
      TS(27);
      if (tid == 0) __atomic_store_n(flg, 1u, __ATOMIC_RELAXED);
    } else {
      unsigned sv;
      LSPIN(sv, flg, sv != 0u);
    }
    STLD();
    if (wid < 4) {
      sfor<8>([&](auto kc) __attribute__((always_inline)) {
        constexpr int k = decltype(kc)::value;
        (void)acc, (void)idt, (void)pv, (void)ga, (void)x5, (void)xw1;
        if constexpr (k < 6) {
          MFMA(acc[0][5 + k], idt[0], pv[k]);
          MFMA(acc[1][5 + k], idt[1], pv[k]);
        }
        if constexpr (k >= 2) {
          constexpr int j = k - 2;
          asm volatile("" : "+v"(acc[0][5 + j]), "+v"(acc[1][5 + j]));
          const u32x4 v = pk8((j < 5 ? acc[0][j % 5] : x5[0]) + ga[0] * acc[0][5 + j], (j < 5 ? acc[1][j % 5] : x5[1]) + ga[1] * acc[1][5 + j]);
          asm volatile("ds_write_b128 %0, %1 offset:%2" ::"v"(xw1), "v"(v), "n"(j * 4096) : "memory");
        }
      });
    }
    {
      TS(6);
      LBAR();
      asm volatile("ds_read_b128 %0, %8\n\tds_read_b128 %1, %8 offset:1024\n\tds_read_b128 %2, %8 offset:2048\n\tds_read_b128 %3, %8 offset:3072\n\ts_waitcnt lgkmcnt(0)"
                   : "=&v"(xq[0]), "=&v"(xq[1]), "=&v"(xq[2]), "=&v"(xq[3]), "+v"(sa4[0]), "+v"(sa4[1]), "+v"(sa4[2]), "+v"(sa4[3])
                   : "v"(xa)
                   : "memory");
      if (!act) {
        asm volatile("s_waitcnt vmcnt(0)" ::"v"(sr[0]), "v"(sr[1]) : "memory");
        TSE();
        return;
      }
      f32x4 s4 = {0.f, 0.f, 0.f, 0.f}, gr = {0.f, 0.f, 0.f, 0.f};
#pragma unroll
      for (int q = 0; q < 4; ++q) {
        gr = MM(xq[q], xq[q], gr);
        s4 = MM(sa4[q], xq[q], s4);
      }
      const u32x4 gd = __builtin_bit_cast(u32x4, gr) & dk;
      const float m = s4[2] * (1.0f / 128);
      float q2 = ar4(__uint_as_float((gd[0] | gd[1]) | (gd[2] | gd[3]))) - s4[2] * m, lg = (s4[0] + s4[1]) - m * ce;
      if (__any(!(s4[2] * m <= 256.0f * q2))) {
        f32x4 a = {0.f, 0.f, 0.f, 0.f}, l = {0.f, 0.f, 0.f, 0.f};
#pragma unroll
        for (int q = 0; q < 4; ++q) {
          const u32x4 ch = *reinterpret_cast<const u32x4*>(shm + SCW + g4 * 16 + q * 64), cl = *reinterpret_cast<const u32x4*>(shm + SCW + 256 + g4 * 16 + q * 64);
          const f32x4 d0 = lo4(xq[q]) - m, d1 = hi4(xq[q]) - m;
          a += d0 * d0 + d1 * d1;
          l += d0 * (lo4(ch) + lo4(cl)) + d1 * (hi4(ch) + hi4(cl));
        }
        q2 = ar4((a[0] + a[1]) + (a[2] + a[3]));
        lg = ar4((l[0] + l[1]) + (l[2] + l[3]));
      }
      const u32x4 own = {nsent(m), nsent(q2), nsent(lg), 0u};
      if (g4 == 0) ST16(so, own, ws + WS_STAT, 0);
      TS(7);
      SKEWPT();
      const bool k0 = g4 * 2 == mt, k1 = g4 * 2 + 1 == mt;
#define STOK() (sr[0] = k0 ? own : sr[0], sr[1] = k1 ? own : sr[1], __all(max(max(max(sr[0][0], sr[0][1]), sr[0][2]), max(max(sr[1][0], sr[1][1]), sr[1][2])) != SENT))
      asm volatile("s_waitcnt vmcnt(1)\n\t; AUDIT_OK %0 %1" : "+v"(sr[0]), "+v"(sr[1])::"memory");
      bool pub = STOK();
      if (!pub) {
        pub = mt == (rg & 7);
        if (OPT_SD) __builtin_amdgcn_s_sleep(OPT_SD);
        POLL { PDBG(it);
          STLD();
          asm volatile("s_waitcnt vmcnt(0)" : "+v"(sr[0]), "+v"(sr[1])::"memory");
          if (STOK()) break;
          __builtin_amdgcn_s_sleep(1);
        }
      }
      TS(9);
      const f32x2 mk = {__uint_as_float(sr[0][0]), __uint_as_float(sr[1][0])};
      const f32x2 qk = {__uint_as_float(sr[0][1]), __uint_as_float(sr[1][1])};
      const f32x2 lk = {__uint_as_float(sr[0][2]), __uint_as_float(sr[1][2])};
      const float mean = ar4(mk[0] + mk[1]) * (1.0f / 8);
      const f32x2 dm = mk - mean;
      const f32x2 a = qk + 128.0f * (dm * dm), l = lk + dm * cj;
      const float vf = ar4(a[0] + a[1]) * (1.0f / D);
      const float rstd = __builtin_amdgcn_rsqf(vf + 1e-5f);
      const float rd = rstd * ar4(l[0] + l[1]) + sw;
      const unsigned rb = f2bf(rd);
      const unsigned ord = rb ^ ((rb & 0x8000u) ? 0xffffu : 0x8000u);
      const unsigned key = (ord << 16) | (0xffffu - (unsigned)row0);
      unsigned t1 = key, t2 = 0u;
      TOP2(0xB1); TOP2(0x4E); TOP2(0x141); TOP2(0x140);
      if (lane == 0 && pub) ST4(reinterpret_cast<u64*>(ws + WS_KEY) + rg * 16, mk64(~t2, ~t1));
      TS(1);
      SKEWPT();
      if (key >= t2) {
        const int sl = rg * 2 + (key == t2);
#pragma unroll
        for (int q = 0; q < 4; ++q) ST16(sl * 2048 + col1 * 2, nsent4(xq[q]), ws + WS_CAND, q * 64);
        if (g4 == 0 && pub) ST16(sl * 16, (u32x4{nsent(mean), nsent(rstd), 0u, 0u}), ws + WS_HDR, 0);
      }
      TS(8);
#pragma unroll
      for (int q = 0; q < 4; ++q) {
        const u32x4 cq = *reinterpret_cast<const u32x4*>(shm + TV + q * 64 + g4 * 16), sbq = *reinterpret_cast<const u32x4*>(shm + TV + 256 + q * 64 + g4 * 16);
        const u32x4 gbq = *reinterpret_cast<const u32x4*>(shm + TV + 512 + q * 64 + g4 * 16);
        const f32x4 x0 = lo4(xq[q]), x1 = hi4(xq[q]);
        const f32x4 h0 = ((x0 - mean) * rstd) * lo4(cq) + lo4(sbq), h1 = ((x1 - mean) * rstd) * hi4(cq) + hi4(sbq);
        ST16((row0 * D + col1) * 2, pk8(x0 + lo4(gbq) * h0, x1 + hi4(gbq) * h1), out, q * 64);
      }
      asm volatile(".Lcpf_e2:");
      WAIT_V(0);
      if (lane == 0) ST4(reinterpret_cast<unsigned*>(ws + WS_DON) + (rg * 8 + mt) * 2, 1u);
      TS(16);
    }
    TSE();
    return;
  }
  {
    const int hid = b - 192, hi = j8 - 24;
    constexpr int RNG = 65536, SSI = MISC, GP = MISC + 2048, SSA = MISC + 3072, HS = MISC + 5120, SX1 = MISC + 9216, ACT = MISC + 13312,
                  HFLG = MISC + 13568, RR = MISC + 13824;
    unsigned* flg = reinterpret_cast<unsigned*>(shm + HFLG);
    const int ec = xcd * 7 + hi, hd = hi == 7 ? 0 : (hi + 7 - ((3 + xcd) & 7) % 7) % 7;
#define HPF(g)                                                     \
  do {                                                             \
    const int u_ = wid * 6 + (hd + 2 * (g)) % 6;                   \
    if (hd && u_ < 32) PFL(PFG(u_, g));                            \
  } while (0)
    WHLD(4);
    const int cb0 = (hd ? (hd > 2 ? hd + 1 : hd - 1) : 2 + (hi == 7)) * 8 + ((xcd + hd) & 7);
    const unsigned om = (unsigned)((wid * 16 + (lane >> 3)) * 12288 + (lane & 7) * 16);
    const unsigned og = (unsigned)((wid * 8 + (lane >> 3)) * 7168 + (lane & 7) * 16);
    const unsigned od = (unsigned)((wid * 8 + (lane >> 3)) * 2048 + (lane & 7) * 16);
    const char* pm = reinterpret_cast<const char*>(wmod) + cb0 * 128;
    const char* pg = reinterpret_cast<const char*>(wg) + ec * 128;
    const char* pu = reinterpret_cast<const char*>(wu) + ec * 128;
    const char* pd = reinterpret_cast<const char*>(wd) + (size_t)ec * 131072;
    const unsigned ow = (unsigned)(((wid & 1) * 16 + (lane >> 3)) * 12288 + (wid >> 1) * 128 + (lane & 7) * 16);
    const char* pw = reinterpret_cast<const char*>(wmod) + 8192 + xcd * 512;
#define RQM(r)                                                                                   \
  do {                                                                                           \
    const char* p_ = pm + (size_t)(r)*1572864;                                                   \
    GLDSP(lds0 + RNG + ((r)&3) * 16384 + wid * 2048, om, p_, WPOL);                              \
    GLDSP(lds0 + RNG + ((r)&3) * 16384 + wid * 2048 + 1024, om, p_ + 98304, WPOL);               \
  } while (0)
#define RQE(r)                                                                                                                \
  do {                                                                                                                        \
    if (NOEXP) break;                                                                                                         \
    GLDSP(lds0 + RNG + ((r)&3) * 16384 + wid * 1024, (r) < 16 ? og : od, (r) < 16 ? pg + (r)*458752 : pd + ((r)-16) * 256, WPOL);   \
    GLDSP(lds0 + RNG + ((r)&3) * 16384 + 8192 + wid * 1024, (r) < 16 ? og : od, (r) < 16 ? pu + (r)*458752 : pd + ((r)-16) * 256 + 128, WPOL); \
  } while (0)
    PBAR(4);
    XFLAG();
    const u32x4 sv = {SENT, SENT, SENT, SENT};
    auto PUBS = [&](int cb, unsigned v, unsigned p, unsigned r) __attribute__((always_inline)) {
      const int vi = cb >> 4, col = (cb & 15) * 64 + lane;
      ST4(modv + cb * 64 + lane, v);
      if (vi == 4) ST4(modv + 6144 + col, p);
      if (vi == 4 ? (lane & 31) == 0 : vi == 3 && lane == 0) ST4(modv + 7168 + (vi == 4 ? col >> 5 : 32 + (cb & 15)), r);
    };
    if (wid == 4 || (hi == 7 && wid < 4)) PUBS(wid == 4 ? cb0 : 64 + xcd * 4 + wid, SENT, SENT, SENT);
    if (tid >= 480) ST4(reinterpret_cast<unsigned*>(ws + WS_Q) + hsl * 32 + tid - 480, SENT);
    unsigned cv, wv;
    asm volatile("global_load_dword %0, %2, %3\n\tglobal_load_dword %1, %2, %4" : "=&v"(cv), "=&v"(wv) : "v"(tid * 4), "s"(cvec), "s"(wr_) : "memory");
    if (hi < 7) {
      PFL(PFG(hi * 4 + (wid & 3), 2 + (wid >> 2)));
      if (hd == 0) PFL(PFG(28 + (wid & 3), 2 + (wid >> 2)));
      const char* ap = ws + WS_ARM + (size_t)(ec * 8 + wid) * 3328;
#pragma unroll
      for (int j = 0; j < 4; ++j) ST16(lane * 16, sv, ap, j < 3 ? j * 1024 : 2304);
      HPF(4);
      WAIT_V(5);
    } else {
      WAIT_V(0);
    }
    asm volatile("; AUDIT_OK %0 %1" : "+v"(cv), "+v"(wv)::"memory");
    *reinterpret_cast<unsigned*>(shm + SSI + tid * 4) = pack2(silu_bf(bflo(cv)), silu_bf(bfhi(cv)));
    *reinterpret_cast<unsigned*>(shm + SX1 + tid * 4) = wv;
    LBAR();
    TS(50);
    const unsigned ha = (unsigned)(((l16 >> 2) * 8 + (lane & 3)) * 2048 + wid * 256);
#define HFR(j) *reinterpret_cast<const u32x4*>(shm + ha + ((j)&1) * 8192 + ((j) >> 2) * 128 + (((((j) >> 1) & 1) * 4 + g4) ^ (lane & 7)) * 16)
    HPF(5);
#pragma unroll
    for (int j = 0; j < 8; ++j) ST16(lane * 16, HFR(j), ws + WS_WH + (size_t)hsl * 65536 + wid * 8192 + 4096, j * 1024 - 4096);
    HPF(6);
    RQM(0);
    RQM(1);
    RQM(2);
    const unsigned tm = lds0 + RNG + ((wid >> 2) * 64 + g4 * 8 + ((lane >> 2) & 3)) * 128 + (wid & 3) * 32 + (lane & 3) * 8;
    f32x4 ma = {0.f, 0.f, 0.f, 0.f};
#define GVR(i)                                                                                                                               \
  do {                                                                                                                                       \
    _Pragma("unroll") for (int f = 0; f < 2; ++f)                                                                                            \
        ma = MM(trd(tm + ((i)&3) * 16384 + f * 4096), *reinterpret_cast<const u32x4*>(shm + SSI + ((i)*128 + (wid >> 2) * 64 + f * 32 + g4 * 8) * 2), ma); \
    if ((i) == 7 && l16 == 0) *reinterpret_cast<f32x4*>(shm + GP + wid * 64 + g4 * 16) = ma;                                                 \
  } while (0)
    const float* gp = reinterpret_cast<const float*>(shm + GP);
    auto PUB = [&](int cb, float s) __attribute__((always_inline)) {
      const int vi = cb >> 4;
      const float v = rbf(((vi == 1 || vi == 4) ? 1.0f : 0.0f) + rbf(s));
      const float p = v * bf2f(*reinterpret_cast<const u16*>(shm + SX1 + ((cb & 15) * 64 + lane) * 2));
      const float r16 = row_sum(p), r32 = swap16_add(r16, r16);
      PUBS(cb, nsent(v), nsent(p), nsent(vi == 4 ? r32 : swap32_add(r32, r32)));
    };
    {
      sfor<8>([&](auto ii) __attribute__((always_inline)) {
        constexpr int i = decltype(ii)::value;
        (void)ow, (void)pw, (void)om, (void)pm, (void)og, (void)od, (void)pg, (void)pu, (void)pd, (void)lds0, (void)wid, (void)pfl, (void)Yx, (void)l16, (void)g4, (void)xcd, (void)hfl, (void)lane, (void)hid;
        TSCAP();
        WAIT_V(4);
        LBAR();
        if (wid == 2) TS(i);
        if constexpr (i == 0) { if (tid == 0) ST4(reinterpret_cast<u64*>(hfl + hid * HST), tag); }
        if constexpr (i == 0) HPF(7);
        if constexpr (i < 5) {
          RQM(i + 3);
        } else {
          const bool e = hid < 56;
          const char* p_ = pw + (i - 5) * 393216;
          GLDSP(lds0 + RNG + (i - 5) * 16384 + wid * (e ? 1024 : 2048), e ? og : ow, e ? pg + (i - 5) * 458752 : p_, WPOL);
          GLDSP(lds0 + RNG + (i - 5) * 16384 + wid * (e ? 1024 : 2048) + (e ? 8192 : 1024), e ? og : ow, e ? pu + (i - 5) * 458752 : p_ + 98304, WPOL);
        }
        GVR(i);
      });
    }
    TS(51);
#define PUB0() do { if (wid == 0) PUB(cb0, gp[lane] + gp[64 + lane]); } while (0)
    u64 sf, hf;
#define SFLD()                                                                                                                     \
  asm volatile("s_mov_b64 exec, 3\n\tglobal_atomic_add_x2 %0, %2, %3, %4 sc0\n\ts_mov_b64 exec, 0xff\n\t"                          \
               "global_load_dwordx2 %1, %2, %5 sc1\n\ts_mov_b64 exec, -1"                                                          \
               : "=&v"(sf), "=&v"(hf) : "v"(lane * 128), "v"(0ull), "s"(ws + WS_SFL + wid * 256), "s"(ws + WS_HFL + wid * 1024) : "memory")
    static_assert(HST == 32, "");
    auto SAW = [&]() __attribute__((always_inline)) {
      POLL { PDBG(it);
        if (((__builtin_amdgcn_ballot_w64(sf != tag) & 3u) | (__builtin_amdgcn_ballot_w64(hf != tag) & 0xffu)) == 0) break;
        __builtin_amdgcn_s_sleep(4);
        SFLD();
        asm volatile("s_waitcnt vmcnt(0)" : "+v"(sf), "+v"(hf)::"memory");
      }
    };
    u32x2 s2;
#define SALD() asm volatile("global_load_dwordx2 %0, %1, %2 sc1" : "=v"(s2) : "v"(tid * 8), "s"(modv) : "memory")
    auto QMM = [&]() __attribute__((always_inline)) {
      *reinterpret_cast<unsigned*>(shm + SSA + tid * 4) = hi2(s2);
      f32x4 qa[2] = {};
#pragma unroll
      for (int j = 0; j < 8; ++j) qa[j & 1] = MM(HFR(j), *reinterpret_cast<const u32x4*>(shm + SSA + wid * 256 + (j >> 1) * 64 + g4 * 16), qa[j & 1]);
      if (l16 < 2) *reinterpret_cast<f32x4*>(shm + HS + wid * 128 + g4 * 32 + l16 * 16) = l16 ? qa[1] : qa[0];
    };
    auto QPUB = [&]() __attribute__((always_inline)) {
      if (tid >= 480) {
        float s = 0.f;
#pragma unroll
        for (int w = 0; w < 8; ++w) s += *reinterpret_cast<const float*>(shm + HS + w * 128 + (tid - 480) * 4);
        ST4(reinterpret_cast<unsigned*>(ws + WS_Q) + hsl * 32 + tid - 480, nsent(s));
      }
    };
    u32x4 GU[32], DN[16];
    if (hid < 56) {
      const unsigned tg = lds0 + RNG + (g4 * 8 + ((lane >> 2) & 3)) * 128 + ((lane >> 1) & 1) * 8192 + wid * 16 + (lane & 1) * 8;
      const unsigned td = lds0 + RNG + (g4 * 8 + ((lane >> 2) & 3)) * 128 + ((wid >> 1) & 1) * 8192 + (wid & 1) * 64 + (lane & 3) * 8 + (wid >> 2) * 16384;
      sfor<24>([&](auto rr) __attribute__((always_inline)) {
        constexpr int r = decltype(rr)::value;
        (void)td, (void)tg, (void)GU, (void)DN, (void)og, (void)od, (void)pg, (void)pu, (void)pd, (void)lds0, (void)wid, (void)pfl, (void)Yx, (void)l16, (void)g4, (void)xcd;
        (void)s2, (void)modv, (void)tid;
        (void)hid, (void)lane, (void)sf, (void)hf;
        (void)sv, (void)hi, (void)ws;
#define EX(r) (((r) >= 0 && (r) < 6 ? 2 : 0) + ((r) == Q_AT - 6 ? 2 : 0) + ((r) == Q_AT - 3))
        WAIT_V(r == 23 ? 0 : 4 + EX(r - 2) + EX(r - 1));
        LBAR();
        TSCAP();
        if (wid == 3) TS(r);
        if constexpr (r == 0) PUB0();
        if constexpr (r == 1) {
          if (cb0 < 16 && wid == 0) {
            WAIT_V(4);
            if (lane == 0) ST4(reinterpret_cast<u64*>(ws + WS_SFL) + cb0 * 16, tag);
          }
        }
        if constexpr (r == Q_AT - 6) SFLD();
        if constexpr (r == Q_AT - 3) {
          asm volatile("" : "+v"(sf), "+v"(hf));
          SAW();
          SALD();
        }
        if constexpr (r < 6) {
          const char* ap = ws + WS_XCH + xcd * 589824 + min(hi * 8 + wid + (NOARM ? 0 : r) * 56, 287) * 2048;
          ST16(lane * 16, sv, ap, 0);
          ST16(lane * 16, sv, ap, 1024);
        }
        if constexpr (r < 17) RQE(r + 3);
        if constexpr (r == 18 || r == 20) { RQE(r + 2); RQE(r + 3); }
        if constexpr (r < 16) {
          GU[2 * r] = trd(tg + (r & 3) * 16384);
          GU[2 * r + 1] = trd(tg + (r & 3) * 16384 + 4096);
          asm volatile("" : "+v"(GU[2 * r]), "+v"(GU[2 * r + 1]));
        } else if constexpr (r & 1) {
#pragma unroll
          for (int j = 0; j < 4; ++j) {
            DN[(r - 17) * 2 + j] = trd(td + (r & 2) * 16384 + (j & 1) * 4096 + (j >> 1) * 32);
            asm volatile("" : "+v"(DN[(r - 17) * 2 + j]));
          }
        }
        if constexpr (r == Q_AT) {
          asm volatile("" : "+v"(s2));
          QMM();
        }
        if constexpr (r == Q_AT + 1) QPUB();
      });
      TS(52);
    } else {
      const unsigned tw = lds0 + RNG + (wid >> 1) * 4096 + (g4 * 8 + ((lane >> 2) & 3)) * 128 + (wid & 1) * 64 + (lane & 3) * 8;
      f32x4 mb[2] = {};
      auto WR = [&](int r0, int r1) __attribute__((always_inline)) {
#pragma unroll 1
        for (int r = r0; r < r1; ++r) {
          WAIT_V(4);
          LBAR();
          if (wid == 2 && r < 22) TSD(r + 8);
          const char* p_ = pw + min(r + 3, 31) * 393216;
          GLDSP(lds0 + RNG + ((r + 3) & 3) * 16384 + wid * 2048, ow, p_, WPOL);
          GLDSP(lds0 + RNG + ((r + 3) & 3) * 16384 + wid * 2048 + 1024, ow, p_ + 98304, WPOL);
          const u32x4 sf = *reinterpret_cast<const u32x4*>(shm + SSI + (r * 32 + g4 * 8) * 2);
          mb[0] = MM(trd(tw + (r & 3) * 16384), sf, mb[0]);
          mb[1] = MM(trd(tw + (r & 3) * 16384 + 32), sf, mb[1]);
        }
      };
      WR(0, 1);
      PUB0();
      WR(1, MQ_AT - 6);
      SFLD();
      WR(MQ_AT - 6, MQ_AT - 3);
      asm volatile("; AUDIT_OK %0 %1" : "+v"(sf), "+v"(hf));
      SAW();
      SALD();
      WR(MQ_AT - 3, MQ_AT);
      asm volatile("; AUDIT_OK %0" : "+v"(s2));
      QMM();
      WR(MQ_AT, 32);
      if (l16 < 2) *reinterpret_cast<f32x4*>(shm + GP + wid * 128 + l16 * 64 + g4 * 16) = l16 ? mb[1] : mb[0];
      LBAR();
      if (wid < 4) PUB(64 + xcd * 4 + wid, gp[tid]);
      QPUB();
      TS(52);
      WAIT_V(0);
      TSE();
      return;
    }
    bool early = false;
    if (tid == 0) {
      POLL { PDBG(it);
        unsigned g;
        asm volatile("global_atomic_add %0, %1, %1, %2 sc0\n\ts_waitcnt vmcnt(0)" : "=&v"(g) : "v"(0u), "s"(ws + WS_WAK + hid * 128) : "memory");
        if (g != SENT) break;
        early = true;
        __builtin_amdgcn_s_sleep(OPT_KS);
      }
    }
    BARRIER();
    TS(21);
    const int tok = hid & 1, col = ((hid >> 1) & 15) * 64 + lane;
    u32x4 c4, s4;
    unsigned gbu;
    POLL { PDBG(it);
      asm volatile("global_load_dwordx4 %0, %3, %5 sc1\n\tglobal_load_dwordx4 %1, %3, %6 sc1\n\tglobal_load_dword %2, %4, %7 sc1\n\ts_waitcnt vmcnt(0)"
                   : "=&v"(c4), "=&v"(s4), "=&v"(gbu) : "v"((tid & 255) * 16), "v"(col * 4), "s"(modv + 4096), "s"(modv + 3072), "s"(modv + 5120) : "memory");
      if (__all(max(max(max4(c4), max4(s4)), gbu) != SENT)) break;
      __builtin_amdgcn_s_sleep(8);
    }
    const f32x4 cb2 = __builtin_bit_cast(f32x4, c4), sb2 = __builtin_bit_cast(f32x4, s4);
    unsigned hra = lds0 + HS + g4 * 32 + (lane & 1) * 16, hwa = lds0 + HS + ((tid & 255) >> 1) * 32 + (wid >> 2) * 16 + (tid & 1) * 8;
    unsigned pto = (lane & 1) * 4096 + ((l16 >> 2) * 256 + wid * 32 + (lane & 2) * 8 + g4 * 4) * 4, awa = lds0 + ACT + wid * 32 + g4 * 16 + l16 * 2;
    asm volatile("" : "+v"(hra), "+v"(hwa), "+v"(pto), "+v"(awa));
    const float gb = __uint_as_float(gbu);
    if (wid == 0) {
      u32x2 g0 = {SENT, SENT}, g1 = {SENT, SENT};
      const char* gp = ws + WS_KEY + lane * 128;
      if (__any(early)) __builtin_amdgcn_s_sleep(OPT_KD);
      TS(20);
      POLL { PDBG(it);
        if (max2(g0) == SENT) asm volatile("global_load_dwordx2 %0, %1, off sc1" : "+v"(g0) : "v"(gp) : "memory");
        if (max2(g1) == SENT) asm volatile("global_load_dwordx2 %0, %1, off sc1" : "+v"(g1) : "v"(gp + 8192) : "memory");
        asm volatile("s_waitcnt vmcnt(0)" : "+v"(g0), "+v"(g1)::"memory");
        if (__all(max(max2(g0), max2(g1)) != SENT)) break;
        __builtin_amdgcn_s_sleep(1);
      }
      g0 = ~g0;
      g1 = ~g1;
      const unsigned a1 = max(g0[0], g1[0]), a2 = max(min(g0[0], g1[0]), max(g0[1], g1[1]));
      const unsigned k1 = wave_max_u(a1);
      const unsigned k2 = wave_max_u(a1 == k1 ? a2 : a1);
      if (lane == 0) *reinterpret_cast<u32x2*>(flg) = u32x2{k1, k2};
      TS(22);
    }
    LBAR();
    const unsigned k1 = flg[0], k2 = flg[1];
    {
      const int rg1 = key_row(k1) >> 4, rg2 = key_row(k2) >> 4;
      const int sl = wid >= 4 ? rg2 * 2 + (rg2 == rg1) : rg1 * 2;
      u32x2 v, hd;
      POLL { PDBG(it);
        asm volatile("global_load_dwordx2 %0, %2, %4 sc1\n\tglobal_load_dwordx2 %1, %3, %5 sc1\n\ts_waitcnt vmcnt(0)"
                     : "=&v"(v), "=&v"(hd) : "v"(sl * 2048 + (tid & 255) * 8), "v"(sl * 16), "s"(ws + WS_CAND), "s"(ws + WS_HDR) : "memory");
        if (__all(max(max2(v), max2(hd)) != SENT)) break;
        __builtin_amdgcn_s_sleep(1);
      }
      TS(23);
      const float mean = __uint_as_float(hd[0]), rstd = __uint_as_float(hd[1]);
      const f32x4 h = ((f32x4{bflo(v[0]), bfhi(v[0]), bflo(v[1]), bfhi(v[1])} - mean) * rstd) * cb2 + sb2;
      asm volatile("ds_write_b64 %0, %1" ::"v"(hwa), "v"(u32x2{pack2(h[0], h[1]), pack2(h[2], h[3])}) : "memory");
      *reinterpret_cast<u32x2*>(shm + SX1 + tid * 8) = v;
    }
    SKEWPT();
    LBAR();
    {
      f32x4 d;
      u32x4 hb[8];
      sfor<39>([&](auto ff) __attribute__((always_inline)) {
        constexpr int f = decltype(ff)::value, c = f - 7;
        (void)d, (void)hb, (void)GU, (void)hra;
        if constexpr (c >= 0) {
          WAIT_L(f <= 32 ? 6 : 38 - f);
          if constexpr (c == 0) MFMA0(d, hb[0], GU[0]);
          else MFMA(d, hb[c & 7], GU[c]);
        }
        if constexpr (f < 32) DSR(hb[f & 7], hra, f * 128);
      });
      asm volatile("s_nop 7" : "+v"(d));
      const float dv = rbf((g4 & 1) ? d[1] : d[0]), uv = dpp_f<0x128>(dv);
      const unsigned av = f2bf(rbf(dv * __builtin_amdgcn_rcpf(1.0f + __expf(-dv))) * uv);
      asm volatile("s_mov_b64 exec, 0xff00ff\n\tds_write_b16 %0, %1\n\ts_mov_b64 exec, -1" ::"v"(awa), "v"(av) : "memory");
    }
    TS(24);
    LBAR();
    {
      u32x4 a0, a1;
      asm volatile("ds_read_b128 %0, %2 offset:%3\n\tds_read_b128 %1, %2 offset:%4\n\ts_waitcnt lgkmcnt(0)" : "=&v"(a0), "=&v"(a1) : "v"(hra), "n"(ACT - HS), "n"(ACT - HS + 128) : "memory");
      u32x4 o = {};
#pragma unroll
      for (int c = 0; c < 8; ++c) {
        const u32x4 d = __builtin_bit_cast(u32x4, MM(DN[2 * c + 1], a1, MM(DN[2 * c], a0, (f32x4{0.f, 0.f, 0.f, 0.f}))));
        o = (l16 >> 1) == c ? d : o;
      }
      ST16(pto, nsent4(o), ws + WS_PART + ec * 8192, 0);
    }
    TS(25);
    SKEWPT();
    if (hid >= 32) {
      if (hi == 6 && wid == 1 && lane < 43) {
        char* fp = ws + (lane < 32 ? WS_XFL + (xcd * 32 + lane) * (XST * 4) : lane < 40 ? WS_HFL + (xcd * 8 + lane - 32) * (HST * 4) : lane < 42 ? WS_SFL + (xcd * 2 + lane - 40) * 128 : WS_EPO);
        if (lane < 42 || xcd == 0) ST4(reinterpret_cast<u64*>(fp), lane < 42 ? ~tag : tag);
      }
      TSE();
      return;
    }
    const unsigned kk = tok ? k2 : k1;
    const int row = key_row(kk);
    float* rr = reinterpret_cast<float*>(shm + RR);
    if (wid == 7) {
      POLL { PDBG(it);
        unsigned g;
        asm volatile("global_load_dword %0, %1, %2 sc1\n\ts_waitcnt vmcnt(0)" : "=&v"(g) : "v"(((row >> 4) * 8 + (hid >> 2)) * 8), "s"(ws + WS_DON) : "memory");
        if (__all(g != SENT)) break;
        __builtin_amdgcn_s_sleep(2);
      }
    } else {
      const unsigned po = (unsigned)((wid * 8 + g4) * 8192 + tok * 4096 + (hid >> 1) * 256 + l16 * 16);
      u32x4 v[2];
      if (OPT_RD) __builtin_amdgcn_s_sleep(OPT_RD);
      POLL { PDBG(it);
        asm volatile("global_load_dwordx4 %0, %2, %3 sc1\n\tglobal_load_dwordx4 %1, %2, %4 sc1\n\ts_waitcnt vmcnt(0)"
                     : "=&v"(v[0]), "=&v"(v[1])
                     : "v"(po), "s"(ws + WS_PART), "s"(ws + WS_PART + 32768)
                     : "memory");
        if (__all(max(max4(v[0]), max4(v[1])) != SENT)) break;
        __builtin_amdgcn_s_sleep(OPT_RR);
      }
      TS(28);
      const f32x4 t = __builtin_bit_cast(f32x4, v[0]) + __builtin_bit_cast(f32x4, v[1]);
      rr[(l16 * 4 + g4) * 8 + wid] = swap16_add(swap32_add(t[0], t[2]), swap32_add(t[1], t[3]));
    }
    const float x1 = bf2f(*reinterpret_cast<const u16*>(shm + SX1 + tok * 2048 + col * 2));
    const float h2 = bf2f(*reinterpret_cast<const u16*>(shm + HS + (col >> 3) * 32 + tok * 16 + (col & 7) * 2));
    float rsg = rbf(1.0f / (1.0f + __expf(-key_val(kk))));
    asm volatile("" : "+v"(rsg));
    LBAR();
    asm volatile(".Lcpf_e1:");
    TS(5);
    if (wid == 0) {
      const f32x4 r0 = *reinterpret_cast<const f32x4*>(rr + lane * 8), r1 = *reinterpret_cast<const f32x4*>(rr + lane * 8 + 4);
      const float sum = ((r0[0] + r0[1]) + (r0[2] + r0[3])) + ((r1[0] + r1[1]) + r1[2]);
      out[(size_t)row * D + col] = (u16)f2bf(x1 + gb * rbf(h2 + rbf(rbf(sum) * rsg)));
    }
    TS(15);
    TSE();
  }
}

extern "C" void kernel_launch(void* const* d_in, const int* in_sizes, int n_in, void* d_out, int out_size, void* d_ws, size_t ws_size,
                              hipStream_t stream) {
  const u16* x = (const u16*)d_in[0];
  const u16* wsr = (const u16*)d_in[1];
  const u16* wsi = (const u16*)d_in[2];
  const u16* whr = (const u16*)d_in[3];
  const u16* whi = (const u16*)d_in[4];
  const u16* w_r = (const u16*)d_in[6];
  const u16* w_gate = (const u16*)d_in[7];
  const u16* w_up = (const u16*)d_in[8];
  const u16* w_down = (const u16*)d_in[9];
  const u16* c = (const u16*)d_in[10];
  const u16* w_mod = (const u16*)d_in[11];
  u16* out = (u16*)d_out;
  k_main<<<256, 512, 0, stream>>>(x, (char*)d_ws, wsr, wsi, c, w_mod, whr, whi, w_r, w_gate, w_up, w_down, out);
}
```

```cpp
#include <hip/hip_runtime.h>
#include <hip/hip_bf16.h>
#include <stdint.h>
#include <utility>

typedef unsigned short u16;
typedef unsigned long long u64;
using bf16x8 = __attribute__((ext_vector_type(8))) short;
using bf16v8 = __attribute__((ext_vector_type(8))) __bf16;
using f32x4 = __attribute__((ext_vector_type(4))) float;
using f32x2 = __attribute__((ext_vector_type(2))) float;
using u32x4 = __attribute__((ext_vector_type(4))) unsigned;
using u32x2 = __attribute__((ext_vector_type(2))) unsigned;

constexpr int S = 2048, D = 1024, F = 3584;

__device__ __forceinline__ float bf2f(unsigned v) { return __uint_as_float(v << 16); }
__device__ __forceinline__ float bflo(unsigned v) { return __uint_as_float(v << 16); }
__device__ __forceinline__ float bfhi(unsigned v) { return __uint_as_float(v & 0xffff0000u); }
__device__ __forceinline__ unsigned f2bf(float f) {
  __hip_bfloat16 b = __float2bfloat16(f);
  return (unsigned)*reinterpret_cast<u16*>(&b);
}
__device__ __forceinline__ float rbf(float f) { return bf2f(f2bf(f)); }
using bf16v2 = __attribute__((ext_vector_type(2))) __bf16;
__device__ __forceinline__ unsigned pack2(float lo, float hi) {
  return __builtin_bit_cast(unsigned, __builtin_convertvector(f32x2{lo, hi}, bf16v2));
}

template <int CTRL>
__device__ __forceinline__ float dpp_f(float v) {
  return __int_as_float(__builtin_amdgcn_mov_dpp(__float_as_int(v), CTRL, 0xf, 0xf, true));
}
template <int CTRL>
__device__ __forceinline__ float dpp_add(float v) { return v + dpp_f<CTRL>(v); }
__device__ __forceinline__ float row_sum(float v) { return dpp_add<0x140>(dpp_add<0x141>(dpp_add<0x4E>(dpp_add<0xB1>(v)))); }
template <int CTRL>
__device__ __forceinline__ unsigned dpp_u(unsigned v) { return (unsigned)__builtin_amdgcn_mov_dpp((int)v, CTRL, 0xf, 0xf, true); }
__device__ __forceinline__ unsigned wave_max_u(unsigned v) {
  v = max(v, dpp_u<0xB1>(v));
  v = max(v, dpp_u<0x4E>(v));
  v = max(v, dpp_u<0x141>(v));
  v = max(v, dpp_u<0x140>(v));
  const unsigned a = (unsigned)__builtin_amdgcn_readlane((int)v, 0), b = (unsigned)__builtin_amdgcn_readlane((int)v, 16);
  const unsigned c = (unsigned)__builtin_amdgcn_readlane((int)v, 32), d = (unsigned)__builtin_amdgcn_readlane((int)v, 48);
  return max(max(a, b), max(c, d));
}
__device__ __forceinline__ u64 mk64(unsigned hi, unsigned lo) { return ((u64)hi << 32) | lo; }
__device__ __forceinline__ float swap32_add(float a, float b) {
  auto r = __builtin_amdgcn_permlane32_swap(__float_as_uint(a), __float_as_uint(b), false, false);
  return __uint_as_float(r[0]) + __uint_as_float(r[1]);
}
__device__ __forceinline__ float swap16_add(float a, float b) {
  auto r = __builtin_amdgcn_permlane16_swap(__float_as_uint(a), __float_as_uint(b), false, false);
  return __uint_as_float(r[0]) + __uint_as_float(r[1]);
}
__device__ __forceinline__ float ar4(float v) {
  const float t = swap32_add(v, v);
  return swap16_add(t, t);
}
__device__ __forceinline__ float wave_sum_s(float v) {
  v = row_sum(v);
  v += __int_as_float(__builtin_amdgcn_update_dpp(0, __float_as_int(v), 0x142, 0xa, 0xf, false));
  v += __int_as_float(__builtin_amdgcn_update_dpp(0, __float_as_int(v), 0x143, 0xc, 0xf, false));
  return __int_as_float(__builtin_amdgcn_readlane(__float_as_int(v), 63));
}
__device__ __forceinline__ float silu_bf(float c) { return rbf(c * __builtin_amdgcn_rcpf(1.0f + __expf(-c))); }
__device__ __forceinline__ float dsum(unsigned v, float a) {
  return __builtin_amdgcn_fdot2_f32_bf16(__builtin_bit_cast(bf16v2, v), __builtin_bit_cast(bf16v2, 0x3f803f80u), a, false);
}
template <class Fn, int... I>
__device__ __forceinline__ void sfor_(Fn&& f, std::integer_sequence<int, I...>) { (f(std::integral_constant<int, I>{}), ...); }
template <int N, class Fn>
__device__ __forceinline__ void sfor(Fn&& f) { sfor_(f, std::make_integer_sequence<int, N>{}); }
constexpr unsigned SENT = 0xFFFFFFFFu;
__device__ __forceinline__ f32x2 unpk(unsigned v) { return f32x2{bflo(v), bfhi(v)}; }

constexpr size_t WS_EPO = 0;
constexpr size_t WS_XFL = 4096;
constexpr size_t WS_HFL = 40960;
constexpr size_t WS_XF2 = WS_HFL + 512;
constexpr size_t WS_SFL = WS_HFL + 8192;
constexpr size_t WS_MOD = 65536;
constexpr size_t WS_Q = WS_MOD + 32768;
constexpr size_t WS_ARM = WS_Q + 8192;
constexpr size_t WS_RS = WS_ARM;
constexpr size_t WS_KEY = WS_RS + 16384;
constexpr size_t WS_HDR = WS_KEY + 16384;
constexpr size_t WS_UFL = WS_HDR + 4096;
constexpr size_t WS_DON = WS_UFL + 98304;
constexpr size_t WS_STAT = WS_DON + 12288;
constexpr size_t WS_CAND = WS_STAT + 262144;
constexpr size_t WS_PART = WS_CAND + 524288;
constexpr size_t WS_XAF = WS_PART + 458752;
constexpr size_t WS_WAK = WS_XAF + 24576;
static_assert(WS_WAK + 8192 <= WS_ARM + 448 * 3328, "armed region");
constexpr size_t WS_XCH = 2u << 20;
constexpr size_t WS_H = 8u << 20;
constexpr size_t WS_WH = 12u << 20;
constexpr size_t WS_U = 16u << 20;
constexpr size_t WS_DBG = 64u << 20;
#define NEWDESIGN 1

#define PCAP (1 << 20)
#define PDBG(it)
#define TS(i)
#define TSCAP()
#define TSC0()
#define TSC1(i)
#define TSD(i)
#define TSE()
__device__ __forceinline__ void st16_wt(void* p, u32x4 v) {
  asm volatile("global_store_dwordx4 %0, %1, off sc1\n\ts_nop 1" ::"v"(p), "v"(v) : "memory");
}
#define ST16(voff, v, base, off) asm volatile("global_store_dwordx4 %0, %1, %2 offset:%3 sc1\n\ts_nop 1" ::"v"(voff), "v"(v), "s"(base), "n"(off) : "memory")
#define ST4(p, v) __hip_atomic_store(p, v, __ATOMIC_RELAXED, __HIP_MEMORY_SCOPE_AGENT)
#define WAIT_V(n) asm volatile("s_waitcnt vmcnt(%0)" ::"n"(n) : "memory")
#define WAIT_L(n) asm volatile("s_waitcnt lgkmcnt(%0)" ::"n"(n) : "memory")
#define BARRIER() asm volatile("s_barrier" ::: "memory")
#define LBAR() asm volatile("s_waitcnt lgkmcnt(0)\n\ts_barrier" ::: "memory")
#define DSR(dst, addr, off) asm volatile("ds_read_b128 %0, %1 offset:%2" : "=v"(dst) : "v"(addr), "n"(off) : "memory")
#define MFMA(c, a, b) asm volatile("v_mfma_f32_16x16x32_bf16 %0, %1, %2, %0" : "+v"(c) : "v"(a), "v"(b))
#define MFMA0(c, a, b) asm volatile("v_mfma_f32_16x16x32_bf16 %0, %1, %2, 0" : "=&v"(c) : "v"(a), "v"(b))
#define GLDSP(m0v, voff, gbase, pol) \
  asm volatile("s_mov_b32 m0, %0\n\ts_nop 0\n\tglobal_load_lds_dwordx4 %1, %2" pol ::"s"(m0v), "v"(voff), "s"(gbase) : "memory")
#define GLDS(m0v, voff, gbase) GLDSP(m0v, voff, gbase, "")
#define WPOL " nt"
#define LDX(dst, voff, base, off) asm volatile("global_load_dwordx4 %0, %1, %2 offset:%3" : "=v"(dst) : "v"(voff), "s"(base), "n"(off) : "memory")
#define LDXS(dst, voff, base, off) asm volatile("global_load_dwordx4 %0, %1, %2 offset:%3 sc1" : "=v"(dst) : "v"(voff), "s"(base), "n"(off) : "memory")
#define BF8(v) __builtin_bit_cast(bf16v8, v)
#define MM(a, b, c) __builtin_amdgcn_mfma_f32_16x16x32_bf16(BF8(a), BF8(b), c, 0, 0, 0)
#define LSPIN(v, p, cond)                                   \
  _Pragma("unroll 1") for (int it_ = 0; it_ < (1 << 26); ++it_) { \
    v = __atomic_load_n(p, __ATOMIC_RELAXED);               \
    if (cond) break;                                        \
    __builtin_amdgcn_s_sleep(1);                            \
  }
#define LSPIN0(v, p, cond)                                  \
  _Pragma("unroll 1") for (int it_ = 0; it_ < (1 << 28); ++it_) { \
    v = __atomic_load_n(p, __ATOMIC_RELAXED);               \
    if (cond) break;                                        \
  }
#define W6(a) "+v"(a[0]), "+v"(a[1]), "+v"(a[2]), "+v"(a[3]), "+v"(a[4]), "+v"(a[5])
#define W8(a) W6(a), "+v"(a[6]), "+v"(a[7])
#define PFL(ptr) asm volatile("s_mov_b32 m0, %0\n\ts_nop 0\n\tglobal_load_lds_dword %1, off" ::"s"(pfl), "v"(ptr) : "memory")
#define HOP(k, nx) asm volatile(".Lhop%0:\n\ts_cbranch_execz .Lhop%1" ::"n"(k), "n"(nx))
using s16x4 = __attribute__((ext_vector_type(4))) short;
__device__ __forceinline__ u32x4 trd(unsigned a) {
  const u32x2 lo = __builtin_bit_cast(u32x2, __builtin_amdgcn_ds_read_tr16_b64_v4i16((__attribute__((address_space(3))) s16x4*)(size_t)a));
  const u32x2 hi = __builtin_bit_cast(u32x2, __builtin_amdgcn_ds_read_tr16_b64_v4i16((__attribute__((address_space(3))) s16x4*)(size_t)(a + 512)));
  return u32x4{lo[0], lo[1], hi[0], hi[1]};
}
__device__ __forceinline__ int key_row(unsigned k) { return (int)((0xffffu - (k & 0xffffu)) & 2047u); }
__device__ __forceinline__ float key_val(unsigned k) {
  const unsigned ord = k >> 16;
  return bf2f(ord ^ ((ord & 0x8000u) ? 0x8000u : 0xffffu));
}
__device__ __forceinline__ unsigned nsent(float v) { return min(__float_as_uint(v), SENT - 1u); }
__device__ __forceinline__ f32x4 lo4(u32x4 q) { return f32x4{bflo(q[0]), bfhi(q[0]), bflo(q[1]), bfhi(q[1])}; }
__device__ __forceinline__ f32x4 hi4(u32x4 q) { return f32x4{bflo(q[2]), bfhi(q[2]), bflo(q[3]), bfhi(q[3])}; }
__device__ __forceinline__ u32x4 pk8(f32x4 a, f32x4 b) { return u32x4{pack2(a[0], a[1]), pack2(a[2], a[3]), pack2(b[0], b[1]), pack2(b[2], b[3])}; }
__device__ __forceinline__ u32x4 nsent4(u32x4 v) { return u32x4{min(v[0], SENT - 1u), min(v[1], SENT - 1u), min(v[2], SENT - 1u), min(v[3], SENT - 1u)}; }
__device__ __forceinline__ unsigned max4(u32x4 v) { return max(max(v[0], v[1]), max(v[2], v[3])); }
__device__ __forceinline__ unsigned max2(u32x2 v) { return max(v[0], v[1]); }
__device__ __forceinline__ unsigned hi2(u32x2 v) { return (v[0] >> 16) | (v[1] & 0xffff0000u); }
#define TOP2(CTRL)                                                  \
  do {                                                              \
    const unsigned o1 = dpp_u<CTRL>(t1), o2 = dpp_u<CTRL>(t2);      \
    t2 = max(min(t1, o1), max(t2, o2));                             \
    t1 = max(t1, o1);                                               \
  } while (0)
#define POLL _Pragma("unroll 1") for (int it = 0; it < PCAP; ++it)
#define OPT_RD 10
#define OPT_RR 2
#define OPT_SD 16
#define OPT_KS 2
#define P_D3 16
#define P_D4 2
#define U_D 36
#define XJ0 2
#define XJ1 5
#define XJ2 9
#define XJ3 10
#define TP_P0 309
#define TP_XL 346
#define CPF_K 24
#define CPF_N 10
#define HP4 0
#define HP5 1
#define HP6 2
#define HP7 3
#define CPF_B 10
#define NOEXP 0
#define NOARM 0
#define HST 2
#define XST 32
#define UST 32
#define OPT_KD 32
#define KORD 1
#define UF_AT 11
#define P_NT 5
#define P_D5 4
#define PF23 1
#define MQ_AT 31
#define MP_AT 16
#define P_AT 6
#define H_AT 24
#define W_NQ 0
#define XQ_AT 24
#define U_R 1
#define A_PR 4
#define WK_AT 1
#define ZD 1
#define U_BM 0xffd0u
static_assert(__builtin_popcount(U_BM) / 2 == 5, "barriers 34 .. 43 or 44");
#define A_LD 3
#define HOPS 0
#define XQ0 2
#define XQ1 4
#define XQ2 18
#define XQ3 20
#define ABL 0
#define F_AT 3
#define ST_E 0
#define PUB_ALL 0
#define XA_G 0
#define XA_D 12
#define PF_N 2
#define SF_1 SF_LD
#define TLBW 0
#define WK_LD 1
#define P_E 0
#define NORS 0
#define LSLP 0
#define Q_AT 9
#define B_VAT 42
constexpr int SL = 22528, NS = 6, MISC = NS * SL;
constexpr int ah(int e) { return e < 7 ? 4 : e < 28 ? 5 + (e - 7) % 3 : 4 + (e - 28) % 4; }
constexpr int ap(int e) { return e < 7 ? e : e < 28 ? (e - 7) / 3 : 7 + (e - 28) / 4; }
constexpr int E1 = 286, E3 = E1 + 66, NE = E3 + 6;
constexpr int eh(int e) { return e < E1 ? e / 11 : 26 + (e - E1) % 6; }
constexpr int ep(int e) { return e < E1 ? e % 11 : e < E3 ? (e - E1) / 6 : 5 + (e - E3); }

__global__ __launch_bounds__(512, 2) void k_main(const u16* __restrict__ x, char* ws, const u16* __restrict__ Y0, const u16* __restrict__ Y1,
                                              const u16* __restrict__ cvec, const u16* __restrict__ wmod, const u16* __restrict__ X0,
                                              const u16* __restrict__ X1, const u16* __restrict__ wr_, const u16* __restrict__ wg,
                                              const u16* __restrict__ wu, const u16* __restrict__ wd, u16* __restrict__ out) {
  constexpr int STG = 114688;
  __shared__ __attribute__((aligned(1024))) char shm[MISC + 16384];
  const int b = blockIdx.x, xcd = b & 7, j8 = b >> 3;
  const int cc = xcd >> 2, qx = xcd & 3;
  const int tid = threadIdx.x, lane = tid & 63;
  const int wid = __builtin_amdgcn_readfirstlane(tid >> 6);
  const unsigned lds0 = (unsigned)(size_t)(__attribute__((address_space(3))) char*)shm;
  const char* cpf;
  {
    unsigned long long pc;
    unsigned len;
    asm volatile("s_getpc_b64 %0\n.Lcpf_here:\n\ts_mov_b32 %1, .Lcpf_e1-.Lcpf_here\n\ts_max_u32 %1, %1, .Lcpf_e2-.Lcpf_here" : "=s"(pc), "=s"(len)::"scc");
    cpf = reinterpret_cast<const char*>(pc) + min((unsigned)((lane * 32 + j8) * 128), len);
  }
  const unsigned pfl = lds0 + MISC + 16128;
  if (wid == 4 && lane < CPF_N) PFL(cpf);
  u32x4 xv[2];
  {
    const unsigned xo = (unsigned)((b * 8 + wid) * D + lane * 8) * 2;
    asm volatile("global_load_dwordx4 %0, %2, %3\n\tglobal_load_dwordx4 %1, %2, %3 offset:1024" : "=&v"(xv[0]), "=&v"(xv[1]) : "v"(xo), "s"(x) : "memory");
  }
  asm volatile("" ::"s"(ws), "s"(Y0), "s"(Y1), "s"(X0), "s"(X1));
  unsigned* xfl = reinterpret_cast<unsigned*>(ws + WS_XFL);
  unsigned* hfl = reinterpret_cast<unsigned*>(ws + WS_HFL);
  unsigned* modv = reinterpret_cast<unsigned*>(ws + WS_MOD);
#define SKEWPT()
  const int g4 = lane >> 4, l16 = lane & 15;
  u64 tag0, tag;
  asm volatile("global_load_dwordx2 %0, %1, %2 sc1" : "=v"(tag0) : "v"(0u), "s"(ws) : "memory");
  const char* Yx = reinterpret_cast<const char*>(cc ? Y1 : Y0) + (size_t)qx * 2097152;
#define PFG(r16, g) (Yx + (size_t)(((r16) * 16 + l16) * 4096 + g4 * 128 + ((((g) + qx) & 7) << 9)))
  unsigned ta = lds0 + STG + ((lane >> 2) & 3) * 2368 + (wid * 128 + (lane >> 5) * 32 + (lane & 3) * 8 + (g4 & 1) * 4) * 2;
  unsigned oo = (unsigned)((wid * 4 + (lane >> 5)) * 131072 + (b >> 2) * 2048 + (g4 & 1) * 1024 + ((b & 3) * 16 + l16) * 16), oo2 = oo + 262144;
  unsigned wa = lds0 + STG + wid * 2368 + lane * 16;
  asm volatile("" : "+v"(ta), "+v"(oo), "+v"(oo2), "+v"(wa));
  const int hsl = (b & 32) + xcd * 4 + (j8 & 3);
  const char* hp = reinterpret_cast<const char*>((b & 32) ? X1 : X0) + (size_t)(hsl & 31) * 65536 + wid * 8192;
#define WHLD(i0)                                                                                                                  \
  _Pragma("unroll") for (int i = i0; i < i0 + 4; ++i)                                                                             \
      GLDS(lds0 + wid * 8192 + i * 1024, (unsigned)((lane ^ ((i >> 1) | ((wid & 2) << 1))) * 16), hp + i * 1024)
  if (wid >= 8 - PF_N) PFL(PFG(j8, 7 - wid));
  if (j8 >= 24) {
    WHLD(0);
    WAIT_V(5);
  } else if (wid >= 8 - PF_N) {
    WAIT_V(2);
  } else {
    WAIT_V(1);
  }
  {
    asm volatile("; AUDIT_OK %0 %1" : "+v"(xv[0]), "+v"(xv[1])::"memory");
    TS(32);
    const float s0 = dsum(xv[0][3], dsum(xv[0][2], dsum(xv[0][1], dsum(xv[0][0], 0.f))));
    const float s1 = dsum(xv[1][3], dsum(xv[1][2], dsum(xv[1][1], dsum(xv[1][0], 0.f))));
    const float mf = wave_sum_s(s0 + s1) * (1.0f / D), m = rbf(mf);
    const f32x4 d0 = lo4(xv[0]) - m, d1 = hi4(xv[0]) - m, d2 = lo4(xv[1]) - m, d3 = hi4(xv[1]) - m;
    const f32x4 q = (d0 * d0 + d1 * d1) + (d2 * d2 + d3 * d3);
    const float vf = wave_sum_s((q[0] + q[1]) + (q[2] + q[3])) * (1.0f / D) - (mf - m) * (mf - m);
    const float rden = __builtin_amdgcn_rcpf(rbf(__builtin_amdgcn_sqrtf(rbf(rbf(vf) + 1e-5f))));
    asm volatile("ds_write_b128 %0, %1" ::"v"(wa), "v"(pk8(d0 * rden, d1 * rden)) : "memory");
    asm volatile("ds_write_b128 %0, %1 offset:1024" ::"v"(wa), "v"(pk8(d2 * rden, d3 * rden)) : "memory");
    LBAR();
    TS(34);
    u32x2 t[4];
    asm volatile(
        "ds_read_b64_tr_b16 %0, %4\n\tds_read_b64_tr_b16 %1, %4 offset:%5\n\t"
        "ds_read_b64_tr_b16 %2, %4 offset:128\n\tds_read_b64_tr_b16 %3, %4 offset:%6\n\ts_waitcnt lgkmcnt(0)"
        : "=&v"(t[0]), "=&v"(t[1]), "=&v"(t[2]), "=&v"(t[3])
        : "v"(ta), "n"(4 * 2368), "n"(4 * 2368 + 128)
        : "memory");
    asm volatile("global_store_dwordx4 %0, %2, %4 sc1\n\tglobal_store_dwordx4 %1, %3, %4 sc1\n\ts_nop 1" ::"v"(oo), "v"(oo2),
                 "v"(u32x4{t[0][0], t[0][1], t[1][0], t[1][1]}), "v"(u32x4{t[2][0], t[2][1], t[3][0], t[3][1]}), "s"(ws + WS_H)
                 : "memory");
  }
#define PBAR(n)                                                                                                          \
  do {                                                                                                                   \
    asm volatile("s_waitcnt vmcnt(%2)\n\tv_lshl_add_u64 %0, %1, 0, 1" : "=v"(tag) : "v"(tag0), "n"(n) : "memory");       \
    TS(36);                                                                                                              \
    BARRIER();                                                                                                           \
    TS(37);                                                                                                              \
  } while (0)
#define XFLAG() do { if ((tid | 1) == 385) ST4(reinterpret_cast<u64*>(ws + ((tid & 1) ? WS_XF2 + b * 8 : WS_XFL + b * 128)), tag); } while (0)
  if (j8 < 24) {
    const int mt = (j8 * 11) >> 5, nt = j8 - mt * 3, grp = xcd * 3 + nt;
#define FROW(f) (nt * 176 + (f) * 16 - (nt == 2 && (f) > 5 ? 16 : 0))
#define PROW(p) FROW(cc ? 10 - (p) : (p))
    constexpr int SCA = MISC, TGA = MISC + 512, TV = MISC + 1024, TSUM = MISC + 1792, FLG = MISC + 1856, SCW = MISC + 2048, RKA = MISC + 3072,
                  RKB = MISC + 5120, RKZ = MISC + 6656, IDT = MISC + 7168, ZSC = MISC + 9216;
    const bool zd = ZD && nt == 2;
    unsigned* flg = reinterpret_cast<unsigned*>(shm + FLG);
    unsigned* ufl = reinterpret_cast<unsigned*>(ws + WS_UFL) + grp * 1024;
    const char* Ug = ws + WS_U + (size_t)grp * (16 * SL + 1024) + 1024;
    const unsigned perm = KORD ? (unsigned)mt | (((0x76543210u & ((1u << (4 * mt)) - 1u)) << 4) | (0x76543210u & (0xfffffff0u << (4 * mt))))
                               : __builtin_rotateright32(0x76543210u, 4 * mt);
#define KT(i) ((perm >> (4 * (i))) & 7u)
    const int kk = wid < 4 ? wid : wid - 2;
    const bool act = (wid < 4 || wid >= 6) && (kk || (cc == 0 && nt < 2));
    const int rg = qx * 32 + (PROW(5 + kk) >> 4);
    u32x4 pv[6];
    f32x4 acc[2][11], ga[2], x5[2];
    bf16x8 idt[2];
    unsigned spo = (unsigned)(((rg * 8 + g4 * 2) * 16 + l16) * 16);
    u32x4 sr[2];
#define STLD() asm volatile("global_load_dwordx4 %0, %2, %3 sc1\n\tglobal_load_dwordx4 %1, %2, %3 offset:256 sc1" : "=&v"(sr[0]), "=&v"(sr[1]) : "v"(spo), "s"(ws + WS_STAT) : "memory")
    const char* xr = ws + WS_XCH + (size_t)(b * 4 + (wid & 3)) * 6144 + 3072;
#define XCHLD()                                                                                                       \
  asm volatile(                                                                                                       \
      "global_load_dwordx4 %0, %6, %7 offset:2048 sc1\n\tglobal_load_dwordx4 %1, %6, %7 offset:1024 sc1\n\t"          \
      "global_load_dwordx4 %2, %6, %7 sc1\n\tglobal_load_dwordx4 %3, %6, %7 offset:-1024 sc1\n\t"                     \
      "global_load_dwordx4 %4, %6, %7 offset:-2048 sc1\n\tglobal_load_dwordx4 %5, %6, %7 offset:-3072 sc1"            \
      : "=&v"(pv[0]), "=&v"(pv[1]), "=&v"(pv[2]), "=&v"(pv[3]), "=&v"(pv[4]), "=&v"(pv[5])                            \
      : "v"(lane * 16), "s"(xr)                                                                                       \
      : "memory")
    if (wid < 4) {
      bf16x8 X[4][2][2], Y[8];
      u32x4 xq[6];
      const unsigned xo0 = lane * 16 + 4096;

#define XLD(dst, XP, XH, i) LDXS(dst, xo0, XP, ((XH)&3) * 2048 + (i)*1024 - 4096)
      int xt = lane < 16 ? (int)WS_H + (mt * 4 + wid) * 131072 + (((qx * 2 + lane) & 15) << 13) : (int)WS_WH + (cc * 32 + mt * 4 + wid) * 65536 + (KT(lane & 7) << 13);
      asm volatile("" : "+v"(xt));
#define XT(j) (ws + (unsigned)__builtin_amdgcn_readlane(xt, j))
#define WP(h) XT(16 + ((h) >> 2))
#define XS(h) X[((h) >> 1) & 3][(h)&1]
      {
        unsigned ye = lds0 + l16 * 128 + (g4 ^ (lane & 7)) * 16, yo = ye ^ 64;
        const char* xpa = XT(0);
        const char* xpb = XT(1);
#pragma unroll
        for (int i = 0; i < 22; ++i) {
          acc[i / 11][i % 11] = f32x4{0.f, 0.f, 0.f, 0.f};
          asm volatile("" : "+v"(acc[i / 11][i % 11]));
        }
        __builtin_amdgcn_s_setprio(1);
        PBAR(0);
        if (wid == 3) {
          const unsigned* fp = xfl + (qx * 32 + lane) * XST;
          if (P_D3) __builtin_amdgcn_s_sleep(P_D3);
          POLL { PDBG(it);
            u64 f;
            asm volatile("global_load_dwordx2 %0, %1, off sc1\n\ts_waitcnt vmcnt(0)" : "=&v"(f) : "v"(fp) : "memory");
            if (__all(f == tag)) break;
            __builtin_amdgcn_s_sleep(P_D4);
          }
        }
        if (HOPS) {
          asm volatile("s_cmp_eq_u32 %0, 3\n\ts_cbranch_scc1 .Lhop%1\n\ts_mov_b64 exec, 0\n\ts_cmp_lt_u32 %0, 1\n\ts_cbranch_scc1 .Lhop0\n\t"
                       "s_cmp_eq_u32 %0, 1\n\ts_cbranch_scc1 .Lhop15\n\ts_branch .Lhop30\n.Lhop%1:\n\ts_mov_b64 exec, -1" ::"s"(wid), "n"(900) : "scc");
        }
        TS(38);
        BARRIER();
        TS(39);
#pragma unroll
        for (int h = 0; h < 7; ++h) {
          XLD(XS(h)[0], h < 4 ? xpa : xpb, h, 0);
          XLD(XS(h)[1], h < 4 ? xpa : xpb, h, 1);
        }
#pragma unroll
        for (int j = 0; j < 6; ++j) DSR(xq[j], lds0 + 5 * SL + l16 * 256 + (((wid * 4 + g4) ^ (lane & 7)) << 4), j * 4096);
#pragma unroll
        for (int j = 0; j < 7; ++j) DSR(Y[j], ye, j * 2048);
        unsigned so = 0, a0 = ye, a1 = yo, yb[2], tq, u = 2;
#define STEP2(S0, XA, XB)                                                                                                      \
  sfor<44>([&](auto nn) __attribute__((always_inline)) {                                                                       \
    constexpr int n = (S0)*22 + decltype(nn)::value, q = n % 22, h = n / 11, p = n % 11, s = n / 22;                           \
    (void)acc, (void)X, (void)Y, (void)a0, (void)a1, (void)xo0, (void)XA, (void)XB, (void)so, (void)ye, (void)yo, (void)u, (void)xt, (void)tq, (void)ws; \
    if constexpr (q == 11) BARRIER();                                                                                          \
    if constexpr (q == 0 || q == 10) asm volatile("s_waitcnt vmcnt(12) lgkmcnt(5)" ::: "memory");                              \
    else if constexpr ((q & 1) == 0) WAIT_L(5);                                                                                \
    MFMA(acc[0][p], XS(h)[0], Y[n & 7]);                                                                                       \
    DSR(Y[(n + 7) & 7], (q < 4 || q >= 15) ? a0 : a1, ((p + 7) % 11) * 2048);                                                  \
    MFMA(acc[1][p], XS(h)[1], Y[n & 7]);                                                                                       \
    if constexpr (q == XQ0 || q == XQ1) XLD(XS(h + 7)[q == XQ1], (s & 1) ? XB : XA, h + 7, q == XQ1);                          \
    if constexpr (q == XQ2 || q == XQ3) XLD(XS(h + 7)[q == XQ3], XB, h + 7, q == XQ3);                                         \
    if constexpr (q == 0 && !(s & 1)) asm volatile("v_readlane_b32 %0, %2, %1\n\ts_add_u32 %1, %1, 1" : "=&s"(tq), "+s"(u) : "v"(xt) : "scc"); \
    if constexpr (q == 0 && (s & 1)) asm volatile("s_add_u32 %0, %0, %1" : "+s"(so) : "n"(SL) : "scc");                        \
    if constexpr (q == 6 && !(s & 1)) asm volatile("s_add_u32 %0, %0, %1" : "+s"(so) : "n"(SL) : "scc");                       \
    if constexpr (q == 8 && (s & 1)) asm volatile("s_cmp_lg_u32 %0, %1\n\ts_cselect_b32 %0, %0, 0" : "+s"(so) : "n"(NS * SL) : "scc"); \
    if constexpr (q == 8 && !(s & 1)) { XB = ws + tq; asm volatile("" : "+s"(XB)); }                                           \
    if constexpr (q == 12) asm volatile("v_add_u32 %0, %1, %2" : "=v"(a0) : "s"(so), "v"(ye));                                 \
    if constexpr (q == 16) asm volatile("v_add_u32 %0, %1, %2" : "=v"(a1) : "s"(so), "v"(yo));                                 \
  })
#define YRD(e) DSR(Y[(e)&7], yb[((32 + eh(e) / 2) % NS) / 2 - 1], ((32 + eh(e) / 2) & 1) * SL + ep(e) * 2048 + (eh(e) & 1) * 1024)
        const char* xw = ws + WS_XCH + (size_t)((b ^ 4) * 4 + wid) * 6144 + 3072;
        unsigned rka, rkb;
        u32x4 ov;
        bf16x8 qf[2];
        auto BP = [&](auto nn) __attribute__((always_inline)) {
          constexpr int n = decltype(nn)::value, h = eh(n), p = ep(n), e = n + 7, m = n - E1 - 7;
          (void)ga, (void)qf, (void)idt, (void)rka, (void)rkb, (void)xq, (void)ov, (void)xw, (void)x, (void)pv, (void)xr, (void)ufl;
          (void)acc, (void)X, (void)Y, (void)yb, (void)xo0, (void)xt, (void)ws, (void)lane, (void)mt, (void)nt, (void)cc, (void)lds0, (void)wid, (void)g4, (void)perm;
          TSCAP();
          if constexpr (n == UF_AT) {
            TS(45);
            WAIT_V(n < 9 ? (n ? n - 1 : 0) : 8);
            if (lane == 0) ST4(ufl + (mt * 4 + wid) * UST, 1u);
            TS(46);
          }
          if constexpr (n == 37) TS(48);
          if constexpr (n == 37 || n == 275 || e == E3) BARRIER();
          if constexpr (n == 37) TS(4);
          if constexpr (n == E1) { TS(3); SKEWPT(); }
          if constexpr (n < E3 && p == 0) WAIT_V(n == 0 ? 17 : h <= 25 ? 12 : 2 * (31 - h));
          if constexpr (n == TP_XL) XCHLD();
          if constexpr ((n & 1) == 0) WAIT_L(e < NE ? 5 : NE - 2 - n);
          if constexpr (n < 11) MFMA0(acc[0][p], XS(h)[0], Y[n & 7]);
          else if constexpr (n < E3) MFMA(acc[0][p], XS(h)[0], Y[n & 7]);
          else MFMA(acc[0][p], qf[0], Y[n & 7]);
          if constexpr (e == E3) {
            DSR(qf[0], rka, 0);
            DSR(qf[1], rka, 256);
            DSR(ga[0], lds0 + TGA + wid * 128 + g4 * 32, 0);
            DSR(ga[1], lds0 + TGA + wid * 128 + g4 * 32, 16);
            DSR(idt[0], yb[1], IDT - 4 * SL);
            DSR(idt[1], yb[1], IDT - 4 * SL + 1024);
          }
          if constexpr (e < E3) YRD(e);
          else if constexpr (e < NE) DSR(Y[e & 7], rkb, (e - E3) * 256);
          if constexpr (n < 11) MFMA0(acc[1][p], XS(h)[1], Y[n & 7]);
          else if constexpr (n < E3) MFMA(acc[1][p], XS(h)[1], Y[n & 7]);
          else MFMA(acc[1][p], qf[1], Y[n & 7]);
          if constexpr (n >= 1 && n < 9) XLD(XS(4 + (n - 1) / 2)[(n - 1) & 1], WP(4), (n - 1) / 2, (n - 1) & 1);
          if constexpr (h >= 1 && h <= 24) {
            if constexpr (p == ((h & 1) ? XJ2 : XJ0)) XLD(XS(h + 7)[0], WP(h + 7), h + 7, 0);
            if constexpr (p == ((h & 1) ? XJ3 : XJ1)) XLD(XS(h + 7)[1], WP(h + 7), h + 7, 1);
          }
          if constexpr (m >= 0 && m < 36) {
            constexpr int q = m / 6, r = m % 6;
            if constexpr (r < 2) {
              asm volatile("v_cvt_pk_bf16_f32 %0, %2, %3\n\tv_cvt_pk_bf16_f32 %1, %4, %5" : "=&v"(ov[2 * r]), "=&v"(ov[2 * r + 1])
                           : "v"(acc[r][q][0]), "v"(acc[r][q][1]), "v"(acc[r][q][2]), "v"(acc[r][q][3]));
              if constexpr (r == 1) asm volatile("v_min_u32 %0, -2, %0\n\tv_min_u32 %1, -2, %1" : "+v"(ov[0]), "+v"(ov[1]));
            } else if constexpr (r == 2) {
              asm volatile("v_min_u32 %0, -2, %0\n\tv_min_u32 %1, -2, %1" : "+v"(ov[2]), "+v"(ov[3]));
              ST16(lane * 16, ov, xw, q * 1024 - 3072);
              if constexpr (q == 5) TS(10);
            } else if constexpr (r < 5 && q < 5) {
              constexpr int i = r - 3;
              asm volatile("v_lshlrev_b32 %0, 16, %4\n\tv_and_b32 %1, 0xffff0000, %4\n\tv_lshlrev_b32 %2, 16, %5\n\tv_and_b32 %3, 0xffff0000, %5"
                           : "=&v"(acc[i][q][0]), "=&v"(acc[i][q][1]), "=&v"(acc[i][q][2]), "=&v"(acc[i][q][3])
                           : "v"(xq[q][2 * i]), "v"(xq[q][2 * i + 1]));
            }
          }
        };
        do {
          STEP2(0, xpb, xpa);
          if (__builtin_expect(u == 17, 0)) {
            TS(57);
            f32x4 c0, c1;
            const char* up = Ug + (mt * 2 + (wid >> 1)) * SL + (wid & 1) * 1024 + 4096;
            const unsigned ul = lds0 + (2 + (wid >> 1)) * SL + (wid & 1) * 1024 + lane * 16;
            u32x4 uv;
            DSR(c0, lds0 + SCA + wid * 128 + g4 * 32, 0);
            DSR(c1, lds0 + SCA + wid * 128 + g4 * 32, 16);
#define UMUL(j, k) asm volatile("v_mul_f32 %0, %0, %1" : "+v"(acc[(k) >> 2][j][(k)&3]) : "v"(((k) >> 2 ? c1 : c0)[(k)&3]))
#define UCVT(j, k) asm volatile("v_cvt_pk_bf16_f32 %0, %1, %2" : "=v"(uv[k]) : "v"(acc[(k) >> 1][j][((k)&1) * 2]), "v"(acc[(k) >> 1][j][((k)&1) * 2 + 1]))
#define UEP(j, r)                                                                                                                          \
  do {                                                                                                                                     \
    if constexpr ((r) == 0) { UMUL(j, 0); UMUL(j, 1); }                                                                                    \
    if constexpr ((r) == 1) { UMUL(j, 2); UMUL(j, 3); UMUL(j, 4); }                                                                        \
    if constexpr ((r) == 2) { UMUL(j, 5); UMUL(j, 6); }                                                                                    \
    if constexpr ((r) == 3) { UMUL(j, 7); UCVT(j, 0); UCVT(j, 1); }                                                                        \
    if constexpr ((r) == 4) { UCVT(j, 2); UCVT(j, 3); }                                                                                    \
    if constexpr ((r) == 5)                                                                                                                \
      asm volatile("global_store_dwordx4 %0, %2, %3 offset:%4 sc1\n\tds_write_b128 %1, %2 offset:%5\n\ts_nop 0"                        \
                   ::"v"(lane * 16), "v"(ul), "v"(uv), "s"(up + ((j) >> 2) * 8192), "n"(((j)&3) * 2048 - 4096), "n"((j)*2048) : "memory"); \
  } while (0)
            sfor<44>([&](auto nn) __attribute__((always_inline)) {
              constexpr int n = decltype(nn)::value, h = ah(n), p = ap(n), e = n + 7, j = n < 32 ? (n - 11) / 3 : 7 + (n - 33) / 4, r = n < 32 ? (n - 11) % 3 : (n - 33) % 4;
              constexpr bool ue = n >= 11 && n != 32 && r < 3;
              (void)up, (void)ul, (void)acc, (void)X, (void)Y, (void)ye, (void)yo, (void)c0, (void)c1, (void)xo0, (void)xpa, (void)lane, (void)uv;
              if constexpr (p == 0) WAIT_V(n < 8 ? 12 : 2 * (13 - n));
              if constexpr ((n & 1) == 0) WAIT_L(e < 44 ? 5 : 42 - n);
              MFMA(acc[0][p], XS(h)[0], Y[(n + 4) & 7]);
              if constexpr (e < 44) DSR(Y[(e + 4) & 7], (ah(e) & 1) ? yo : ye, ((ah(e) >> 1) & 1) * SL + ap(e) * 2048);
              if constexpr (ue) UEP(j, 2 * r);
              MFMA(acc[1][p], XS(h)[1], Y[(n + 4) & 7]);
              if constexpr (n < 2) XLD(XS(3)[n], xpa, 3, n);
              if constexpr (ue) UEP(j, 2 * r + 1);
            });
            TS(0);
            SKEWPT();
            asm volatile("s_nop 4");
            UEP(10, 0); UEP(10, 1); UEP(10, 2); UEP(10, 3); UEP(10, 4); UEP(10, 5);
            ye = lds0 + lane * 16;
            yo = ye + 1024;
            so = 4 * SL;
            a0 = ye + 4 * SL;
            a1 = yo + 4 * SL;
            yb[0] = ye + 2 * SL;
            yb[1] = a0;
            asm volatile("" : "+v"(yb[0]), "+v"(yb[1]), "+v"(ye), "+v"(yo));
            LBAR();
            TS(44);
            sfor<7>([&](auto ee) __attribute__((always_inline)) { (void)Y, (void)yb; YRD(decltype(ee)::value); });
            sfor<44>(BP);
            u = 19;
            xpa = WP(8);
          }
          STEP2(2, xpa, xpb);
        } while (u != 24);
        TS(2);
        yb[0] = a0;
        yb[1] = ye + 4 * SL;
        asm volatile("" : W6(xq));
        rka = lds0 + (g4 ? RKZ : RKA + wid * 512 + l16 * 16);
        rkb = lds0 + RKB + l16 * 16;
        sfor<NE - 264>([&](auto nn) __attribute__((always_inline)) { BP(std::integral_constant<int, 264 + decltype(nn)::value>{}); });
        __builtin_amdgcn_s_setprio(0);
        TSC1(59);
        TS(13);
        SKEWPT();
        x5[0] = lo4(xq[5]);
        x5[1] = hi4(xq[5]);
        asm volatile("" : "+v"(x5[0]), "+v"(x5[1]), "+v"(spo));
      }
    } else if (wid < 6) {
      const int hw = wid - 4;
      __builtin_amdgcn_s_setprio(2);
      unsigned voy[11];
#pragma unroll
      for (int i = 0; i < 11; ++i) voy[i] = (unsigned)((PROW(i) + hw * 8 + (lane >> 3)) * 4096 + ((lane & 7) ^ (lane >> 3)) * 16);
      const unsigned lbase = lds0 + hw * 1024;
      if (zd) voy[5] = (lane & 7) * 16;
      auto stage = [&](int n, int slot) {
        const char* yp = Yx + (((n + qx * 4) & 31) << 7);
        const unsigned l = lbase + slot * SL;
#pragma unroll
        for (int i = 0; i < 11; ++i) GLDS(i == 5 && zd ? lbase + ZSC : l + i * 2048, voy[i], yp);
      };
      const unsigned xo = (unsigned)(((qx * 512 + hw * 4 + g4) * D + mt * 128) * 2 + ((l16 ^ (hw * 4 + g4)) << 4));
      asm volatile("s_waitcnt vmcnt(0)" ::"v"(voy[0]), "v"(voy[10]), "v"(tag0), "v"(xo) : "memory");
      BARRIER();
#pragma unroll
      for (int i = 0; i < 12; ++i) GLDS(lds0 + 5 * SL + i * 2048 + hw * 1024, xo, x + (PROW(5 + i / 2) + (i & 1) * 8) * D);
#pragma unroll 1
      for (int s = 0; s <= A_LD; ++s) stage(s, s);
      WAIT_V(11 * A_LD);
      TS(38);
      BARRIER();
      int slot = A_LD + 1;
#pragma unroll 1
      for (int k = 1; k <= 31 - A_LD; ++k) {
        WAIT_V(11 * (A_LD - 1));
        BARRIER();
        if (hw == 1) TSD(k);
        if (LSLP) __builtin_amdgcn_s_sleep(LSLP);
        stage(k + A_LD, slot);
        slot = slot == NS - 1 ? 0 : slot + 1;
        if (k == CPF_K && hw == 1 && lane < CPF_N) PFL(cpf);
      }
      if (A_LD > 3) { WAIT_V(33); BARRIER(); }
      if (A_LD > 2) { WAIT_V(22); BARRIER(); }
      WAIT_V(0); BARRIER();
      const unsigned* ufp = ufl + (KT(min(lane >> 2, 6) + 1) * 4 + (lane & 3)) * UST;
      unsigned miss = ~0u;
      BARRIER();
      TS(47);
      if (U_D) __builtin_amdgcn_s_sleep(U_D);
      slot = 34 % NS;
#pragma unroll 1
      for (int n = 2; n < 16; ++n) {
        const bool bar = (U_BM >> n) & 1;
        const int lead = n - 3 - __builtin_popcount(U_BM & ((1u << n) - 1u));
#define UBAR() do { if (lead < 3) { if (lead == 2) WAIT_V(22); else if (lead == 1) WAIT_V(11); else WAIT_V(0); } WAIT_V(33); BARRIER(); } while (0)
        if (bar && n < 8) UBAR();
        POLL { PDBG(it);
          if (((miss >> (2 * n - 4)) & 3u) == 0) break;
          if (it) __builtin_amdgcn_s_sleep(U_R);
          unsigned f;
          asm volatile("global_load_dword %0, %1, off sc1\n\ts_waitcnt vmcnt(0)" : "=&v"(f) : "v"(ufp) : "memory");
          miss = (unsigned)__builtin_amdgcn_ballot_w64(f == SENT);
        }
        if (n == 2) TS(48);
        const char* yp = Ug + (KT(n >> 1) * 2 + (n & 1)) * SL + hw * 11264;
        unsigned l = lds0 + slot * SL + hw * 11264;
        slot = slot == NS - 1 ? 0 : slot + 1;
        if (n == CPF_B && hw == 1 && lane < CPF_N) PFL(cpf);
        asm volatile("" : "+s"(yp), "+s"(l));
        if (bar && n >= 8) {
          UBAR();
          if (hw == 1) TSD(n + 44);
          if (LSLP) __builtin_amdgcn_s_sleep(LSLP);
        }
#define G1(o) "global_load_lds_dwordx4 %0, %1 offset:" #o " sc1\n\t"
        asm volatile("s_mov_b32 m0, %2\n\ts_nop 0\n\t" G1(0) G1(1024) G1(2048) G1(3072) ::"v"(lane * 16), "s"(yp), "s"(l) : "memory");
        asm volatile("s_mov_b32 m0, %2\n\ts_nop 0\n\t" G1(0) G1(1024) G1(2048) G1(3072) ::"v"(lane * 16 + 4096), "s"(yp), "s"(l + 4096) : "memory");
        asm volatile("s_mov_b32 m0, %2\n\ts_nop 0\n\t" G1(0) G1(1024) G1(2048) ::"v"(lane * 16 + 8192), "s"(yp), "s"(l + 8192) : "memory");
      }
      if (__builtin_popcount(U_BM) == 10) {
        WAIT_V(33);
        BARRIER();
      }
      WAIT_V(0);
      BARRIER();
      BARRIER();
      if (WK_AT == 0 && b < 56 && tid == 256) ST4(reinterpret_cast<unsigned*>(ws + WS_WAK) + b * 32, 1u);
      BARRIER();
      if (WK_AT == 1 && b < 56 && tid == 256) ST4(reinterpret_cast<unsigned*>(ws + WS_WAK) + b * 32, 1u);
      TSE();
      return;
    } else {
      const int aw = wid - 6, rp = min(mt * 2 + aw, 10);
      PBAR(0);
      XFLAG();
      if (zd) {
#pragma unroll
        for (int s = 0; s < 5; ++s) *reinterpret_cast<u32x4*>(shm + s * SL + 10240 + aw * 1024 + lane * 16) = u32x4{0u, 0u, 0u, 0u};
      }
      LBAR();
      int wq = nt * 2 + aw;
#define WPF()                                                                                                                   \
  do {                                                                                                                          \
    if (wq < W_NQ) PFL(ws + WS_WH + (size_t)(cc * 32 + mt * 4 + (wq & 3)) * 65536 + (KT(wq >> 2) << 13) + lane * 128);          \
    wq += 6;                                                                                                                    \
  } while (0)
      float rs[4] = {};
      const unsigned ra = lds0 + rp * 2048 + lane * 16;
      unsigned so = 0;
#define RSUM()                                                                                                                        \
  do {                                                                                                                                \
    u32x4 R_[2];                                                                                                                      \
    if (NORS) break;                                                                                                                  \
    asm volatile("ds_read_b128 %0, %2\n\tds_read_b128 %1, %2 offset:1024\n\ts_waitcnt lgkmcnt(0)" : "=&v"(R_[0]), "=&v"(R_[1]) : "v"(ra + so) : "memory"); \
    _Pragma("unroll") for (int j = 0; j < 8; ++j) {                                                                                   \
      asm volatile("v_add_f32 %0, %0, %1" : "+v"(rs[(j >> 2) * 2]) : "v"(R_[j >> 2][j & 3] << 16));                                   \
      asm volatile("v_add_f32 %0, %0, %1" : "+v"(rs[(j >> 2) * 2 + 1]) : "v"(R_[j >> 2][j & 3] & 0xffff0000u));                       \
    }                                                                                                                                 \
    so = so == (NS - 1) * SL ? 0 : so + SL;                                                                                           \
  } while (0)
      {
#pragma unroll 1
        for (int bi = 0; bi < F_AT; ++bi) {
          RSUM();
          BARRIER();
        }
        u32x4 f;
#define FLD() asm volatile("global_load_dwordx4 %0, %1, %2 sc1" : "=v"(f) : "v"(lane * 16), "s"(ws + WS_XF2 + aw * 1024) : "memory")
        FLD();
        if (zd) *reinterpret_cast<u32x4*>(shm + 5 * SL + 10240 + aw * 1024 + lane * 16) = u32x4{0u, 0u, 0u, 0u};
#pragma unroll 1
        for (int bi = F_AT; bi < 4; ++bi) {
          RSUM();
          BARRIER();
        }
        POLL { PDBG(it);
          asm volatile("s_waitcnt vmcnt(0)" : "+v"(f)::"memory");
          if (__all(mk64(f[1], f[0]) == tag && mk64(f[3], f[2]) == tag)) break;
          __builtin_amdgcn_s_sleep(1);
          FLD();
        }
        asm volatile("s_waitcnt vmcnt(0)" : "+v"(f)::"memory");
      }
#pragma unroll 1
      for (int bi = 4; bi < H_AT; ++bi) {
        RSUM();
        BARRIER();
      }
      {
        u64 f;
        const unsigned* fp = hfl + lane * HST;
        asm volatile("global_load_dwordx2 %0, %1, off sc1" : "=v"(f) : "v"(fp) : "memory");
        RSUM();
        BARRIER();
        RSUM();
        BARRIER();
        POLL { PDBG(it);
          asm volatile("s_waitcnt vmcnt(0)" : "+v"(f)::"memory");
          if (__all(f == tag)) break;
          __builtin_amdgcn_s_sleep(1);
          asm volatile("global_load_dwordx2 %0, %1, off sc1" : "=v"(f) : "v"(fp) : "memory");
        }
        asm volatile("s_waitcnt vmcnt(0)" : "+v"(f)::"memory");
        unsigned c;
        const unsigned* cp = modv + 1024 + mt * 128 + aw * 64 + lane;
        asm volatile("global_load_dword %0, %1, off sc1" : "=v"(c) : "v"(cp) : "memory");
        if (TLBW && aw == 0 && lane < 3) PFL(lane == 0 ? Ug - 128 : lane == 1 ? Ug + 16 * SL : ws + WS_WH + (size_t)(cc * 32 + mt * 4) * 65536);
        WPF();
        RSUM();
        BARRIER();
        WPF();
        RSUM();
        BARRIER();
        POLL { PDBG(it);
          asm volatile("s_waitcnt vmcnt(0)" : "+v"(c)::"memory");
          if (__all(c != SENT)) break;
          __builtin_amdgcn_s_sleep(1);
          asm volatile("global_load_dword %0, %1, off sc1" : "=v"(c) : "v"(cp) : "memory");
        }
        asm volatile("s_waitcnt vmcnt(0)" : "+v"(c)::"memory");
        *reinterpret_cast<unsigned*>(shm + SCA + (aw * 64 + lane) * 4) = c ^ (cc ? 0x80000000u : 0u);
        flg[0] = 0u;
      }
#pragma unroll 1
      for (int bi = H_AT + 4; bi < 32; ++bi) {
        WPF();
        RSUM();
        if (bi < 30) BARRIER();
      }
      unsigned* rsb = reinterpret_cast<unsigned*>(ws + WS_RS) + qx * 512;
      {
        const float v0 = dpp_add<0x141>(dpp_add<0x4E>(dpp_add<0xB1>(rs[0] + rs[1])));
        const float v1 = dpp_add<0x141>(dpp_add<0x4E>(dpp_add<0xB1>(rs[2] + rs[3])));
        if ((lane & 7) < 2 && mt * 2 + aw < 11 && !(nt == 2 && rp == 5)) ST4(rsb + cc * S + PROW(rp) + (lane & 1) * 8 + (lane >> 3), nsent((lane & 1) ? v1 : v0));
      }
      BARRIER();
      if (XA_G) {
        __builtin_amdgcn_s_sleep(XA_D);
        const char* ap = ws + WS_XCH + (b ^ 4) * 24576 + aw * 12288 + 4096;
#pragma unroll
        for (int j = 0; j < 12; ++j) ST16(lane * 16, (u32x4{SENT, SENT, SENT, SENT}), ap + (j >> 3) * 8192, (j & 7) * 1024 - 4096);
        BARRIER();
        BARRIER();
        WAIT_V(0);
        if (lane == 0) ST4(reinterpret_cast<unsigned*>(ws + WS_XAF) + b * 32 + aw, 1u);
      }
#pragma unroll 1
      for (int k = XA_G ? 36 : 34; k <= (aw ? B_VAT : 45); ++k) BARRIER();
      u32x2 m[6];
      const unsigned lr = min(lane, 47);
      const unsigned o0 = aw ? (unsigned)(WS_Q - WS_MOD) + mt * 512 + lane * 8 : 8192u + mt * 512 + lane * 8;
      const unsigned o1 = aw ? (unsigned)(WS_RS - WS_MOD) + (qx * 512 + PROW(5 + (lr >> 3)) + (lr & 7) * 2) * 4 : o0 + 8192;
      const unsigned o2 = aw ? o1 + 8192 : o0 + 16384, o3 = aw ? (XA_G ? (unsigned)(WS_XAF - WS_MOD) + (b ^ 4) * 128 : o1) : 28672u + min(lane, 23) * 8;
#define VLD()                                                                                                                     \
  asm volatile("global_load_dwordx2 %0, %6, %10 sc1\n\tglobal_load_dwordx2 %1, %6, %11 sc1\n\tglobal_load_dwordx2 %2, %7, %10 sc1\n\t" \
               "global_load_dwordx2 %3, %7, %11 sc1\n\tglobal_load_dwordx2 %4, %8, %10 sc1\n\tglobal_load_dwordx2 %5, %9, %10 sc1"    \
               : "=&v"(m[0]), "=&v"(m[1]), "=&v"(m[2]), "=&v"(m[3]), "=&v"(m[4]), "=&v"(m[5])                                     \
               : "v"(o0), "v"(o1), "v"(o2), "v"(o3), "s"(modv), "s"(modv + 1024)                                                  \
               : "memory")
      VLD();
      if (aw) {
#pragma unroll
        for (int k = B_VAT + 1; k <= 44; ++k) BARRIER();
      }
      TS(30);
      POLL { PDBG(it);
        asm volatile("s_waitcnt vmcnt(0)" : W6(m)::"memory");
        if (__all(max(max(max(max2(m[0]), max2(m[1])), max(max2(m[2]), aw ? 0u : max2(m[3]))), max(max2(m[4]), max2(m[5]))) != SENT)) break;
        __builtin_amdgcn_s_sleep(2);
        VLD();
      }
      asm volatile("s_waitcnt vmcnt(0)" : W6(m)::"memory");
      if (aw == 0) {
        *reinterpret_cast<u32x2*>(shm + TGA + lane * 8) = m[0];
        *reinterpret_cast<unsigned*>(shm + TV + lane * 4) = hi2(m[2]);
        *reinterpret_cast<unsigned*>(shm + TV + 256 + lane * 4) = hi2(m[1]);
        *reinterpret_cast<unsigned*>(shm + TV + 512 + lane * 4) = hi2(m[3]);
        const float p0 = __uint_as_float(m[4][0]), p1 = __uint_as_float(m[4][1]);
        const unsigned hh = pack2(p0, p1);
        *reinterpret_cast<unsigned*>(shm + SCW + lane * 4) = hh;
        *reinterpret_cast<unsigned*>(shm + SCW + 256 + lane * 4) = pack2(p0 - bflo(hh), p1 - bfhi(hh));
        *reinterpret_cast<u32x2*>(shm + SCW + 512 + lane * 8) = u32x2{0x3f803f80u, 0x3f803f80u};
        const float u = __uint_as_float(m[5][0]) + __uint_as_float(m[5][1]), u2 = dpp_add<0xB1>(u), u8 = dpp_add<0x141>(dpp_add<0x4E>(u2));
        if (lane < 17 && !(lane & 1)) *reinterpret_cast<float*>(shm + TSUM + lane * 2) = lane < 16 ? u2 : u8;
      } else {
        BARRIER();
#pragma unroll
        for (int j = 0; j < 2; ++j) {
          const float r0 = __uint_as_float(m[0][j]), r1 = __uint_as_float(m[1][j]);
          const unsigned a0 = pack2(r0, r0), a1 = pack2(r1, r1);
          *reinterpret_cast<u32x4*>(shm + RKA + ((lane >> 4) * 32 + ((lane >> 1) & 1) * 16 + ((lane >> 2) & 3) * 4 + (lane & 1) * 2 + j) * 16) =
              u32x4{a0, pack2(r0 - bflo(a0), r0 - bflo(a0)), a1, pack2(r1 - bflo(a1), r1 - bflo(a1))};
          const float s0 = __uint_as_float(m[2][j]), s1 = -__uint_as_float(m[4][j]);
          const unsigned b0 = pack2(s0, s0 - rbf(s0)), b1 = pack2(s1, s1 - rbf(s1));
          *reinterpret_cast<u32x4*>(shm + RKB + (lr * 2 + j) * 16) = u32x4{b0, b0, b1, b1};
        }
        *reinterpret_cast<u32x2*>(shm + RKZ + lane * 8) = u32x2{0u, 0u};
        const unsigned one = g4 == (l16 >> 2) ? ((lane & 1) ? 0x3f800000u : 0x3f80u) : 0u, e0 = (lane & 2) ? 0u : one, e1 = (lane & 2) ? one : 0u;
        *reinterpret_cast<u32x4*>(shm + IDT + lane * 16) = u32x4{e0, e1, 0u, 0u};
        *reinterpret_cast<u32x4*>(shm + IDT + 1024 + lane * 16) = u32x4{0u, 0u, e0, e1};
      }
      TS(17);
      LBAR();
    }
    const int row0 = rg * 16 + l16, col1 = mt * 128 + g4 * 8;
    u32x4 xq[4], sa4[4];
#pragma unroll
    for (int q = 0; q < 4; ++q) DSR(sa4[q], lds0 + SCW + (lane & 3) * 256 + g4 * 16, q * 64);
    f32x2 cj = *reinterpret_cast<const f32x2*>(shm + TSUM + g4 * 8);
    float sw = *reinterpret_cast<const float*>(shm + TSUM + 32), ce = *reinterpret_cast<const float*>(shm + TSUM + mt * 4);
    u32x4 dk;
#pragma unroll
    for (int q = 0; q < 4; ++q) dk[q] = l16 == g4 * 4 + q ? ~0u : 0u;
    unsigned xw1 = lds0 + tid * 16;
    unsigned xa = lds0 + kk * 4096 + lane * 16, so = (unsigned)(((rg * 8 + mt) * 16 + lane) * 16);
    asm volatile("" : "+v"(dk), "+v"(cj), "+v"(sw), "+v"(ce), "+v"(xa), "+v"(so), "+v"(xw1));
    if (wid < 4) {
      POLL { PDBG(it);
        asm volatile("s_waitcnt vmcnt(0)" : W6(pv)::"memory");
        if (__all(max(max(max(max4(pv[0]), max4(pv[1])), max(max4(pv[2]), max4(pv[3]))), max(max4(pv[4]), max4(pv[5]))) != SENT)) break;
        __builtin_amdgcn_s_sleep(1);
        XCHLD();
      }
      asm volatile("s_waitcnt vmcnt(0)" : W6(pv)::"memory");
      TS(27);
      if (ST_E && tid == 0) __atomic_store_n(flg, 1u, __ATOMIC_RELAXED);
    } else if (ST_E) {
      unsigned sv;
      LSPIN(sv, flg, sv != 0u);
    }
    if (ST_E) STLD();
    else sr[0] = sr[1] = u32x4{SENT, SENT, SENT, SENT};
    if (wid < 4) {
      sfor<8>([&](auto kc) __attribute__((always_inline)) {
        constexpr int k = decltype(kc)::value;
        (void)acc, (void)idt, (void)pv, (void)ga, (void)x5, (void)xw1;
        if constexpr (k < 6) {
          MFMA(acc[0][5 + k], idt[0], pv[k]);
          MFMA(acc[1][5 + k], idt[1], pv[k]);
        }
        if constexpr (k >= 2) {
          constexpr int j = k - 2;
          asm volatile("" : "+v"(acc[0][5 + j]), "+v"(acc[1][5 + j]));
          const u32x4 v = pk8((j < 5 ? acc[0][j % 5] : x5[0]) + ga[0] * acc[0][5 + j], (j < 5 ? acc[1][j % 5] : x5[1]) + ga[1] * acc[1][5 + j]);
          asm volatile("ds_write_b128 %0, %1 offset:%2" ::"v"(xw1), "v"(v), "n"(j * 4096) : "memory");
        }
      });
    }
    {
      TS(6);
      LBAR();
      asm volatile("ds_read_b128 %0, %8\n\tds_read_b128 %1, %8 offset:1024\n\tds_read_b128 %2, %8 offset:2048\n\tds_read_b128 %3, %8 offset:3072\n\ts_waitcnt lgkmcnt(0)"
                   : "=&v"(xq[0]), "=&v"(xq[1]), "=&v"(xq[2]), "=&v"(xq[3]), "+v"(sa4[0]), "+v"(sa4[1]), "+v"(sa4[2]), "+v"(sa4[3])
                   : "v"(xa)
                   : "memory");
      if (!act) {
        asm volatile("s_waitcnt vmcnt(0)" ::"v"(sr[0]), "v"(sr[1]) : "memory");
        TSE();
        return;
      }
      f32x4 s4 = {0.f, 0.f, 0.f, 0.f}, gr = {0.f, 0.f, 0.f, 0.f};
#pragma unroll
      for (int q = 0; q < 4; ++q) {
        gr = MM(xq[q], xq[q], gr);
        s4 = MM(sa4[q], xq[q], s4);
      }
      const u32x4 gd = __builtin_bit_cast(u32x4, gr) & dk;
      const float m = s4[2] * (1.0f / 128);
      float q2 = ar4(__uint_as_float((gd[0] | gd[1]) | (gd[2] | gd[3]))) - s4[2] * m, lg = (s4[0] + s4[1]) - m * ce;
      if (__any(!(s4[2] * m <= 256.0f * q2))) {
        f32x4 a = {0.f, 0.f, 0.f, 0.f}, l = {0.f, 0.f, 0.f, 0.f};
#pragma unroll
        for (int q = 0; q < 4; ++q) {
          const u32x4 ch = *reinterpret_cast<const u32x4*>(shm + SCW + g4 * 16 + q * 64), cl = *reinterpret_cast<const u32x4*>(shm + SCW + 256 + g4 * 16 + q * 64);
          const f32x4 d0 = lo4(xq[q]) - m, d1 = hi4(xq[q]) - m;
          a += d0 * d0 + d1 * d1;
          l += d0 * (lo4(ch) + lo4(cl)) + d1 * (hi4(ch) + hi4(cl));
        }
        q2 = ar4((a[0] + a[1]) + (a[2] + a[3]));
        lg = ar4((l[0] + l[1]) + (l[2] + l[3]));
      }
      const u32x4 own = {nsent(m), nsent(q2), nsent(lg), 0u};
      if (g4 == 0) ST16(so, own, ws + WS_STAT, 0);
      TS(7);
      SKEWPT();
      const bool k0 = g4 * 2 == mt, k1 = g4 * 2 + 1 == mt;
#define STOK() (sr[0] = k0 ? own : sr[0], sr[1] = k1 ? own : sr[1], __all(max(max(max(sr[0][0], sr[0][1]), sr[0][2]), max(max(sr[1][0], sr[1][1]), sr[1][2])) != SENT))
      if (ST_E) asm volatile("s_waitcnt vmcnt(1)\n\t; AUDIT_OK %0 %1" : "+v"(sr[0]), "+v"(sr[1])::"memory");
      bool pub = ST_E && STOK();
      if (!pub) {
        pub = PUB_ALL || mt == (rg & 7);
        if (OPT_SD) __builtin_amdgcn_s_sleep(OPT_SD);
        POLL { PDBG(it);
          STLD();
          asm volatile("s_waitcnt vmcnt(0)" : "+v"(sr[0]), "+v"(sr[1])::"memory");
          if (STOK()) break;
          __builtin_amdgcn_s_sleep(1);
        }
      }
      TS(9);
      const f32x2 mk = {__uint_as_float(sr[0][0]), __uint_as_float(sr[1][0])};
      const f32x2 qk = {__uint_as_float(sr[0][1]), __uint_as_float(sr[1][1])};
      const f32x2 lk = {__uint_as_float(sr[0][2]), __uint_as_float(sr[1][2])};
      const float mean = ar4(mk[0] + mk[1]) * (1.0f / 8);
      const f32x2 dm = mk - mean;
      const f32x2 a = qk + 128.0f * (dm * dm), l = lk + dm * cj;
      const float vf = ar4(a[0] + a[1]) * (1.0f / D);
      const float rstd = __builtin_amdgcn_rsqf(vf + 1e-5f);
      const float rd = rstd * ar4(l[0] + l[1]) + sw;
      const unsigned rb = f2bf(rd);
      const unsigned ord = rb ^ ((rb & 0x8000u) ? 0xffffu : 0x8000u);
      const unsigned key = (ord << 16) | (0xffffu - (unsigned)row0);
      unsigned t1 = key, t2 = 0u;
      TOP2(0xB1); TOP2(0x4E); TOP2(0x141); TOP2(0x140);
      if (lane == 0 && pub) ST4(reinterpret_cast<u64*>(ws + WS_KEY) + rg * 16, mk64(~t2, ~t1));
      TS(1);
      SKEWPT();
      if (key >= t2) {
        const int sl = rg * 2 + (key == t2);
#pragma unroll
        for (int q = 0; q < 4; ++q) ST16(sl * 2048 + col1 * 2, nsent4(xq[q]), ws + WS_CAND, q * 64);
        if (g4 == 0 && pub) ST16(sl * 16, (u32x4{nsent(mean), nsent(rstd), 0u, 0u}), ws + WS_HDR, 0);
      }
      TS(8);
#pragma unroll
      for (int q = 0; q < 4; ++q) {
        const u32x4 cq = *reinterpret_cast<const u32x4*>(shm + TV + q * 64 + g4 * 16), sbq = *reinterpret_cast<const u32x4*>(shm + TV + 256 + q * 64 + g4 * 16);
        const u32x4 gbq = *reinterpret_cast<const u32x4*>(shm + TV + 512 + q * 64 + g4 * 16);
        const f32x4 x0 = lo4(xq[q]), x1 = hi4(xq[q]);
        const f32x4 h0 = ((x0 - mean) * rstd) * lo4(cq) + lo4(sbq), h1 = ((x1 - mean) * rstd) * hi4(cq) + hi4(sbq);
        ST16((row0 * D + col1) * 2, pk8(x0 + lo4(gbq) * h0, x1 + hi4(gbq) * h1), out, q * 64);
      }
      asm volatile(".Lcpf_e2:");
      WAIT_V(0);
      if (lane == 0) ST4(reinterpret_cast<unsigned*>(ws + WS_DON) + (rg * 8 + mt) * 2, 1u);
      TS(16);
    }
    TSE();
    return;
  }
  {
    const int hid = b - 192, hi = j8 - 24;
    constexpr int RNG = 65536, SSI = MISC, GP = MISC + 2048, SSA = MISC + 3072, HS = MISC + 5120, SX1 = MISC + 9216, ACT = MISC + 13312,
                  HFLG = MISC + 13568, RR = MISC + 13824;
    unsigned* flg = reinterpret_cast<unsigned*>(shm + HFLG);
    const int ec = xcd * 7 + hi, hd = hi == 7 ? 7 : (hi + 7 - ((3 + xcd) & 7) % 7) % 7;
    const bool pon = wid < ((0x06555542 >> (4 * hd)) & 7);
    const char* pfh = PFG((hd < 2 ? 2 * hd : 5 * hd - 4) + wid, 8 - qx);
#define HPF(g) do { if (pon && !((ABL & 2) && (g) > 3)) PFL(pfh + ((((g) + qx) & 7) << 9)); } while (0)
    WHLD(4);
    const int cb0 = xcd * 8 + ((hd + 2) & 7);
    const unsigned om = (unsigned)((wid * 16 + (lane >> 3)) * 12288 + (lane & 7) * 16);
    const unsigned og = (unsigned)((wid * 8 + (lane >> 3)) * 7168 + (lane & 7) * 16);
    const unsigned od = (unsigned)((wid * 8 + (lane >> 3)) * 2048 + (lane & 7) * 16);
    const char* pm = reinterpret_cast<const char*>(wmod) + cb0 * 128;
    const char* pg = reinterpret_cast<const char*>(wg) + ec * 128;
    const char* pu = reinterpret_cast<const char*>(wu) + ec * 128;
    const char* pd = reinterpret_cast<const char*>(wd) + (size_t)ec * 131072;
    const unsigned ow = (unsigned)(((wid & 1) * 16 + (lane >> 3)) * 12288 + (wid >> 1) * 128 + (lane & 7) * 16);
    const char* pw = reinterpret_cast<const char*>(wmod) + 8192 + xcd * 512;
#define RQM(r)                                                                                   \
  do {                                                                                           \
    const char* p_ = pm + (size_t)(r)*1572864;                                                   \
    GLDSP(lds0 + RNG + ((r)&3) * 16384 + wid * 2048, om, p_, WPOL);                              \
    GLDSP(lds0 + RNG + ((r)&3) * 16384 + wid * 2048 + 1024, om, p_ + 98304, WPOL);               \
  } while (0)
#define RQE(r)                                                                                                                \
  do {                                                                                                                        \
    if (NOEXP) break;                                                                                                         \
    GLDSP(lds0 + RNG + ((r)&3) * 16384 + wid * 1024, (r) < 16 ? og : od, (r) < 16 ? pg + (r)*458752 : pd + ((r)-16) * 256, WPOL);   \
    GLDSP(lds0 + RNG + ((r)&3) * 16384 + 8192 + wid * 1024, (r) < 16 ? og : od, (r) < 16 ? pu + (r)*458752 : pd + ((r)-16) * 256 + 128, WPOL); \
  } while (0)
    PBAR(4);
    XFLAG();
    const u32x4 sv = {SENT, SENT, SENT, SENT};
    auto PUBS = [&](int cb, unsigned v, unsigned p, unsigned r) __attribute__((always_inline)) {
      const int vi = cb >> 4, col = (cb & 15) * 64 + lane;
      ST4(modv + cb * 64 + lane, v);
      if (vi == 4) ST4(modv + 6144 + col, p);
      if (vi == 4 ? (lane & 31) == 0 : vi == 3 && lane == 0) ST4(modv + 7168 + (vi == 4 ? col >> 5 : 32 + (cb & 15)), r);
    };
    if (wid == 4 || (hi == 7 && wid < 4)) PUBS(wid == 4 ? cb0 : 64 + xcd * 4 + wid, SENT, SENT, SENT);
    if (tid >= 480) ST4(reinterpret_cast<unsigned*>(ws + WS_Q) + hsl * 32 + tid - 480, SENT);
    unsigned cv, wv;
    asm volatile("global_load_dword %0, %2, %3\n\tglobal_load_dword %1, %2, %4" : "=&v"(cv), "=&v"(wv) : "v"(tid * 4), "s"(cvec), "s"(wr_) : "memory");
    if (hi < 7) {
      if (PF_N < 3) HPF(2);
      HPF(3);
      const char* ap = ws + WS_ARM + (size_t)(ec * 8 + wid) * 3328;
#pragma unroll
      for (int j = 0; j < 4; ++j) ST16(lane * 16, sv, ap, j < 3 ? j * 1024 : 2304);
      HPF(4);
      WAIT_V(4);
    } else {
      WAIT_V(0);
    }
    asm volatile("; AUDIT_OK %0 %1" : "+v"(cv), "+v"(wv)::"memory");
    *reinterpret_cast<unsigned*>(shm + SSI + tid * 4) = pack2(silu_bf(bflo(cv)), silu_bf(bfhi(cv)));
    *reinterpret_cast<unsigned*>(shm + SX1 + tid * 4) = wv;
    LBAR();
    TS(50);
    const unsigned ha = (unsigned)(((l16 >> 2) * 8 + (lane & 3)) * 2048 + wid * 256);
#define HFR(j) *reinterpret_cast<const u32x4*>(shm + ha + ((j)&1) * 8192 + ((j) >> 2) * 128 + (((((j) >> 1) & 1) * 4 + g4) ^ (lane & 7)) * 16)
    HPF(5);
#pragma unroll
    for (int j = 0; j < ((ABL & 1) ? 0 : 8); ++j) ST16(lane * 16, HFR(j), ws + WS_WH + (size_t)hsl * 65536 + wid * 8192 + 4096, j * 1024 - 4096);
    HPF(6);
    RQM(0);
    RQM(1);
    RQM(2);
    const unsigned tm = lds0 + RNG + ((wid >> 2) * 64 + g4 * 8 + ((lane >> 2) & 3)) * 128 + (wid & 3) * 32 + (lane & 3) * 8;
    f32x4 ma = {0.f, 0.f, 0.f, 0.f};
#define GVR(i)                                                                                                                               \
  do {                                                                                                                                       \
    _Pragma("unroll") for (int f = 0; f < 2; ++f)                                                                                            \
        ma = MM(trd(tm + ((i)&3) * 16384 + f * 4096), *reinterpret_cast<const u32x4*>(shm + SSI + ((i)*128 + (wid >> 2) * 64 + f * 32 + g4 * 8) * 2), ma); \
    if ((i) == 7 && l16 == 0) *reinterpret_cast<f32x4*>(shm + GP + wid * 64 + g4 * 16) = ma;                                                 \
  } while (0)
    const float* gp = reinterpret_cast<const float*>(shm + GP);
    auto PUB = [&](int cb, float s) __attribute__((always_inline)) {
      const int vi = cb >> 4;
      const float v = rbf(((vi == 1 || vi == 4) ? 1.0f : 0.0f) + rbf(s));
      const float p = v * bf2f(*reinterpret_cast<const u16*>(shm + SX1 + ((cb & 15) * 64 + lane) * 2));
      const float r16 = row_sum(p), r32 = swap16_add(r16, r16);
      PUBS(cb, nsent(v), nsent(p), nsent(vi == 4 ? r32 : swap32_add(r32, r32)));
    };
    {
      sfor<8>([&](auto ii) __attribute__((always_inline)) {
        constexpr int i = decltype(ii)::value;
        (void)ow, (void)pw, (void)om, (void)pm, (void)og, (void)od, (void)pg, (void)pu, (void)pd, (void)lds0, (void)wid, (void)pfl, (void)Yx, (void)l16, (void)g4, (void)xcd, (void)hfl, (void)lane, (void)hid;
        TSCAP();
        WAIT_V(4);
        LBAR();
        if (wid == 2) TS(i);
        if constexpr (i == 0) { if (tid == 0) ST4(reinterpret_cast<u64*>(hfl + hid * HST), tag); }
        if constexpr (i == 0) HPF(7);
        if constexpr (i < 5) {
          RQM(i + 3);
        } else {
          const bool e = hid < 56;
          const char* p_ = pw + (i - 5) * 393216;
          GLDSP(lds0 + RNG + (i - 5) * 16384 + wid * (e ? 1024 : 2048), e ? og : ow, e ? pg + (i - 5) * 458752 : p_, WPOL);
          GLDSP(lds0 + RNG + (i - 5) * 16384 + wid * (e ? 1024 : 2048) + (e ? 8192 : 1024), e ? og : ow, e ? pu + (i - 5) * 458752 : p_ + 98304, WPOL);
        }
        GVR(i);
      });
    }
    TS(51);
#define PUB0() do { if (wid == 0) PUB(cb0, gp[lane] + gp[64 + lane]); } while (0)
    u64 sf, hf;
#define SFLD(OP)                                                                                                                   \
  asm volatile("s_mov_b64 exec, 3\n\t" OP "\n\ts_mov_b64 exec, 0xff\n\tglobal_load_dwordx2 %1, %6, %5 sc1\n\ts_mov_b64 exec, -1"   \
               : "=&v"(sf), "=&v"(hf) : "v"(lane * 128), "v"(0ull), "s"(ws + WS_SFL + wid * 256), "s"(ws + WS_HFL + wid * 64), "v"(lane * 8) : "memory")
#define SF_LD "global_load_dwordx2 %0, %2, %4 sc1"
#define SF_AT "global_atomic_add_x2 %0, %2, %3, %4 sc0"
    auto SAW = [&]() __attribute__((always_inline)) {
      POLL { PDBG(it);
        if (((__builtin_amdgcn_ballot_w64(sf != tag) & 3u) | (__builtin_amdgcn_ballot_w64(hf != tag) & 0xffu)) == 0) break;
        __builtin_amdgcn_s_sleep(4);
        SFLD(SF_LD);
        asm volatile("s_waitcnt vmcnt(0)" : "+v"(sf), "+v"(hf)::"memory");
      }
    };
    u32x2 s2;
#define SALD() asm volatile("global_load_dwordx2 %0, %1, %2 sc1" : "=v"(s2) : "v"(tid * 8), "s"(modv) : "memory")
    auto QMM = [&]() __attribute__((always_inline)) {
      *reinterpret_cast<unsigned*>(shm + SSA + tid * 4) = hi2(s2);
      f32x4 qa[2] = {};
#pragma unroll
      for (int j = 0; j < 8; ++j) qa[j & 1] = MM(HFR(j), *reinterpret_cast<const u32x4*>(shm + SSA + wid * 256 + (j >> 1) * 64 + g4 * 16), qa[j & 1]);
      if (l16 < 2) *reinterpret_cast<f32x4*>(shm + HS + wid * 128 + g4 * 32 + l16 * 16) = l16 ? qa[1] : qa[0];
    };
    auto QPUB = [&]() __attribute__((always_inline)) {
      if (tid >= 480) {
        float s = 0.f;
#pragma unroll
        for (int w = 0; w < 8; ++w) s += *reinterpret_cast<const float*>(shm + HS + w * 128 + (tid - 480) * 4);
        ST4(reinterpret_cast<unsigned*>(ws + WS_Q) + hsl * 32 + tid - 480, nsent(s));
      }
    };
    u32x4 GU[32], DN[16];
    if (hid < 56) {
      const unsigned tg = lds0 + RNG + (g4 * 8 + ((lane >> 2) & 3)) * 128 + ((lane >> 1) & 1) * 8192 + wid * 16 + (lane & 1) * 8;
      const unsigned td = lds0 + RNG + (g4 * 8 + ((lane >> 2) & 3)) * 128 + ((wid >> 1) & 1) * 8192 + (wid & 1) * 64 + (lane & 3) * 8 + (wid >> 2) * 16384;
      sfor<24>([&](auto rr) __attribute__((always_inline)) {
        constexpr int r = decltype(rr)::value;
        (void)td, (void)tg, (void)GU, (void)DN, (void)og, (void)od, (void)pg, (void)pu, (void)pd, (void)lds0, (void)wid, (void)pfl, (void)Yx, (void)l16, (void)g4, (void)xcd;
        (void)s2, (void)modv, (void)tid;
        (void)hid, (void)lane, (void)sf, (void)hf;
        (void)sv, (void)hi, (void)ws;
#define EX(r) ((!XA_G && (r) >= 0 && (r) < 12 / A_PR ? A_PR : 0) + ((r) == Q_AT - 6 ? 2 : 0) + ((r) == Q_AT - 3))
        WAIT_V(r == 23 ? 0 : 4 + EX(r - 2) + EX(r - 1));
        LBAR();
        TSCAP();
        if (wid == 3) TS(r);
        if constexpr (r == 0) PUB0();
        if constexpr (r == 1) {
          if (cb0 < 16 && wid == 0) {
            WAIT_V(2 + EX(0));
            if (lane == 0) ST4(reinterpret_cast<u64*>(ws + WS_SFL) + cb0 * 16, tag);
          }
        }
        if constexpr (r == Q_AT - 6) SFLD(SF_1);
        if constexpr (r == Q_AT - 3) {
          asm volatile("" : "+v"(sf), "+v"(hf));
          SAW();
          SALD();
        }
        if constexpr (!XA_G && r < 12 / A_PR) {
          const char* ap = ws + WS_XCH + xcd * 589824 + min(hi * 8 + wid + r * 56, 576 / A_PR - 1) * (A_PR * 1024);
#pragma unroll
          for (int j = 0; j < A_PR; ++j) ST16(lane * 16, sv, ap, j * 1024);
        }
        if constexpr (r < 17) RQE(r + 3);
        if constexpr (r == 18 || r == 20) { RQE(r + 2); RQE(r + 3); }
        if constexpr (r < 16) {
          GU[2 * r] = trd(tg + (r & 3) * 16384);
          GU[2 * r + 1] = trd(tg + (r & 3) * 16384 + 4096);
          asm volatile("" : "+v"(GU[2 * r]), "+v"(GU[2 * r + 1]));
        } else if constexpr (r & 1) {
#pragma unroll
          for (int j = 0; j < 4; ++j) {
            DN[(r - 17) * 2 + j] = trd(td + (r & 2) * 16384 + (j & 1) * 4096 + (j >> 1) * 32);
            asm volatile("" : "+v"(DN[(r - 17) * 2 + j]));
          }
        }
        if constexpr (r == Q_AT) {
          asm volatile("" : "+v"(s2));
          QMM();
        }
        if constexpr (r == Q_AT + 1) QPUB();
      });
      TS(52);
    } else {
      const unsigned tw = lds0 + RNG + (wid >> 1) * 4096 + (g4 * 8 + ((lane >> 2) & 3)) * 128 + (wid & 1) * 64 + (lane & 3) * 8;
      f32x4 mb[2] = {};
      auto WR = [&](int r0, int r1) __attribute__((always_inline)) {
#pragma unroll 1
        for (int r = r0; r < r1; ++r) {
          WAIT_V(4);
          LBAR();
          if (wid == 2 && r < 22) TSD(r + 8);
          const char* p_ = pw + min(r + 3, 31) * 393216;
          GLDSP(lds0 + RNG + ((r + 3) & 3) * 16384 + wid * 2048, ow, p_, WPOL);
          GLDSP(lds0 + RNG + ((r + 3) & 3) * 16384 + wid * 2048 + 1024, ow, p_ + 98304, WPOL);
          const u32x4 sf = *reinterpret_cast<const u32x4*>(shm + SSI + (r * 32 + g4 * 8) * 2);
          mb[0] = MM(trd(tw + (r & 3) * 16384), sf, mb[0]);
          mb[1] = MM(trd(tw + (r & 3) * 16384 + 32), sf, mb[1]);
        }
      };
      WR(0, 1);
      PUB0();
      WR(1, 4);
      if (cb0 < 16 && tid == 0) ST4(reinterpret_cast<u64*>(ws + WS_SFL) + cb0 * 16, tag);
      WR(4, MQ_AT - 6);
      SFLD(SF_LD);
      WR(MQ_AT - 6, MQ_AT - 3);
      asm volatile("; AUDIT_OK %0 %1" : "+v"(sf), "+v"(hf));
      SAW();
      SALD();
      WR(MQ_AT - 3, MQ_AT);
      asm volatile("; AUDIT_OK %0" : "+v"(s2));
      QMM();
      WR(MQ_AT, 32);
      if (l16 < 2) *reinterpret_cast<f32x4*>(shm + GP + wid * 128 + l16 * 64 + g4 * 16) = l16 ? mb[1] : mb[0];
      LBAR();
      if (wid < 4) PUB(64 + xcd * 4 + wid, gp[tid]);
      QPUB();
      TS(52);
      WAIT_V(0);
      TSE();
      return;
    }
    bool early = false;
    if (tid == 0) {
      POLL { PDBG(it);
        unsigned g;
        if (WK_LD) asm volatile("global_load_dword %0, %1, %2 sc1\n\ts_waitcnt vmcnt(0)" : "=&v"(g) : "v"(0u), "s"(ws + WS_WAK + hid * 128) : "memory");
        else asm volatile("global_atomic_add %0, %1, %1, %2 sc0\n\ts_waitcnt vmcnt(0)" : "=&v"(g) : "v"(0u), "s"(ws + WS_WAK + hid * 128) : "memory");
        if (g != SENT) break;
        early = true;
        __builtin_amdgcn_s_sleep(OPT_KS);
      }
    }
    BARRIER();
    TS(21);
    const int tok = hid & 1, col = ((hid >> 1) & 15) * 64 + lane;
    u32x4 c4, s4;
    unsigned gbu;
    POLL { PDBG(it);
      asm volatile("global_load_dwordx4 %0, %3, %5 sc1\n\tglobal_load_dwordx4 %1, %3, %6 sc1\n\tglobal_load_dword %2, %4, %7 sc1\n\ts_waitcnt vmcnt(0)"
                   : "=&v"(c4), "=&v"(s4), "=&v"(gbu) : "v"((tid & 255) * 16), "v"(col * 4), "s"(modv + 4096), "s"(modv + 3072), "s"(modv + 5120) : "memory");
      if (__all(max(max(max4(c4), max4(s4)), gbu) != SENT)) break;
      __builtin_amdgcn_s_sleep(8);
    }
    const f32x4 cb2 = __builtin_bit_cast(f32x4, c4), sb2 = __builtin_bit_cast(f32x4, s4);
    unsigned hra = lds0 + HS + g4 * 32 + (lane & 1) * 16, hwa = lds0 + HS + ((tid & 255) >> 1) * 32 + (wid >> 2) * 16 + (tid & 1) * 8;
    unsigned pto = (lane & 1) * 4096 + ((l16 >> 2) * 256 + wid * 32 + (lane & 2) * 8 + g4 * 4) * 4, awa = lds0 + ACT + wid * 32 + g4 * 16 + l16 * 2;
    asm volatile("" : "+v"(hra), "+v"(hwa), "+v"(pto), "+v"(awa));
    const float gb = __uint_as_float(gbu);
    if (wid == 0) {
      u32x2 g0 = {SENT, SENT}, g1 = {SENT, SENT};
      const char* gp = ws + WS_KEY + lane * 128;
      if (__any(early)) __builtin_amdgcn_s_sleep(OPT_KD);
      TS(20);
      POLL { PDBG(it);
        if (max2(g0) == SENT) asm volatile("global_load_dwordx2 %0, %1, off sc1" : "+v"(g0) : "v"(gp) : "memory");
        if (max2(g1) == SENT) asm volatile("global_load_dwordx2 %0, %1, off sc1" : "+v"(g1) : "v"(gp + 8192) : "memory");
        asm volatile("s_waitcnt vmcnt(0)" : "+v"(g0), "+v"(g1)::"memory");
        if (__all(max(max2(g0), max2(g1)) != SENT)) break;
        __builtin_amdgcn_s_sleep(1);
      }
      g0 = ~g0;
      g1 = ~g1;
      const unsigned a1 = max(g0[0], g1[0]), a2 = max(min(g0[0], g1[0]), max(g0[1], g1[1]));
      const unsigned k1 = wave_max_u(a1);
      const unsigned k2 = wave_max_u(a1 == k1 ? a2 : a1);
      if (lane == 0) *reinterpret_cast<u32x2*>(flg) = u32x2{k1, k2};
      TS(22);
    }
    LBAR();
    const unsigned k1 = flg[0], k2 = flg[1];
    {
      const int rg1 = key_row(k1) >> 4, rg2 = key_row(k2) >> 4;
      const int sl = wid >= 4 ? rg2 * 2 + (rg2 == rg1) : rg1 * 2;
      u32x2 v, hd;
      POLL { PDBG(it);
        asm volatile("global_load_dwordx2 %0, %2, %4 sc1\n\tglobal_load_dwordx2 %1, %3, %5 sc1\n\ts_waitcnt vmcnt(0)"
                     : "=&v"(v), "=&v"(hd) : "v"(sl * 2048 + (tid & 255) * 8), "v"(sl * 16), "s"(ws + WS_CAND), "s"(ws + WS_HDR) : "memory");
        if (__all(max(max2(v), max2(hd)) != SENT)) break;
        __builtin_amdgcn_s_sleep(1);
      }
      TS(23);
      const float mean = __uint_as_float(hd[0]), rstd = __uint_as_float(hd[1]);
      const f32x4 h = ((f32x4{bflo(v[0]), bfhi(v[0]), bflo(v[1]), bfhi(v[1])} - mean) * rstd) * cb2 + sb2;
      asm volatile("ds_write_b64 %0, %1" ::"v"(hwa), "v"(u32x2{pack2(h[0], h[1]), pack2(h[2], h[3])}) : "memory");
      *reinterpret_cast<u32x2*>(shm + SX1 + tid * 8) = v;
    }
    SKEWPT();
    LBAR();
    {
      f32x4 d;
      u32x4 hb[8];
      sfor<39>([&](auto ff) __attribute__((always_inline)) {
        constexpr int f = decltype(ff)::value, c = f - 7;
        (void)d, (void)hb, (void)GU, (void)hra;
        if constexpr (c >= 0) {
          WAIT_L(f <= 32 ? 6 : 38 - f);
          if constexpr (c == 0) MFMA0(d, hb[0], GU[0]);
          else MFMA(d, hb[c & 7], GU[c]);
        }
        if constexpr (f < 32) DSR(hb[f & 7], hra, f * 128);
      });
      asm volatile("s_nop 7" : "+v"(d));
      const float dv = rbf((g4 & 1) ? d[1] : d[0]), uv = dpp_f<0x128>(dv);
      const unsigned av = f2bf(rbf(dv * __builtin_amdgcn_rcpf(1.0f + __expf(-dv))) * uv);
      asm volatile("s_mov_b64 exec, 0xff00ff\n\tds_write_b16 %0, %1\n\ts_mov_b64 exec, -1" ::"v"(awa), "v"(av) : "memory");
    }
    TS(24);
    LBAR();
    {
      u32x4 a0, a1;
      asm volatile("ds_read_b128 %0, %2 offset:%3\n\tds_read_b128 %1, %2 offset:%4\n\ts_waitcnt lgkmcnt(0)" : "=&v"(a0), "=&v"(a1) : "v"(hra), "n"(ACT - HS), "n"(ACT - HS + 128) : "memory");
      u32x4 o = {};
#pragma unroll
      for (int c = 0; c < 8; ++c) {
        const u32x4 d = __builtin_bit_cast(u32x4, MM(DN[2 * c + 1], a1, MM(DN[2 * c], a0, (f32x4{0.f, 0.f, 0.f, 0.f}))));
        o = (l16 >> 1) == c ? d : o;
      }
      ST16(pto, nsent4(o), ws + WS_PART + ec * 8192, 0);
    }
    TS(25);
    SKEWPT();
    if (hid >= 32) {
      if (hi == 6 && wid == 1 && lane < 35) {
        char* fp = ws + (lane < 32 ? WS_XFL + (xcd * 32 + lane) * 128 : lane < 34 ? WS_SFL + (xcd * 2 + lane - 32) * 128 : WS_EPO);
        if (lane < 34 || xcd == 0) ST4(reinterpret_cast<u64*>(fp), lane < 34 ? ~tag : tag);
      }
      if (hid == 40 && tid < 320) ST4(reinterpret_cast<u64*>(ws + WS_HFL) + tid, ~tag);
      TSE();
      return;
    }
    const unsigned kk = tok ? k2 : k1;
    const int row = key_row(kk);
    float* rr = reinterpret_cast<float*>(shm + RR);
    if (wid == 7) {
      POLL { PDBG(it);
        unsigned g;
        asm volatile("global_load_dword %0, %1, %2 sc1\n\ts_waitcnt vmcnt(0)" : "=&v"(g) : "v"(((row >> 4) * 8 + (hid >> 2)) * 8), "s"(ws + WS_DON) : "memory");
        if (__all(g != SENT)) break;
        __builtin_amdgcn_s_sleep(2);
      }
    } else {
      const unsigned po = (unsigned)((wid * 8 + g4) * 8192 + tok * 4096 + (hid >> 1) * 256 + l16 * 16);
      u32x4 v[2];
      if (OPT_RD) __builtin_amdgcn_s_sleep(OPT_RD);
      POLL { PDBG(it);
        asm volatile("global_load_dwordx4 %0, %2, %3 sc1\n\tglobal_load_dwordx4 %1, %2, %4 sc1\n\ts_waitcnt vmcnt(0)"
                     : "=&v"(v[0]), "=&v"(v[1])
                     : "v"(po), "s"(ws + WS_PART), "s"(ws + WS_PART + 32768)
                     : "memory");
        if (__all(max(max4(v[0]), max4(v[1])) != SENT)) break;
        __builtin_amdgcn_s_sleep(OPT_RR);
      }
      TS(28);
      const f32x4 t = __builtin_bit_cast(f32x4, v[0]) + __builtin_bit_cast(f32x4, v[1]);
      rr[(l16 * 4 + g4) * 8 + wid] = swap16_add(swap32_add(t[0], t[2]), swap32_add(t[1], t[3]));
    }
    const float x1 = bf2f(*reinterpret_cast<const u16*>(shm + SX1 + tok * 2048 + col * 2));
    const float h2 = bf2f(*reinterpret_cast<const u16*>(shm + HS + (col >> 3) * 32 + tok * 16 + (col & 7) * 2));
    float rsg = rbf(1.0f / (1.0f + __expf(-key_val(kk))));
    asm volatile("" : "+v"(rsg));
    LBAR();
    asm volatile(".Lcpf_e1:");
    TS(5);
    if (wid == 0) {
      const f32x4 r0 = *reinterpret_cast<const f32x4*>(rr + lane * 8), r1 = *reinterpret_cast<const f32x4*>(rr + lane * 8 + 4);
      const float sum = ((r0[0] + r0[1]) + (r0[2] + r0[3])) + ((r1[0] + r1[1]) + r1[2]);
      out[(size_t)row * D + col] = (u16)f2bf(x1 + gb * rbf(h2 + rbf(rbf(sum) * rsg)));
    }
    TS(15);
    TSE();
  }
}

extern "C" void kernel_launch(void* const* d_in, const int* in_sizes, int n_in, void* d_out, int out_size, void* d_ws, size_t ws_size,
                              hipStream_t stream) {
  const u16* x = (const u16*)d_in[0];
  const u16* wsr = (const u16*)d_in[1];
  const u16* wsi = (const u16*)d_in[2];
  const u16* whr = (const u16*)d_in[3];
  const u16* whi = (const u16*)d_in[4];
  const u16* w_r = (const u16*)d_in[6];
  const u16* w_gate = (const u16*)d_in[7];
  const u16* w_up = (const u16*)d_in[8];
  const u16* w_down = (const u16*)d_in[9];
  const u16* c = (const u16*)d_in[10];
  const u16* w_mod = (const u16*)d_in[11];
  u16* out = (u16*)d_out;
  k_main<<<256, 512, 0, stream>>>(x, (char*)d_ws, wsr, wsi, c, w_mod, whr, whi, w_r, w_gate, w_up, w_down, out);
}
```

```cpp
#include <hip/hip_runtime.h>
#include <hip/hip_bf16.h>
#include <stdint.h>
#include <utility>

typedef unsigned short u16;
typedef unsigned long long u64;
using bf16x8 = __attribute__((ext_vector_type(8))) short;
using bf16v8 = __attribute__((ext_vector_type(8))) __bf16;
using f32x4 = __attribute__((ext_vector_type(4))) float;
using f32x2 = __attribute__((ext_vector_type(2))) float;
using u32x4 = __attribute__((ext_vector_type(4))) unsigned;
using u32x2 = __attribute__((ext_vector_type(2))) unsigned;

constexpr int S = 2048, D = 1024, F = 3584;

__device__ __forceinline__ float bf2f(unsigned v) { return __uint_as_float(v << 16); }
__device__ __forceinline__ float bflo(unsigned v) { return __uint_as_float(v << 16); }
__device__ __forceinline__ float bfhi(unsigned v) { return __uint_as_float(v & 0xffff0000u); }
__device__ __forceinline__ unsigned f2bf(float f) {
  __hip_bfloat16 b = __float2bfloat16(f);
  return (unsigned)*reinterpret_cast<u16*>(&b);
}
__device__ __forceinline__ float rbf(float f) { return bf2f(f2bf(f)); }
using bf16v2 = __attribute__((ext_vector_type(2))) __bf16;
__device__ __forceinline__ unsigned pack2(float lo, float hi) {
  return __builtin_bit_cast(unsigned, __builtin_convertvector(f32x2{lo, hi}, bf16v2));
}

template <int CTRL>
__device__ __forceinline__ float dpp_f(float v) {
  return __int_as_float(__builtin_amdgcn_mov_dpp(__float_as_int(v), CTRL, 0xf, 0xf, true));
}
template <int CTRL>
__device__ __forceinline__ float dpp_add(float v) { return v + dpp_f<CTRL>(v); }
__device__ __forceinline__ float row_sum(float v) { return dpp_add<0x140>(dpp_add<0x141>(dpp_add<0x4E>(dpp_add<0xB1>(v)))); }
template <int CTRL>
__device__ __forceinline__ unsigned dpp_u(unsigned v) { return (unsigned)__builtin_amdgcn_mov_dpp((int)v, CTRL, 0xf, 0xf, true); }
__device__ __forceinline__ unsigned wave_max_u(unsigned v) {
  v = max(v, dpp_u<0xB1>(v));
  v = max(v, dpp_u<0x4E>(v));
  v = max(v, dpp_u<0x141>(v));
  v = max(v, dpp_u<0x140>(v));
  const unsigned a = (unsigned)__builtin_amdgcn_readlane((int)v, 0), b = (unsigned)__builtin_amdgcn_readlane((int)v, 16);
  const unsigned c = (unsigned)__builtin_amdgcn_readlane((int)v, 32), d = (unsigned)__builtin_amdgcn_readlane((int)v, 48);
  return max(max(a, b), max(c, d));
}
__device__ __forceinline__ u64 mk64(unsigned hi, unsigned lo) { return ((u64)hi << 32) | lo; }
__device__ __forceinline__ float swap32_add(float a, float b) {
  auto r = __builtin_amdgcn_permlane32_swap(__float_as_uint(a), __float_as_uint(b), false, false);
  return __uint_as_float(r[0]) + __uint_as_float(r[1]);
}
__device__ __forceinline__ float swap16_add(float a, float b) {
  auto r = __builtin_amdgcn_permlane16_swap(__float_as_uint(a), __float_as_uint(b), false, false);
  return __uint_as_float(r[0]) + __uint_as_float(r[1]);
}
__device__ __forceinline__ float ar4(float v) {
  const float t = swap32_add(v, v);
  return swap16_add(t, t);
}
__device__ __forceinline__ float wave_sum_s(float v) {
  v = row_sum(v);
  v += __int_as_float(__builtin_amdgcn_update_dpp(0, __float_as_int(v), 0x142, 0xa, 0xf, false));
  v += __int_as_float(__builtin_amdgcn_update_dpp(0, __float_as_int(v), 0x143, 0xc, 0xf, false));
  return __int_as_float(__builtin_amdgcn_readlane(__float_as_int(v), 63));
}
__device__ __forceinline__ float silu_bf(float c) { return rbf(c * __builtin_amdgcn_rcpf(1.0f + __expf(-c))); }
__device__ __forceinline__ float dsum(unsigned v, float a) {
  return __builtin_amdgcn_fdot2_f32_bf16(__builtin_bit_cast(bf16v2, v), __builtin_bit_cast(bf16v2, 0x3f803f80u), a, false);
}
template <class Fn, int... I>
__device__ __forceinline__ void sfor_(Fn&& f, std::integer_sequence<int, I...>) { (f(std::integral_constant<int, I>{}), ...); }
template <int N, class Fn>
__device__ __forceinline__ void sfor(Fn&& f) { sfor_(f, std::make_integer_sequence<int, N>{}); }
constexpr unsigned SENT = 0xFFFFFFFFu;
__device__ __forceinline__ f32x2 unpk(unsigned v) { return f32x2{bflo(v), bfhi(v)}; }

constexpr size_t WS_EPO = 0;
constexpr size_t WS_XFL = 4096;
constexpr size_t WS_HFL = 40960;
constexpr size_t WS_XF2 = WS_HFL + 512;
constexpr size_t WS_SFL = WS_HFL + 8192;
constexpr size_t WS_MOD = 65536;
constexpr size_t WS_Q = WS_MOD + 32768;
constexpr size_t WS_ARM = WS_Q + 8192;
constexpr size_t WS_RS = WS_ARM;
constexpr size_t WS_KEY = WS_RS + 16384;
constexpr size_t WS_HDR = WS_KEY + 16384;
constexpr size_t WS_UFL = WS_HDR + 4096;
constexpr size_t WS_DON = WS_UFL + 98304;
constexpr size_t WS_STAT = WS_DON + 12288;
constexpr size_t WS_CAND = WS_STAT + 262144;
constexpr size_t WS_PART = WS_CAND + 524288;
constexpr size_t WS_XAF = WS_PART + 458752;
constexpr size_t WS_WAK = WS_XAF + 24576;
static_assert(WS_WAK + 8192 <= WS_ARM + 448 * 3328, "armed region");
constexpr size_t WS_XCH = 2u << 20;
constexpr size_t WS_H = 8u << 20;
constexpr size_t WS_WH = 12u << 20;
constexpr size_t WS_U = 16u << 20;
constexpr size_t WS_DBG = 64u << 20;
#define NEWDESIGN 1

#define PCAP (1 << 20)
#define PDBG(it)
#define TS(i)
#define TSCAP()
#define TSC0()
#define TSC1(i)
#define TSD(i)
#define TSE()
__device__ __forceinline__ void st16_wt(void* p, u32x4 v) {
  asm volatile("global_store_dwordx4 %0, %1, off sc1\n\ts_nop 1" ::"v"(p), "v"(v) : "memory");
}
#define ST16(voff, v, base, off) asm volatile("global_store_dwordx4 %0, %1, %2 offset:%3 sc1\n\ts_nop 1" ::"v"(voff), "v"(v), "s"(base), "n"(off) : "memory")
#define ST4(p, v) __hip_atomic_store(p, v, __ATOMIC_RELAXED, __HIP_MEMORY_SCOPE_AGENT)
#define WAIT_V(n) asm volatile("s_waitcnt vmcnt(%0)" ::"n"(n) : "memory")
#define WAIT_L(n) asm volatile("s_waitcnt lgkmcnt(%0)" ::"n"(n) : "memory")
#define BARRIER() asm volatile("s_barrier" ::: "memory")
#define LBAR() asm volatile("s_waitcnt lgkmcnt(0)\n\ts_barrier" ::: "memory")
#define DSR(dst, addr, off) asm volatile("ds_read_b128 %0, %1 offset:%2" : "=v"(dst) : "v"(addr), "n"(off) : "memory")
#define MFMA(c, a, b) asm volatile("v_mfma_f32_16x16x32_bf16 %0, %1, %2, %0" : "+v"(c) : "v"(a), "v"(b))
#define MFMAI(c, a, b) asm volatile("v_mfma_f32_16x16x32_bf16 %0, %1, %2, %0" ::"v"(c), "v"(a), "v"(b))
#define MFMA0(c, a, b) asm volatile("v_mfma_f32_16x16x32_bf16 %0, %1, %2, 0" : "=&v"(c) : "v"(a), "v"(b))
#define GLDSP(m0v, voff, gbase, pol) \
  asm volatile("s_mov_b32 m0, %0\n\ts_nop 0\n\tglobal_load_lds_dwordx4 %1, %2" pol ::"s"(m0v), "v"(voff), "s"(gbase) : "memory")
#define GLDS(m0v, voff, gbase) GLDSP(m0v, voff, gbase, "")
#define WPOL " nt"
#define LDX(dst, voff, base, off) asm volatile("global_load_dwordx4 %0, %1, %2 offset:%3" : "=v"(dst) : "v"(voff), "s"(base), "n"(off) : "memory")
#define LDXS(dst, voff, base, off) asm volatile("global_load_dwordx4 %0, %1, %2 offset:%3 sc1" : "=v"(dst) : "v"(voff), "s"(base), "n"(off) : "memory")
#define BF8(v) __builtin_bit_cast(bf16v8, v)
#define MM(a, b, c) __builtin_amdgcn_mfma_f32_16x16x32_bf16(BF8(a), BF8(b), c, 0, 0, 0)
#define LSPIN(v, p, cond)                                   \
  _Pragma("unroll 1") for (int it_ = 0; it_ < (1 << 26); ++it_) { \
    v = __atomic_load_n(p, __ATOMIC_RELAXED);               \
    if (cond) break;                                        \
    __builtin_amdgcn_s_sleep(1);                            \
  }
#define LSPIN0(v, p, cond)                                  \
  _Pragma("unroll 1") for (int it_ = 0; it_ < (1 << 28); ++it_) { \
    v = __atomic_load_n(p, __ATOMIC_RELAXED);               \
    if (cond) break;                                        \
  }
#define W6(a) "+v"(a[0]), "+v"(a[1]), "+v"(a[2]), "+v"(a[3]), "+v"(a[4]), "+v"(a[5])
#define W8(a) W6(a), "+v"(a[6]), "+v"(a[7])
#define PFL(ptr) asm volatile("s_mov_b32 m0, %0\n\ts_nop 0\n\tglobal_load_lds_dword %1, off" ::"s"(pfl), "v"(ptr) : "memory")
#define HOP(k, nx) asm volatile(".Lhop%0:\n\ts_cbranch_execz .Lhop%1" ::"n"(k), "n"(nx))
using s16x4 = __attribute__((ext_vector_type(4))) short;
__device__ __forceinline__ u32x4 trd(unsigned a) {
  const u32x2 lo = __builtin_bit_cast(u32x2, __builtin_amdgcn_ds_read_tr16_b64_v4i16((__attribute__((address_space(3))) s16x4*)(size_t)a));
  const u32x2 hi = __builtin_bit_cast(u32x2, __builtin_amdgcn_ds_read_tr16_b64_v4i16((__attribute__((address_space(3))) s16x4*)(size_t)(a + 512)));
  return u32x4{lo[0], lo[1], hi[0], hi[1]};
}
using u32x8 = __attribute__((ext_vector_type(8))) unsigned;
__device__ __forceinline__ u32x8 trd2(unsigned a) {
  const u32x4 lo = trd(a), hi = trd(a + 4096);
  return u32x8{lo[0], lo[1], lo[2], lo[3], hi[0], hi[1], hi[2], hi[3]};
}
#define SMF(c, a, b, i) asm volatile("v_smfmac_f32_16x16x64_bf16 %0, %1, %2, %3" ::"v"(c), "v"(a), "v"(b), "v"(i))
__device__ __forceinline__ int key_row(unsigned k) { return (int)((0xffffu - (k & 0xffffu)) & 2047u); }
__device__ __forceinline__ float key_val(unsigned k) {
  const unsigned ord = k >> 16;
  return bf2f(ord ^ ((ord & 0x8000u) ? 0x8000u : 0xffffu));
}
__device__ __forceinline__ unsigned nsent(float v) { return min(__float_as_uint(v), SENT - 1u); }
__device__ __forceinline__ f32x4 lo4(u32x4 q) { return f32x4{bflo(q[0]), bfhi(q[0]), bflo(q[1]), bfhi(q[1])}; }
__device__ __forceinline__ f32x4 hi4(u32x4 q) { return f32x4{bflo(q[2]), bfhi(q[2]), bflo(q[3]), bfhi(q[3])}; }
__device__ __forceinline__ u32x4 pk8(f32x4 a, f32x4 b) { return u32x4{pack2(a[0], a[1]), pack2(a[2], a[3]), pack2(b[0], b[1]), pack2(b[2], b[3])}; }
__device__ __forceinline__ u32x4 nsent4(u32x4 v) { return u32x4{min(v[0], SENT - 1u), min(v[1], SENT - 1u), min(v[2], SENT - 1u), min(v[3], SENT - 1u)}; }
__device__ __forceinline__ unsigned max4(u32x4 v) { return max(max(v[0], v[1]), max(v[2], v[3])); }
__device__ __forceinline__ unsigned max2(u32x2 v) { return max(v[0], v[1]); }
__device__ __forceinline__ unsigned hi2(u32x2 v) { return (v[0] >> 16) | (v[1] & 0xffff0000u); }
#define TOP2(CTRL)                                                  \
  do {                                                              \
    const unsigned o1 = dpp_u<CTRL>(t1), o2 = dpp_u<CTRL>(t2);      \
    t2 = max(min(t1, o1), max(t2, o2));                             \
    t1 = max(t1, o1);                                               \
  } while (0)
#define POLL _Pragma("unroll 1") for (int it = 0; it < PCAP; ++it)
#define OPT_RD 10
#define OPT_RR 2
#define OPT_SD 16
#define OPT_KS 2
#define P_D3 16
#define P_D4 2
#define U_D 28
#define XJ0 2
#define XJ1 5
#define XJ2 9
#define XJ3 10
#define TP_P0 309
#define TP_XL 344
#define CPF_K 24
#define CPF_N 10
#define HP4 0
#define HP5 1
#define HP6 2
#define HP7 3
#define CPF_B 10
#define NOEXP 0
#define NOARM 0
#define HST 2
#define XST 32
#define UST 32
#define OPT_KD 32
#define KORD 1
#define UF_AT 8
#define P_NT 5
#define P_D5 4
#define PF23 1
#define MQ_AT 31
#define MP_AT 16
#define P_AT 6
#define H_AT 24
#define W_NQ 0
#define XQ_AT 24
#define U_R 1
#define A_PR 4
#define WK_AT 1
#define ZD 1
#define U_BM 0xffd0u
static_assert(__builtin_popcount(U_BM) / 2 == 5, "barriers 34 .. 43 or 44");
#define A_LD 3
#define HOPS 0
#define XQ0 2
#define XQ1 4
#define XQ2 18
#define XQ3 20
#define ABL 0
#define F_AT 3
#define ST_E 0
#define PUB_ALL 0
#define XA_G 0
#define XA_D 12
#define PF_N 3
#define SF_1 SF_LD
#define TLBW 0
#define WK_LD 1
#define HX0 9
#define HXS 2
static_assert(HX0 + 13 * HXS < 44 && UF_AT <= 11, "");
#define U_M1 0xfu
#define X_AT 12
static_assert(A_LD <= 3, "five slots");
#define QM 3
#define PF_E PF_N
#define B46 339
#define OUT_D 20
#define HOPX 1
#define RT_D 0
#define P_E 0
#define NORS 0
#define LSLP 0
#define Q_AT 9
#define B_VAT 42
constexpr int SL = 22528, NS = 5, XR = NS * SL, MISC = XR + 24576;
constexpr int ah(int e) { return e < 7 ? 4 : e < 28 ? 5 + (e - 7) % 3 : 4 + (e - 28) % 4; }
constexpr int ap(int e) { return e < 7 ? e : e < 28 ? (e - 7) / 3 : 7 + (e - 28) / 4; }
constexpr int E0 = 264, E1 = E0 + 25, E2 = E1 + 24, E3 = E0 + 88, NE = E3 + 6;
constexpr int eh(int e) { return e < E0 + 7 ? e / 11 : e < E1 ? 25 + (e - E0 - 7) / 6 : e < E2 ? 28 + (e - E1) % 4 : 24 + (e - E2 + 1) % 8; }
constexpr int ep(int e) { return e < E0 + 7 ? e % 11 : e < E1 ? (e - E0 - 7) % 6 : e < E2 ? (e - E1) / 4 : e < E3 ? 6 + (e - E2 + 1) / 8 : 5 + (e - E3); }

__global__ __launch_bounds__(512, 2) void k_main(const u16* __restrict__ x, char* ws, const u16* __restrict__ Y0, const u16* __restrict__ Y1,
                                              const u16* __restrict__ cvec, const u16* __restrict__ wmod, const u16* __restrict__ X0,
                                              const u16* __restrict__ X1, const u16* __restrict__ wr_, const u16* __restrict__ wg,
                                              const u16* __restrict__ wu, const u16* __restrict__ wd, u16* __restrict__ out) {
  constexpr int STG = 114688;
  __shared__ __attribute__((aligned(1024))) char shm[MISC + 16384];
  const int b = blockIdx.x, xcd = b & 7, j8 = b >> 3;
  const int cc = xcd >> 2, qx = xcd & 3, qs = xcd & QM;
  const int tid = threadIdx.x, lane = tid & 63;
  const int wid = __builtin_amdgcn_readfirstlane(tid >> 6);
  const unsigned lds0 = (unsigned)(size_t)(__attribute__((address_space(3))) char*)shm;
  const char* cpf;
  {
    unsigned long long pc;
    unsigned len;
    asm volatile("s_getpc_b64 %0\n.Lcpf_here:\n\ts_mov_b32 %1, .Lcpf_e1-.Lcpf_here\n\ts_max_u32 %1, %1, .Lcpf_e2-.Lcpf_here" : "=s"(pc), "=s"(len)::"scc");
    cpf = reinterpret_cast<const char*>(pc) + min((unsigned)((lane * 32 + j8) * 128), len);
  }
  const unsigned pfl = lds0 + MISC + 16128;
  if (wid == 4 && lane < CPF_N) PFL(cpf);
  u32x4 xv[2];
  {
    const unsigned xo = (unsigned)((b * 8 + wid) * D + lane * 8) * 2;
    asm volatile("global_load_dwordx4 %0, %2, %3\n\tglobal_load_dwordx4 %1, %2, %3 offset:1024" : "=&v"(xv[0]), "=&v"(xv[1]) : "v"(xo), "s"(x) : "memory");
  }
  asm volatile("" ::"s"(ws), "s"(Y0), "s"(Y1), "s"(X0), "s"(X1));
  unsigned* xfl = reinterpret_cast<unsigned*>(ws + WS_XFL);
  unsigned* hfl = reinterpret_cast<unsigned*>(ws + WS_HFL);
  unsigned* modv = reinterpret_cast<unsigned*>(ws + WS_MOD);
#define SKEWPT()
  const int g4 = lane >> 4, l16 = lane & 15;
  u64 tag0, tag;
  asm volatile("global_load_dwordx2 %0, %1, %2 sc1" : "=v"(tag0) : "v"(0u), "s"(ws) : "memory");
  const char* Yx = reinterpret_cast<const char*>(cc ? Y1 : Y0) + (size_t)qx * 2097152;
#define PFG(r16, g) (Yx + (size_t)(((r16) * 16 + l16) * 4096 + g4 * 128 + ((((g) + qs) & 7) << 9)))
  unsigned ta = lds0 + STG + ((lane >> 2) & 3) * 2368 + (wid * 128 + (lane >> 5) * 32 + (lane & 3) * 8 + (g4 & 1) * 4) * 2;
  unsigned oo = (unsigned)((wid * 4 + (lane >> 5)) * 131072 + (b >> 2) * 2048 + (g4 & 1) * 1024 + ((b & 3) * 16 + l16) * 16), oo2 = oo + 262144;
  unsigned wa = lds0 + STG + wid * 2368 + lane * 16;
  asm volatile("" : "+v"(ta), "+v"(oo), "+v"(oo2), "+v"(wa));
  const int hsl = (b & 32) + xcd * 4 + (j8 & 3);
  const char* hp = reinterpret_cast<const char*>((b & 32) ? X1 : X0) + (size_t)(hsl & 31) * 65536 + wid * 8192;
#define WHLD(i0)                                                                                                                  \
  _Pragma("unroll") for (int i = i0; i < i0 + 4; ++i)                                                                             \
      GLDS(lds0 + wid * 8192 + i * 1024, (unsigned)((lane ^ ((i >> 1) | ((wid & 2) << 1))) * 16), hp + i * 1024)
  const char* pf0 = PFG(j8, 7 - wid);
  if (wid >= 8 - PF_E) PFL(pf0);
  if (j8 >= 24) {
    WHLD(0);
    WAIT_V(5);
  } else if (wid >= 8 - PF_E) {
    WAIT_V(2);
  } else {
    WAIT_V(1);
  }
  {
    asm volatile("; AUDIT_OK %0 %1" : "+v"(xv[0]), "+v"(xv[1])::"memory");
    TS(32);
    const float s0 = dsum(xv[0][3], dsum(xv[0][2], dsum(xv[0][1], dsum(xv[0][0], 0.f))));
    const float s1 = dsum(xv[1][3], dsum(xv[1][2], dsum(xv[1][1], dsum(xv[1][0], 0.f))));
    const float mf = wave_sum_s(s0 + s1) * (1.0f / D), m = rbf(mf);
    const f32x4 d0 = lo4(xv[0]) - m, d1 = hi4(xv[0]) - m, d2 = lo4(xv[1]) - m, d3 = hi4(xv[1]) - m;
    const f32x4 q = (d0 * d0 + d1 * d1) + (d2 * d2 + d3 * d3);
    const float vf = wave_sum_s((q[0] + q[1]) + (q[2] + q[3])) * (1.0f / D) - (mf - m) * (mf - m);
    const float rden = __builtin_amdgcn_rcpf(rbf(__builtin_amdgcn_sqrtf(rbf(rbf(vf) + 1e-5f))));
    asm volatile("ds_write_b128 %0, %1" ::"v"(wa), "v"(pk8(d0 * rden, d1 * rden)) : "memory");
    asm volatile("ds_write_b128 %0, %1 offset:1024" ::"v"(wa), "v"(pk8(d2 * rden, d3 * rden)) : "memory");
    LBAR();
    TS(34);
    u32x2 t[4];
    asm volatile(
        "ds_read_b64_tr_b16 %0, %4\n\tds_read_b64_tr_b16 %1, %4 offset:%5\n\t"
        "ds_read_b64_tr_b16 %2, %4 offset:128\n\tds_read_b64_tr_b16 %3, %4 offset:%6\n\ts_waitcnt lgkmcnt(0)"
        : "=&v"(t[0]), "=&v"(t[1]), "=&v"(t[2]), "=&v"(t[3])
        : "v"(ta), "n"(4 * 2368), "n"(4 * 2368 + 128)
        : "memory");
    asm volatile("global_store_dwordx4 %0, %2, %4 sc1\n\tglobal_store_dwordx4 %1, %3, %4 sc1\n\ts_nop 1" ::"v"(oo), "v"(oo2),
                 "v"(u32x4{t[0][0], t[0][1], t[1][0], t[1][1]}), "v"(u32x4{t[2][0], t[2][1], t[3][0], t[3][1]}), "s"(ws + WS_H)
                 : "memory");
    if (PF_E < PF_N) PFL((wid >= 8 - PF_N && wid < 8 - PF_E) ? pf0 : reinterpret_cast<const char*>(x));
  }
#define PBAR(n)                                                                                                          \
  do {                                                                                                                   \
    asm volatile("s_waitcnt vmcnt(%2)\n\tv_lshl_add_u64 %0, %1, 0, 1" : "=v"(tag) : "v"(tag0), "n"((n) + (PF_E < PF_N)) : "memory"); \
    TS(36);                                                                                                              \
    BARRIER();                                                                                                           \
    TS(37);                                                                                                              \
  } while (0)
#define XFLAG() do { if ((tid | 1) == 385) ST4(reinterpret_cast<u64*>(ws + ((tid & 1) ? WS_XF2 + b * 8 : WS_XFL + b * 128)), tag); } while (0)
  if (j8 < 24) {
    const int mt = (j8 * 11) >> 5, nt = j8 - mt * 3, grp = xcd * 3 + nt;
#define FROW(f) (nt * 176 + (f) * 16 - (nt == 2 && (f) > 5 ? 16 : 0))
#define PROW(p) FROW(cc ? 10 - (p) : (p))
    constexpr int SCA = MISC, TGA = MISC + 512, TV = MISC + 1024, TSUM = MISC + 1792, FLG = MISC + 1856, SCW = MISC + 2048, RKA = MISC + 3072,
                  RKB = MISC + 5120, RKZ = MISC + 6656, IDT = MISC + 7168, ZSC = MISC + 9216;
    const bool zd = ZD && nt == 2;
    unsigned* flg = reinterpret_cast<unsigned*>(shm + FLG);
    unsigned* ufl = reinterpret_cast<unsigned*>(ws + WS_UFL) + grp * 1024;
    const char* Ug = ws + WS_U + (size_t)grp * (16 * SL + 1024) + 1024;
    const unsigned perm = KORD ? (unsigned)mt | (((0x76543210u & ((1u << (4 * mt)) - 1u)) << 4) | (0x76543210u & (0xfffffff0u << (4 * mt))))
                               : __builtin_rotateright32(0x76543210u, 4 * mt);
#define KT(i) ((perm >> (4 * (i))) & 7u)
    const int kk = wid < 4 ? wid : wid - 2;
    const bool act = (wid < 4 || wid >= 6) && (kk || (cc == 0 && nt < 2));
    const int rg = qx * 32 + (PROW(5 + kk) >> 4);
    u32x4 pv[6];
    f32x4 acc[2][11], ga[2], x5[2];
    bf16x8 idt[2];
    unsigned spo = (unsigned)(((rg * 8 + g4 * 2) * 16 + l16) * 16);
    u32x4 sr[2], dk;
#pragma unroll
    for (int q = 0; q < 4; ++q) dk[q] = l16 == g4 * 4 + q ? ~0u : 0u;
    unsigned xw1 = lds0 + XR + tid * 16, xa = lds0 + XR + kk * 4096 + lane * 16, sto = (unsigned)(((rg * 8 + mt) * 16 + lane) * 16);
    unsigned saa = lds0 + SCW + (lane & 3) * 256 + g4 * 16;
    asm volatile("" : "+v"(dk), "+v"(xa), "+v"(sto), "+v"(xw1), "+v"(spo), "+v"(saa));
#define STLD() asm volatile("global_load_dwordx4 %0, %2, %3 sc1\n\tglobal_load_dwordx4 %1, %2, %3 offset:256 sc1" : "=&v"(sr[0]), "=&v"(sr[1]) : "v"(spo), "s"(ws + WS_STAT) : "memory")
    const char* xr = ws + WS_XCH + (size_t)(b * 4 + (wid & 3)) * 6144 + 3072;
#define XCHLD()                                                                                                       \
  asm volatile(                                                                                                       \
      "global_load_dwordx4 %0, %6, %7 offset:2048 sc1\n\tglobal_load_dwordx4 %1, %6, %7 offset:1024 sc1\n\t"          \
      "global_load_dwordx4 %2, %6, %7 sc1\n\tglobal_load_dwordx4 %3, %6, %7 offset:-1024 sc1\n\t"                     \
      "global_load_dwordx4 %4, %6, %7 offset:-2048 sc1\n\tglobal_load_dwordx4 %5, %6, %7 offset:-3072 sc1"            \
      : "=&v"(pv[0]), "=&v"(pv[1]), "=&v"(pv[2]), "=&v"(pv[3]), "=&v"(pv[4]), "=&v"(pv[5])                            \
      : "v"(lane * 16), "s"(xr)                                                                                       \
      : "memory")
    if (wid < 4) {
      bf16x8 X[4][2][2], Y[8];
      u32x4 xq[6];
      const unsigned xo0 = lane * 16 + 4096;

#define XLD(dst, XP, XH, i) LDXS(dst, xo0, XP, ((XH)&3) * 2048 + (i)*1024 - 4096)
      int xt = lane < 16 ? (int)WS_H + (mt * 4 + wid) * 131072 + (((qs * 2 + lane) & 15) << 13) : (int)WS_WH + (cc * 32 + mt * 4 + wid) * 65536 + (KT(lane & 7) << 13);
      asm volatile("" : "+v"(xt));
#define XT(j) (ws + (unsigned)__builtin_amdgcn_readlane(xt, j))
#define WP(h) XT(16 + ((h) >> 2))
#define XS(h) X[((h) >> 1) & 3][(h)&1]
      {
        unsigned ye = lds0 + l16 * 128 + (g4 ^ (lane & 7)) * 16, yo = ye ^ 64;
        const char* xpa = XT(0);
        const char* xpb = XT(1);
#pragma unroll
        for (int i = 0; i < 22; ++i) {
          acc[i / 11][i % 11] = f32x4{0.f, 0.f, 0.f, 0.f};
          asm volatile("" : "+v"(acc[i / 11][i % 11]));
        }
        const char* xw = ws + WS_XCH + (size_t)((b ^ 4) * 4 + wid) * 6144 + 3072;
        unsigned xra = lds0 + XR + l16 * 256 + (((wid * 4 + g4) ^ (lane & 7)) << 4), gaa = lds0 + TGA + wid * 128 + g4 * 32;
        unsigned rka = lds0 + (g4 ? RKZ : RKA + wid * 512 + l16 * 16), rkb = lds0 + RKB + l16 * 16;
        asm volatile("" : "+s"(xw), "+s"(xr), "+v"(xra), "+v"(gaa), "+v"(rka), "+v"(rkb));
        __builtin_amdgcn_s_setprio(1);
        PBAR(0);
        if (wid == 3) {
          const unsigned* fp = xfl + (qs * 32 + lane) * XST;
          if (P_D3) __builtin_amdgcn_s_sleep(P_D3);
          POLL { PDBG(it);
            u64 f;
            asm volatile("global_load_dwordx2 %0, %1, off sc1\n\ts_waitcnt vmcnt(0)" : "=&v"(f) : "v"(fp) : "memory");
            if (__all(f == tag)) break;
            __builtin_amdgcn_s_sleep(P_D4);
          }
        }
        if (HOPS) {
          asm volatile("s_cmp_eq_u32 %0, 3\n\ts_cbranch_scc1 .Lhop%1\n\ts_mov_b64 exec, 0\n\ts_cmp_lt_u32 %0, 1\n\ts_cbranch_scc1 .Lhop0\n\t"
                       "s_cmp_eq_u32 %0, 1\n\ts_cbranch_scc1 .Lhop15\n\ts_branch .Lhop30\n.Lhop%1:\n\ts_mov_b64 exec, -1" ::"s"(wid), "n"(900) : "scc");
        }
        TS(38);
        BARRIER();
        TS(39);
#pragma unroll
        for (int h = 0; h < 7; ++h) {
          XLD(XS(h)[0], h < 4 ? xpa : xpb, h, 0);
          XLD(XS(h)[1], h < 4 ? xpa : xpb, h, 1);
        }
#pragma unroll
        for (int j = 0; j < 7; ++j) DSR(Y[j], ye, j * 2048);
        unsigned so = 0, a0 = ye, a1 = yo, yb[2], tq, u = 2;
#define STEP2(S0, XA, XB)                                                                                                      \
  sfor<44>([&](auto nn) __attribute__((always_inline)) {                                                                       \
    constexpr int n = (S0)*22 + decltype(nn)::value, q = n % 22, h = n / 11, p = n % 11, s = n / 22;                           \
    (void)acc, (void)X, (void)Y, (void)a0, (void)a1, (void)xo0, (void)XA, (void)XB, (void)so, (void)ye, (void)yo, (void)u, (void)xt, (void)tq, (void)ws; \
    if constexpr (q == 11) BARRIER();                                                                                          \
    if constexpr (q == 0 || q == 10) asm volatile("s_waitcnt vmcnt(12) lgkmcnt(5)" ::: "memory");                              \
    else if constexpr ((q & 1) == 0) WAIT_L(5);                                                                                \
    MFMA(acc[0][p], XS(h)[0], Y[n & 7]);                                                                                       \
    DSR(Y[(n + 7) & 7], (q < 4 || q >= 15) ? a0 : a1, ((p + 7) % 11) * 2048);                                                  \
    MFMA(acc[1][p], XS(h)[1], Y[n & 7]);                                                                                       \
    if constexpr (q == XQ0 || q == XQ1) XLD(XS(h + 7)[q == XQ1], (s & 1) ? XB : XA, h + 7, q == XQ1);                          \
    if constexpr (q == XQ2 || q == XQ3) XLD(XS(h + 7)[q == XQ3], XB, h + 7, q == XQ3);                                         \
    if constexpr (q == 0 && !(s & 1)) asm volatile("v_readlane_b32 %0, %2, %1\n\ts_add_u32 %1, %1, 1" : "=&s"(tq), "+s"(u) : "v"(xt) : "scc"); \
    if constexpr (q == 6) asm volatile("s_add_u32 %0, %0, %1" : "+s"(so) : "n"(SL) : "scc");                                   \
    if constexpr (q == 8) asm volatile("s_cmp_lg_u32 %0, %1\n\ts_cselect_b32 %0, %0, 0" : "+s"(so) : "n"(NS * SL) : "scc"); \
    if constexpr (q == 14 && !(s & 1)) { asm volatile("" : "+s"(tq)); XB = ws + tq; asm volatile("" : "+s"(XB)); }             \
    if constexpr (q == 12) asm volatile("v_add_u32 %0, %1, %2" : "=v"(a0) : "s"(so), "v"(ye));                                 \
    if constexpr (q == 16) asm volatile("v_add_u32 %0, %1, %2" : "=v"(a1) : "s"(so), "v"(yo));                                 \
  })
#define YSL(e) ((32 + eh(e) / 2) % NS)
#define YRD(e) DSR(Y[(e)&7], YSL(e) < 2 ? ye : yb[YSL(e) / 4], (YSL(e) & 1) * SL + ep(e) * 2048 + (eh(e) & 1) * 1024)

        u32x4 ov;
        bf16x8 qf[2];
        auto OPS = [&](auto kc) __attribute__((always_inline)) {
          constexpr int k = decltype(kc)::value, q = k / 10, r = k % 10, c = k - 60;
          (void)ov, (void)acc, (void)xw, (void)lane, (void)xq, (void)x5;
          if constexpr (k < 60) {
            if constexpr (r < 4) asm volatile("v_cvt_pk_bf16_f32 %0, %1, %2" : "=v"(ov[r]) : "v"(acc[r / 2][q][(r & 1) * 2]), "v"(acc[r / 2][q][(r & 1) * 2 + 1]));
            else if constexpr (r < 8) asm volatile("v_min_u32 %0, -2, %0" : "+v"(ov[r - 4]));
            else if constexpr (r == 9) asm volatile("global_store_dwordx4 %0, %1, %2 offset:%3 sc1" ::"v"(lane * 16), "v"(ov), "s"(xw), "n"(q * 1024 - 3072) : "memory");
          } else if constexpr (c >= 40 && c < 48) {
            if constexpr (c & 1) asm volatile("v_and_b32 %0, 0xffff0000, %1" : "=v"(x5[(c >> 2) & 1][c & 3]) : "v"(xq[5][(c >> 1) & 3]));
            else asm volatile("v_lshlrev_b32 %0, 16, %1" : "=v"(x5[(c >> 2) & 1][c & 3]) : "v"(xq[5][(c >> 1) & 3]));
          } else if constexpr (c < 40) {
            if constexpr (c & 1) asm volatile("v_and_b32 %0, 0xffff0000, %1" : "=v"(acc[(c >> 2) & 1][c >> 3][c & 3]) : "v"(xq[c >> 3][(c >> 1) & 3]));
            else asm volatile("v_lshlrev_b32 %0, 16, %1" : "=v"(acc[(c >> 2) & 1][c >> 3][c & 3]) : "v"(xq[c >> 3][(c >> 1) & 3]));
          }
        };
#define IC(v) std::integral_constant<int, (v)>{}
#define FILL(g)                                                                              \
  do {                                                                                       \
    if constexpr (m >= 0) {                                                                  \
      OPS(IC(m / 2 * 5 + (m & 1) * 3 + (g)));                                                \
      if constexpr ((g) && !(m & 1)) OPS(IC(m / 2 * 5 + 2));                                 \
    }                                                                                        \
  } while (0)
        auto BP = [&](auto nn) __attribute__((always_inline)) {
          constexpr int n = decltype(nn)::value, h = eh(n), p = ep(n), e = n + 7, m = n - E1 - 5;
          (void)ga, (void)qf, (void)idt, (void)rka, (void)rkb, (void)gaa, (void)xq, (void)ov, (void)xw, (void)x, (void)pv, (void)xr, (void)ufl;
          (void)acc, (void)X, (void)Y, (void)yb, (void)ye, (void)xra, (void)xo0, (void)xt, (void)ws, (void)lane, (void)mt, (void)nt, (void)cc, (void)lds0, (void)wid, (void)g4, (void)perm;
          (void)OPS;
          TSCAP();
          if constexpr (n == E1 + 30) TS(10);
          if constexpr (n == UF_AT) {
            TS(45);
            WAIT_V(n > HX0 ? (n - HX0 + HXS - 1) / HXS : 0);
            if (lane == 0) ST4(ufl + (mt * 4 + wid) * UST, 1u);
            TS(46);
          }
          if constexpr (n == 37) TS(48);
          if constexpr (n == 37 || n == E0 + 5 || n == E0 + 17 || n == B46) BARRIER();
          if constexpr (n == 37) TS(4);
          if constexpr (n == E1) { TS(3); SKEWPT(); }
          if constexpr (n < E3 && p == 0 && (n == 0 || n >= 44)) WAIT_V(n == 0 ? 17 : h <= 25 ? 12 : 2 * (31 - h));
          if constexpr ((n & 1) == 0) WAIT_L(e < NE ? 5 : NE - 2 - n);
          if constexpr (n < 11) MFMA0(acc[0][p], XS(h)[0], Y[n & 7]);
          else if constexpr (n >= E1 && n < E3 && h < 31) MFMAI(acc[0][p], XS(h)[0], Y[n & 7]);
          else if constexpr (n < E3) MFMA(acc[0][p], XS(h)[0], Y[n & 7]);
          else MFMA(acc[0][p], qf[0], Y[n & 7]);
          if constexpr (e < E3) YRD(e);
          else if constexpr (e < NE) DSR(Y[e & 7], rkb, (e - E3) * 256);
          FILL(0);
          if constexpr (n < 11) MFMA0(acc[1][p], XS(h)[1], Y[n & 7]);
          else if constexpr (n >= E1 && n < E3 && h < 31) MFMAI(acc[1][p], XS(h)[1], Y[n & 7]);
          else if constexpr (n < E3) MFMA(acc[1][p], XS(h)[1], Y[n & 7]);
          else MFMA(acc[1][p], qf[1], Y[n & 7]);
          if constexpr (n >= HX0 && n < HX0 + 14 * HXS && (n - HX0) % HXS == 0) {
            constexpr int k = (n - HX0) / HXS;
            static_assert(k < 8 || n >= 11 * (k / 2 - 3), "slot still in use");
            XLD(XS(4 + k / 2)[k & 1], WP(4 + k / 2), k / 2, k & 1);
          }
          if constexpr (n >= 272 && n < 294 && n % 4 == 0) DSR(xq[(n - 272) / 4], xra, (n - 272) / 4 * 4096);
          if constexpr (n == E0 || n == E0 + 2) XLD(XS(31)[(n - E0) / 2], WP(31), 31, (n - E0) / 2);
          FILL(1);
          if constexpr (n == B46 || n == B46 + 1) DSR(qf[n - B46], rka, (n - B46) * 256);
          if constexpr (n == B46 + 2 || n == B46 + 3) DSR(ga[n - B46 - 2], gaa, (n - B46 - 2) * 16);
          if constexpr (n == B46 + 4 || n == B46 + 5) DSR(idt[n - B46 - 4], yb[1], IDT - 4 * SL + (n - B46 - 4) * 1024);
          if constexpr (n >= TP_XL && n < TP_XL + 6) LDXS(pv[TP_XL + 5 - n], lane * 16, xr, (n - TP_XL) * 1024 - 3072);
        };
        do {
          STEP2(0, xpb, xpa);
          if (__builtin_expect(u == 17, 0)) {
            TS(57);
            TSC1(40);
            TSC0();
            f32x4 c0, c1;
            const char* up = Ug + (mt * 2 + (wid >> 1)) * SL + (wid & 1) * 1024 + 4096;
            const unsigned ul = lds0 + (2 + (wid >> 1)) * SL + (wid & 1) * 1024 + lane * 16;
            u32x4 uv;
            DSR(c0, lds0 + SCA + wid * 128 + g4 * 32, 0);
            DSR(c1, lds0 + SCA + wid * 128 + g4 * 32, 16);
#define UMUL(j, k) asm volatile("v_mul_f32 %0, %0, %1" : "+v"(acc[(k) >> 2][j][(k)&3]) : "v"(((k) >> 2 ? c1 : c0)[(k)&3]))
#define UCVT(j, k) asm volatile("v_cvt_pk_bf16_f32 %0, %1, %2" : "=v"(uv[k]) : "v"(acc[(k) >> 1][j][((k)&1) * 2]), "v"(acc[(k) >> 1][j][((k)&1) * 2 + 1]))
#define UEP(j, r)                                                                                                                          \
  do {                                                                                                                                     \
    if constexpr ((r) == 0) { UMUL(j, 0); UMUL(j, 1); }                                                                                    \
    if constexpr ((r) == 1) { UMUL(j, 2); UMUL(j, 3); UMUL(j, 4); }                                                                        \
    if constexpr ((r) == 2) { UMUL(j, 5); UMUL(j, 6); }                                                                                    \
    if constexpr ((r) == 3) { UMUL(j, 7); UCVT(j, 0); UCVT(j, 1); }                                                                        \
    if constexpr ((r) == 4) { UCVT(j, 2); UCVT(j, 3); }                                                                                    \
    if constexpr ((r) == 5)                                                                                                                \
      asm volatile("global_store_dwordx4 %0, %2, %3 offset:%4 sc1\n\tds_write_b128 %1, %2 offset:%5\n\ts_nop 0"                        \
                   ::"v"(lane * 16), "v"(ul), "v"(uv), "s"(up + ((j) >> 2) * 8192), "n"(((j)&3) * 2048 - 4096), "n"((j)*2048) : "memory"); \
  } while (0)
            sfor<44>([&](auto nn) __attribute__((always_inline)) {
              constexpr int n = decltype(nn)::value, h = ah(n), p = ap(n), e = n + 7, j = n < 32 ? (n - 11) / 3 : 7 + (n - 33) / 4, r = n < 32 ? (n - 11) % 3 : (n - 33) % 4;
              constexpr bool ue = n >= 11 && n != 32 && r < 3;
              (void)up, (void)ul, (void)acc, (void)X, (void)Y, (void)ye, (void)yo, (void)c0, (void)c1, (void)xo0, (void)xpa, (void)lane, (void)uv;
              if constexpr (p == 0) WAIT_V(n < 8 ? 12 : 2 * (13 - n));
              if constexpr ((n & 1) == 0) WAIT_L(e < 44 ? 5 : 42 - n);
              if constexpr (h < 7) MFMAI(acc[0][p], XS(h)[0], Y[(n + 4) & 7]);
              else MFMA(acc[0][p], XS(h)[0], Y[(n + 4) & 7]);
              if constexpr (e < 44) DSR(Y[(e + 4) & 7], (ah(e) & 1) ? yo : ye, ((ah(e) >> 1) & 1) * SL + ap(e) * 2048);
              if constexpr (ue) UEP(j, 2 * r);
              if constexpr (h < 7) MFMAI(acc[1][p], XS(h)[1], Y[(n + 4) & 7]);
              else MFMA(acc[1][p], XS(h)[1], Y[(n + 4) & 7]);
              if constexpr (n < 2) XLD(XS(3)[n], xpa, 3, n);
              if constexpr (ue) UEP(j, 2 * r + 1);
            });
            TSC1(41);
            TS(0);
            SKEWPT();
            asm volatile("s_nop 4");
            UEP(10, 0); UEP(10, 1); UEP(10, 2); UEP(10, 3); UEP(10, 4); UEP(10, 5);
            ye = lds0 + lane * 16;
            yo = ye + 1024;
            so = 4 * SL;
            a0 = ye + 4 * SL;
            a1 = yo + 4 * SL;
            yb[0] = ye + 2 * SL;
            yb[1] = a0;
            asm volatile("" : "+v"(yb[0]), "+v"(yb[1]), "+v"(ye), "+v"(yo));
            LBAR();
            TS(44);
            sfor<7>([&](auto ee) __attribute__((always_inline)) { (void)Y, (void)yb, (void)ye; YRD(decltype(ee)::value); });
            sfor<44>(BP);
            u = 19;
            xpa = WP(8);
          }
          STEP2(2, xpa, xpb);
        } while (u != 24);
        TS(2);
        sfor<NE - 264>([&](auto nn) __attribute__((always_inline)) { BP(std::integral_constant<int, 264 + decltype(nn)::value>{}); });
        __builtin_amdgcn_s_setprio(0);
        TSC1(59);
        TS(13);
        SKEWPT();
      }
    } else if (wid < 6) {
      const int hw = wid - 4;
      __builtin_amdgcn_s_setprio(2);
      unsigned voy[11];
#pragma unroll
      for (int i = 0; i < 11; ++i) voy[i] = (unsigned)((PROW(i) + hw * 8 + (lane >> 3)) * 4096 + ((lane & 7) ^ (lane >> 3)) * 16);
      const unsigned lbase = lds0 + hw * 1024;
      if (zd) voy[5] = (lane & 7) * 16;
      auto stage = [&](int n, int slot) {
        const char* yp = Yx + (((n + qs * 4) & 31) << 7);
        const unsigned l = lbase + slot * SL;
#pragma unroll
        for (int i = 0; i < 11; ++i) GLDS(i == 5 && zd ? lbase + ZSC : l + i * 2048, voy[i], yp);
      };
      asm volatile("s_waitcnt vmcnt(%3)" ::"v"(voy[0]), "v"(voy[10]), "v"(tag0), "n"(PF_E < PF_N ? 1 : 0) : "memory");
      BARRIER();
#pragma unroll 1
      for (int s = 0; s <= A_LD; ++s) stage(s, s);
      WAIT_V(11 * A_LD);
      TS(38);
      BARRIER();
      int slot = A_LD + 1;
#pragma unroll 1
      for (int k = 1; k <= 31 - A_LD; ++k) {
        WAIT_V(11 * (A_LD - 1));
        BARRIER();
        if (hw == 1) TSD(k);
        if (LSLP) __builtin_amdgcn_s_sleep(LSLP);
        stage(k + A_LD, slot);
        slot = slot == NS - 1 ? 0 : slot + 1;
        if (k == CPF_K && hw == 1 && lane < CPF_N) PFL(cpf);
      }
      if (A_LD > 3) { WAIT_V(33); BARRIER(); }
      if (A_LD > 2) { WAIT_V(22); BARRIER(); }
      WAIT_V(0); BARRIER();
      const unsigned* ufp = ufl + (KT(min(lane >> 2, 6) + 1) * 4 + (lane & 3)) * UST;
      unsigned miss = ~0u;
      const char* y1 = Ug + KT(1) * 2 * SL + hw * 11264;
      asm volatile("" : "+s"(y1), "+v"(ufp));
      BARRIER();
      TS(47);
      if (U_D) __builtin_amdgcn_s_sleep(U_D);
      auto UPOLL = [&](unsigned need, unsigned em0) __attribute__((always_inline)) {
        POLL { PDBG(it);
          if ((miss & need) == 0) break;
          if (it) __builtin_amdgcn_s_sleep(U_R);
          unsigned f;
          const u64 em = miss & em0;
          asm volatile("s_mov_b64 exec, %2\n\tglobal_load_dword %0, %1, off sc1\n\ts_mov_b64 exec, -1\n\ts_waitcnt vmcnt(0)" : "=&v"(f) : "v"(ufp), "s"(em) : "memory");
          miss &= ~(unsigned)em | (unsigned)__builtin_amdgcn_ballot_w64(f == SENT);
        }
      };
#define G1(o) "global_load_lds_dwordx4 %0, %1 offset:" #o " sc1\n\t"
#define ULD(yp, l)                                                                                                                          \
  do {                                                                                                                                      \
    asm volatile("s_mov_b32 m0, %2\n\ts_nop 0\n\t" G1(0) G1(1024) G1(2048) G1(3072)::"v"(lane * 16), "s"(yp), "s"(l) : "memory");            \
    asm volatile("s_mov_b32 m0, %2\n\ts_nop 0\n\t" G1(0) G1(1024) G1(2048) G1(3072)::"v"(lane * 16 + 4096), "s"(yp), "s"((l) + 4096) : "memory"); \
    asm volatile("s_mov_b32 m0, %2\n\ts_nop 0\n\t" G1(0) G1(1024) G1(2048)::"v"(lane * 16 + 8192), "s"(yp), "s"((l) + 8192) : "memory");     \
  } while (0)
      {
        UPOLL(3u, 15u);
        TS(48);
        ULD(y1, lds0 + 4 * SL + hw * 11264);
        UPOLL(15u, 15u);
        unsigned f;
        asm volatile("global_load_dword %0, %1, off sc1" : "=v"(f) : "v"(ufp) : "memory");
        ULD(y1 + SL, lds0 + hw * 11264);
        asm volatile("s_waitcnt vmcnt(11)" : "+v"(f)::"memory");
        BARRIER();
        miss = (unsigned)__builtin_amdgcn_ballot_w64(f == SENT);
      }
      slot = 1;
#pragma unroll 1
      for (int n = 4; n < 16; ++n) {
        UPOLL(3u << (2 * n - 4), ~0u);
        const char* yp = Ug + (KT(n >> 1) * 2 + (n & 1)) * SL + hw * 11264;
        unsigned l = lds0 + slot * SL + hw * 11264;
        slot = slot == NS - 1 ? 0 : slot + 1;
        if (n == CPF_B && hw == 1 && lane < CPF_N) PFL(cpf);
        asm volatile("" : "+s"(yp), "+s"(l));
        if (n >= 6) {
          WAIT_V(22);
          BARRIER();
          if (hw == 1) TSD(n + 44);
        }
        ULD(yp, l);
      }
      WAIT_V(22);
      BARRIER();
      WAIT_V(0);
      BARRIER();
      BARRIER();
      if (WK_AT == 0 && b < 56 && tid == 256) ST4(reinterpret_cast<unsigned*>(ws + WS_WAK) + b * 32, 1u);
      BARRIER();
      if (WK_AT == 1 && b < 56 && tid == 256) ST4(reinterpret_cast<unsigned*>(ws + WS_WAK) + b * 32, 1u);
      TSE();
      return;
    } else {
      const int aw = wid - 6, rp = min(mt * 2 + aw, 10);
      PBAR(0);
      XFLAG();
      if (zd) {
#pragma unroll
        for (int s = 0; s < NS; ++s) *reinterpret_cast<u32x4*>(shm + s * SL + 10240 + aw * 1024 + lane * 16) = u32x4{0u, 0u, 0u, 0u};
      }
      LBAR();
      int wq = nt * 2 + aw;
#define WPF()                                                                                                                   \
  do {                                                                                                                          \
    if (wq < W_NQ) PFL(ws + WS_WH + (size_t)(cc * 32 + mt * 4 + (wq & 3)) * 65536 + (KT(wq >> 2) << 13) + lane * 128);          \
    wq += 6;                                                                                                                    \
  } while (0)
      float rs[4] = {};
      const unsigned ra = lds0 + rp * 2048 + lane * 16;
      unsigned so = 0;
#define RSUM()                                                                                                                        \
  do {                                                                                                                                \
    u32x4 R_[2];                                                                                                                      \
    if (NORS) break;                                                                                                                  \
    asm volatile("ds_read_b128 %0, %2\n\tds_read_b128 %1, %2 offset:1024\n\ts_waitcnt lgkmcnt(0)" : "=&v"(R_[0]), "=&v"(R_[1]) : "v"(ra + so) : "memory"); \
    _Pragma("unroll") for (int j = 0; j < 8; ++j) {                                                                                   \
      asm volatile("v_add_f32 %0, %0, %1" : "+v"(rs[(j >> 2) * 2]) : "v"(R_[j >> 2][j & 3] << 16));                                   \
      asm volatile("v_add_f32 %0, %0, %1" : "+v"(rs[(j >> 2) * 2 + 1]) : "v"(R_[j >> 2][j & 3] & 0xffff0000u));                       \
    }                                                                                                                                 \
    so = so == (NS - 1) * SL ? 0 : so + SL;                                                                                           \
  } while (0)
      {
#pragma unroll 1
        for (int bi = 0; bi < F_AT; ++bi) {
          RSUM();
          BARRIER();
        }
        u32x4 f;
#define FLD() asm volatile("global_load_dwordx4 %0, %1, %2 sc1" : "=v"(f) : "v"(lane * 16), "s"(ws + WS_XF2 + aw * 1024) : "memory")
        FLD();
#pragma unroll 1
        for (int bi = F_AT; bi < 4; ++bi) {
          RSUM();
          BARRIER();
        }
        POLL { PDBG(it);
          asm volatile("s_waitcnt vmcnt(0)" : "+v"(f)::"memory");
          if (__all(mk64(f[1], f[0]) == tag && mk64(f[3], f[2]) == tag)) break;
          __builtin_amdgcn_s_sleep(1);
          FLD();
        }
        asm volatile("s_waitcnt vmcnt(0)" : "+v"(f)::"memory");
      }
      const unsigned xo = (unsigned)(((qx * 512 + aw * 4 + g4) * D + mt * 128) * 2 + ((l16 ^ (aw * 4 + g4)) << 4));
#pragma unroll 1
      for (int bi = 4; bi < H_AT; ++bi) {
        RSUM();
        const int i = bi - X_AT;
        if (i >= 0 && i < 12) GLDS(lds0 + XR + i * 2048 + aw * 1024, xo, x + (PROW(5 + i / 2) + (i & 1) * 8) * D);
        BARRIER();
      }
      {
        u64 f;
        const unsigned* fp = hfl + lane * HST;
        asm volatile("global_load_dwordx2 %0, %1, off sc1" : "=v"(f) : "v"(fp) : "memory");
        RSUM();
        BARRIER();
        RSUM();
        BARRIER();
        POLL { PDBG(it);
          asm volatile("s_waitcnt vmcnt(0)" : "+v"(f)::"memory");
          if (__all(f == tag)) break;
          __builtin_amdgcn_s_sleep(1);
          asm volatile("global_load_dwordx2 %0, %1, off sc1" : "=v"(f) : "v"(fp) : "memory");
        }
        asm volatile("s_waitcnt vmcnt(0)" : "+v"(f)::"memory");
        unsigned c;
        const unsigned* cp = modv + 1024 + mt * 128 + aw * 64 + lane;
        asm volatile("global_load_dword %0, %1, off sc1" : "=v"(c) : "v"(cp) : "memory");
        if (TLBW && aw == 0 && lane < 3) PFL(lane == 0 ? Ug - 128 : lane == 1 ? Ug + 16 * SL : ws + WS_WH + (size_t)(cc * 32 + mt * 4) * 65536);
        WPF();
        RSUM();
        BARRIER();
        WPF();
        RSUM();
        BARRIER();
        POLL { PDBG(it);
          asm volatile("s_waitcnt vmcnt(0)" : "+v"(c)::"memory");
          if (__all(c != SENT)) break;
          __builtin_amdgcn_s_sleep(1);
          asm volatile("global_load_dword %0, %1, off sc1" : "=v"(c) : "v"(cp) : "memory");
        }
        asm volatile("s_waitcnt vmcnt(0)" : "+v"(c)::"memory");
        *reinterpret_cast<unsigned*>(shm + SCA + (aw * 64 + lane) * 4) = c ^ (cc ? 0x80000000u : 0u);
        flg[0] = 0u;
      }
#pragma unroll 1
      for (int bi = H_AT + 4; bi < 32; ++bi) {
        WPF();
        RSUM();
        if (bi < 30) BARRIER();
      }
      unsigned* rsb = reinterpret_cast<unsigned*>(ws + WS_RS) + qx * 512;
      {
        const float v0 = dpp_add<0x141>(dpp_add<0x4E>(dpp_add<0xB1>(rs[0] + rs[1])));
        const float v1 = dpp_add<0x141>(dpp_add<0x4E>(dpp_add<0xB1>(rs[2] + rs[3])));
        if ((lane & 7) < 2 && mt * 2 + aw < 11 && !(nt == 2 && rp == 5)) ST4(rsb + cc * S + PROW(rp) + (lane & 1) * 8 + (lane >> 3), nsent((lane & 1) ? v1 : v0));
      }
      BARRIER();
#pragma unroll 1
      for (int k = 34; k <= (aw ? B_VAT : 46); ++k) BARRIER();
      u32x2 m[6];
      const unsigned lr = min(lane, 47);
      const unsigned o0 = aw ? (unsigned)(WS_Q - WS_MOD) + mt * 512 + lane * 8 : 8192u + mt * 512 + lane * 8;
      const unsigned o1 = aw ? (unsigned)(WS_RS - WS_MOD) + (qx * 512 + PROW(5 + (lr >> 3)) + (lr & 7) * 2) * 4 : o0 + 8192;
      const unsigned o2 = aw ? o1 + 8192 : o0 + 16384, o3 = aw ? (XA_G ? (unsigned)(WS_XAF - WS_MOD) + (b ^ 4) * 128 : o1) : 28672u + min(lane, 23) * 8;
#define VLD()                                                                                                                     \
  asm volatile("global_load_dwordx2 %0, %6, %10 sc1\n\tglobal_load_dwordx2 %1, %6, %11 sc1\n\tglobal_load_dwordx2 %2, %7, %10 sc1\n\t" \
               "global_load_dwordx2 %3, %7, %11 sc1\n\tglobal_load_dwordx2 %4, %8, %10 sc1\n\tglobal_load_dwordx2 %5, %9, %10 sc1"    \
               : "=&v"(m[0]), "=&v"(m[1]), "=&v"(m[2]), "=&v"(m[3]), "=&v"(m[4]), "=&v"(m[5])                                     \
               : "v"(o0), "v"(o1), "v"(o2), "v"(o3), "s"(modv), "s"(modv + 1024)                                                  \
               : "memory")
      VLD();
      if (aw) {
#pragma unroll
        for (int k = B_VAT + 1; k <= 45; ++k) BARRIER();
      }
      TS(30);
      POLL { PDBG(it);
        asm volatile("s_waitcnt vmcnt(0)" : W6(m)::"memory");
        if (__all(max(max(max(max2(m[0]), max2(m[1])), max(max2(m[2]), aw ? 0u : max2(m[3]))), max(max2(m[4]), max2(m[5]))) != SENT)) break;
        __builtin_amdgcn_s_sleep(2);
        VLD();
      }
      asm volatile("s_waitcnt vmcnt(0)" : W6(m)::"memory");
      if (aw == 0) {
        *reinterpret_cast<u32x2*>(shm + TGA + lane * 8) = m[0];
        *reinterpret_cast<unsigned*>(shm + TV + lane * 4) = hi2(m[2]);
        *reinterpret_cast<unsigned*>(shm + TV + 256 + lane * 4) = hi2(m[1]);
        *reinterpret_cast<unsigned*>(shm + TV + 512 + lane * 4) = hi2(m[3]);
        const float p0 = __uint_as_float(m[4][0]), p1 = __uint_as_float(m[4][1]);
        const unsigned hh = pack2(p0, p1);
        *reinterpret_cast<unsigned*>(shm + SCW + lane * 4) = hh;
        *reinterpret_cast<unsigned*>(shm + SCW + 256 + lane * 4) = pack2(p0 - bflo(hh), p1 - bfhi(hh));
        *reinterpret_cast<u32x2*>(shm + SCW + 512 + lane * 8) = u32x2{0x3f803f80u, 0x3f803f80u};
        const float u = __uint_as_float(m[5][0]) + __uint_as_float(m[5][1]), u2 = dpp_add<0xB1>(u), u8 = dpp_add<0x141>(dpp_add<0x4E>(u2));
        if (lane < 17 && !(lane & 1)) *reinterpret_cast<float*>(shm + TSUM + lane * 2) = lane < 16 ? u2 : u8;
      } else {
        BARRIER();
#pragma unroll
        for (int j = 0; j < 2; ++j) {
          const float r0 = __uint_as_float(m[0][j]), r1 = __uint_as_float(m[1][j]);
          const unsigned a0 = pack2(r0, r0), a1 = pack2(r1, r1);
          *reinterpret_cast<u32x4*>(shm + RKA + ((lane >> 4) * 32 + ((lane >> 1) & 1) * 16 + ((lane >> 2) & 3) * 4 + (lane & 1) * 2 + j) * 16) =
              u32x4{a0, pack2(r0 - bflo(a0), r0 - bflo(a0)), a1, pack2(r1 - bflo(a1), r1 - bflo(a1))};
          const float s0 = __uint_as_float(m[2][j]), s1 = -__uint_as_float(m[4][j]);
          const unsigned b0 = pack2(s0, s0 - rbf(s0)), b1 = pack2(s1, s1 - rbf(s1));
          *reinterpret_cast<u32x4*>(shm + RKB + (lr * 2 + j) * 16) = u32x4{b0, b0, b1, b1};
        }
        *reinterpret_cast<u32x2*>(shm + RKZ + lane * 8) = u32x2{0u, 0u};
        const unsigned one = g4 == (l16 >> 2) ? ((lane & 1) ? 0x3f800000u : 0x3f80u) : 0u, e0 = (lane & 2) ? 0u : one, e1 = (lane & 2) ? one : 0u;
        *reinterpret_cast<u32x4*>(shm + IDT + lane * 16) = u32x4{e0, e1, 0u, 0u};
        *reinterpret_cast<u32x4*>(shm + IDT + 1024 + lane * 16) = u32x4{0u, 0u, e0, e1};
      }
      TS(17);
      LBAR();
    }
    const int row0 = rg * 16 + l16, col1 = mt * 128 + g4 * 8;
    u32x4 xq[4], sa4[4];
#pragma unroll
    for (int q = 0; q < 4; ++q) DSR(sa4[q], saa, q * 64);
    f32x2 cj = *reinterpret_cast<const f32x2*>(shm + TSUM + g4 * 8);
    float sw = *reinterpret_cast<const float*>(shm + TSUM + 32), ce = *reinterpret_cast<const float*>(shm + TSUM + mt * 4);
    asm volatile("" : "+v"(cj), "+v"(sw), "+v"(ce));
    if (wid < 4) {
      POLL { PDBG(it);
        asm volatile("s_waitcnt vmcnt(0)" : W6(pv)::"memory");
        if (__all(max(max(max(max4(pv[0]), max4(pv[1])), max(max4(pv[2]), max4(pv[3]))), max(max4(pv[4]), max4(pv[5]))) != SENT)) break;
        __builtin_amdgcn_s_sleep(1);
        XCHLD();
      }
      asm volatile("s_waitcnt vmcnt(0)" : W6(pv)::"memory");
      TS(27);
      if (ST_E && tid == 0) __atomic_store_n(flg, 1u, __ATOMIC_RELAXED);
    } else if (ST_E) {
      unsigned sv;
      LSPIN(sv, flg, sv != 0u);
    }
    if (ST_E) STLD();
    else sr[0] = sr[1] = u32x4{SENT, SENT, SENT, SENT};
    if (wid < 4) {
      sfor<8>([&](auto kc) __attribute__((always_inline)) {
        constexpr int k = decltype(kc)::value;
        (void)acc, (void)idt, (void)pv, (void)ga, (void)x5, (void)xw1;
        if constexpr (k < 6) {
          MFMA(acc[0][5 + k], idt[0], pv[k]);
          MFMA(acc[1][5 + k], idt[1], pv[k]);
        }
        if constexpr (k >= 2) {
          constexpr int j = k - 2;
          asm volatile("" : "+v"(acc[0][5 + j]), "+v"(acc[1][5 + j]));
          const u32x4 v = pk8((j < 5 ? acc[0][j % 5] : x5[0]) + ga[0] * acc[0][5 + j], (j < 5 ? acc[1][j % 5] : x5[1]) + ga[1] * acc[1][5 + j]);
          asm volatile("ds_write_b128 %0, %1 offset:%2" ::"v"(xw1), "v"(v), "n"(j * 4096) : "memory");
        }
      });
    }
    {
      TS(6);
      LBAR();
      asm volatile("ds_read_b128 %0, %8\n\tds_read_b128 %1, %8 offset:1024\n\tds_read_b128 %2, %8 offset:2048\n\tds_read_b128 %3, %8 offset:3072\n\ts_waitcnt lgkmcnt(0)"
                   : "=&v"(xq[0]), "=&v"(xq[1]), "=&v"(xq[2]), "=&v"(xq[3]), "+v"(sa4[0]), "+v"(sa4[1]), "+v"(sa4[2]), "+v"(sa4[3])
                   : "v"(xa)
                   : "memory");
      if (!act) {
        asm volatile("s_waitcnt vmcnt(0)" ::"v"(sr[0]), "v"(sr[1]) : "memory");
        TSE();
        return;
      }
      f32x4 s4 = {0.f, 0.f, 0.f, 0.f}, gr = {0.f, 0.f, 0.f, 0.f};
#pragma unroll
      for (int q = 0; q < 4; ++q) {
        gr = MM(xq[q], xq[q], gr);
        s4 = MM(sa4[q], xq[q], s4);
      }
      const u32x4 gd = __builtin_bit_cast(u32x4, gr) & dk;
      const float m = s4[2] * (1.0f / 128);
      float q2 = ar4(__uint_as_float((gd[0] | gd[1]) | (gd[2] | gd[3]))) - s4[2] * m, lg = (s4[0] + s4[1]) - m * ce;
      if (__any(!(s4[2] * m <= 256.0f * q2))) {
        f32x4 a = {0.f, 0.f, 0.f, 0.f}, l = {0.f, 0.f, 0.f, 0.f};
#pragma unroll
        for (int q = 0; q < 4; ++q) {
          const u32x4 ch = *reinterpret_cast<const u32x4*>(shm + SCW + g4 * 16 + q * 64), cl = *reinterpret_cast<const u32x4*>(shm + SCW + 256 + g4 * 16 + q * 64);
          const f32x4 d0 = lo4(xq[q]) - m, d1 = hi4(xq[q]) - m;
          a += d0 * d0 + d1 * d1;
          l += d0 * (lo4(ch) + lo4(cl)) + d1 * (hi4(ch) + hi4(cl));
        }
        q2 = ar4((a[0] + a[1]) + (a[2] + a[3]));
        lg = ar4((l[0] + l[1]) + (l[2] + l[3]));
      }
      const u32x4 own = {nsent(m), nsent(q2), nsent(lg), 0u};
      if (g4 == 0) ST16(sto, own, ws + WS_STAT, 0);
      TS(7);
      SKEWPT();
      const bool k0 = g4 * 2 == mt, k1 = g4 * 2 + 1 == mt;
#define STOK() (sr[0] = k0 ? own : sr[0], sr[1] = k1 ? own : sr[1], __all(max(max(max(sr[0][0], sr[0][1]), sr[0][2]), max(max(sr[1][0], sr[1][1]), sr[1][2])) != SENT))
      if (ST_E) asm volatile("s_waitcnt vmcnt(1)\n\t; AUDIT_OK %0 %1" : "+v"(sr[0]), "+v"(sr[1])::"memory");
      bool pub = ST_E && STOK();
      if (!pub) {
        pub = PUB_ALL || mt == (rg & 7);
        if (OPT_SD) __builtin_amdgcn_s_sleep(OPT_SD);
        POLL { PDBG(it);
          STLD();
          asm volatile("s_waitcnt vmcnt(0)" : "+v"(sr[0]), "+v"(sr[1])::"memory");
          if (STOK()) break;
          __builtin_amdgcn_s_sleep(1);
        }
      }
      TS(9);
      const f32x2 mk = {__uint_as_float(sr[0][0]), __uint_as_float(sr[1][0])};
      const f32x2 qk = {__uint_as_float(sr[0][1]), __uint_as_float(sr[1][1])};
      const f32x2 lk = {__uint_as_float(sr[0][2]), __uint_as_float(sr[1][2])};
      const float mean = ar4(mk[0] + mk[1]) * (1.0f / 8);
      const f32x2 dm = mk - mean;
      const f32x2 a = qk + 128.0f * (dm * dm), l = lk + dm * cj;
      const float vf = ar4(a[0] + a[1]) * (1.0f / D);
      const float rstd = __builtin_amdgcn_rsqf(vf + 1e-5f);
      const float rd = rstd * ar4(l[0] + l[1]) + sw;
      const unsigned rb = f2bf(rd);
      const unsigned ord = rb ^ ((rb & 0x8000u) ? 0xffffu : 0x8000u);
      const unsigned key = (ord << 16) | (0xffffu - (unsigned)row0);
      unsigned t1 = key, t2 = 0u;
      TOP2(0xB1); TOP2(0x4E); TOP2(0x141); TOP2(0x140);
      if (lane == 0 && pub) ST4(reinterpret_cast<u64*>(ws + WS_KEY) + rg * 16, mk64(~t2, ~t1));
      TS(1);
      SKEWPT();
      if (key >= t2) {
        const int sl = rg * 2 + (key == t2);
#pragma unroll
        for (int q = 0; q < 4; ++q) ST16(sl * 2048 + col1 * 2, nsent4(xq[q]), ws + WS_CAND, q * 64);
        if (g4 == 0 && pub) ST16(sl * 16, (u32x4{nsent(mean), nsent(rstd), 0u, 0u}), ws + WS_HDR, 0);
      }
      TS(8);
      if (OUT_D) __builtin_amdgcn_s_sleep(OUT_D);
#pragma unroll
      for (int q = 0; q < 4; ++q) {
        const u32x4 cq = *reinterpret_cast<const u32x4*>(shm + TV + q * 64 + g4 * 16), sbq = *reinterpret_cast<const u32x4*>(shm + TV + 256 + q * 64 + g4 * 16);
        const u32x4 gbq = *reinterpret_cast<const u32x4*>(shm + TV + 512 + q * 64 + g4 * 16);
        const f32x4 x0 = lo4(xq[q]), x1 = hi4(xq[q]);
        const f32x4 h0 = ((x0 - mean) * rstd) * lo4(cq) + lo4(sbq), h1 = ((x1 - mean) * rstd) * hi4(cq) + hi4(sbq);
        ST16((row0 * D + col1) * 2, pk8(x0 + lo4(gbq) * h0, x1 + hi4(gbq) * h1), out, q * 64);
      }
      asm volatile(".Lcpf_e2:");
      WAIT_V(0);
      if (lane == 0) ST4(reinterpret_cast<unsigned*>(ws + WS_DON) + (rg * 8 + mt) * 2, 1u);
      TS(16);
    }
    TSE();
    return;
  }
  {
    const int hid = b - 192, hi = j8 - 24;
    constexpr int RNG = 65536, SSI = MISC, GP = MISC + 2048, SSA = MISC + 3072, HS = MISC + 5120, SX1 = MISC + 9216, ACT = MISC + 13312,
                  HFLG = MISC + 13568, RR = MISC + 13824;
    unsigned* flg = reinterpret_cast<unsigned*>(shm + HFLG);
    const int ec = xcd * 7 + hi, hd = hi == 7 ? 7 : (hi + 7 - ((3 + xcd) & 7) % 7) % 7;
    const bool pon = wid < ((0x06555542 >> (4 * hd)) & 7);
    const char* pfh = PFG((hd < 2 ? 2 * hd : 5 * hd - 4) + wid, 8 - qs);
#define HPF(g) do { if (pon && !((ABL & 2) && (g) > 3)) PFL(pfh + ((((g) + qs) & 7) << 9)); } while (0)
    WHLD(4);
    const int cb0 = xcd * 8 + ((hd + 2) & 7);
    const unsigned om = (unsigned)((wid * 16 + (lane >> 3)) * 12288 + (lane & 7) * 16);
    const unsigned og = (unsigned)((wid * 8 + (lane >> 3)) * 7168 + (lane & 7) * 16);
    const unsigned od = (unsigned)((wid * 8 + (lane >> 3)) * 2048 + (lane & 7) * 16);
    const char* pm = reinterpret_cast<const char*>(wmod) + cb0 * 128;
    const char* pg = reinterpret_cast<const char*>(wg) + ec * 128;
    const char* pu = reinterpret_cast<const char*>(wu) + ec * 128;
    const char* pd = reinterpret_cast<const char*>(wd) + (size_t)ec * 131072;
    const unsigned ow = (unsigned)(((wid & 1) * 16 + (lane >> 3)) * 12288 + (wid >> 1) * 128 + (lane & 7) * 16);
    const char* pw = reinterpret_cast<const char*>(wmod) + 8192 + xcd * 512;
#define RQM(r)                                                                                   \
  do {                                                                                           \
    const char* p_ = pm + (size_t)(r)*1572864;                                                   \
    GLDSP(lds0 + RNG + ((r)&3) * 16384 + wid * 2048, om, p_, WPOL);                              \
    GLDSP(lds0 + RNG + ((r)&3) * 16384 + wid * 2048 + 1024, om, p_ + 98304, WPOL);               \
  } while (0)
#define RQE(r)                                                                                                                \
  do {                                                                                                                        \
    if (NOEXP) break;                                                                                                         \
    GLDSP(lds0 + RNG + ((r)&3) * 16384 + wid * 1024, (r) < 16 ? og : od, (r) < 16 ? pg + (r)*458752 : pd + ((r)-16) * 256, WPOL);   \
    GLDSP(lds0 + RNG + ((r)&3) * 16384 + 8192 + wid * 1024, (r) < 16 ? og : od, (r) < 16 ? pu + (r)*458752 : pd + ((r)-16) * 256 + 128, WPOL); \
  } while (0)
    PBAR(4);
    XFLAG();
    const u32x4 sv = {SENT, SENT, SENT, SENT};
    auto PUBS = [&](int cb, unsigned v, unsigned p, unsigned r) __attribute__((always_inline)) {
      const int vi = cb >> 4, col = (cb & 15) * 64 + lane;
      ST4(modv + cb * 64 + lane, v);
      if (vi == 4) ST4(modv + 6144 + col, p);
      if (vi == 4 ? (lane & 31) == 0 : vi == 3 && lane == 0) ST4(modv + 7168 + (vi == 4 ? col >> 5 : 32 + (cb & 15)), r);
    };
    if (wid == 4 || (hi == 7 && wid < 4)) PUBS(wid == 4 ? cb0 : 64 + xcd * 4 + wid, SENT, SENT, SENT);
    if (tid >= 480) ST4(reinterpret_cast<unsigned*>(ws + WS_Q) + hsl * 32 + tid - 480, SENT);
    unsigned cv, wv;
    asm volatile("global_load_dword %0, %2, %3\n\tglobal_load_dword %1, %2, %4" : "=&v"(cv), "=&v"(wv) : "v"(tid * 4), "s"(cvec), "s"(wr_) : "memory");
    if (hi < 7) {
      if (PF_N < 3) HPF(2);
      HPF(3);
      const char* ap = ws + WS_ARM + (size_t)(ec * 8 + wid) * 3328;
#pragma unroll
      for (int j = 0; j < 4; ++j) ST16(lane * 16, sv, ap, j < 3 ? j * 1024 : 2304);
      HPF(4);
      WAIT_V(4);
    } else {
      WAIT_V(0);
    }
    asm volatile("; AUDIT_OK %0 %1" : "+v"(cv), "+v"(wv)::"memory");
    *reinterpret_cast<unsigned*>(shm + SSI + tid * 4) = pack2(silu_bf(bflo(cv)), silu_bf(bfhi(cv)));
    *reinterpret_cast<unsigned*>(shm + SX1 + tid * 4) = wv;
    LBAR();
    TS(50);
    const unsigned ha = (unsigned)(((l16 >> 2) * 8 + (lane & 3)) * 2048 + wid * 256);
#define HFR(j) *reinterpret_cast<const u32x4*>(shm + ha + ((j)&1) * 8192 + ((j) >> 2) * 128 + (((((j) >> 1) & 1) * 4 + g4) ^ (lane & 7)) * 16)
    HPF(5);
#pragma unroll
    for (int j = 0; j < ((ABL & 1) ? 0 : 8); ++j) ST16(lane * 16, HFR(j), ws + WS_WH + (size_t)hsl * 65536 + wid * 8192 + 4096, j * 1024 - 4096);
    HPF(6);
    RQM(0);
    RQM(1);
    RQM(2);
    const unsigned tm = lds0 + RNG + ((wid >> 2) * 64 + g4 * 8 + ((lane >> 2) & 3)) * 128 + (wid & 3) * 32 + (lane & 3) * 8;
    f32x4 ma = {0.f, 0.f, 0.f, 0.f};
#define GVR(i)                                                                                                                               \
  do {                                                                                                                                       \
    _Pragma("unroll") for (int f = 0; f < 2; ++f)                                                                                            \
        ma = MM(trd(tm + ((i)&3) * 16384 + f * 4096), *reinterpret_cast<const u32x4*>(shm + SSI + ((i)*128 + (wid >> 2) * 64 + f * 32 + g4 * 8) * 2), ma); \
    if ((i) == 7 && l16 == 0) *reinterpret_cast<f32x4*>(shm + GP + wid * 64 + g4 * 16) = ma;                                                 \
  } while (0)
    const float* gp = reinterpret_cast<const float*>(shm + GP);
    auto PUB = [&](int cb, float s) __attribute__((always_inline)) {
      const int vi = cb >> 4;
      const float v = rbf(((vi == 1 || vi == 4) ? 1.0f : 0.0f) + rbf(s));
      const float p = v * bf2f(*reinterpret_cast<const u16*>(shm + SX1 + ((cb & 15) * 64 + lane) * 2));
      const float r16 = row_sum(p), r32 = swap16_add(r16, r16);
      PUBS(cb, nsent(v), nsent(p), nsent(vi == 4 ? r32 : swap32_add(r32, r32)));
    };
    {
      sfor<8>([&](auto ii) __attribute__((always_inline)) {
        constexpr int i = decltype(ii)::value;
        (void)ow, (void)pw, (void)om, (void)pm, (void)og, (void)od, (void)pg, (void)pu, (void)pd, (void)lds0, (void)wid, (void)pfl, (void)Yx, (void)l16, (void)g4, (void)xcd, (void)hfl, (void)lane, (void)hid;
        TSCAP();
        WAIT_V(4);
        LBAR();
        if (wid == 2) TS(i);
        if constexpr (i == 0) { if (tid == 0) ST4(reinterpret_cast<u64*>(hfl + hid * HST), tag); }
        if constexpr (i == 0) HPF(7);
        if constexpr (i < 5) {
          RQM(i + 3);
        } else {
          const bool e = hid < 56;
          const char* p_ = pw + (i - 5) * 393216;
          GLDSP(lds0 + RNG + (i - 5) * 16384 + wid * (e ? 1024 : 2048), e ? og : ow, e ? pg + (i - 5) * 458752 : p_, WPOL);
          GLDSP(lds0 + RNG + (i - 5) * 16384 + wid * (e ? 1024 : 2048) + (e ? 8192 : 1024), e ? og : ow, e ? pu + (i - 5) * 458752 : p_ + 98304, WPOL);
        }
        GVR(i);
      });
    }
    TS(51);
#define PUB0() do { if (wid == 0) PUB(cb0, gp[lane] + gp[64 + lane]); } while (0)
    u64 sf, hf;
#define SFLD(OP)                                                                                                                   \
  asm volatile("s_mov_b64 exec, 3\n\t" OP "\n\ts_mov_b64 exec, 0xff\n\tglobal_load_dwordx2 %1, %6, %5 sc1\n\ts_mov_b64 exec, -1"   \
               : "=&v"(sf), "=&v"(hf) : "v"(lane * 128), "v"(0ull), "s"(ws + WS_SFL + wid * 256), "s"(ws + WS_HFL + wid * 64), "v"(lane * 8) : "memory")
#define SF_LD "global_load_dwordx2 %0, %2, %4 sc1"
#define SF_AT "global_atomic_add_x2 %0, %2, %3, %4 sc0"
    auto SAW = [&]() __attribute__((always_inline)) {
      POLL { PDBG(it);
        if (((__builtin_amdgcn_ballot_w64(sf != tag) & 3u) | (__builtin_amdgcn_ballot_w64(hf != tag) & 0xffu)) == 0) break;
        __builtin_amdgcn_s_sleep(4);
        SFLD(SF_LD);
        asm volatile("s_waitcnt vmcnt(0)" : "+v"(sf), "+v"(hf)::"memory");
      }
    };
    u32x2 s2;
#define SALD() asm volatile("global_load_dwordx2 %0, %1, %2 sc1" : "=v"(s2) : "v"(tid * 8), "s"(modv) : "memory")
    auto QMM = [&]() __attribute__((always_inline)) {
      *reinterpret_cast<unsigned*>(shm + SSA + tid * 4) = hi2(s2);
      f32x4 qa[2] = {};
#pragma unroll
      for (int j = 0; j < 8; ++j) qa[j & 1] = MM(HFR(j), *reinterpret_cast<const u32x4*>(shm + SSA + wid * 256 + (j >> 1) * 64 + g4 * 16), qa[j & 1]);
      if (l16 < 2) *reinterpret_cast<f32x4*>(shm + HS + wid * 128 + g4 * 32 + l16 * 16) = l16 ? qa[1] : qa[0];
    };
    auto QPUB = [&]() __attribute__((always_inline)) {
      if (tid >= 480) {
        float s = 0.f;
#pragma unroll
        for (int w = 0; w < 8; ++w) s += *reinterpret_cast<const float*>(shm + HS + w * 128 + (tid - 480) * 4);
        ST4(reinterpret_cast<unsigned*>(ws + WS_Q) + hsl * 32 + tid - 480, nsent(s));
      }
    };
    u32x8 GU[16], DN[8];
    if (hid < 56) {
      const unsigned tg = lds0 + RNG + (g4 * 8 + ((lane >> 2) & 3)) * 128 + ((lane >> 1) & 1) * 8192 + wid * 16 + (lane & 1) * 8;
      const unsigned td = lds0 + RNG + (g4 * 8 + ((lane >> 2) & 3)) * 128 + ((wid >> 1) & 1) * 8192 + (wid & 1) * 64 + (lane & 3) * 8 + (wid >> 2) * 16384;
      sfor<24>([&](auto rr) __attribute__((always_inline)) {
        constexpr int r = decltype(rr)::value;
        (void)td, (void)tg, (void)GU, (void)DN, (void)og, (void)od, (void)pg, (void)pu, (void)pd, (void)lds0, (void)wid, (void)pfl, (void)Yx, (void)l16, (void)g4, (void)xcd;
        (void)s2, (void)modv, (void)tid;
        (void)hid, (void)lane, (void)sf, (void)hf;
        (void)sv, (void)hi, (void)ws;
#define EX(r) ((!XA_G && (r) >= 0 && (r) < 12 / A_PR ? A_PR : 0) + ((r) == Q_AT - 6 ? 2 : 0) + ((r) == Q_AT - 3))
        WAIT_V(r == 23 ? 0 : 4 + EX(r - 2) + EX(r - 1));
        LBAR();
        TSCAP();
        if (wid == 3) TS(r);
        if constexpr (r == 0) PUB0();
        if constexpr (r == 1) {
          if (cb0 < 16 && wid == 0) {
            WAIT_V(2 + EX(0));
            if (lane == 0) ST4(reinterpret_cast<u64*>(ws + WS_SFL) + cb0 * 16, tag);
          }
        }
        if constexpr (r == Q_AT - 6) SFLD(SF_1);
        if constexpr (r == Q_AT - 3) {
          asm volatile("" : "+v"(sf), "+v"(hf));
          SAW();
          SALD();
        }
        if constexpr (!XA_G && r < 12 / A_PR) {
          const char* ap = ws + WS_XCH + xcd * 589824 + min(hi * 8 + wid + r * 56, 576 / A_PR - 1) * (A_PR * 1024);
#pragma unroll
          for (int j = 0; j < A_PR; ++j) ST16(lane * 16, sv, ap, j * 1024);
        }
        if constexpr (r < 17) RQE(r + 3);
        if constexpr (r == 18 || r == 20) { RQE(r + 2); RQE(r + 3); }
        if constexpr (r < 16) {
          GU[r] = trd2(tg + (r & 3) * 16384);
          asm volatile("" : "+v"(GU[r]));
        } else if constexpr (r & 1) {
#pragma unroll
          for (int j = 0; j < 2; ++j) {
            DN[r - 17 + j] = trd2(td + (r & 2) * 16384 + j * 32);
            asm volatile("" : "+v"(DN[r - 17 + j]));
          }
        }
        if constexpr (r == Q_AT) {
          asm volatile("" : "+v"(s2));
          QMM();
        }
        if constexpr (r == Q_AT + 1) QPUB();
      });
      TS(52);
    } else {
      const unsigned tw = lds0 + RNG + (wid >> 1) * 4096 + (g4 * 8 + ((lane >> 2) & 3)) * 128 + (wid & 1) * 64 + (lane & 3) * 8;
      f32x4 mb[2] = {};
      auto WR = [&](int r0, int r1) __attribute__((always_inline)) {
#pragma unroll 1
        for (int r = r0; r < r1; ++r) {
          WAIT_V(4);
          LBAR();
          if (wid == 2 && r < 22) TSD(r + 8);
          const char* p_ = pw + min(r + 3, 31) * 393216;
          GLDSP(lds0 + RNG + ((r + 3) & 3) * 16384 + wid * 2048, ow, p_, WPOL);
          GLDSP(lds0 + RNG + ((r + 3) & 3) * 16384 + wid * 2048 + 1024, ow, p_ + 98304, WPOL);
          const u32x4 sf = *reinterpret_cast<const u32x4*>(shm + SSI + (r * 32 + g4 * 8) * 2);
          mb[0] = MM(trd(tw + (r & 3) * 16384), sf, mb[0]);
          mb[1] = MM(trd(tw + (r & 3) * 16384 + 32), sf, mb[1]);
        }
      };
      WR(0, 1);
      PUB0();
      WR(1, 4);
      if (cb0 < 16 && tid == 0) ST4(reinterpret_cast<u64*>(ws + WS_SFL) + cb0 * 16, tag);
      WR(4, MQ_AT - 6);
      SFLD(SF_LD);
      WR(MQ_AT - 6, MQ_AT - 3);
      asm volatile("; AUDIT_OK %0 %1" : "+v"(sf), "+v"(hf));
      SAW();
      SALD();
      WR(MQ_AT - 3, MQ_AT);
      asm volatile("; AUDIT_OK %0" : "+v"(s2));
      QMM();
      WR(MQ_AT, 32);
      if (l16 < 2) *reinterpret_cast<f32x4*>(shm + GP + wid * 128 + l16 * 64 + g4 * 16) = l16 ? mb[1] : mb[0];
      LBAR();
      if (wid < 4) PUB(64 + xcd * 4 + wid, gp[tid]);
      QPUB();
      TS(52);
      WAIT_V(0);
      TSE();
      return;
    }
    bool early = false;
    if (tid == 0) {
      POLL { PDBG(it);
        unsigned g;
        if (WK_LD) asm volatile("global_load_dword %0, %1, %2 sc1\n\ts_waitcnt vmcnt(0)" : "=&v"(g) : "v"(0u), "s"(ws + WS_WAK + hid * 128) : "memory");
        else asm volatile("global_atomic_add %0, %1, %1, %2 sc0\n\ts_waitcnt vmcnt(0)" : "=&v"(g) : "v"(0u), "s"(ws + WS_WAK + hid * 128) : "memory");
        if (g != SENT) break;
        early = true;
        __builtin_amdgcn_s_sleep(OPT_KS);
      }
    }
    BARRIER();
    TS(21);
    const int tok = hid & 1, col = (hid & 16) * 32 + (lane & 2) * 128 + (hid & 14) * 16 + (lane & 1) * 16 + (lane >> 2);
    u32x4 c4, s4;
    unsigned gbu;
    POLL { PDBG(it);
      asm volatile("global_load_dwordx4 %0, %3, %5 sc1\n\tglobal_load_dwordx4 %1, %3, %6 sc1\n\tglobal_load_dword %2, %4, %7 sc1\n\ts_waitcnt vmcnt(0)"
                   : "=&v"(c4), "=&v"(s4), "=&v"(gbu) : "v"((tid & 255) * 16), "v"(col * 4), "s"(modv + 4096), "s"(modv + 3072), "s"(modv + 5120) : "memory");
      if (__all(max(max(max4(c4), max4(s4)), gbu) != SENT)) break;
      __builtin_amdgcn_s_sleep(8);
    }
    const f32x4 cb2 = __builtin_bit_cast(f32x4, c4), sb2 = __builtin_bit_cast(f32x4, s4);
    unsigned hra = lds0 + HS + g4 * 64 + (lane & 4) * 8 + (lane & 1) * 16, hwa = lds0 + HS + ((tid & 255) >> 2) * 64 + (wid >> 2) * 32 + (tid & 3) * 4;
    unsigned pto = (g4 & 1) * 4096 + ((g4 >> 1) * 8 + wid) * 256 + l16 * 16, six = (lane & 1) ? 0xEEEEu : 0x4444u;
    unsigned awa = lds0 + ACT + (wid >> 1) * 64 + g4 * 32 + (lane & 2) * 8 + (wid & 1) * 8 + (lane & 4) + (lane & 1) * 2;
    f32x4 d = {0.f, 0.f, 0.f, 0.f};
    asm volatile("" : "+v"(hra), "+v"(hwa), "+v"(pto), "+v"(awa), "+v"(six), "+v"(d));
    const float gb = __uint_as_float(gbu);
    if (wid == 0) {
      u32x2 g0 = {SENT, SENT}, g1 = {SENT, SENT};
      const char* gp = ws + WS_KEY + lane * 128;
      if (__any(early)) __builtin_amdgcn_s_sleep(OPT_KD);
      TS(20);
      POLL { PDBG(it);
        if (max2(g0) == SENT) asm volatile("global_load_dwordx2 %0, %1, off sc1" : "+v"(g0) : "v"(gp) : "memory");
        if (max2(g1) == SENT) asm volatile("global_load_dwordx2 %0, %1, off sc1" : "+v"(g1) : "v"(gp + 8192) : "memory");
        asm volatile("s_waitcnt vmcnt(0)" : "+v"(g0), "+v"(g1)::"memory");
        if (__all(max(max2(g0), max2(g1)) != SENT)) break;
        __builtin_amdgcn_s_sleep(1);
      }
      g0 = ~g0;
      g1 = ~g1;
      const unsigned a1 = max(g0[0], g1[0]), a2 = max(min(g0[0], g1[0]), max(g0[1], g1[1]));
      const unsigned k1 = wave_max_u(a1);
      const unsigned k2 = wave_max_u(a1 == k1 ? a2 : a1);
      if (lane == 0) *reinterpret_cast<u32x2*>(flg) = u32x2{k1, k2};
      TS(22);
    }
    LBAR();
    const unsigned k1 = flg[0], k2 = flg[1];
    {
      const int rg1 = key_row(k1) >> 4, rg2 = key_row(k2) >> 4;
      const int sl = wid >= 4 ? rg2 * 2 + (rg2 == rg1) : rg1 * 2;
      u32x2 v, hd;
      POLL { PDBG(it);
        asm volatile("global_load_dwordx2 %0, %2, %4 sc1\n\tglobal_load_dwordx2 %1, %3, %5 sc1\n\ts_waitcnt vmcnt(0)"
                     : "=&v"(v), "=&v"(hd) : "v"(sl * 2048 + (tid & 255) * 8), "v"(sl * 16), "s"(ws + WS_CAND), "s"(ws + WS_HDR) : "memory");
        if (__all(max(max2(v), max2(hd)) != SENT)) break;
        __builtin_amdgcn_s_sleep(1);
      }
      TS(23);
      const float mean = __uint_as_float(hd[0]), rstd = __uint_as_float(hd[1]);
      const f32x4 h = ((f32x4{bflo(v[0]), bfhi(v[0]), bflo(v[1]), bfhi(v[1])} - mean) * rstd) * cb2 + sb2;
      asm volatile("ds_write2_b32 %0, %1, %2 offset1:4" ::"v"(hwa), "v"(pack2(h[0], h[1])), "v"(pack2(h[2], h[3])) : "memory");
      *reinterpret_cast<u32x2*>(shm + SX1 + tid * 8) = v;
    }
    SKEWPT();
    TS(26);
    LBAR();
    TS(27);
    f32x4 dn[8];
    {
      u32x4 hb[8];
      sfor<23>([&](auto ff) __attribute__((always_inline)) {
        constexpr int f = decltype(ff)::value, c = f - 7;
        (void)d, (void)hb, (void)GU, (void)hra, (void)six;
        if constexpr (c >= 0) {
          WAIT_L(f <= 16 ? 6 : 22 - f);
          SMF(d, hb[c & 7], GU[c], six);
        }
        if constexpr (f < 16) DSR(hb[f & 7], hra, f * 256);
      });
      asm volatile("s_nop 7" : "+v"(d));
      TS(29);
      const float dv = rbf(d[0] + d[1]), uv = dpp_f<0x128>(dv);
      const unsigned av = f2bf(rbf(dv * __builtin_amdgcn_rcpf(1.0f + __expf(-dv))) * uv);
      asm volatile("s_mov_b64 exec, 0xff00ff\n\tds_write_b16 %0, %1\n\ts_mov_b64 exec, -1" ::"v"(awa), "v"(av) : "memory");
    }
#pragma unroll
    for (int c = 0; c < 8; ++c) asm volatile("v_mov_b64 %0, 0" : "=v"(dn[c].lo));
    asm volatile("" : W8(dn));
    TS(24);
    LBAR();
    {
      u32x4 a0;
      asm volatile("ds_read_b128 %0, %1 offset:%2\n\ts_waitcnt lgkmcnt(0)" : "=&v"(a0) : "v"(hra), "n"(ACT - HS) : "memory");
#pragma unroll
      for (int c = 0; c < 8; ++c) SMF(dn[(c >> 1) + (c & 1) * 4], a0, DN[(c >> 1) + (c & 1) * 4], six);
      asm volatile("s_nop 7" : W8(dn));
      u32x4 o;
#pragma unroll
      for (int i = 0; i < 4; ++i) o[i] = nsent((g4 & 2) ? dn[4 + i][0] + dn[4 + i][1] : dn[i][0] + dn[i][1]);
      ST16(pto, o, ws + WS_PART + ec * 8192, 0);
    }
    TS(25);
    SKEWPT();
    if (hid >= 32) {
      if (hi == 6 && wid == 1 && lane < 35) {
        char* fp = ws + (lane < 32 ? WS_XFL + (xcd * 32 + lane) * 128 : lane < 34 ? WS_SFL + (xcd * 2 + lane - 32) * 128 : WS_EPO);
        if (lane < 34 || xcd == 0) ST4(reinterpret_cast<u64*>(fp), lane < 34 ? ~tag : tag);
      }
      if (hid == 40 && tid < 320) ST4(reinterpret_cast<u64*>(ws + WS_HFL) + tid, ~tag);
      TSE();
      return;
    }
    const unsigned kk = tok ? k2 : k1;
    const int row = key_row(kk);
    float* rr = reinterpret_cast<float*>(shm + RR);
    float x1 = bf2f(*reinterpret_cast<const u16*>(shm + SX1 + tok * 2048 + col * 2));
    float h2 = bf2f(*reinterpret_cast<const u16*>(shm + HS + (col >> 4) * 64 + tok * 32 + (col & 2) * 8 + (col & 12) + (col & 1) * 2));
    float rsg = rbf(1.0f / (1.0f + __expf(-key_val(kk))));
    unsigned oo3 = (unsigned)(row * D + col) * 2, rra = lds0 + RR + lane * 32;
    asm volatile("" : "+v"(rsg), "+v"(x1), "+v"(h2), "+v"(oo3), "+v"(rra));
    if (wid == 7) {
      POLL { PDBG(it);
        unsigned g;
        asm volatile("global_load_dword %0, %1, %2 sc1\n\ts_waitcnt vmcnt(0)" : "=&v"(g) : "v"(((row >> 4) * 8 + (col >> 7)) * 8), "s"(ws + WS_DON) : "memory");
        if (__all(g != SENT)) break;
        __builtin_amdgcn_s_sleep(2);
      }
    } else {
      const unsigned po = (unsigned)((wid * 8 + g4) * 8192 + tok * 4096 + (hid >> 1) * 256 + l16 * 16);
      u32x4 v[2];
      if (OPT_RD) __builtin_amdgcn_s_sleep(OPT_RD);
      POLL { PDBG(it);
        asm volatile("global_load_dwordx4 %0, %2, %3 sc1\n\tglobal_load_dwordx4 %1, %2, %4 sc1\n\ts_waitcnt vmcnt(0)"
                     : "=&v"(v[0]), "=&v"(v[1])
                     : "v"(po), "s"(ws + WS_PART), "s"(ws + WS_PART + 32768)
                     : "memory");
        if (__all(max(max4(v[0]), max4(v[1])) != SENT)) break;
        __builtin_amdgcn_s_sleep(OPT_RR);
      }
      TS(28);
      const f32x4 t = __builtin_bit_cast(f32x4, v[0]) + __builtin_bit_cast(f32x4, v[1]);
      rr[(l16 * 4 + g4) * 8 + wid] = swap16_add(swap32_add(t[0], t[2]), swap32_add(t[1], t[3]));
    }
    LBAR();
    asm volatile(".Lcpf_e1:");
    TS(5);
    if (wid == 0) {
      f32x4 r0, r1;
      asm volatile("ds_read_b128 %0, %2\n\tds_read_b128 %1, %2 offset:16\n\ts_waitcnt lgkmcnt(0)" : "=&v"(r0), "=&v"(r1) : "v"(rra) : "memory");
      const float sum = ((r0[0] + r0[1]) + (r0[2] + r0[3])) + ((r1[0] + r1[1]) + r1[2]);
      asm volatile("global_store_short %0, %1, %2" ::"v"(oo3), "v"(f2bf(x1 + gb * rbf(h2 + rbf(rbf(sum) * rsg)))), "s"(out) : "memory");
    }
    TS(15);
    TSE();
  }
}

extern "C" void kernel_launch(void* const* d_in, const int* in_sizes, int n_in, void* d_out, int out_size, void* d_ws, size_t ws_size,
                              hipStream_t stream) {
  const u16* x = (const u16*)d_in[0];
  const u16* wsr = (const u16*)d_in[1];
  const u16* wsi = (const u16*)d_in[2];
  const u16* whr = (const u16*)d_in[3];
  const u16* whi = (const u16*)d_in[4];
  const u16* w_r = (const u16*)d_in[6];
  const u16* w_gate = (const u16*)d_in[7];
  const u16* w_up = (const u16*)d_in[8];
  const u16* w_down = (const u16*)d_in[9];
  const u16* c = (const u16*)d_in[10];
  const u16* w_mod = (const u16*)d_in[11];
  u16* out = (u16*)d_out;
  k_main<<<256, 512, 0, stream>>>(x, (char*)d_ws, wsr, wsi, c, w_mod, whr, whi, w_r, w_gate, w_up, w_down, out);
}
```

```cpp
#include <hip/hip_runtime.h>
#include <hip/hip_bf16.h>
#include <stdint.h>
#include <utility>

typedef unsigned short u16;
typedef unsigned long long u64;
using bf16x8 = __attribute__((ext_vector_type(8))) short;
using bf16v8 = __attribute__((ext_vector_type(8))) __bf16;
using f32x4 = __attribute__((ext_vector_type(4))) float;
using f32x2 = __attribute__((ext_vector_type(2))) float;
using u32x4 = __attribute__((ext_vector_type(4))) unsigned;
using u32x2 = __attribute__((ext_vector_type(2))) unsigned;

constexpr int S = 2048, D = 1024, F = 3584;

__device__ __forceinline__ float bf2f(unsigned v) { return __uint_as_float(v << 16); }
__device__ __forceinline__ float bflo(unsigned v) { return __uint_as_float(v << 16); }
__device__ __forceinline__ float bfhi(unsigned v) { return __uint_as_float(v & 0xffff0000u); }
__device__ __forceinline__ unsigned f2bf(float f) {
  __hip_bfloat16 b = __float2bfloat16(f);
  return (unsigned)*reinterpret_cast<u16*>(&b);
}
__device__ __forceinline__ float rbf(float f) { return bf2f(f2bf(f)); }
using bf16v2 = __attribute__((ext_vector_type(2))) __bf16;
__device__ __forceinline__ unsigned pack2(float lo, float hi) {
  return __builtin_bit_cast(unsigned, __builtin_convertvector(f32x2{lo, hi}, bf16v2));
}

template <int CTRL>
__device__ __forceinline__ float dpp_f(float v) {
  return __int_as_float(__builtin_amdgcn_mov_dpp(__float_as_int(v), CTRL, 0xf, 0xf, true));
}
template <int CTRL>
__device__ __forceinline__ float dpp_add(float v) { return v + dpp_f<CTRL>(v); }
__device__ __forceinline__ float row_sum(float v) { return dpp_add<0x140>(dpp_add<0x141>(dpp_add<0x4E>(dpp_add<0xB1>(v)))); }
template <int CTRL>
__device__ __forceinline__ unsigned dpp_u(unsigned v) { return (unsigned)__builtin_amdgcn_mov_dpp((int)v, CTRL, 0xf, 0xf, true); }
__device__ __forceinline__ unsigned wave_max_u(unsigned v) {
  v = max(v, dpp_u<0xB1>(v));
  v = max(v, dpp_u<0x4E>(v));
  v = max(v, dpp_u<0x141>(v));
  v = max(v, dpp_u<0x140>(v));
  const unsigned a = (unsigned)__builtin_amdgcn_readlane((int)v, 0), b = (unsigned)__builtin_amdgcn_readlane((int)v, 16);
  const unsigned c = (unsigned)__builtin_amdgcn_readlane((int)v, 32), d = (unsigned)__builtin_amdgcn_readlane((int)v, 48);
  return max(max(a, b), max(c, d));
}
__device__ __forceinline__ u64 mk64(unsigned hi, unsigned lo) { return ((u64)hi << 32) | lo; }
__device__ __forceinline__ float swap32_add(float a, float b) {
  auto r = __builtin_amdgcn_permlane32_swap(__float_as_uint(a), __float_as_uint(b), false, false);
  return __uint_as_float(r[0]) + __uint_as_float(r[1]);
}
__device__ __forceinline__ float swap16_add(float a, float b) {
  auto r = __builtin_amdgcn_permlane16_swap(__float_as_uint(a), __float_as_uint(b), false, false);
  return __uint_as_float(r[0]) + __uint_as_float(r[1]);
}
__device__ __forceinline__ float ar4(float v) {
  const float t = swap32_add(v, v);
  return swap16_add(t, t);
}
__device__ __forceinline__ float wave_sum_s(float v) {
  v = row_sum(v);
  v += __int_as_float(__builtin_amdgcn_update_dpp(0, __float_as_int(v), 0x142, 0xa, 0xf, false));
  v += __int_as_float(__builtin_amdgcn_update_dpp(0, __float_as_int(v), 0x143, 0xc, 0xf, false));
  return __int_as_float(__builtin_amdgcn_readlane(__float_as_int(v), 63));
}
__device__ __forceinline__ float silu_bf(float c) { return rbf(c * __builtin_amdgcn_rcpf(1.0f + __expf(-c))); }
__device__ __forceinline__ float dsum(unsigned v, float a) {
  return __builtin_amdgcn_fdot2_f32_bf16(__builtin_bit_cast(bf16v2, v), __builtin_bit_cast(bf16v2, 0x3f803f80u), a, false);
}
template <class Fn, int... I>
__device__ __forceinline__ void sfor_(Fn&& f, std::integer_sequence<int, I...>) { (f(std::integral_constant<int, I>{}), ...); }
template <int N, class Fn>
__device__ __forceinline__ void sfor(Fn&& f) { sfor_(f, std::make_integer_sequence<int, N>{}); }
constexpr unsigned SENT = 0xFFFFFFFFu;
__device__ __forceinline__ f32x2 unpk(unsigned v) { return f32x2{bflo(v), bfhi(v)}; }

constexpr size_t WS_EPO = 0;
constexpr size_t WS_XFL = 4096;
constexpr size_t WS_HFL = 40960;
constexpr size_t WS_XF2 = WS_HFL + 512;
constexpr size_t WS_SFL = WS_HFL + 8192;
constexpr size_t WS_MOD = 65536;
constexpr size_t WS_Q = WS_MOD + 32768;
constexpr size_t WS_ARM = WS_Q + 8192;
constexpr size_t WS_RS = WS_ARM;
constexpr size_t WS_KEY = WS_RS + 16384;
constexpr size_t WS_HDR = WS_KEY + 16384;
constexpr size_t WS_UFL = WS_HDR + 4096;
constexpr size_t WS_DON = WS_UFL + 98304;
constexpr size_t WS_STAT = WS_DON + 12288;
constexpr size_t WS_CAND = WS_STAT + 262144;
constexpr size_t WS_PART = WS_CAND + 524288;
constexpr size_t WS_XAF = WS_PART + 458752;
constexpr size_t WS_WAK = WS_XAF + 24576;
static_assert(WS_WAK + 8192 <= WS_ARM + 448 * 3328, "armed region");
constexpr size_t WS_XCH = 2u << 20;
constexpr size_t WS_H = 8u << 20;
constexpr size_t WS_WH = 12u << 20;
constexpr size_t WS_U = 16u << 20;
constexpr size_t WS_DBG = 64u << 20;
#define NEWDESIGN 1

#define PCAP (1 << 20)
#define PDBG(it)
#define TS(i)
#define TSCAP()
#define TSC0()
#define TSC1(i)
#define TSD(i)
#define TSE()
__device__ __forceinline__ void st16_wt(void* p, u32x4 v) {
  asm volatile("global_store_dwordx4 %0, %1, off sc1\n\ts_nop 1" ::"v"(p), "v"(v) : "memory");
}
#define ST16(voff, v, base, off) asm volatile("global_store_dwordx4 %0, %1, %2 offset:%3 sc1\n\ts_nop 1" ::"v"(voff), "v"(v), "s"(base), "n"(off) : "memory")
#define ST4(p, v) __hip_atomic_store(p, v, __ATOMIC_RELAXED, __HIP_MEMORY_SCOPE_AGENT)
#define WAIT_V(n) asm volatile("s_waitcnt vmcnt(%0)" ::"n"(n) : "memory")
#define WAIT_L(n) asm volatile("s_waitcnt lgkmcnt(%0)" ::"n"(n) : "memory")
#define BARRIER() asm volatile("s_barrier" ::: "memory")
#define LBAR() asm volatile("s_waitcnt lgkmcnt(0)\n\ts_barrier" ::: "memory")
#define DSR(dst, addr, off) asm volatile("ds_read_b128 %0, %1 offset:%2" : "=v"(dst) : "v"(addr), "n"(off) : "memory")
#define MFMA(c, a, b) asm volatile("v_mfma_f32_16x16x32_bf16 %0, %1, %2, %0" : "+v"(c) : "v"(a), "v"(b))
#define MFMAI(c, a, b) asm volatile("v_mfma_f32_16x16x32_bf16 %0, %1, %2, %0" ::"v"(c), "v"(a), "v"(b))
#define MFMA0(c, a, b) asm volatile("v_mfma_f32_16x16x32_bf16 %0, %1, %2, 0" : "=&v"(c) : "v"(a), "v"(b))
#define GLDSP(m0v, voff, gbase, pol) \
  asm volatile("s_mov_b32 m0, %0\n\ts_nop 0\n\tglobal_load_lds_dwordx4 %1, %2" pol ::"s"(m0v), "v"(voff), "s"(gbase) : "memory")
#define GLDS(m0v, voff, gbase) GLDSP(m0v, voff, gbase, "")
#define WPOL " nt"
#define LDX(dst, voff, base, off) asm volatile("global_load_dwordx4 %0, %1, %2 offset:%3" : "=v"(dst) : "v"(voff), "s"(base), "n"(off) : "memory")
#define LDXS(dst, voff, base, off) asm volatile("global_load_dwordx4 %0, %1, %2 offset:%3 sc1" : "=v"(dst) : "v"(voff), "s"(base), "n"(off) : "memory")
#define BF8(v) __builtin_bit_cast(bf16v8, v)
#define MM(a, b, c) __builtin_amdgcn_mfma_f32_16x16x32_bf16(BF8(a), BF8(b), c, 0, 0, 0)
#define LSPIN(v, p, cond)                                   \
  _Pragma("unroll 1") for (int it_ = 0; it_ < (1 << 26); ++it_) { \
    v = __atomic_load_n(p, __ATOMIC_RELAXED);               \
    if (cond) break;                                        \
    __builtin_amdgcn_s_sleep(1);                            \
  }
#define LSPIN0(v, p, cond)                                  \
  _Pragma("unroll 1") for (int it_ = 0; it_ < (1 << 28); ++it_) { \
    v = __atomic_load_n(p, __ATOMIC_RELAXED);               \
    if (cond) break;                                        \
  }
#define W6(a) "+v"(a[0]), "+v"(a[1]), "+v"(a[2]), "+v"(a[3]), "+v"(a[4]), "+v"(a[5])
#define W8(a) W6(a), "+v"(a[6]), "+v"(a[7])
#define PFL(ptr) asm volatile("s_mov_b32 m0, %0\n\ts_nop 0\n\tglobal_load_lds_dword %1, off" ::"s"(pfl), "v"(ptr) : "memory")
#define HOP(k, nx) asm volatile(".Lhop%0:\n\ts_cbranch_execz .Lhop%1" ::"n"(k), "n"(nx))
using s16x4 = __attribute__((ext_vector_type(4))) short;
__device__ __forceinline__ u32x4 trd(unsigned a) {
  const u32x2 lo = __builtin_bit_cast(u32x2, __builtin_amdgcn_ds_read_tr16_b64_v4i16((__attribute__((address_space(3))) s16x4*)(size_t)a));
  const u32x2 hi = __builtin_bit_cast(u32x2, __builtin_amdgcn_ds_read_tr16_b64_v4i16((__attribute__((address_space(3))) s16x4*)(size_t)(a + 512)));
  return u32x4{lo[0], lo[1], hi[0], hi[1]};
}
using u32x8 = __attribute__((ext_vector_type(8))) unsigned;
__device__ __forceinline__ u32x8 trd2(unsigned a) {
  const u32x4 lo = trd(a), hi = trd(a + 4096);
  return u32x8{lo[0], lo[1], lo[2], lo[3], hi[0], hi[1], hi[2], hi[3]};
}
#define SMF(c, a, b, i) asm volatile("v_smfmac_f32_16x16x64_bf16 %0, %1, %2, %3" ::"v"(c), "v"(a), "v"(b), "v"(i))
__device__ __forceinline__ int key_row(unsigned k) { return (int)((0xffffu - (k & 0xffffu)) & 2047u); }
__device__ __forceinline__ float key_val(unsigned k) {
  const unsigned ord = k >> 16;
  return bf2f(ord ^ ((ord & 0x8000u) ? 0x8000u : 0xffffu));
}
__device__ __forceinline__ unsigned nsent(float v) { return min(__float_as_uint(v), SENT - 1u); }
__device__ __forceinline__ f32x4 lo4(u32x4 q) { return f32x4{bflo(q[0]), bfhi(q[0]), bflo(q[1]), bfhi(q[1])}; }
__device__ __forceinline__ f32x4 hi4(u32x4 q) { return f32x4{bflo(q[2]), bfhi(q[2]), bflo(q[3]), bfhi(q[3])}; }
__device__ __forceinline__ u32x4 pk8(f32x4 a, f32x4 b) { return u32x4{pack2(a[0], a[1]), pack2(a[2], a[3]), pack2(b[0], b[1]), pack2(b[2], b[3])}; }
__device__ __forceinline__ u32x4 nsent4(u32x4 v) { return u32x4{min(v[0], SENT - 1u), min(v[1], SENT - 1u), min(v[2], SENT - 1u), min(v[3], SENT - 1u)}; }
__device__ __forceinline__ unsigned max4(u32x4 v) { return max(max(v[0], v[1]), max(v[2], v[3])); }
__device__ __forceinline__ unsigned max2(u32x2 v) { return max(v[0], v[1]); }
__device__ __forceinline__ unsigned hi2(u32x2 v) { return (v[0] >> 16) | (v[1] & 0xffff0000u); }
#define TOP2(CTRL)                                                  \
  do {                                                              \
    const unsigned o1 = dpp_u<CTRL>(t1), o2 = dpp_u<CTRL>(t2);      \
    t2 = max(min(t1, o1), max(t2, o2));                             \
    t1 = max(t1, o1);                                               \
  } while (0)
#define POLL _Pragma("unroll 1") for (int it = 0; it < PCAP; ++it)
#define OPT_RD 10
#define OPT_RR 2
#define OPT_SD 16
#define OPT_KS 2
#define P_D3 16
#define P_D4 2
#define U_D 28
#define XJ0 2
#define XJ1 5
#define XJ2 9
#define XJ3 10
#define TP_P0 309
#define TP_XL 340
#define CPF_K 24
#define CPF_N 10
#define HP4 0
#define HP5 1
#define HP6 2
#define HP7 3
#define CPF_B 10
#define NOEXP 0
#define NOARM 0
#define HST 2
#define XST 32
#define UST 32
#define OPT_KD 32
#define KORD 1
#define UF_AT 8
#define P_NT 5
#define P_D5 4
#define PF23 1
#define MQ_AT 31
#define MP_AT 16
#define P_AT 6
#define H_AT 24
#define W_NQ 0
#define XQ_AT 24
#define U_R 1
#define A_PR 4
#define WK_AT 1
#define ZD 1
#define U_BM 0xffd0u
static_assert(__builtin_popcount(U_BM) / 2 == 5, "barriers 34 .. 43 or 44");
#define A_LD 3
#define HOPS 0
#define XQ0 2
#define XQ1 4
#define XQ2 18
#define XQ3 20
#define ABL 0
#define F_AT 3
#define ST_E 0
#define PUB_ALL 0
#define XA_G 0
#define XA_D 12
#define PF_N 3
#define SF_1 SF_LD
#define TLBW 0
#define WK_LD 1
#define HX0 9
#define HXS 2
static_assert(HX0 + 11 * HXS < 44 && UF_AT <= 22, "");
#define U_M1 0xfu
#define X_AT 12
static_assert(A_LD <= 3, "five slots");
#define QM 3
#define PF_E PF_N
#define B46 339
#define OUT_D 20
#define HOPX 1
#define RT_D 0
#define FPOL ""
#define TP_X2 350
#define X_R 1
#define P_E 0
#define NORS 0
#define LSLP 0
#define Q_AT 9
#define B_VAT 42
constexpr int SL = 22528, NS = 5, XR = NS * SL, MISC = XR + 24576;
constexpr int as(int c) { return c < 3 ? 2 : c < 6 ? 3 : 2 + (c & 1); }
constexpr int ap(int c) { return c < 3 ? c : c < 6 ? c - 3 : c / 2; }
constexpr int ug(int j) { return j < 9 ? 18 + 7 * j : 82; }
constexpr int E0 = 264, E3 = E0 + 88, NE = E3 + 6;
constexpr int es(int c) { return c < 132 ? c / 11 : c < 144 ? 12 + (c - 132) / 6 : c < 156 ? 14 + (c & 1) : 12 + (c & 3); }
constexpr int ep(int c) { return c < 132 ? c % 11 : c < 144 ? (c - 132) % 6 : c < 156 ? (c - 144) / 2 : 6 + (c - 156) / 4; }

__global__ __launch_bounds__(512, 2) void k_main(const u16* __restrict__ x, char* ws, const u16* __restrict__ Y0, const u16* __restrict__ Y1,
                                              const u16* __restrict__ cvec, const u16* __restrict__ wmod, const u16* __restrict__ X0,
                                              const u16* __restrict__ X1, const u16* __restrict__ wr_, const u16* __restrict__ wg,
                                              const u16* __restrict__ wu, const u16* __restrict__ wd, u16* __restrict__ out) {
  constexpr int STG = 114688;
  __shared__ __attribute__((aligned(1024))) char shm[MISC + 16384];
  const int b = blockIdx.x, xcd = b & 7, j8 = b >> 3;
  const int cc = xcd >> 2, qx = xcd & 3, qs = xcd & QM;
  const int tid = threadIdx.x, lane = tid & 63;
  const int wid = __builtin_amdgcn_readfirstlane(tid >> 6);
  const unsigned lds0 = (unsigned)(size_t)(__attribute__((address_space(3))) char*)shm;
  const char* cpf;
  {
    unsigned long long pc;
    unsigned len;
    asm volatile("s_getpc_b64 %0\n.Lcpf_here:\n\ts_mov_b32 %1, .Lcpf_e1-.Lcpf_here\n\ts_max_u32 %1, %1, .Lcpf_e2-.Lcpf_here" : "=s"(pc), "=s"(len)::"scc");
    cpf = reinterpret_cast<const char*>(pc) + min((unsigned)((lane * 32 + j8) * 128), len);
  }
  const unsigned pfl = lds0 + MISC + 16128;
  if (wid == 4 && lane < CPF_N) PFL(cpf);
  u32x4 xv[2];
  {
    const unsigned xo = (unsigned)((b * 8 + wid) * D + lane * 8) * 2;
    asm volatile("global_load_dwordx4 %0, %2, %3\n\tglobal_load_dwordx4 %1, %2, %3 offset:1024" : "=&v"(xv[0]), "=&v"(xv[1]) : "v"(xo), "s"(x) : "memory");
  }
  asm volatile("" ::"s"(ws), "s"(Y0), "s"(Y1), "s"(X0), "s"(X1));
  unsigned* xfl = reinterpret_cast<unsigned*>(ws + WS_XFL);
  unsigned* hfl = reinterpret_cast<unsigned*>(ws + WS_HFL);
  unsigned* modv = reinterpret_cast<unsigned*>(ws + WS_MOD);
#define SKEWPT()
  const int g4 = lane >> 4, l16 = lane & 15;
  u64 tag0, tag;
  asm volatile("global_load_dwordx2 %0, %1, %2 sc1" : "=v"(tag0) : "v"(0u), "s"(ws) : "memory");
  const char* Yx = reinterpret_cast<const char*>(cc ? Y1 : Y0) + (size_t)qx * 2097152;
#define PFG(r16, g) (Yx + (size_t)(((r16) * 16 + l16) * 4096 + g4 * 128 + ((((g) + qs) & 7) << 9)))
  unsigned ta = lds0 + STG + ((lane >> 2) & 3) * 2368 + (wid * 128 + (lane >> 5) * 32 + (lane & 3) * 8 + (g4 & 1) * 4) * 2;
  unsigned oo = (unsigned)((wid * 4 + (lane >> 5)) * 131072 + (b >> 2) * 2048 + (g4 & 1) * 1024 + ((b & 3) * 16 + l16) * 16), oo2 = oo + 262144;
  unsigned wa = lds0 + STG + wid * 2368 + lane * 16;
  asm volatile("" : "+v"(ta), "+v"(oo), "+v"(oo2), "+v"(wa));
  const int hsl = (b & 32) + xcd * 4 + (j8 & 3);
  const char* hp = reinterpret_cast<const char*>((b & 32) ? X1 : X0) + (size_t)(hsl & 31) * 65536 + wid * 8192;
#define WHLD(i0)                                                                                                                  \
  _Pragma("unroll") for (int i = i0; i < i0 + 4; ++i)                                                                             \
      GLDS(lds0 + wid * 8192 + i * 1024, (unsigned)((lane ^ ((i >> 1) | ((wid & 2) << 1))) * 16), hp + i * 1024)
  const char* pf0 = PFG(j8, 7 - wid);
  if (wid >= 8 - PF_E) PFL(pf0);
  if (j8 >= 24) {
    WHLD(0);
    WAIT_V(5);
  } else if (wid >= 8 - PF_E) {
    WAIT_V(2);
  } else {
    WAIT_V(1);
  }
  {
    asm volatile("; AUDIT_OK %0 %1" : "+v"(xv[0]), "+v"(xv[1])::"memory");
    TS(32);
    const float s0 = dsum(xv[0][3], dsum(xv[0][2], dsum(xv[0][1], dsum(xv[0][0], 0.f))));
    const float s1 = dsum(xv[1][3], dsum(xv[1][2], dsum(xv[1][1], dsum(xv[1][0], 0.f))));
    const float mf = wave_sum_s(s0 + s1) * (1.0f / D), m = rbf(mf);
    const f32x4 d0 = lo4(xv[0]) - m, d1 = hi4(xv[0]) - m, d2 = lo4(xv[1]) - m, d3 = hi4(xv[1]) - m;
    const f32x4 q = (d0 * d0 + d1 * d1) + (d2 * d2 + d3 * d3);
    const float vf = wave_sum_s((q[0] + q[1]) + (q[2] + q[3])) * (1.0f / D) - (mf - m) * (mf - m);
    const float rden = __builtin_amdgcn_rcpf(rbf(__builtin_amdgcn_sqrtf(rbf(rbf(vf) + 1e-5f))));
    asm volatile("ds_write_b128 %0, %1" ::"v"(wa), "v"(pk8(d0 * rden, d1 * rden)) : "memory");
    asm volatile("ds_write_b128 %0, %1 offset:1024" ::"v"(wa), "v"(pk8(d2 * rden, d3 * rden)) : "memory");
    LBAR();
    TS(34);
    u32x2 t[4];
    asm volatile(
        "ds_read_b64_tr_b16 %0, %4\n\tds_read_b64_tr_b16 %1, %4 offset:%5\n\t"
        "ds_read_b64_tr_b16 %2, %4 offset:128\n\tds_read_b64_tr_b16 %3, %4 offset:%6\n\ts_waitcnt lgkmcnt(0)"
        : "=&v"(t[0]), "=&v"(t[1]), "=&v"(t[2]), "=&v"(t[3])
        : "v"(ta), "n"(4 * 2368), "n"(4 * 2368 + 128)
        : "memory");
    asm volatile("global_store_dwordx4 %0, %2, %4 sc1\n\tglobal_store_dwordx4 %1, %3, %4 sc1\n\ts_nop 1" ::"v"(oo), "v"(oo2),
                 "v"(u32x4{t[0][0], t[0][1], t[1][0], t[1][1]}), "v"(u32x4{t[2][0], t[2][1], t[3][0], t[3][1]}), "s"(ws + WS_H)
                 : "memory");
    if (PF_E < PF_N) PFL((wid >= 8 - PF_N && wid < 8 - PF_E) ? pf0 : reinterpret_cast<const char*>(x));
  }
#define PBAR(n)                                                                                                          \
  do {                                                                                                                   \
    asm volatile("s_waitcnt vmcnt(%2)\n\tv_lshl_add_u64 %0, %1, 0, 1" : "=v"(tag) : "v"(tag0), "n"((n) + (PF_E < PF_N)) : "memory"); \
    TS(36);                                                                                                              \
    BARRIER();                                                                                                           \
    TS(37);                                                                                                              \
  } while (0)
#define XFLAG() do { if ((tid | 1) == 385) ST4(reinterpret_cast<u64*>(ws + ((tid & 1) ? WS_XF2 + b * 8 : WS_XFL + b * 128)), tag); } while (0)
  if (j8 < 24) {
    const int mt = (j8 * 11) >> 5, nt = j8 - mt * 3, grp = xcd * 3 + nt;
#define FROW(f) (nt * 176 + (f) * 16 - (nt == 2 && (f) > 5 ? 16 : 0))
#define PROW(p) FROW(cc ? 10 - (p) : (p))
    constexpr int SCA = MISC, TGA = MISC + 512, TV = MISC + 1024, TSUM = MISC + 1792, FLG = MISC + 1856, SCW = MISC + 2048, RKA = MISC + 3072,
                  RKB = MISC + 5120, RKZ = MISC + 6656, IDT = MISC + 7168, ZSC = MISC + 9216;
    const bool zd = ZD && nt == 2;
    unsigned* ufl = reinterpret_cast<unsigned*>(ws + WS_UFL) + grp * 1024;
    const char* Ug = ws + WS_U + (size_t)grp * (16 * SL + 1024) + 1024;
    const unsigned perm = KORD ? (unsigned)mt | (((0x76543210u & ((1u << (4 * mt)) - 1u)) << 4) | (0x76543210u & (0xfffffff0u << (4 * mt))))
                               : __builtin_rotateright32(0x76543210u, 4 * mt);
#define KT(i) ((perm >> (4 * (i))) & 7u)
    const int kk = wid < 4 ? wid : wid - 2;
    const bool act = (wid < 4 || wid >= 6) && (kk || (cc == 0 && nt < 2));
    const int rg = qx * 32 + (PROW(5 + kk) >> 4);
    u32x4 pv[6];
    f32x4 acc[2][11], ga[2], x5[2];
    bf16x8 idt[2];
    unsigned spo = (unsigned)(((rg * 8 + g4 * 2) * 16 + l16) * 16);
    u32x4 sr[2], dk;
#pragma unroll
    for (int q = 0; q < 4; ++q) dk[q] = l16 == g4 * 4 + q ? ~0u : 0u;
    unsigned xw1 = lds0 + XR + tid * 16, xa = lds0 + XR + kk * 4096 + lane * 16, sto = (unsigned)(((rg * 8 + mt) * 16 + lane) * 16);
    unsigned saa = lds0 + SCW + (lane & 3) * 256 + g4 * 16;
    asm volatile("" : "+v"(dk), "+v"(xa), "+v"(sto), "+v"(xw1), "+v"(spo), "+v"(saa));
#define STLD() asm volatile("global_load_dwordx4 %0, %2, %3 sc1\n\tglobal_load_dwordx4 %1, %2, %3 offset:256 sc1" : "=&v"(sr[0]), "=&v"(sr[1]) : "v"(spo), "s"(ws + WS_STAT) : "memory")
    const char* xr = ws + WS_XCH + (size_t)(b * 4 + (wid & 3)) * 6144 + 3072;
#define XCHLD()                                                                                                       \
  asm volatile(                                                                                                       \
      "global_load_dwordx4 %0, %6, %7 offset:2048 sc1\n\tglobal_load_dwordx4 %1, %6, %7 offset:1024 sc1\n\t"          \
      "global_load_dwordx4 %2, %6, %7 sc1\n\tglobal_load_dwordx4 %3, %6, %7 offset:-1024 sc1\n\t"                     \
      "global_load_dwordx4 %4, %6, %7 offset:-2048 sc1\n\tglobal_load_dwordx4 %5, %6, %7 offset:-3072 sc1"            \
      : "=&v"(pv[0]), "=&v"(pv[1]), "=&v"(pv[2]), "=&v"(pv[3]), "=&v"(pv[4]), "=&v"(pv[5])                            \
      : "v"(lane * 16), "s"(xr)                                                                                       \
      : "memory")
    if (wid < 4) {
      bf16x8 X[4][2][2], Y[8];
      u32x4 xq[6];
      const unsigned xo0 = lane * 16 + 4096;

#define XLD(dst, XP, XH, i) LDXS(dst, xo0, XP, ((XH)&3) * 2048 + (i)*1024 - 4096)
      int xt = lane < 16 ? (int)WS_H + (mt * 4 + wid) * 131072 + (((qs * 2 + lane) & 15) << 13) : (int)WS_WH + (cc * 32 + mt * 4 + wid) * 65536 + (KT(lane & 7) << 13);
      asm volatile("" : "+v"(xt));
#define XT(j) (ws + (unsigned)__builtin_amdgcn_readlane(xt, j))
#define WP(h) XT(16 + ((h) >> 2))
#define XS(h) X[((h) >> 1) & 3][(h)&1]
      {
        unsigned ye = lds0 + l16 * 128 + (g4 ^ (lane & 7)) * 16, yo = ye ^ 64;
        const char* xpa = XT(0);
        const char* xpb = XT(1);
#pragma unroll
        for (int i = 0; i < 22; ++i) {
          acc[i / 11][i % 11] = f32x4{0.f, 0.f, 0.f, 0.f};
          asm volatile("" : "+v"(acc[i / 11][i % 11]));
        }
        const char* xw = ws + WS_XCH + (size_t)((b ^ 4) * 4 + wid) * 6144 + 3072;
        unsigned xra = lds0 + XR + l16 * 256 + (((wid * 4 + g4) ^ (lane & 7)) << 4), gaa = lds0 + TGA + wid * 128 + g4 * 32;
        unsigned rka = lds0 + (g4 ? RKZ : RKA + wid * 512 + l16 * 16), rkb = lds0 + RKB + l16 * 16;
        asm volatile("" : "+s"(xw), "+s"(xr), "+v"(xra), "+v"(gaa), "+v"(rka), "+v"(rkb));
        __builtin_amdgcn_s_setprio(1);
        PBAR(0);
        if (wid == 3) {
          const unsigned* fp = xfl + (qs * 32 + lane) * XST;
          if (P_D3) __builtin_amdgcn_s_sleep(P_D3);
          POLL { PDBG(it);
            u64 f;
            asm volatile("global_load_dwordx2 %0, %1, off sc1\n\ts_waitcnt vmcnt(0)" : "=&v"(f) : "v"(fp) : "memory");
            if (__all(f == tag)) break;
            __builtin_amdgcn_s_sleep(P_D4);
          }
        }
        TS(38);
        BARRIER();
        TS(39);
#pragma unroll
        for (int h = 0; h < 6; ++h) {
          XLD(XS(h)[0], h < 4 ? xpa : xpb, h, 0);
          XLD(XS(h)[1], h < 4 ? xpa : xpb, h, 1);
        }
#pragma unroll
        for (int j = 0; j < 6; ++j) DSR(Y[j], (j & 1) ? yo : ye, (j >> 1) * 2048);
        unsigned so = 0, a0 = ye, a1 = yo, yb[2], tq, u = 2;
#define STEP2(S0, XA, XB)                                                                                                      \
  sfor<44>([&](auto nn) __attribute__((always_inline)) {                                                                       \
    constexpr int n = (S0)*22 + decltype(nn)::value, q = n % 22, p = q / 2, s = n / 22, i = n & 1;                             \
    constexpr int k = q == XQ0 ? 0 : q == XQ1 ? 1 : q == XQ2 ? 2 : q == XQ3 ? 3 : -1;                                          \
    (void)acc, (void)X, (void)Y, (void)a0, (void)a1, (void)xo0, (void)XA, (void)XB, (void)so, (void)ye, (void)yo, (void)u, (void)xt, (void)tq, (void)ws; \
    if constexpr (q == 11) BARRIER();                                                                                          \
    if constexpr (q == 0) asm volatile("s_waitcnt vmcnt(8) lgkmcnt(4)" ::: "memory");                                          \
    else if constexpr (!i) WAIT_L(4);                                                                                          \
    MFMAI(acc[i][p], X[s][0][i], Y[n & 6]);                                                                                    \
    DSR(Y[(n + 6) & 7], i ? a1 : a0, ((p + 3) % 11) * 2048);                                                                   \
    MFMA(acc[i][p], X[s][1][i], Y[(n & 6) + 1]);                                                                               \
    if constexpr (k >= 0) XLD(X[(s + 3) & 3][k / 2][k & 1], (s & 1) ? XB : XA, 2 * s + 6 + k / 2, k & 1);                      \
    if constexpr (q == 0 && !(s & 1)) asm volatile("v_readlane_b32 %0, %2, %1\n\ts_add_u32 %1, %1, 1" : "=&s"(tq), "+s"(u) : "v"(xt) : "scc"); \
    if constexpr (q == 6) asm volatile("s_add_u32 %0, %0, %1" : "+s"(so) : "n"(SL) : "scc");                                   \
    if constexpr (q == 8) asm volatile("s_cmp_lg_u32 %0, %1\n\ts_cselect_b32 %0, %0, 0" : "+s"(so) : "n"(NS * SL) : "scc");    \
    if constexpr (q == 12 && !(s & 1)) { asm volatile("" : "+s"(tq)); XB = ws + tq; asm volatile("" : "+s"(XB)); }             \
    if constexpr (q == 14) asm volatile("v_add_u32 %0, %1, %2" : "=v"(a0) : "s"(so), "v"(ye));                                 \
    if constexpr (q == 16) asm volatile("v_add_u32 %0, %1, %2" : "=v"(a1) : "s"(so), "v"(yo));                                 \
  })
#define YSL(e) ((32 + es((e) / 2)) % NS)
#define YRD(e) DSR(Y[(e)&7], YSL(e) < 2 ? ye : yb[YSL(e) / 4], (YSL(e) & 1) * SL + ep((e) / 2) * 2048 + ((e)&1) * 1024)

        u32x4 ov;
        bf16x8 qf[2];
        auto OPS = [&](auto kc) __attribute__((always_inline)) {
          constexpr int k = decltype(kc)::value, q = k / 10, r = k % 10, c = k - 60;
          (void)ov, (void)acc, (void)xw, (void)lane, (void)xq, (void)x5;
          if constexpr (k < 60) {
            if constexpr (r < 4) asm volatile("v_cvt_pk_bf16_f32 %0, %1, %2" : "=v"(ov[r]) : "v"(acc[r / 2][q][(r & 1) * 2]), "v"(acc[r / 2][q][(r & 1) * 2 + 1]));
            else if constexpr (r < 8) asm volatile("v_min_u32 %0, -2, %0" : "+v"(ov[r - 4]));
            else if constexpr (r == 9) asm volatile("global_store_dwordx4 %0, %1, %2 offset:%3 sc1" ::"v"(lane * 16), "v"(ov), "s"(xw), "n"(q * 1024 - 3072) : "memory");
          } else if constexpr (c >= 40 && c < 48) {
            if constexpr (c & 1) asm volatile("v_and_b32 %0, 0xffff0000, %1" : "=v"(x5[(c >> 2) & 1][c & 3]) : "v"(xq[5][(c >> 1) & 3]));
            else asm volatile("v_lshlrev_b32 %0, 16, %1" : "=v"(x5[(c >> 2) & 1][c & 3]) : "v"(xq[5][(c >> 1) & 3]));
          } else if constexpr (c < 40) {
            if constexpr (c & 1) asm volatile("v_and_b32 %0, 0xffff0000, %1" : "=v"(acc[(c >> 2) & 1][c >> 3][c & 3]) : "v"(xq[c >> 3][(c >> 1) & 3]));
            else asm volatile("v_lshlrev_b32 %0, 16, %1" : "=v"(acc[(c >> 2) & 1][c >> 3][c & 3]) : "v"(xq[c >> 3][(c >> 1) & 3]));
          }
        };
#define IC(v) std::integral_constant<int, (v)>{}
#define FILL(g)                                                                              \
  do {                                                                                       \
    if constexpr (m >= 0) {                                                                  \
      OPS(IC(m / 2 * 5 + (m & 1) * 3 + (g)));                                                \
      if constexpr ((g) && !(m & 1)) OPS(IC(m / 2 * 5 + 2));                                 \
    }                                                                                        \
  } while (0)
        auto BP = [&](auto nn) __attribute__((always_inline)) {
          constexpr int n = decltype(nn)::value, s = es(n / 2), p = n < E3 ? ep(n / 2) : 5 + n - E3, i = n & 1, e = n + 6, m = n - E0 - 30;
          (void)ga, (void)qf, (void)idt, (void)rka, (void)rkb, (void)gaa, (void)xq, (void)ov, (void)xw, (void)x, (void)pv, (void)xr, (void)ufl;
          (void)acc, (void)X, (void)Y, (void)yb, (void)ye, (void)xra, (void)xo0, (void)xt, (void)ws, (void)lane, (void)mt, (void)nt, (void)cc, (void)lds0, (void)wid, (void)g4, (void)perm;
          (void)OPS;
          TSCAP();
          if constexpr (n == E0 + 55) TS(10);
          if constexpr (n == UF_AT) {
            TS(45);
            WAIT_V(n > HX0 ? (n - HX0 + HXS - 1) / HXS : 0);
            if (lane == 0) ST4(ufl + (mt * 4 + wid) * UST, 1u);
            TS(46);
          }
          if constexpr (n == 37) TS(48);
          if constexpr (n == 37 || n == E0 + 5 || n == E0 + 17 || n == B46) BARRIER();
          if constexpr (n == 37) TS(4);
          if constexpr (n == E0 + 25) { TS(3); SKEWPT(); }
          if constexpr (p == 0 && !i && n != 22) WAIT_V(n == 0 ? 15 : s < 14 ? 8 : 4 * (15 - s));
          if constexpr (!i) WAIT_L(e < NE ? 4 : NE - 2 - n);
          if constexpr (n < 22) MFMA0(acc[i][p], X[0][0][i], Y[n & 6]);
          else if constexpr (n < E3) MFMAI(acc[i][p], X[s & 3][0][i], Y[n & 6]);
          else MFMA(acc[0][p], qf[0], Y[n & 7]);
          if constexpr (e < E3) YRD(e);
          else if constexpr (e < NE) DSR(Y[e & 7], rkb, (e - E3) * 256);
          FILL(0);
          if constexpr (n < E3 && (s == 1 || s == 15)) MFMA(acc[i][p], X[s & 3][1][i], Y[(n & 6) + 1]);
          else if constexpr (n < E3) MFMAI(acc[i][p], X[s & 3][1][i], Y[(n & 6) + 1]);
          else MFMA(acc[1][p], qf[1], Y[n & 7]);
          if constexpr (n >= HX0 && n < HX0 + 12 * HXS && (n - HX0) % HXS == 0) {
            constexpr int k = (n - HX0) / HXS;
            static_assert(k < 8 || n >= 22, "slot still in use");
            XLD(XS(4 + k / 2)[k & 1], WP(4 + k / 2), k / 2, k & 1);
          }
          if constexpr (n >= 272 && n < 294 && n % 4 == 0) DSR(xq[(n - 272) / 4], xra, (n - 272) / 4 * 4096);
          if constexpr (n >= E0 && n < E0 + 4) XLD(X[3][(n - E0) / 2][i], WP(31), 30 + (n - E0) / 2, i);
          FILL(1);
          if constexpr (n == B46 || n == B46 + 1) DSR(qf[n - B46], rka, (n - B46) * 256);
          if constexpr (n == B46 + 2 || n == B46 + 3) DSR(ga[n - B46 - 2], gaa, (n - B46 - 2) * 16);
          if constexpr (n == B46 + 4 || n == B46 + 5) DSR(idt[n - B46 - 4], yb[1], IDT - 4 * SL + (n - B46 - 4) * 1024);
          if constexpr (n >= TP_XL && n < TP_XL + 4) LDXS(pv[TP_XL + 5 - n], lane * 16, xr, (n - TP_XL) * 1024 - 3072);
          if constexpr (n == TP_X2 || n == TP_X2 + 1) LDXS(pv[TP_X2 + 1 - n], lane * 16, xr, (n - TP_X2) * 1024 + 1024);
        };
        do {
          STEP2(0, xpb, xpa);
          if (__builtin_expect(u == 17, 0)) {
            TS(57);
            TSC1(40);
            TSC0();
            f32x4 c0, c1;
            const char* up = Ug + (mt * 2 + (wid >> 1)) * SL + (wid & 1) * 1024 + 4096;
            const unsigned ul = lds0 + (2 + (wid >> 1)) * SL + (wid & 1) * 1024 + lane * 16;
            u32x4 uv;
            DSR(c0, lds0 + SCA + wid * 128 + g4 * 32, 0);
            DSR(c1, lds0 + SCA + wid * 128 + g4 * 32, 16);
#define UMUL(j, k) asm volatile("v_mul_f32 %0, %0, %1" : "+v"(acc[(k) >> 2][j][(k)&3]) : "v"(((k) >> 2 ? c1 : c0)[(k)&3]))
#define UCVT(j, k) asm volatile("v_cvt_pk_bf16_f32 %0, %1, %2" : "=v"(uv[k]) : "v"(acc[(k) >> 1][j][((k)&1) * 2]), "v"(acc[(k) >> 1][j][((k)&1) * 2 + 1]))
            auto UEP = [&](auto jc, auto rc) __attribute__((always_inline)) {
              constexpr int j = decltype(jc)::value, r = decltype(rc)::value;
              (void)acc, (void)c0, (void)c1, (void)uv, (void)lane, (void)ul, (void)up;
              if constexpr (r < 4) { UMUL(j, 2 * r); UMUL(j, 2 * r + 1); }
              else if constexpr (r < 6) { UCVT(j, 2 * r - 8); UCVT(j, 2 * r - 7); }
              else asm volatile("global_store_dwordx4 %0, %2, %3 offset:%4 sc1\n\tds_write_b128 %1, %2 offset:%5"
                                ::"v"(lane * 16), "v"(ul), "v"(uv), "s"(up + (j >> 2) * 8192), "n"((j & 3) * 2048 - 4096), "n"(j * 2048) : "memory");
            };
#define UGAP(G) do { constexpr int j_ = (G) < 82 ? ((G)-18) / 7 : 9; if constexpr ((G) >= 18 && (G) != 81) UEP(IC(j_), IC((G)-ug(j_))); } while (0)
            sfor<44>([&](auto nn) __attribute__((always_inline)) {
              constexpr int n = decltype(nn)::value, s = as(n / 2), p = ap(n / 2), i = n & 1, e = n + 6;
              (void)acc, (void)X, (void)Y, (void)ye, (void)yo, (void)xo0, (void)xpa, (void)UEP;
              if constexpr (n == 0 || n == 6) WAIT_V(8);
              if constexpr (!i) WAIT_L(e < 44 ? 4 : 42 - n);
              MFMAI(acc[i][p], X[s][0][i], Y[(n + 4) & 6]);
              if constexpr (e < 44) DSR(Y[(e + 4) & 7], i ? yo : ye, (as(e / 2) & 1) * SL + ap(e / 2) * 2048);
              UGAP(2 * n);
              if constexpr (s == 2) MFMAI(acc[i][p], X[s][1][i], Y[((n + 4) & 6) + 1]);
              else MFMA(acc[i][p], X[s][1][i], Y[((n + 4) & 6) + 1]);
              if constexpr (n < 4) XLD(X[1][n / 2][i], xpa, 2 + n / 2, i);
              UGAP(2 * n + 1);
            });
            TSC1(41);
            TS(0);
            SKEWPT();
            UEP(IC(9), IC(6));
            asm volatile("s_nop 1");
            sfor<7>([&](auto rc) __attribute__((always_inline)) { UEP(IC(10), rc); });
            ye = lds0 + lane * 16;
            yo = ye + 1024;
            so = 4 * SL;
            a0 = ye + 4 * SL;
            a1 = yo + 4 * SL;
            yb[0] = ye + 2 * SL;
            yb[1] = a0;
            asm volatile("" : "+v"(yb[0]), "+v"(yb[1]), "+v"(ye), "+v"(yo));
            LBAR();
            TS(44);
            sfor<6>([&](auto ee) __attribute__((always_inline)) { (void)Y, (void)yb, (void)ye; YRD(decltype(ee)::value); });
            sfor<44>(BP);
            u = 19;
            xpa = WP(8);
          }
          STEP2(2, xpa, xpb);
        } while (u != 24);
        TS(2);
        sfor<NE - 264>([&](auto nn) __attribute__((always_inline)) { BP(std::integral_constant<int, 264 + decltype(nn)::value>{}); });
        __builtin_amdgcn_s_setprio(0);
        TSC1(59);
        TS(13);
        SKEWPT();
      }
    } else if (wid < 6) {
      const int hw = wid - 4;
      __builtin_amdgcn_s_setprio(2);
      unsigned voy[11];
#pragma unroll
      for (int i = 0; i < 11; ++i) voy[i] = (unsigned)((PROW(i) + hw * 8 + (lane >> 3)) * 4096 + ((lane & 7) ^ (lane >> 3)) * 16);
      const unsigned lbase = lds0 + hw * 1024;
      if (zd) voy[5] = (lane & 7) * 16;
      auto stage = [&](int n, int slot) {
        const char* yp = Yx + (((n + qs * 4) & 31) << 7);
        const unsigned l = lbase + slot * SL;
#pragma unroll
        for (int i = 0; i < 11; ++i) GLDS(i == 5 && zd ? lbase + ZSC : l + i * 2048, voy[i], yp);
      };
      asm volatile("s_waitcnt vmcnt(%3)" ::"v"(voy[0]), "v"(voy[10]), "v"(tag0), "n"(PF_E < PF_N ? 1 : 0) : "memory");
      BARRIER();
#pragma unroll 1
      for (int s = 0; s <= A_LD; ++s) stage(s, s);
      WAIT_V(11 * A_LD);
      TS(38);
      BARRIER();
      int slot = A_LD + 1;
#pragma unroll 1
      for (int k = 1; k <= 31 - A_LD; ++k) {
        WAIT_V(11 * (A_LD - 1));
        BARRIER();
        if (hw == 1) TSD(k);
        if (LSLP) __builtin_amdgcn_s_sleep(LSLP);
        stage(k + A_LD, slot);
        slot = slot == NS - 1 ? 0 : slot + 1;
        if (k == CPF_K && hw == 1 && lane < CPF_N) PFL(cpf);
      }
      if (A_LD > 3) { WAIT_V(33); BARRIER(); }
      if (A_LD > 2) { WAIT_V(22); BARRIER(); }
      WAIT_V(0); BARRIER();
      const unsigned* ufp = ufl + (KT(min(lane >> 2, 6) + 1) * 4 + (lane & 3)) * UST;
      unsigned miss = ~0u;
      const char* y1 = Ug + KT(1) * 2 * SL + hw * 11264;
      asm volatile("" : "+s"(y1), "+v"(ufp));
      BARRIER();
      TS(47);
      if (U_D) __builtin_amdgcn_s_sleep(U_D);
      auto UPOLL = [&](unsigned need, unsigned em0) __attribute__((always_inline)) {
        POLL { PDBG(it);
          if ((miss & need) == 0) break;
          if (it) __builtin_amdgcn_s_sleep(U_R);
          unsigned f;
          const u64 em = miss & em0;
          asm volatile("s_mov_b64 exec, %2\n\tglobal_load_dword %0, %1, off sc1\n\ts_mov_b64 exec, -1\n\ts_waitcnt vmcnt(0)" : "=&v"(f) : "v"(ufp), "s"(em) : "memory");
          miss &= ~(unsigned)em | (unsigned)__builtin_amdgcn_ballot_w64(f == SENT);
        }
      };
#define G1(o) "global_load_lds_dwordx4 %0, %1 offset:" #o " sc1\n\t"
#define ULD(yp, l)                                                                                                                          \
  do {                                                                                                                                      \
    asm volatile("s_mov_b32 m0, %2\n\ts_nop 0\n\t" G1(0) G1(1024) G1(2048) G1(3072)::"v"(lane * 16), "s"(yp), "s"(l) : "memory");            \
    asm volatile("s_mov_b32 m0, %2\n\ts_nop 0\n\t" G1(0) G1(1024) G1(2048) G1(3072)::"v"(lane * 16 + 4096), "s"(yp), "s"((l) + 4096) : "memory"); \
    asm volatile("s_mov_b32 m0, %2\n\ts_nop 0\n\t" G1(0) G1(1024) G1(2048)::"v"(lane * 16 + 8192), "s"(yp), "s"((l) + 8192) : "memory");     \
  } while (0)
      {
        UPOLL(3u, 15u);
        TS(48);
        ULD(y1, lds0 + 4 * SL + hw * 11264);
        UPOLL(15u, 15u);
        unsigned f;
        asm volatile("global_load_dword %0, %1, off sc1" : "=v"(f) : "v"(ufp) : "memory");
        ULD(y1 + SL, lds0 + hw * 11264);
        asm volatile("s_waitcnt vmcnt(11)" : "+v"(f)::"memory");
        BARRIER();
        miss = (unsigned)__builtin_amdgcn_ballot_w64(f == SENT);
      }
      slot = 1;
#pragma unroll 1
      for (int n = 4; n < 16; ++n) {
        UPOLL(3u << (2 * n - 4), ~0u);
        const char* yp = Ug + (KT(n >> 1) * 2 + (n & 1)) * SL + hw * 11264;
        unsigned l = lds0 + slot * SL + hw * 11264;
        slot = slot == NS - 1 ? 0 : slot + 1;
        if (n == CPF_B && hw == 1 && lane < CPF_N) PFL(cpf);
        asm volatile("" : "+s"(yp), "+s"(l));
        if (n >= 6) {
          WAIT_V(22);
          BARRIER();
          if (hw == 1) TSD(n + 44);
        }
        ULD(yp, l);
      }
      WAIT_V(22);
      BARRIER();
      WAIT_V(0);
      BARRIER();
      BARRIER();
      if (WK_AT == 0 && b < 56 && tid == 256) ST4(reinterpret_cast<unsigned*>(ws + WS_WAK) + b * 32, 1u);
      BARRIER();
      if (WK_AT == 1 && b < 56 && tid == 256) ST4(reinterpret_cast<unsigned*>(ws + WS_WAK) + b * 32, 1u);
      TSE();
      return;
    } else {
      const int aw = wid - 6, rp = min(mt * 2 + aw, 10);
      PBAR(0);
      XFLAG();
      if (zd) {
#pragma unroll
        for (int s = 0; s < NS; ++s) *reinterpret_cast<u32x4*>(shm + s * SL + 10240 + aw * 1024 + lane * 16) = u32x4{0u, 0u, 0u, 0u};
      }
      LBAR();
      int wq = nt * 2 + aw;
#define WPF()                                                                                                                   \
  do {                                                                                                                          \
    if (wq < W_NQ) PFL(ws + WS_WH + (size_t)(cc * 32 + mt * 4 + (wq & 3)) * 65536 + (KT(wq >> 2) << 13) + lane * 128);          \
    wq += 6;                                                                                                                    \
  } while (0)
      float rs[4] = {};
      const unsigned ra = lds0 + rp * 2048 + lane * 16;
      unsigned so = 0;
#define RSUM()                                                                                                                        \
  do {                                                                                                                                \
    u32x4 R_[2];                                                                                                                      \
    if (NORS) break;                                                                                                                  \
    asm volatile("ds_read_b128 %0, %2\n\tds_read_b128 %1, %2 offset:1024\n\ts_waitcnt lgkmcnt(0)" : "=&v"(R_[0]), "=&v"(R_[1]) : "v"(ra + so) : "memory"); \
    _Pragma("unroll") for (int j = 0; j < 8; ++j) {                                                                                   \
      asm volatile("v_add_f32 %0, %0, %1" : "+v"(rs[(j >> 2) * 2]) : "v"(R_[j >> 2][j & 3] << 16));                                   \
      asm volatile("v_add_f32 %0, %0, %1" : "+v"(rs[(j >> 2) * 2 + 1]) : "v"(R_[j >> 2][j & 3] & 0xffff0000u));                       \
    }                                                                                                                                 \
    so = so == (NS - 1) * SL ? 0 : so + SL;                                                                                           \
  } while (0)
      {
#pragma unroll 1
        for (int bi = 0; bi < F_AT; ++bi) {
          RSUM();
          BARRIER();
        }
        u32x4 f;
#define FLD() asm volatile("global_load_dwordx4 %0, %1, %2 sc1" : "=v"(f) : "v"(lane * 16), "s"(ws + WS_XF2 + aw * 1024) : "memory")
        FLD();
#pragma unroll 1
        for (int bi = F_AT; bi < 4; ++bi) {
          RSUM();
          BARRIER();
        }
        POLL { PDBG(it);
          asm volatile("s_waitcnt vmcnt(0)" : "+v"(f)::"memory");
          if (__all(mk64(f[1], f[0]) == tag && mk64(f[3], f[2]) == tag)) break;
          __builtin_amdgcn_s_sleep(1);
          FLD();
        }
        asm volatile("s_waitcnt vmcnt(0)" : "+v"(f)::"memory");
      }
      const unsigned xo = (unsigned)(((qx * 512 + aw * 4 + g4) * D + mt * 128) * 2 + ((l16 ^ (aw * 4 + g4)) << 4));
#pragma unroll 1
      for (int bi = 4; bi < H_AT; ++bi) {
        RSUM();
        const int i = bi - X_AT;
        if (i >= 0 && i < 12) GLDS(lds0 + XR + i * 2048 + aw * 1024, xo, x + (PROW(5 + i / 2) + (i & 1) * 8) * D);
        BARRIER();
      }
      {
        u64 f;
        const unsigned* fp = hfl + lane * HST;
        asm volatile("global_load_dwordx2 %0, %1, off sc1" : "=v"(f) : "v"(fp) : "memory");
        RSUM();
        BARRIER();
        RSUM();
        BARRIER();
        POLL { PDBG(it);
          asm volatile("s_waitcnt vmcnt(0)" : "+v"(f)::"memory");
          if (__all(f == tag)) break;
          __builtin_amdgcn_s_sleep(1);
          asm volatile("global_load_dwordx2 %0, %1, off sc1" : "=v"(f) : "v"(fp) : "memory");
        }
        asm volatile("s_waitcnt vmcnt(0)" : "+v"(f)::"memory");
        unsigned c;
        const unsigned* cp = modv + 1024 + mt * 128 + aw * 64 + lane;
        asm volatile("global_load_dword %0, %1, off sc1" : "=v"(c) : "v"(cp) : "memory");
        if (TLBW && aw == 0 && lane < 3) PFL(lane == 0 ? Ug - 128 : lane == 1 ? Ug + 16 * SL : ws + WS_WH + (size_t)(cc * 32 + mt * 4) * 65536);
        WPF();
        RSUM();
        BARRIER();
        WPF();
        RSUM();
        BARRIER();
        POLL { PDBG(it);
          asm volatile("s_waitcnt vmcnt(0)" : "+v"(c)::"memory");
          if (__all(c != SENT)) break;
          __builtin_amdgcn_s_sleep(1);
          asm volatile("global_load_dword %0, %1, off sc1" : "=v"(c) : "v"(cp) : "memory");
        }
        asm volatile("s_waitcnt vmcnt(0)" : "+v"(c)::"memory");
        *reinterpret_cast<unsigned*>(shm + SCA + (aw * 64 + lane) * 4) = c ^ (cc ? 0x80000000u : 0u);
      }
#pragma unroll 1
      for (int bi = H_AT + 4; bi < 32; ++bi) {
        WPF();
        RSUM();
        if (bi < 30) BARRIER();
      }
      unsigned* rsb = reinterpret_cast<unsigned*>(ws + WS_RS) + qx * 512;
      {
        const float v0 = dpp_add<0x141>(dpp_add<0x4E>(dpp_add<0xB1>(rs[0] + rs[1])));
        const float v1 = dpp_add<0x141>(dpp_add<0x4E>(dpp_add<0xB1>(rs[2] + rs[3])));
        if ((lane & 7) < 2 && mt * 2 + aw < 11 && !(nt == 2 && rp == 5)) ST4(rsb + cc * S + PROW(rp) + (lane & 1) * 8 + (lane >> 3), nsent((lane & 1) ? v1 : v0));
      }
      BARRIER();
#pragma unroll 1
      for (int k = 34; k <= (aw ? B_VAT : 46); ++k) BARRIER();
      u32x2 m[6];
      const unsigned lr = min(lane, 47);
      const unsigned o0 = aw ? (unsigned)(WS_Q - WS_MOD) + mt * 512 + lane * 8 : 8192u + mt * 512 + lane * 8;
      const unsigned o1 = aw ? (unsigned)(WS_RS - WS_MOD) + (qx * 512 + PROW(5 + (lr >> 3)) + (lr & 7) * 2) * 4 : o0 + 8192;
      const unsigned o2 = aw ? o1 + 8192 : o0 + 16384, o3 = aw ? (XA_G ? (unsigned)(WS_XAF - WS_MOD) + (b ^ 4) * 128 : o1) : 28672u + min(lane, 23) * 8;
#define VLD()                                                                                                                     \
  asm volatile("global_load_dwordx2 %0, %6, %10 sc1\n\tglobal_load_dwordx2 %1, %6, %11 sc1\n\tglobal_load_dwordx2 %2, %7, %10 sc1\n\t" \
               "global_load_dwordx2 %3, %7, %11 sc1\n\tglobal_load_dwordx2 %4, %8, %10 sc1\n\tglobal_load_dwordx2 %5, %9, %10 sc1"    \
               : "=&v"(m[0]), "=&v"(m[1]), "=&v"(m[2]), "=&v"(m[3]), "=&v"(m[4]), "=&v"(m[5])                                     \
               : "v"(o0), "v"(o1), "v"(o2), "v"(o3), "s"(modv), "s"(modv + 1024)                                                  \
               : "memory")
      VLD();
      if (aw) {
#pragma unroll
        for (int k = B_VAT + 1; k <= 45; ++k) BARRIER();
      }
      TS(30);
      POLL { PDBG(it);
        asm volatile("s_waitcnt vmcnt(0)" : W6(m)::"memory");
        if (__all(max(max(max(max2(m[0]), max2(m[1])), max(max2(m[2]), aw ? 0u : max2(m[3]))), max(max2(m[4]), max2(m[5]))) != SENT)) break;
        __builtin_amdgcn_s_sleep(2);
        VLD();
      }
      asm volatile("s_waitcnt vmcnt(0)" : W6(m)::"memory");
      if (aw == 0) {
        *reinterpret_cast<u32x2*>(shm + TGA + lane * 8) = m[0];
        *reinterpret_cast<unsigned*>(shm + TV + lane * 4) = hi2(m[2]);
        *reinterpret_cast<unsigned*>(shm + TV + 256 + lane * 4) = hi2(m[1]);
        *reinterpret_cast<unsigned*>(shm + TV + 512 + lane * 4) = hi2(m[3]);
        const float p0 = __uint_as_float(m[4][0]), p1 = __uint_as_float(m[4][1]);
        const unsigned hh = pack2(p0, p1);
        *reinterpret_cast<unsigned*>(shm + SCW + lane * 4) = hh;
        *reinterpret_cast<unsigned*>(shm + SCW + 256 + lane * 4) = pack2(p0 - bflo(hh), p1 - bfhi(hh));
        *reinterpret_cast<u32x2*>(shm + SCW + 512 + lane * 8) = u32x2{0x3f803f80u, 0x3f803f80u};
        const float u = __uint_as_float(m[5][0]) + __uint_as_float(m[5][1]), u2 = dpp_add<0xB1>(u), u8 = dpp_add<0x141>(dpp_add<0x4E>(u2));
        if (lane < 17 && !(lane & 1)) *reinterpret_cast<float*>(shm + TSUM + lane * 2) = lane < 16 ? u2 : u8;
      } else {
        BARRIER();
#pragma unroll
        for (int j = 0; j < 2; ++j) {
          const float r0 = __uint_as_float(m[0][j]), r1 = __uint_as_float(m[1][j]);
          const unsigned a0 = pack2(r0, r0), a1 = pack2(r1, r1);
          *reinterpret_cast<u32x4*>(shm + RKA + ((lane >> 4) * 32 + ((lane >> 1) & 1) * 16 + ((lane >> 2) & 3) * 4 + (lane & 1) * 2 + j) * 16) =
              u32x4{a0, pack2(r0 - bflo(a0), r0 - bflo(a0)), a1, pack2(r1 - bflo(a1), r1 - bflo(a1))};
          const float s0 = __uint_as_float(m[2][j]), s1 = -__uint_as_float(m[4][j]);
          const unsigned b0 = pack2(s0, s0 - rbf(s0)), b1 = pack2(s1, s1 - rbf(s1));
          *reinterpret_cast<u32x4*>(shm + RKB + (lr * 2 + j) * 16) = u32x4{b0, b0, b1, b1};
        }
        *reinterpret_cast<u32x2*>(shm + RKZ + lane * 8) = u32x2{0u, 0u};
        const unsigned one = g4 == (l16 >> 2) ? ((lane & 1) ? 0x3f800000u : 0x3f80u) : 0u, e0 = (lane & 2) ? 0u : one, e1 = (lane & 2) ? one : 0u;
        *reinterpret_cast<u32x4*>(shm + IDT + lane * 16) = u32x4{e0, e1, 0u, 0u};
        *reinterpret_cast<u32x4*>(shm + IDT + 1024 + lane * 16) = u32x4{0u, 0u, e0, e1};
      }
      TS(17);
      LBAR();
    }
    const int row0 = rg * 16 + l16, col1 = mt * 128 + g4 * 8;
    u32x4 xq[4], sa4[4];
#pragma unroll
    for (int q = 0; q < 4; ++q) DSR(sa4[q], saa, q * 64);
    f32x2 cj = *reinterpret_cast<const f32x2*>(shm + TSUM + g4 * 8);
    float sw = *reinterpret_cast<const float*>(shm + TSUM + 32), ce = *reinterpret_cast<const float*>(shm + TSUM + mt * 4);
    asm volatile("" : "+v"(cj), "+v"(sw), "+v"(ce));
    if (wid < 4) {
      auto X1A = [&](auto kc) __attribute__((always_inline)) {
        constexpr int k = decltype(kc)::value;
        (void)acc, (void)idt, (void)pv;
        MFMA(acc[0][5 + k], idt[0], pv[k]);
        MFMA(acc[1][5 + k], idt[1], pv[k]);
      };
      auto X1B = [&](auto kc) __attribute__((always_inline)) {
        constexpr int j = decltype(kc)::value;
        (void)acc, (void)ga, (void)x5, (void)xw1;
        asm volatile("" : "+v"(acc[0][5 + j]), "+v"(acc[1][5 + j]));
        const u32x4 v = pk8((j < 5 ? acc[0][j % 5] : x5[0]) + ga[0] * acc[0][5 + j], (j < 5 ? acc[1][j % 5] : x5[1]) + ga[1] * acc[1][5 + j]);
        asm volatile("ds_write_b128 %0, %1 offset:%2" ::"v"(xw1), "v"(v), "n"(j * 4096) : "memory");
      };
      asm volatile("s_waitcnt vmcnt(2)" : "+v"(pv[2]), "+v"(pv[3]), "+v"(pv[4]), "+v"(pv[5])::"memory");
      POLL { PDBG(it);
        if (__all(max(max(max4(pv[2]), max4(pv[3])), max(max4(pv[4]), max4(pv[5]))) != SENT)) break;
        WAIT_V(0);
        __builtin_amdgcn_s_sleep(X_R);
        XCHLD();
        asm volatile("s_waitcnt vmcnt(0)" : W6(pv)::"memory");
      }
      X1A(IC(5)); X1A(IC(4)); X1A(IC(3)); X1B(IC(5)); X1A(IC(2)); X1B(IC(4)); X1B(IC(3));
      asm volatile("s_waitcnt vmcnt(0)" : "+v"(pv[0]), "+v"(pv[1])::"memory");
      POLL { PDBG(it);
        if (__all(max(max4(pv[0]), max4(pv[1])) != SENT)) break;
        __builtin_amdgcn_s_sleep(X_R);
        asm volatile("global_load_dwordx4 %0, %2, %3 offset:2048 sc1\n\tglobal_load_dwordx4 %1, %2, %3 offset:1024 sc1\n\ts_waitcnt vmcnt(0)"
                     : "=&v"(pv[0]), "=&v"(pv[1]) : "v"(lane * 16), "s"(xr) : "memory");
      }
      TS(27);
      X1A(IC(1)); X1A(IC(0)); X1B(IC(2)); X1B(IC(1)); X1B(IC(0));
    }
    {
      TS(6);
      LBAR();
      asm volatile("ds_read_b128 %0, %8\n\tds_read_b128 %1, %8 offset:1024\n\tds_read_b128 %2, %8 offset:2048\n\tds_read_b128 %3, %8 offset:3072\n\ts_waitcnt lgkmcnt(0)"
                   : "=&v"(xq[0]), "=&v"(xq[1]), "=&v"(xq[2]), "=&v"(xq[3]), "+v"(sa4[0]), "+v"(sa4[1]), "+v"(sa4[2]), "+v"(sa4[3])
                   : "v"(xa)
                   : "memory");
      if (!act) {
        TSE();
        return;
      }
      f32x4 s4 = {0.f, 0.f, 0.f, 0.f}, gr = {0.f, 0.f, 0.f, 0.f};
#pragma unroll
      for (int q = 0; q < 4; ++q) {
        gr = MM(xq[q], xq[q], gr);
        s4 = MM(sa4[q], xq[q], s4);
      }
      const u32x4 gd = __builtin_bit_cast(u32x4, gr) & dk;
      const float m = s4[2] * (1.0f / 128);
      float q2 = ar4(__uint_as_float((gd[0] | gd[1]) | (gd[2] | gd[3]))) - s4[2] * m, lg = (s4[0] + s4[1]) - m * ce;
      if (__any(!(s4[2] * m <= 256.0f * q2))) {
        f32x4 a = {0.f, 0.f, 0.f, 0.f}, l = {0.f, 0.f, 0.f, 0.f};
#pragma unroll
        for (int q = 0; q < 4; ++q) {
          const u32x4 ch = *reinterpret_cast<const u32x4*>(shm + SCW + g4 * 16 + q * 64), cl = *reinterpret_cast<const u32x4*>(shm + SCW + 256 + g4 * 16 + q * 64);
          const f32x4 d0 = lo4(xq[q]) - m, d1 = hi4(xq[q]) - m;
          a += d0 * d0 + d1 * d1;
          l += d0 * (lo4(ch) + lo4(cl)) + d1 * (hi4(ch) + hi4(cl));
        }
        q2 = ar4((a[0] + a[1]) + (a[2] + a[3]));
        lg = ar4((l[0] + l[1]) + (l[2] + l[3]));
      }
      const u32x4 own = {nsent(m), nsent(q2), nsent(lg), 0u};
      if (g4 == 0) ST16(sto, own, ws + WS_STAT, 0);
      TS(7);
      SKEWPT();
      const bool k0 = g4 * 2 == mt, k1 = g4 * 2 + 1 == mt;
#define STOK() (sr[0] = k0 ? own : sr[0], sr[1] = k1 ? own : sr[1], __all(max(max(max(sr[0][0], sr[0][1]), sr[0][2]), max(max(sr[1][0], sr[1][1]), sr[1][2])) != SENT))
      const bool pub = mt == (rg & 7);
      if (OPT_SD) __builtin_amdgcn_s_sleep(OPT_SD);
      POLL { PDBG(it);
        STLD();
        asm volatile("s_waitcnt vmcnt(0)" : "+v"(sr[0]), "+v"(sr[1])::"memory");
        if (STOK()) break;
        __builtin_amdgcn_s_sleep(1);
      }
      TS(9);
      const f32x2 mk = {__uint_as_float(sr[0][0]), __uint_as_float(sr[1][0])};
      const f32x2 qk = {__uint_as_float(sr[0][1]), __uint_as_float(sr[1][1])};
      const f32x2 lk = {__uint_as_float(sr[0][2]), __uint_as_float(sr[1][2])};
      const float mean = ar4(mk[0] + mk[1]) * (1.0f / 8);
      const f32x2 dm = mk - mean;
      const f32x2 a = qk + 128.0f * (dm * dm), l = lk + dm * cj;
      const float vf = ar4(a[0] + a[1]) * (1.0f / D);
      const float rstd = __builtin_amdgcn_rsqf(vf + 1e-5f);
      const float rd = rstd * ar4(l[0] + l[1]) + sw;
      const unsigned rb = f2bf(rd);
      const unsigned ord = rb ^ ((rb & 0x8000u) ? 0xffffu : 0x8000u);
      const unsigned key = (ord << 16) | (0xffffu - (unsigned)row0);
      unsigned t1 = key, t2 = 0u;
      TOP2(0xB1); TOP2(0x4E); TOP2(0x141); TOP2(0x140);
      if (lane == 0 && pub) ST4(reinterpret_cast<u64*>(ws + WS_KEY) + rg * 16, mk64(~t2, ~t1));
      TS(1);
      SKEWPT();
      if (key >= t2) {
        const int sl = rg * 2 + (key == t2);
#pragma unroll
        for (int q = 0; q < 4; ++q) ST16(sl * 2048 + col1 * 2, nsent4(xq[q]), ws + WS_CAND, q * 64);
        if (g4 == 0 && pub) ST16(sl * 16, (u32x4{nsent(mean), nsent(rstd), 0u, 0u}), ws + WS_HDR, 0);
      }
      TS(8);
      if (OUT_D) __builtin_amdgcn_s_sleep(OUT_D);
#pragma unroll
      for (int q = 0; q < 4; ++q) {
        const u32x4 cq = *reinterpret_cast<const u32x4*>(shm + TV + q * 64 + g4 * 16), sbq = *reinterpret_cast<const u32x4*>(shm + TV + 256 + q * 64 + g4 * 16);
        const u32x4 gbq = *reinterpret_cast<const u32x4*>(shm + TV + 512 + q * 64 + g4 * 16);
        const f32x4 x0 = lo4(xq[q]), x1 = hi4(xq[q]);
        const f32x4 h0 = ((x0 - mean) * rstd) * lo4(cq) + lo4(sbq), h1 = ((x1 - mean) * rstd) * hi4(cq) + hi4(sbq);
        ST16((row0 * D + col1) * 2, pk8(x0 + lo4(gbq) * h0, x1 + hi4(gbq) * h1), out, q * 64);
      }
      asm volatile(".Lcpf_e2:");
      WAIT_V(0);
      if (lane == 0) ST4(reinterpret_cast<unsigned*>(ws + WS_DON) + (rg * 8 + mt) * 2, 1u);
      TS(16);
    }
    TSE();
    return;
  }
  {
    const int hid = b - 192, hi = j8 - 24;
    constexpr int RNG = 65536, SSI = MISC, GP = MISC + 2048, SSA = MISC + 3072, HS = MISC + 5120, SX1 = MISC + 9216, ACT = MISC + 13312,
                  HFLG = MISC + 13568, RR = MISC + 13824;
    unsigned* flg = reinterpret_cast<unsigned*>(shm + HFLG);
    const int ec = xcd * 7 + hi, hd = hi == 7 ? 7 : (hi + 7 - ((3 + xcd) & 7) % 7) % 7;
    const bool pon = wid < ((0x06555542 >> (4 * hd)) & 7);
    const char* pfh = PFG((hd < 2 ? 2 * hd : 5 * hd - 4) + wid, 8 - qs);
#define HPF(g) do { if (pon && !((ABL & 2) && (g) > 3)) PFL(pfh + ((((g) + qs) & 7) << 9)); } while (0)
    WHLD(4);
    const int cb0 = xcd * 8 + ((hd + 2) & 7);
    const unsigned om = (unsigned)((wid * 16 + (lane >> 3)) * 12288 + (lane & 7) * 16);
    const unsigned og = (unsigned)((wid * 8 + (lane >> 3)) * 7168 + (lane & 7) * 16);
    const unsigned od = (unsigned)((wid * 8 + (lane >> 3)) * 2048 + (lane & 7) * 16);
    const char* pm = reinterpret_cast<const char*>(wmod) + cb0 * 128;
    const char* pg = reinterpret_cast<const char*>(wg) + ec * 128;
    const char* pu = reinterpret_cast<const char*>(wu) + ec * 128;
    const char* pd = reinterpret_cast<const char*>(wd) + (size_t)ec * 131072;
    const unsigned ow = (unsigned)(((wid & 1) * 16 + (lane >> 3)) * 12288 + (wid >> 1) * 128 + (lane & 7) * 16);
    const char* pw = reinterpret_cast<const char*>(wmod) + 8192 + xcd * 512;
#define RQM(r)                                                                                   \
  do {                                                                                           \
    const char* p_ = pm + (size_t)(r)*1572864;                                                   \
    GLDSP(lds0 + RNG + ((r)&3) * 16384 + wid * 2048, om, p_, WPOL);                              \
    GLDSP(lds0 + RNG + ((r)&3) * 16384 + wid * 2048 + 1024, om, p_ + 98304, WPOL);               \
  } while (0)
#define RQE(r)                                                                                                                \
  do {                                                                                                                        \
    if (NOEXP) break;                                                                                                         \
    GLDSP(lds0 + RNG + ((r)&3) * 16384 + wid * 1024, (r) < 16 ? og : od, (r) < 16 ? pg + (r)*458752 : pd + ((r)-16) * 256, WPOL);   \
    GLDSP(lds0 + RNG + ((r)&3) * 16384 + 8192 + wid * 1024, (r) < 16 ? og : od, (r) < 16 ? pu + (r)*458752 : pd + ((r)-16) * 256 + 128, WPOL); \
  } while (0)
    PBAR(4);
    XFLAG();
    const u32x4 sv = {SENT, SENT, SENT, SENT};
    auto PUBS = [&](int cb, unsigned v, unsigned p, unsigned r) __attribute__((always_inline)) {
      const int vi = cb >> 4, col = (cb & 15) * 64 + lane;
      ST4(modv + cb * 64 + lane, v);
      if (vi == 4) ST4(modv + 6144 + col, p);
      if (vi == 4 ? (lane & 31) == 0 : vi == 3 && lane == 0) ST4(modv + 7168 + (vi == 4 ? col >> 5 : 32 + (cb & 15)), r);
    };
    if (wid == 4 || (hi == 7 && wid < 4)) PUBS(wid == 4 ? cb0 : 64 + xcd * 4 + wid, SENT, SENT, SENT);
    if (tid >= 480) ST4(reinterpret_cast<unsigned*>(ws + WS_Q) + hsl * 32 + tid - 480, SENT);
    unsigned cv, wv;
    asm volatile("global_load_dword %0, %2, %3\n\tglobal_load_dword %1, %2, %4" : "=&v"(cv), "=&v"(wv) : "v"(tid * 4), "s"(cvec), "s"(wr_) : "memory");
    if (hi < 7) {
      if (PF_N < 3) HPF(2);
      HPF(3);
      const char* ap = ws + WS_ARM + (size_t)(ec * 8 + wid) * 3328;
#pragma unroll
      for (int j = 0; j < 4; ++j) ST16(lane * 16, sv, ap, j < 3 ? j * 1024 : 2304);
      HPF(4);
      WAIT_V(4);
    } else {
      WAIT_V(0);
    }
    asm volatile("; AUDIT_OK %0 %1" : "+v"(cv), "+v"(wv)::"memory");
    *reinterpret_cast<unsigned*>(shm + SSI + tid * 4) = pack2(silu_bf(bflo(cv)), silu_bf(bfhi(cv)));
    *reinterpret_cast<unsigned*>(shm + SX1 + tid * 4) = wv;
    LBAR();
    TS(50);
    const unsigned ha = (unsigned)(((l16 >> 2) * 8 + (lane & 3)) * 2048 + wid * 256);
#define HFR(j) *reinterpret_cast<const u32x4*>(shm + ha + ((j)&1) * 8192 + ((j) >> 2) * 128 + (((((j) >> 1) & 1) * 4 + g4) ^ (lane & 7)) * 16)
    HPF(5);
#pragma unroll
    for (int j = 0; j < ((ABL & 1) ? 0 : 8); ++j) ST16(lane * 16, HFR(j), ws + WS_WH + (size_t)hsl * 65536 + wid * 8192 + 4096, j * 1024 - 4096);
    HPF(6);
    RQM(0);
    RQM(1);
    RQM(2);
    const unsigned tm = lds0 + RNG + ((wid >> 2) * 64 + g4 * 8 + ((lane >> 2) & 3)) * 128 + (wid & 3) * 32 + (lane & 3) * 8;
    f32x4 ma = {0.f, 0.f, 0.f, 0.f};
#define GVR(i)                                                                                                                               \
  do {                                                                                                                                       \
    _Pragma("unroll") for (int f = 0; f < 2; ++f)                                                                                            \
        ma = MM(trd(tm + ((i)&3) * 16384 + f * 4096), *reinterpret_cast<const u32x4*>(shm + SSI + ((i)*128 + (wid >> 2) * 64 + f * 32 + g4 * 8) * 2), ma); \
    if ((i) == 7 && l16 == 0) *reinterpret_cast<f32x4*>(shm + GP + wid * 64 + g4 * 16) = ma;                                                 \
  } while (0)
    const float* gp = reinterpret_cast<const float*>(shm + GP);
    auto PUB = [&](int cb, float s) __attribute__((always_inline)) {
      const int vi = cb >> 4;
      const float v = rbf(((vi == 1 || vi == 4) ? 1.0f : 0.0f) + rbf(s));
      const float p = v * bf2f(*reinterpret_cast<const u16*>(shm + SX1 + ((cb & 15) * 64 + lane) * 2));
      const float r16 = row_sum(p), r32 = swap16_add(r16, r16);
      PUBS(cb, nsent(v), nsent(p), nsent(vi == 4 ? r32 : swap32_add(r32, r32)));
    };
    {
      sfor<8>([&](auto ii) __attribute__((always_inline)) {
        constexpr int i = decltype(ii)::value;
        (void)ow, (void)pw, (void)om, (void)pm, (void)og, (void)od, (void)pg, (void)pu, (void)pd, (void)lds0, (void)wid, (void)pfl, (void)Yx, (void)l16, (void)g4, (void)xcd, (void)hfl, (void)lane, (void)hid;
        TSCAP();
        WAIT_V(4);
        LBAR();
        if (wid == 2) TS(i);
        if constexpr (i == 0) { if (tid == 0) ST4(reinterpret_cast<u64*>(hfl + hid * HST), tag); }
        if constexpr (i == 0) HPF(7);
        if constexpr (i < 5) {
          RQM(i + 3);
        } else {
          const bool e = hid < 56;
          const char* p_ = pw + (i - 5) * 393216;
          GLDSP(lds0 + RNG + (i - 5) * 16384 + wid * (e ? 1024 : 2048), e ? og : ow, e ? pg + (i - 5) * 458752 : p_, WPOL);
          GLDSP(lds0 + RNG + (i - 5) * 16384 + wid * (e ? 1024 : 2048) + (e ? 8192 : 1024), e ? og : ow, e ? pu + (i - 5) * 458752 : p_ + 98304, WPOL);
        }
        GVR(i);
      });
    }
    TS(51);
#define PUB0() do { if (wid == 0) PUB(cb0, gp[lane] + gp[64 + lane]); } while (0)
    u64 sf, hf;
#define SFLD(OP)                                                                                                                   \
  asm volatile("s_mov_b64 exec, 3\n\t" OP "\n\ts_mov_b64 exec, 0xff\n\tglobal_load_dwordx2 %1, %6, %5 sc1\n\ts_mov_b64 exec, -1"   \
               : "=&v"(sf), "=&v"(hf) : "v"(lane * 128), "v"(0ull), "s"(ws + WS_SFL + wid * 256), "s"(ws + WS_HFL + wid * 64), "v"(lane * 8) : "memory")
#define SF_LD "global_load_dwordx2 %0, %2, %4 sc1"
#define SF_AT "global_atomic_add_x2 %0, %2, %3, %4 sc0"
    auto SAW = [&]() __attribute__((always_inline)) {
      POLL { PDBG(it);
        if (((__builtin_amdgcn_ballot_w64(sf != tag) & 3u) | (__builtin_amdgcn_ballot_w64(hf != tag) & 0xffu)) == 0) break;
        __builtin_amdgcn_s_sleep(4);
        SFLD(SF_LD);
        asm volatile("s_waitcnt vmcnt(0)" : "+v"(sf), "+v"(hf)::"memory");
      }
    };
    u32x2 s2;
#define SALD() asm volatile("global_load_dwordx2 %0, %1, %2 sc1" : "=v"(s2) : "v"(tid * 8), "s"(modv) : "memory")
    auto QMM = [&]() __attribute__((always_inline)) {
      *reinterpret_cast<unsigned*>(shm + SSA + tid * 4) = hi2(s2);
      f32x4 qa[2] = {};
#pragma unroll
      for (int j = 0; j < 8; ++j) qa[j & 1] = MM(HFR(j), *reinterpret_cast<const u32x4*>(shm + SSA + wid * 256 + (j >> 1) * 64 + g4 * 16), qa[j & 1]);
      if (l16 < 2) *reinterpret_cast<f32x4*>(shm + HS + wid * 128 + g4 * 32 + l16 * 16) = l16 ? qa[1] : qa[0];
    };
    auto QPUB = [&]() __attribute__((always_inline)) {
      if (tid >= 480) {
        float s = 0.f;
#pragma unroll
        for (int w = 0; w < 8; ++w) s += *reinterpret_cast<const float*>(shm + HS + w * 128 + (tid - 480) * 4);
        ST4(reinterpret_cast<unsigned*>(ws + WS_Q) + hsl * 32 + tid - 480, nsent(s));
      }
    };
    u32x8 GU[16], DN[8];
    if (hid < 56) {
      const unsigned tg = lds0 + RNG + (g4 * 8 + ((lane >> 2) & 3)) * 128 + ((lane >> 1) & 1) * 8192 + wid * 16 + (lane & 1) * 8;
      const unsigned td = lds0 + RNG + (g4 * 8 + ((lane >> 2) & 3)) * 128 + (wid >> 2) * 8192 + (wid & 3) * 32 + (lane & 3) * 8;
      sfor<24>([&](auto rr) __attribute__((always_inline)) {
        constexpr int r = decltype(rr)::value;
        (void)td, (void)tg, (void)GU, (void)DN, (void)og, (void)od, (void)pg, (void)pu, (void)pd, (void)lds0, (void)wid, (void)pfl, (void)Yx, (void)l16, (void)g4, (void)xcd;
        (void)s2, (void)modv, (void)tid;
        (void)hid, (void)lane, (void)sf, (void)hf;
        (void)sv, (void)hi, (void)ws;
#define EX(r) (((r) < 0 ? 0 : (r) < 2 ? 4 : (r) == 2 ? 2 : 0) + ((r) == Q_AT - 6 ? 2 : 0) + ((r) == Q_AT - 3))
        WAIT_V(r > 21 ? 2 * (23 - r) : 4 + EX(r - 2) + EX(r - 1));
        LBAR();
        TSCAP();
        if (wid == 3) TS(r);
        if constexpr (r == 0) PUB0();
        if constexpr (r == 1) {
          if (cb0 < 16 && wid == 0) {
            WAIT_V(2 + EX(0));
            if (lane == 0) ST4(reinterpret_cast<u64*>(ws + WS_SFL) + cb0 * 16, tag);
          }
        }
        if constexpr (r == Q_AT - 6) SFLD(SF_1);
        if constexpr (r == Q_AT - 3) {
          asm volatile("" : "+v"(sf), "+v"(hf));
          SAW();
          SALD();
        }
        if constexpr (r < 3) {
          const char* ap = ws + WS_XCH + xcd * 589824 + (hi * 8 + wid) * 10240 + r * 4096;
#pragma unroll
          for (int j = 0; j < (r < 2 ? 4 : 2); ++j) ST16(lane * 16, sv, ap, j * 1024);
          if (r == 2 && wid < 3) ST16(lane * 16, sv, ws + WS_XCH + xcd * 589824 + min(560 + hi * 3 + wid, 575) * 1024, 0);
        }
        if constexpr (r < 21) RQE(r + 3);
        if constexpr (r < 16) {
          GU[r] = trd2(tg + (r & 3) * 16384);
          asm volatile("" : "+v"(GU[r]));
        } else {
          DN[r - 16] = trd2(td + (r & 3) * 16384);
          asm volatile("" : "+v"(DN[r - 16]));
        }
        if constexpr (r == Q_AT) {
          asm volatile("" : "+v"(s2));
          QMM();
        }
        if constexpr (r == Q_AT + 1) QPUB();
      });
      TS(52);
    } else {
      const unsigned tw = lds0 + RNG + (wid >> 1) * 4096 + (g4 * 8 + ((lane >> 2) & 3)) * 128 + (wid & 1) * 64 + (lane & 3) * 8;
      f32x4 mb[2] = {};
      auto WR = [&](int r0, int r1) __attribute__((always_inline)) {
#pragma unroll 1
        for (int r = r0; r < r1; ++r) {
          WAIT_V(4);
          LBAR();
          if (wid == 2 && r < 22) TSD(r + 8);
          const char* p_ = pw + min(r + 3, 31) * 393216;
          GLDSP(lds0 + RNG + ((r + 3) & 3) * 16384 + wid * 2048, ow, p_, WPOL);
          GLDSP(lds0 + RNG + ((r + 3) & 3) * 16384 + wid * 2048 + 1024, ow, p_ + 98304, WPOL);
          const u32x4 sf = *reinterpret_cast<const u32x4*>(shm + SSI + (r * 32 + g4 * 8) * 2);
          mb[0] = MM(trd(tw + (r & 3) * 16384), sf, mb[0]);
          mb[1] = MM(trd(tw + (r & 3) * 16384 + 32), sf, mb[1]);
        }
      };
      WR(0, 1);
      PUB0();
      WR(1, 4);
      if (cb0 < 16 && tid == 0) ST4(reinterpret_cast<u64*>(ws + WS_SFL) + cb0 * 16, tag);
      WR(4, MQ_AT - 6);
      SFLD(SF_LD);
      WR(MQ_AT - 6, MQ_AT - 3);
      asm volatile("; AUDIT_OK %0 %1" : "+v"(sf), "+v"(hf));
      SAW();
      SALD();
      WR(MQ_AT - 3, MQ_AT);
      asm volatile("; AUDIT_OK %0" : "+v"(s2));
      QMM();
      WR(MQ_AT, 32);
      if (l16 < 2) *reinterpret_cast<f32x4*>(shm + GP + wid * 128 + l16 * 64 + g4 * 16) = l16 ? mb[1] : mb[0];
      LBAR();
      if (wid < 4) PUB(64 + xcd * 4 + wid, gp[tid]);
      QPUB();
      TS(52);
      WAIT_V(0);
      TSE();
      return;
    }
    bool early = false;
    if (tid == 0) {
      POLL { PDBG(it);
        unsigned g;
        if (WK_LD) asm volatile("global_load_dword %0, %1, %2 sc1\n\ts_waitcnt vmcnt(0)" : "=&v"(g) : "v"(0u), "s"(ws + WS_WAK + hid * 128) : "memory");
        else asm volatile("global_atomic_add %0, %1, %1, %2 sc0\n\ts_waitcnt vmcnt(0)" : "=&v"(g) : "v"(0u), "s"(ws + WS_WAK + hid * 128) : "memory");
        if (g != SENT) break;
        early = true;
        __builtin_amdgcn_s_sleep(OPT_KS);
      }
    }
    BARRIER();
    TS(21);
    const int tok = hid & 1, col = (hid & 16) * 32 + (lane & 3) * 128 + (hid & 14) * 8 + (lane >> 2);
    u32x4 c4, s4;
    unsigned gbu;
    POLL { PDBG(it);
      asm volatile("global_load_dwordx4 %0, %3, %5 sc1\n\tglobal_load_dwordx4 %1, %3, %6 sc1\n\tglobal_load_dword %2, %4, %7 sc1\n\ts_waitcnt vmcnt(0)"
                   : "=&v"(c4), "=&v"(s4), "=&v"(gbu) : "v"((tid & 255) * 16), "v"(col * 4), "s"(modv + 4096), "s"(modv + 3072), "s"(modv + 5120) : "memory");
      if (__all(max(max(max4(c4), max4(s4)), gbu) != SENT)) break;
      __builtin_amdgcn_s_sleep(8);
    }
    const f32x4 cb2 = __builtin_bit_cast(f32x4, c4), sb2 = __builtin_bit_cast(f32x4, s4);
    unsigned hra = lds0 + HS + g4 * 64 + (lane & 4) * 8 + (lane & 1) * 16, hwa = lds0 + HS + ((tid & 255) >> 2) * 64 + (wid >> 2) * 32 + (tid & 3) * 4;
    unsigned pto = (g4 & 1) * 4096 + ((g4 >> 1) * 8 + wid) * 256 + l16 * 16, six = (lane & 1) ? 0xEEEEu : 0x4444u;
    unsigned awa = lds0 + ACT + (wid >> 1) * 64 + g4 * 32 + (lane & 2) * 8 + (wid & 1) * 8 + (lane & 4) + (lane & 1) * 2;
    f32x4 d = {0.f, 0.f, 0.f, 0.f};
    asm volatile("" : "+v"(hra), "+v"(hwa), "+v"(pto), "+v"(awa), "+v"(six), "+v"(d));
    const float gb = __uint_as_float(gbu);
    if (wid == 0) {
      u32x2 g0 = {SENT, SENT}, g1 = {SENT, SENT};
      const char* gp = ws + WS_KEY + lane * 128;
      if (__any(early)) __builtin_amdgcn_s_sleep(OPT_KD);
      TS(20);
      POLL { PDBG(it);
        if (max2(g0) == SENT) asm volatile("global_load_dwordx2 %0, %1, off sc1" : "+v"(g0) : "v"(gp) : "memory");
        if (max2(g1) == SENT) asm volatile("global_load_dwordx2 %0, %1, off sc1" : "+v"(g1) : "v"(gp + 8192) : "memory");
        asm volatile("s_waitcnt vmcnt(0)" : "+v"(g0), "+v"(g1)::"memory");
        if (__all(max(max2(g0), max2(g1)) != SENT)) break;
        __builtin_amdgcn_s_sleep(1);
      }
      g0 = ~g0;
      g1 = ~g1;
      const unsigned a1 = max(g0[0], g1[0]), a2 = max(min(g0[0], g1[0]), max(g0[1], g1[1]));
      const unsigned k1 = wave_max_u(a1);
      const unsigned k2 = wave_max_u(a1 == k1 ? a2 : a1);
      if (lane == 0) *reinterpret_cast<u32x2*>(flg) = u32x2{k1, k2};
      TS(22);
    }
    LBAR();
    const unsigned k1 = flg[0], k2 = flg[1];
    {
      const int rg1 = key_row(k1) >> 4, rg2 = key_row(k2) >> 4;
      const int sl = wid >= 4 ? rg2 * 2 + (rg2 == rg1) : rg1 * 2;
      u32x2 v, hd;
      POLL { PDBG(it);
        asm volatile("global_load_dwordx2 %0, %2, %4 sc1\n\tglobal_load_dwordx2 %1, %3, %5 sc1\n\ts_waitcnt vmcnt(0)"
                     : "=&v"(v), "=&v"(hd) : "v"(sl * 2048 + (tid & 255) * 8), "v"(sl * 16), "s"(ws + WS_CAND), "s"(ws + WS_HDR) : "memory");
        if (__all(max(max2(v), max2(hd)) != SENT)) break;
        __builtin_amdgcn_s_sleep(1);
      }
      TS(23);
      const float mean = __uint_as_float(hd[0]), rstd = __uint_as_float(hd[1]);
      const f32x4 h = ((f32x4{bflo(v[0]), bfhi(v[0]), bflo(v[1]), bfhi(v[1])} - mean) * rstd) * cb2 + sb2;
      asm volatile("ds_write2_b32 %0, %1, %2 offset1:4" ::"v"(hwa), "v"(pack2(h[0], h[1])), "v"(pack2(h[2], h[3])) : "memory");
      *reinterpret_cast<u32x2*>(shm + SX1 + tid * 8) = v;
    }
    SKEWPT();
    TS(26);
    LBAR();
    TS(27);
    f32x4 dn[8];
    {
      u32x4 hb[8];
      sfor<23>([&](auto ff) __attribute__((always_inline)) {
        constexpr int f = decltype(ff)::value, c = f - 7;
        (void)d, (void)hb, (void)GU, (void)hra, (void)six;
        if constexpr (c >= 0) {
          WAIT_L(f <= 16 ? 6 : 22 - f);
          SMF(d, hb[c & 7], GU[c], six);
        }
        if constexpr (f < 16) DSR(hb[f & 7], hra, f * 256);
      });
      asm volatile("s_nop 7" : "+v"(d));
      TS(29);
      const float dv = rbf(d[0] + d[1]), uv = dpp_f<0x128>(dv);
      const unsigned av = f2bf(rbf(dv * __builtin_amdgcn_rcpf(1.0f + __expf(-dv))) * uv);
      asm volatile("s_mov_b64 exec, 0xff00ff\n\tds_write_b16 %0, %1\n\ts_mov_b64 exec, -1" ::"v"(awa), "v"(av) : "memory");
    }
#pragma unroll
    for (int c = 0; c < 8; ++c) asm volatile("v_mov_b64 %0, 0" : "=v"(dn[c].lo));
    asm volatile("" : W8(dn));
    TS(24);
    LBAR();
    {
      u32x4 a0;
      asm volatile("ds_read_b128 %0, %1 offset:%2\n\ts_waitcnt lgkmcnt(0)" : "=&v"(a0) : "v"(hra), "n"(ACT - HS) : "memory");
#pragma unroll
      for (int c = 0; c < 8; ++c) SMF(dn[(c >> 1) + (c & 1) * 4], a0, DN[(c >> 1) + (c & 1) * 4], six);
      asm volatile("s_nop 7" : W8(dn));
      u32x4 o;
#pragma unroll
      for (int i = 0; i < 4; ++i) o[i] = nsent((g4 & 2) ? dn[4 + i][0] + dn[4 + i][1] : dn[i][0] + dn[i][1]);
      ST16(pto, o, ws + WS_PART + ec * 8192, 0);
    }
    TS(25);
    SKEWPT();
    if (hid >= 32) {
      if (hi == 6 && wid == 1 && lane < 35) {
        char* fp = ws + (lane < 32 ? WS_XFL + (xcd * 32 + lane) * 128 : lane < 34 ? WS_SFL + (xcd * 2 + lane - 32) * 128 : WS_EPO);
        if (lane < 34 || xcd == 0) ST4(reinterpret_cast<u64*>(fp), lane < 34 ? ~tag : tag);
      }
      if (hid == 40 && tid < 320) ST4(reinterpret_cast<u64*>(ws + WS_HFL) + tid, ~tag);
      TSE();
      return;
    }
    const unsigned kk = tok ? k2 : k1;
    const int row = key_row(kk);
    float* rr = reinterpret_cast<float*>(shm + RR);
    float x1 = bf2f(*reinterpret_cast<const u16*>(shm + SX1 + tok * 2048 + col * 2));
    float h2 = bf2f(*reinterpret_cast<const u16*>(shm + HS + (col >> 4) * 64 + tok * 32 + (col & 2) * 8 + (col & 12) + (col & 1) * 2));
    float rsg = rbf(1.0f / (1.0f + __expf(-key_val(kk))));
    unsigned oo3 = (unsigned)(row * D + col) * 2, rra = lds0 + RR + lane * 32;
    asm volatile("" : "+v"(rsg), "+v"(x1), "+v"(h2), "+v"(oo3), "+v"(rra));
    if (wid == 7) {
      POLL { PDBG(it);
        unsigned g;
        asm volatile("global_load_dword %0, %1, %2 sc1\n\ts_waitcnt vmcnt(0)" : "=&v"(g) : "v"(((row >> 4) * 8 + (col >> 7)) * 8), "s"(ws + WS_DON) : "memory");
        if (__all(g != SENT)) break;
        __builtin_amdgcn_s_sleep(2);
      }
    } else {
      const unsigned po = (unsigned)((wid * 8 + g4) * 8192 + tok * 4096 + (hid >> 1) * 256 + l16 * 16);
      u32x4 v[2];
      if (OPT_RD) __builtin_amdgcn_s_sleep(OPT_RD);
      POLL { PDBG(it);
        asm volatile("global_load_dwordx4 %0, %2, %3 sc1\n\tglobal_load_dwordx4 %1, %2, %4 sc1\n\ts_waitcnt vmcnt(0)"
                     : "=&v"(v[0]), "=&v"(v[1])
                     : "v"(po), "s"(ws + WS_PART), "s"(ws + WS_PART + 32768)
                     : "memory");
        if (__all(max(max4(v[0]), max4(v[1])) != SENT)) break;
        __builtin_amdgcn_s_sleep(OPT_RR);
      }
      TS(28);
      const f32x4 t = __builtin_bit_cast(f32x4, v[0]) + __builtin_bit_cast(f32x4, v[1]);
      rr[(l16 * 4 + g4) * 8 + wid] = swap16_add(swap32_add(t[0], t[2]), swap32_add(t[1], t[3]));
    }
    LBAR();
    asm volatile(".Lcpf_e1:");
    TS(5);
    if (wid == 0) {
      f32x4 r0, r1;
      asm volatile("ds_read_b128 %0, %2\n\tds_read_b128 %1, %2 offset:16\n\ts_waitcnt lgkmcnt(0)" : "=&v"(r0), "=&v"(r1) : "v"(rra) : "memory");
      const float sum = ((r0[0] + r0[1]) + (r0[2] + r0[3])) + ((r1[0] + r1[1]) + r1[2]);
      asm volatile("global_store_short %0, %1, %2" FPOL ::"v"(oo3), "v"(f2bf(x1 + gb * rbf(h2 + rbf(rbf(sum) * rsg)))), "s"(out) : "memory");
    }
    TS(15);
    TSE();
  }
}

extern "C" void kernel_launch(void* const* d_in, const int* in_sizes, int n_in, void* d_out, int out_size, void* d_ws, size_t ws_size,
                              hipStream_t stream) {
  const u16* x = (const u16*)d_in[0];
  const u16* wsr = (const u16*)d_in[1];
  const u16* wsi = (const u16*)d_in[2];
  const u16* whr = (const u16*)d_in[3];
  const u16* whi = (const u16*)d_in[4];
  const u16* w_r = (const u16*)d_in[6];
  const u16* w_gate = (const u16*)d_in[7];
  const u16* w_up = (const u16*)d_in[8];
  const u16* w_down = (const u16*)d_in[9];
  const u16* c = (const u16*)d_in[10];
  const u16* w_mod = (const u16*)d_in[11];
  u16* out = (u16*)d_out;
  k_main<<<256, 512, 0, stream>>>(x, (char*)d_ws, wsr, wsi, c, w_mod, whr, whi, w_r, w_gate, w_up, w_down, out);
}
```

```cpp
#include <hip/hip_runtime.h>
#include <hip/hip_bf16.h>
#include <stdint.h>
#include <utility>

typedef unsigned short u16;
typedef unsigned long long u64;
using bf16x8 = __attribute__((ext_vector_type(8))) short;
using bf16v8 = __attribute__((ext_vector_type(8))) __bf16;
using f32x4 = __attribute__((ext_vector_type(4))) float;
using f32x2 = __attribute__((ext_vector_type(2))) float;
using u32x4 = __attribute__((ext_vector_type(4))) unsigned;
using u32x2 = __attribute__((ext_vector_type(2))) unsigned;

constexpr int S = 2048, D = 1024, F = 3584;

__device__ __forceinline__ float bf2f(unsigned v) { return __uint_as_float(v << 16); }
__device__ __forceinline__ float bflo(unsigned v) { return __uint_as_float(v << 16); }
__device__ __forceinline__ float bfhi(unsigned v) { return __uint_as_float(v & 0xffff0000u); }
__device__ __forceinline__ unsigned f2bf(float f) {
  __hip_bfloat16 b = __float2bfloat16(f);
  return (unsigned)*reinterpret_cast<u16*>(&b);
}
__device__ __forceinline__ float rbf(float f) { return bf2f(f2bf(f)); }
using bf16v2 = __attribute__((ext_vector_type(2))) __bf16;
__device__ __forceinline__ unsigned pack2(float lo, float hi) {
  return __builtin_bit_cast(unsigned, __builtin_convertvector(f32x2{lo, hi}, bf16v2));
}

template <int CTRL>
__device__ __forceinline__ float dpp_f(float v) {
  return __int_as_float(__builtin_amdgcn_mov_dpp(__float_as_int(v), CTRL, 0xf, 0xf, true));
}
template <int CTRL>
__device__ __forceinline__ float dpp_add(float v) { return v + dpp_f<CTRL>(v); }
__device__ __forceinline__ float row_sum(float v) { return dpp_add<0x140>(dpp_add<0x141>(dpp_add<0x4E>(dpp_add<0xB1>(v)))); }
template <int CTRL>
__device__ __forceinline__ unsigned dpp_u(unsigned v) { return (unsigned)__builtin_amdgcn_mov_dpp((int)v, CTRL, 0xf, 0xf, true); }
__device__ __forceinline__ unsigned wave_max_u(unsigned v) {
  v = max(v, dpp_u<0xB1>(v));
  v = max(v, dpp_u<0x4E>(v));
  v = max(v, dpp_u<0x141>(v));
  v = max(v, dpp_u<0x140>(v));
  const unsigned a = (unsigned)__builtin_amdgcn_readlane((int)v, 0), b = (unsigned)__builtin_amdgcn_readlane((int)v, 16);
  const unsigned c = (unsigned)__builtin_amdgcn_readlane((int)v, 32), d = (unsigned)__builtin_amdgcn_readlane((int)v, 48);
  return max(max(a, b), max(c, d));
}
__device__ __forceinline__ u64 mk64(unsigned hi, unsigned lo) { return ((u64)hi << 32) | lo; }
__device__ __forceinline__ float swap32_add(float a, float b) {
  auto r = __builtin_amdgcn_permlane32_swap(__float_as_uint(a), __float_as_uint(b), false, false);
  return __uint_as_float(r[0]) + __uint_as_float(r[1]);
}
__device__ __forceinline__ float swap16_add(float a, float b) {
  auto r = __builtin_amdgcn_permlane16_swap(__float_as_uint(a), __float_as_uint(b), false, false);
  return __uint_as_float(r[0]) + __uint_as_float(r[1]);
}
__device__ __forceinline__ float ar4(float v) {
  const float t = swap32_add(v, v);
  return swap16_add(t, t);
}
__device__ __forceinline__ float wave_sum_s(float v) {
  v = row_sum(v);
  v += __int_as_float(__builtin_amdgcn_update_dpp(0, __float_as_int(v), 0x142, 0xa, 0xf, false));
  v += __int_as_float(__builtin_amdgcn_update_dpp(0, __float_as_int(v), 0x143, 0xc, 0xf, false));
  return __int_as_float(__builtin_amdgcn_readlane(__float_as_int(v), 63));
}
__device__ __forceinline__ float silu_bf(float c) { return rbf(c * __builtin_amdgcn_rcpf(1.0f + __expf(-c))); }
__device__ __forceinline__ float dsum(unsigned v, float a) {
  return __builtin_amdgcn_fdot2_f32_bf16(__builtin_bit_cast(bf16v2, v), __builtin_bit_cast(bf16v2, 0x3f803f80u), a, false);
}
template <class Fn, int... I>
__device__ __forceinline__ void sfor_(Fn&& f, std::integer_sequence<int, I...>) { (f(std::integral_constant<int, I>{}), ...); }
template <int N, class Fn>
__device__ __forceinline__ void sfor(Fn&& f) { sfor_(f, std::make_integer_sequence<int, N>{}); }
constexpr unsigned SENT = 0xFFFFFFFFu;

constexpr size_t WS_EPO = 0;
constexpr size_t WS_XFL = 4096;
constexpr size_t WS_HFL = 40960;
constexpr size_t WS_XF2 = WS_HFL + 512;
constexpr size_t WS_SFL = WS_HFL + 8192;
constexpr size_t WS_MOD = 65536;
constexpr size_t WS_Q = WS_MOD + 32768;
constexpr size_t WS_ARM = WS_Q + 8192;
constexpr size_t WS_RS = WS_ARM;
constexpr size_t WS_KEY = WS_RS + 16384;
constexpr size_t WS_HDR = WS_KEY + 16384;
constexpr size_t WS_UFL = WS_HDR + 4096;
constexpr size_t WS_DON = WS_UFL + 98304;
constexpr size_t WS_STAT = WS_DON + 12288;
constexpr size_t WS_CAND = WS_STAT + 262144;
constexpr size_t WS_PART = WS_CAND + 524288;
constexpr size_t WS_WAK = WS_PART + 483328;
static_assert(WS_WAK + 8192 <= WS_ARM + 448 * 3328, "armed region");
constexpr size_t WS_XCH = 2u << 20;
constexpr size_t WS_H = 8u << 20;
constexpr size_t WS_WH = 12u << 20;
constexpr size_t WS_U = 16u << 20;
constexpr size_t WS_DBG = 64u << 20;
#define NEWDESIGN 1

#define PCAP (1 << 20)
#define PDBG(it)
#define TS(i)
#define TSCAP()
#define TSC0()
#define TSC1(i)
#define TSD(i)
#define TSE()
#define ST16(voff, v, base, off) asm volatile("global_store_dwordx4 %0, %1, %2 offset:%3 sc1\n\ts_nop 1" ::"v"(voff), "v"(v), "s"(base), "n"(off) : "memory")
#define ST4(p, v) __hip_atomic_store(p, v, __ATOMIC_RELAXED, __HIP_MEMORY_SCOPE_AGENT)
#define WAIT_V(n) asm volatile("s_waitcnt vmcnt(%0)" ::"n"(n) : "memory")
#define WAIT_L(n) asm volatile("s_waitcnt lgkmcnt(%0)" ::"n"(n) : "memory")
#define BARRIER() asm volatile("s_barrier" ::: "memory")
#define LBAR() asm volatile("s_waitcnt lgkmcnt(0)\n\ts_barrier" ::: "memory")
#define DSR(dst, addr, off) asm volatile("ds_read_b128 %0, %1 offset:%2" : "=v"(dst) : "v"(addr), "n"(off) : "memory")
#define MFMA(c, a, b) asm volatile("v_mfma_f32_16x16x32_bf16 %0, %1, %2, %0" : "+v"(c) : "v"(a), "v"(b))
#define MFMAI(c, a, b) asm volatile("v_mfma_f32_16x16x32_bf16 %0, %1, %2, %0" ::"v"(c), "v"(a), "v"(b))
#define MFMA0(c, a, b) asm volatile("v_mfma_f32_16x16x32_bf16 %0, %1, %2, 0" : "=&v"(c) : "v"(a), "v"(b))
#define GLDSP(m0v, voff, gbase, pol) \
  asm volatile("s_mov_b32 m0, %0\n\ts_nop 0\n\tglobal_load_lds_dwordx4 %1, %2" pol ::"s"(m0v), "v"(voff), "s"(gbase) : "memory")
#define GLDS(m0v, voff, gbase) GLDSP(m0v, voff, gbase, "")
#define WPOL " nt"
#define LDXS(dst, voff, base, off) asm volatile("global_load_dwordx4 %0, %1, %2 offset:%3 sc1" : "=v"(dst) : "v"(voff), "s"(base), "n"(off) : "memory")
#define BF8(v) __builtin_bit_cast(bf16v8, v)
#define MM(a, b, c) __builtin_amdgcn_mfma_f32_16x16x32_bf16(BF8(a), BF8(b), c, 0, 0, 0)
#define W6(a) "+v"(a[0]), "+v"(a[1]), "+v"(a[2]), "+v"(a[3]), "+v"(a[4]), "+v"(a[5])
#define W8(a) W6(a), "+v"(a[6]), "+v"(a[7])
#define PFL(ptr) asm volatile("s_mov_b32 m0, %0\n\ts_nop 0\n\tglobal_load_lds_dword %1, off" ::"s"(pfl), "v"(ptr) : "memory")
using s16x4 = __attribute__((ext_vector_type(4))) short;
__device__ __forceinline__ u32x4 trd(unsigned a) {
  const u32x2 lo = __builtin_bit_cast(u32x2, __builtin_amdgcn_ds_read_tr16_b64_v4i16((__attribute__((address_space(3))) s16x4*)(size_t)a));
  const u32x2 hi = __builtin_bit_cast(u32x2, __builtin_amdgcn_ds_read_tr16_b64_v4i16((__attribute__((address_space(3))) s16x4*)(size_t)(a + 512)));
  return u32x4{lo[0], lo[1], hi[0], hi[1]};
}
using u32x8 = __attribute__((ext_vector_type(8))) unsigned;
__device__ __forceinline__ u32x8 trd2(unsigned a) {
  const u32x4 lo = trd(a), hi = trd(a + 4096);
  return u32x8{lo[0], lo[1], lo[2], lo[3], hi[0], hi[1], hi[2], hi[3]};
}
#define SMF(c, a, b, i) asm volatile("v_smfmac_f32_16x16x64_bf16 %0, %1, %2, %3" ::"v"(c), "v"(a), "v"(b), "v"(i))
__device__ __forceinline__ int key_row(unsigned k) { return (int)((0xffffu - (k & 0xffffu)) & 2047u); }
__device__ __forceinline__ float key_val(unsigned k) {
  const unsigned ord = k >> 16;
  return bf2f(ord ^ ((ord & 0x8000u) ? 0x8000u : 0xffffu));
}
__device__ __forceinline__ unsigned nsent(float v) { return min(__float_as_uint(v), SENT - 1u); }
__device__ __forceinline__ f32x4 lo4(u32x4 q) { return f32x4{bflo(q[0]), bfhi(q[0]), bflo(q[1]), bfhi(q[1])}; }
__device__ __forceinline__ f32x4 hi4(u32x4 q) { return f32x4{bflo(q[2]), bfhi(q[2]), bflo(q[3]), bfhi(q[3])}; }
__device__ __forceinline__ u32x4 pk8(f32x4 a, f32x4 b) { return u32x4{pack2(a[0], a[1]), pack2(a[2], a[3]), pack2(b[0], b[1]), pack2(b[2], b[3])}; }
__device__ __forceinline__ u32x4 nsent4(u32x4 v) { return u32x4{min(v[0], SENT - 1u), min(v[1], SENT - 1u), min(v[2], SENT - 1u), min(v[3], SENT - 1u)}; }
__device__ __forceinline__ unsigned max4(u32x4 v) { return max(max(v[0], v[1]), max(v[2], v[3])); }
__device__ __forceinline__ unsigned max2(u32x2 v) { return max(v[0], v[1]); }
__device__ __forceinline__ unsigned hi2(u32x2 v) { return (v[0] >> 16) | (v[1] & 0xffff0000u); }
#define TOP2(CTRL)                                                  \
  do {                                                              \
    const unsigned o1 = dpp_u<CTRL>(t1), o2 = dpp_u<CTRL>(t2);      \
    t2 = max(min(t1, o1), max(t2, o2));                             \
    t1 = max(t1, o1);                                               \
  } while (0)
#define POLL _Pragma("unroll 1") for (int it = 0; it < PCAP; ++it)
#define P_D3 16
#define P_D4 2
#define U_D 28
#define U_R 1
#define X_R 1
#define OPT_SD 16
#define OPT_KS 2
#define OPT_KD 32
#define OPT_RD 10
#define OPT_RR 2
#define OUT_D 20
#define CPF_N 10
#define CPF_K 24
#define CPF_B 10
#define PF_N 3
#define A_LD 3
#define F_AT 3
#define X_AT 12
#define H_AT 24
#define B_VAT 42
#define UF_AT 8
#define B46 339
#define TP_XL 340
#define TP_X2 350
#define Q_AT 9
#define MQ_AT 31
#define XQ0 2
#define XQ1 4
#define XQ2 18
#define XQ3 20
#define HX0 9
#define HXS 2
static_assert(HX0 + 11 * HXS < 44 && UF_AT <= 22 && A_LD <= 3 && H_AT <= 24 && X_AT + 12 <= H_AT && Q_AT >= 7, "");
constexpr int SL = 22528, NS = 5, XR = NS * SL, MISC = XR + 24576;
constexpr int as(int c) { return c < 3 ? 2 : c < 6 ? 3 : 2 + (c & 1); }
constexpr int ap(int c) { return c < 3 ? c : c < 6 ? c - 3 : c / 2; }
constexpr int ug(int j) { return j < 9 ? 18 + 7 * j : 82; }
constexpr int E0 = 264, E3 = E0 + 88, NE = E3 + 6;
constexpr int es(int c) { return c < 132 ? c / 11 : c < 144 ? 12 + (c - 132) / 6 : c < 156 ? 14 + (c & 1) : 12 + (c & 3); }
constexpr int ep(int c) { return c < 132 ? c % 11 : c < 144 ? (c - 132) % 6 : c < 156 ? (c - 144) / 2 : 6 + (c - 156) / 4; }

__global__ __launch_bounds__(512, 2) void k_main(const u16* __restrict__ x, char* ws, const u16* __restrict__ Y0, const u16* __restrict__ Y1,
                                              const u16* __restrict__ cvec, const u16* __restrict__ wmod, const u16* __restrict__ X0,
                                              const u16* __restrict__ X1, const u16* __restrict__ wr_, const u16* __restrict__ wg,
                                              const u16* __restrict__ wu, const u16* __restrict__ wd, u16* __restrict__ out) {
  constexpr int STG = 114688;
  __shared__ __attribute__((aligned(1024))) char shm[MISC + 16384];
  const int b = blockIdx.x, xcd = b & 7, j8 = b >> 3;
  const int cc = xcd >> 2, qx = xcd & 3;
  const int tid = threadIdx.x, lane = tid & 63;
  const int wid = __builtin_amdgcn_readfirstlane(tid >> 6);
  const unsigned lds0 = (unsigned)(size_t)(__attribute__((address_space(3))) char*)shm;
  const char* cpf;
  {
    unsigned long long pc;
    unsigned len;
    asm volatile("s_getpc_b64 %0\n.Lcpf_here:\n\ts_mov_b32 %1, .Lcpf_e1-.Lcpf_here\n\ts_max_u32 %1, %1, .Lcpf_e2-.Lcpf_here" : "=s"(pc), "=s"(len)::"scc");
    cpf = reinterpret_cast<const char*>(pc) + min((unsigned)((lane * 32 + j8) * 128), len);
  }
  const unsigned pfl = lds0 + MISC + 16128;
  if (wid == 4 && lane < CPF_N) PFL(cpf);
  u32x4 xv[2];
  {
    const unsigned xo = (unsigned)((b * 8 + wid) * D + lane * 8) * 2;
    asm volatile("global_load_dwordx4 %0, %2, %3\n\tglobal_load_dwordx4 %1, %2, %3 offset:1024" : "=&v"(xv[0]), "=&v"(xv[1]) : "v"(xo), "s"(x) : "memory");
  }
  asm volatile("" ::"s"(ws), "s"(Y0), "s"(Y1), "s"(X0), "s"(X1));
  unsigned* xfl = reinterpret_cast<unsigned*>(ws + WS_XFL);
  unsigned* hfl = reinterpret_cast<unsigned*>(ws + WS_HFL);
  unsigned* modv = reinterpret_cast<unsigned*>(ws + WS_MOD);
#define SKEWPT()
  const int g4 = lane >> 4, l16 = lane & 15;
  u64 tag0, tag;
  asm volatile("global_load_dwordx2 %0, %1, %2 sc1" : "=v"(tag0) : "v"(0u), "s"(ws) : "memory");
  const char* Yx = reinterpret_cast<const char*>(cc ? Y1 : Y0) + (size_t)qx * 2097152;
#define PFG(r16, g) (Yx + (size_t)(((r16) * 16 + l16) * 4096 + g4 * 128 + ((((g) + qx) & 7) << 9)))
  unsigned ta = lds0 + STG + ((lane >> 2) & 3) * 2368 + (wid * 128 + (lane >> 5) * 32 + (lane & 3) * 8 + (g4 & 1) * 4) * 2;
  unsigned oo = (unsigned)((wid * 4 + (lane >> 5)) * 131072 + (b >> 2) * 2048 + (g4 & 1) * 1024 + ((b & 3) * 16 + l16) * 16), oo2 = oo + 262144;
  unsigned wa = lds0 + STG + wid * 2368 + lane * 16;
  asm volatile("" : "+v"(ta), "+v"(oo), "+v"(oo2), "+v"(wa));
  const int hsl = (b & 32) + xcd * 4 + (j8 & 3);
  const char* hp = reinterpret_cast<const char*>((b & 32) ? X1 : X0) + (size_t)(hsl & 31) * 65536 + wid * 8192;
#define WHLD(i0)                                                                                                                  \
  _Pragma("unroll") for (int i = i0; i < i0 + 4; ++i)                                                                             \
      GLDS(lds0 + wid * 8192 + i * 1024, (unsigned)((lane ^ ((i >> 1) | ((wid & 2) << 1))) * 16), hp + i * 1024)
  if (wid >= 8 - PF_N) PFL(PFG(j8, 7 - wid));
  if (j8 >= 24) {
    WHLD(0);
    WAIT_V(5);
  } else if (wid >= 8 - PF_N) {
    WAIT_V(2);
  } else {
    WAIT_V(1);
  }
  {
    asm volatile("; AUDIT_OK %0 %1" : "+v"(xv[0]), "+v"(xv[1])::"memory");
    TS(32);
    const float s0 = dsum(xv[0][3], dsum(xv[0][2], dsum(xv[0][1], dsum(xv[0][0], 0.f))));
    const float s1 = dsum(xv[1][3], dsum(xv[1][2], dsum(xv[1][1], dsum(xv[1][0], 0.f))));
    const float mf = wave_sum_s(s0 + s1) * (1.0f / D), m = rbf(mf);
    const f32x4 d0 = lo4(xv[0]) - m, d1 = hi4(xv[0]) - m, d2 = lo4(xv[1]) - m, d3 = hi4(xv[1]) - m;
    const f32x4 q = (d0 * d0 + d1 * d1) + (d2 * d2 + d3 * d3);
    const float vf = wave_sum_s((q[0] + q[1]) + (q[2] + q[3])) * (1.0f / D) - (mf - m) * (mf - m);
    const float rden = __builtin_amdgcn_rcpf(rbf(__builtin_amdgcn_sqrtf(rbf(rbf(vf) + 1e-5f))));
    asm volatile("ds_write_b128 %0, %1" ::"v"(wa), "v"(pk8(d0 * rden, d1 * rden)) : "memory");
    asm volatile("ds_write_b128 %0, %1 offset:1024" ::"v"(wa), "v"(pk8(d2 * rden, d3 * rden)) : "memory");
    LBAR();
    TS(34);
    u32x2 t[4];
    asm volatile(
        "ds_read_b64_tr_b16 %0, %4\n\tds_read_b64_tr_b16 %1, %4 offset:%5\n\t"
        "ds_read_b64_tr_b16 %2, %4 offset:128\n\tds_read_b64_tr_b16 %3, %4 offset:%6\n\ts_waitcnt lgkmcnt(0)"
        : "=&v"(t[0]), "=&v"(t[1]), "=&v"(t[2]), "=&v"(t[3])
        : "v"(ta), "n"(4 * 2368), "n"(4 * 2368 + 128)
        : "memory");
    asm volatile("global_store_dwordx4 %0, %2, %4 sc1\n\tglobal_store_dwordx4 %1, %3, %4 sc1\n\ts_nop 1" ::"v"(oo), "v"(oo2),
                 "v"(u32x4{t[0][0], t[0][1], t[1][0], t[1][1]}), "v"(u32x4{t[2][0], t[2][1], t[3][0], t[3][1]}), "s"(ws + WS_H)
                 : "memory");
  }
#define PBAR(n)                                                                                                          \
  do {                                                                                                                   \
    asm volatile("s_waitcnt vmcnt(%2)\n\tv_lshl_add_u64 %0, %1, 0, 1" : "=v"(tag) : "v"(tag0), "n"(n) : "memory");       \
    TS(36);                                                                                                              \
    BARRIER();                                                                                                           \
    TS(37);                                                                                                              \
  } while (0)
#define XFLAG() do { if ((tid | 1) == 385) ST4(reinterpret_cast<u64*>(ws + ((tid & 1) ? WS_XF2 + b * 8 : WS_XFL + b * 128)), tag); } while (0)
  if (j8 < 24) {
    const int mt = (j8 * 11) >> 5, nt = j8 - mt * 3, grp = xcd * 3 + nt;
#define FROW(f) (nt * 176 + (f) * 16 - (nt == 2 && (f) > 5 ? 16 : 0))
#define PROW(p) FROW(cc ? 10 - (p) : (p))
    constexpr int SCA = MISC, TGA = MISC + 512, TV = MISC + 1024, TSUM = MISC + 1792, SCW = MISC + 2048, RKA = MISC + 3072,
                  RKB = MISC + 5120, RKZ = MISC + 6656, IDT = MISC + 7168, ZSC = MISC + 9216;
    const bool zd = nt == 2;
    unsigned* ufl = reinterpret_cast<unsigned*>(ws + WS_UFL) + grp * 1024;
    const char* Ug = ws + WS_U + (size_t)grp * (16 * SL + 1024) + 1024;
    const unsigned perm = (unsigned)mt | (((0x76543210u & ((1u << (4 * mt)) - 1u)) << 4) | (0x76543210u & (0xfffffff0u << (4 * mt))));
#define KT(i) ((perm >> (4 * (i))) & 7u)
    const int kk = wid < 4 ? wid : wid - 2;
    const bool act = (wid < 4 || wid >= 6) && (kk || (cc == 0 && nt < 2));
    const int rg = qx * 32 + (PROW(5 + kk) >> 4);
    u32x4 pv[6];
    f32x4 acc[2][11], ga[2], x5[2];
    bf16x8 idt[2];
    unsigned spo = (unsigned)(((rg * 8 + g4 * 2) * 16 + l16) * 16);
    u32x4 sr[2], dk;
#pragma unroll
    for (int q = 0; q < 4; ++q) dk[q] = l16 == g4 * 4 + q ? ~0u : 0u;
    unsigned xw1 = lds0 + XR + tid * 16, xa = lds0 + XR + kk * 4096 + lane * 16, sto = (unsigned)(((rg * 8 + mt) * 16 + lane) * 16);
    unsigned saa = lds0 + SCW + (lane & 3) * 256 + g4 * 16;
    asm volatile("" : "+v"(dk), "+v"(xa), "+v"(sto), "+v"(xw1), "+v"(spo), "+v"(saa));
#define STLD() asm volatile("global_load_dwordx4 %0, %2, %3 sc1\n\tglobal_load_dwordx4 %1, %2, %3 offset:256 sc1" : "=&v"(sr[0]), "=&v"(sr[1]) : "v"(spo), "s"(ws + WS_STAT) : "memory")
    const char* xr = ws + WS_XCH + (size_t)(b * 4 + (wid & 3)) * 6144 + 3072;
#define XCHLD()                                                                                                       \
  asm volatile(                                                                                                       \
      "global_load_dwordx4 %0, %6, %7 offset:2048 sc1\n\tglobal_load_dwordx4 %1, %6, %7 offset:1024 sc1\n\t"          \
      "global_load_dwordx4 %2, %6, %7 sc1\n\tglobal_load_dwordx4 %3, %6, %7 offset:-1024 sc1\n\t"                     \
      "global_load_dwordx4 %4, %6, %7 offset:-2048 sc1\n\tglobal_load_dwordx4 %5, %6, %7 offset:-3072 sc1"            \
      : "=&v"(pv[0]), "=&v"(pv[1]), "=&v"(pv[2]), "=&v"(pv[3]), "=&v"(pv[4]), "=&v"(pv[5])                            \
      : "v"(lane * 16), "s"(xr)                                                                                       \
      : "memory")
    if (wid < 4) {
      bf16x8 X[4][2][2], Y[8];
      u32x4 xq[6];
      const unsigned xo0 = lane * 16 + 4096;

#define XLD(dst, XP, XH, i) LDXS(dst, xo0, XP, ((XH)&3) * 2048 + (i)*1024 - 4096)
      int xt = lane < 16 ? (int)WS_H + (mt * 4 + wid) * 131072 + (((qx * 2 + lane) & 15) << 13) : (int)WS_WH + (cc * 32 + mt * 4 + wid) * 65536 + (KT(lane & 7) << 13);
      asm volatile("" : "+v"(xt));
#define XT(j) (ws + (unsigned)__builtin_amdgcn_readlane(xt, j))
#define WP(h) XT(16 + ((h) >> 2))
#define XS(h) X[((h) >> 1) & 3][(h)&1]
      {
        unsigned ye = lds0 + l16 * 128 + (g4 ^ (lane & 7)) * 16, yo = ye ^ 64;
        const char* xpa = XT(0);
        const char* xpb = XT(1);
#pragma unroll
        for (int i = 0; i < 22; ++i) {
          acc[i / 11][i % 11] = f32x4{0.f, 0.f, 0.f, 0.f};
          asm volatile("" : "+v"(acc[i / 11][i % 11]));
        }
        const char* xw = ws + WS_XCH + (size_t)((b ^ 4) * 4 + wid) * 6144 + 3072;
        unsigned xra = lds0 + XR + l16 * 256 + (((wid * 4 + g4) ^ (lane & 7)) << 4), gaa = lds0 + TGA + wid * 128 + g4 * 32;
        unsigned rka = lds0 + (g4 ? RKZ : RKA + wid * 512 + l16 * 16), rkb = lds0 + RKB + l16 * 16;
        asm volatile("" : "+s"(xw), "+s"(xr), "+v"(xra), "+v"(gaa), "+v"(rka), "+v"(rkb));
        __builtin_amdgcn_s_setprio(1);
        PBAR(0);
        if (wid == 3) {
          const unsigned* fp = xfl + (qx * 32 + lane) * 32;
          if (P_D3) __builtin_amdgcn_s_sleep(P_D3);
          POLL { PDBG(it);
            u64 f;
            asm volatile("global_load_dwordx2 %0, %1, off sc1\n\ts_waitcnt vmcnt(0)" : "=&v"(f) : "v"(fp) : "memory");
            if (__all(f == tag)) break;
            __builtin_amdgcn_s_sleep(P_D4);
          }
        }
        TS(38);
        BARRIER();
        TS(39);
#pragma unroll
        for (int h = 0; h < 6; ++h) {
          XLD(XS(h)[0], h < 4 ? xpa : xpb, h, 0);
          XLD(XS(h)[1], h < 4 ? xpa : xpb, h, 1);
        }
#pragma unroll
        for (int j = 0; j < 6; ++j) DSR(Y[j], (j & 1) ? yo : ye, (j >> 1) * 2048);
        unsigned so = 0, a0 = ye, a1 = yo, yb[2], tq, u = 2;
#define STEP2(S0, XA, XB)                                                                                                      \
  sfor<44>([&](auto nn) __attribute__((always_inline)) {                                                                       \
    constexpr int n = (S0)*22 + decltype(nn)::value, q = n % 22, p = q / 2, s = n / 22, i = n & 1;                             \
    constexpr int k = q == XQ0 ? 0 : q == XQ1 ? 1 : q == XQ2 ? 2 : q == XQ3 ? 3 : -1;                                          \
    (void)acc, (void)X, (void)Y, (void)a0, (void)a1, (void)xo0, (void)XA, (void)XB, (void)so, (void)ye, (void)yo, (void)u, (void)xt, (void)tq, (void)ws; \
    if constexpr (q == 11) BARRIER();                                                                                          \
    if constexpr (q == 0) asm volatile("s_waitcnt vmcnt(8) lgkmcnt(4)" ::: "memory");                                          \
    else if constexpr (!i) WAIT_L(4);                                                                                          \
    MFMAI(acc[i][p], X[s][0][i], Y[n & 6]);                                                                                    \
    DSR(Y[(n + 6) & 7], i ? a1 : a0, ((p + 3) % 11) * 2048);                                                                   \
    MFMA(acc[i][p], X[s][1][i], Y[(n & 6) + 1]);                                                                               \
    if constexpr (k >= 0) XLD(X[(s + 3) & 3][k / 2][k & 1], (s & 1) ? XB : XA, 2 * s + 6 + k / 2, k & 1);                      \
    if constexpr (q == 0 && !(s & 1)) asm volatile("v_readlane_b32 %0, %2, %1\n\ts_add_u32 %1, %1, 1" : "=&s"(tq), "+s"(u) : "v"(xt) : "scc"); \
    if constexpr (q == 6) asm volatile("s_add_u32 %0, %0, %1" : "+s"(so) : "n"(SL) : "scc");                                   \
    if constexpr (q == 8) asm volatile("s_cmp_lg_u32 %0, %1\n\ts_cselect_b32 %0, %0, 0" : "+s"(so) : "n"(NS * SL) : "scc");    \
    if constexpr (q == 12 && !(s & 1)) { asm volatile("" : "+s"(tq)); XB = ws + tq; asm volatile("" : "+s"(XB)); }             \
    if constexpr (q == 14) asm volatile("v_add_u32 %0, %1, %2" : "=v"(a0) : "s"(so), "v"(ye));                                 \
    if constexpr (q == 16) asm volatile("v_add_u32 %0, %1, %2" : "=v"(a1) : "s"(so), "v"(yo));                                 \
  })
#define YSL(e) ((32 + es((e) / 2)) % NS)
#define YRD(e) DSR(Y[(e)&7], YSL(e) < 2 ? ye : yb[YSL(e) / 4], (YSL(e) & 1) * SL + ep((e) / 2) * 2048 + ((e)&1) * 1024)

        u32x4 ov;
        bf16x8 qf[2];
        auto OPS = [&](auto kc) __attribute__((always_inline)) {
          constexpr int k = decltype(kc)::value, q = k / 10, r = k % 10, c = k - 60;
          (void)ov, (void)acc, (void)xw, (void)lane, (void)xq, (void)x5;
          if constexpr (k < 60) {
            if constexpr (r < 4) asm volatile("v_cvt_pk_bf16_f32 %0, %1, %2" : "=v"(ov[r]) : "v"(acc[r / 2][q][(r & 1) * 2]), "v"(acc[r / 2][q][(r & 1) * 2 + 1]));
            else if constexpr (r < 8) asm volatile("v_min_u32 %0, -2, %0" : "+v"(ov[r - 4]));
            else if constexpr (r == 9) asm volatile("global_store_dwordx4 %0, %1, %2 offset:%3 sc1" ::"v"(lane * 16), "v"(ov), "s"(xw), "n"(q * 1024 - 3072) : "memory");
          } else if constexpr (c >= 40 && c < 48) {
            if constexpr (c & 1) asm volatile("v_and_b32 %0, 0xffff0000, %1" : "=v"(x5[(c >> 2) & 1][c & 3]) : "v"(xq[5][(c >> 1) & 3]));
            else asm volatile("v_lshlrev_b32 %0, 16, %1" : "=v"(x5[(c >> 2) & 1][c & 3]) : "v"(xq[5][(c >> 1) & 3]));
          } else if constexpr (c < 40) {
            if constexpr (c & 1) asm volatile("v_and_b32 %0, 0xffff0000, %1" : "=v"(acc[(c >> 2) & 1][c >> 3][c & 3]) : "v"(xq[c >> 3][(c >> 1) & 3]));
            else asm volatile("v_lshlrev_b32 %0, 16, %1" : "=v"(acc[(c >> 2) & 1][c >> 3][c & 3]) : "v"(xq[c >> 3][(c >> 1) & 3]));
          }
        };
#define IC(v) std::integral_constant<int, (v)>{}
#define FILL(g)                                                                              \
  do {                                                                                       \
    if constexpr (m >= 0) {                                                                  \
      OPS(IC(m / 2 * 5 + (m & 1) * 3 + (g)));                                                \
      if constexpr ((g) && !(m & 1)) OPS(IC(m / 2 * 5 + 2));                                 \
    }                                                                                        \
  } while (0)
        auto BP = [&](auto nn) __attribute__((always_inline)) {
          constexpr int n = decltype(nn)::value, s = es(n / 2), p = n < E3 ? ep(n / 2) : 5 + n - E3, i = n & 1, e = n + 6, m = n - E0 - 30;
          (void)ga, (void)qf, (void)idt, (void)rka, (void)rkb, (void)gaa, (void)xq, (void)ov, (void)xw, (void)x, (void)pv, (void)xr, (void)ufl;
          (void)acc, (void)X, (void)Y, (void)yb, (void)ye, (void)xra, (void)xo0, (void)xt, (void)ws, (void)lane, (void)mt, (void)nt, (void)cc, (void)lds0, (void)wid, (void)g4, (void)perm;
          (void)OPS;
          TSCAP();
          if constexpr (n == E0 + 55) TS(10);
          if constexpr (n == UF_AT) {
            TS(45);
            WAIT_V(n > HX0 ? (n - HX0 + HXS - 1) / HXS : 0);
            if (lane == 0) ST4(ufl + (mt * 4 + wid) * 32, 1u);
            TS(46);
          }
          if constexpr (n == 37) TS(48);
          if constexpr (n == 37 || n == E0 + 5 || n == E0 + 17 || n == B46) BARRIER();
          if constexpr (n == 37) TS(4);
          if constexpr (n == E0 + 25) { TS(3); SKEWPT(); }
          if constexpr (p == 0 && !i && n != 22) WAIT_V(n == 0 ? 15 : s < 14 ? 8 : 4 * (15 - s));
          if constexpr (!i) WAIT_L(e < NE ? 4 : NE - 2 - n);
          if constexpr (n < 22) MFMA0(acc[i][p], X[0][0][i], Y[n & 6]);
          else if constexpr (n < E3) MFMAI(acc[i][p], X[s & 3][0][i], Y[n & 6]);
          else MFMA(acc[0][p], qf[0], Y[n & 7]);
          if constexpr (e < E3) YRD(e);
          else if constexpr (e < NE) DSR(Y[e & 7], rkb, (e - E3) * 256);
          FILL(0);
          if constexpr (n < E3 && (s == 1 || s == 15)) MFMA(acc[i][p], X[s & 3][1][i], Y[(n & 6) + 1]);
          else if constexpr (n < E3) MFMAI(acc[i][p], X[s & 3][1][i], Y[(n & 6) + 1]);
          else MFMA(acc[1][p], qf[1], Y[n & 7]);
          if constexpr (n >= HX0 && n < HX0 + 12 * HXS && (n - HX0) % HXS == 0) {
            constexpr int k = (n - HX0) / HXS;
            static_assert(k < 8 || n >= 22, "slot still in use");
            XLD(XS(4 + k / 2)[k & 1], WP(4 + k / 2), k / 2, k & 1);
          }
          if constexpr (n >= 272 && n < 294 && n % 4 == 0) DSR(xq[(n - 272) / 4], xra, (n - 272) / 4 * 4096);
          if constexpr (n >= E0 && n < E0 + 4) XLD(X[3][(n - E0) / 2][i], WP(31), 30 + (n - E0) / 2, i);
          FILL(1);
          if constexpr (n == B46 || n == B46 + 1) DSR(qf[n - B46], rka, (n - B46) * 256);
          if constexpr (n == B46 + 2 || n == B46 + 3) DSR(ga[n - B46 - 2], gaa, (n - B46 - 2) * 16);
          if constexpr (n == B46 + 4 || n == B46 + 5) DSR(idt[n - B46 - 4], yb[1], IDT - 4 * SL + (n - B46 - 4) * 1024);
          if constexpr (n >= TP_XL && n < TP_XL + 4) LDXS(pv[TP_XL + 5 - n], lane * 16, xr, (n - TP_XL) * 1024 - 3072);
          if constexpr (n == TP_X2 || n == TP_X2 + 1) LDXS(pv[TP_X2 + 1 - n], lane * 16, xr, (n - TP_X2) * 1024 + 1024);
        };
        do {
          STEP2(0, xpb, xpa);
          if (__builtin_expect(u == 17, 0)) {
            TS(57);
            TSC1(40);
            TSC0();
            f32x4 c0, c1;
            const char* up = Ug + (mt * 2 + (wid >> 1)) * SL + (wid & 1) * 1024 + 4096;
            const unsigned ul = lds0 + (2 + (wid >> 1)) * SL + (wid & 1) * 1024 + lane * 16;
            u32x4 uv;
            DSR(c0, lds0 + SCA + wid * 128 + g4 * 32, 0);
            DSR(c1, lds0 + SCA + wid * 128 + g4 * 32, 16);
#define UMUL(j, k) asm volatile("v_mul_f32 %0, %0, %1" : "+v"(acc[(k) >> 2][j][(k)&3]) : "v"(((k) >> 2 ? c1 : c0)[(k)&3]))
#define UCVT(j, k) asm volatile("v_cvt_pk_bf16_f32 %0, %1, %2" : "=v"(uv[k]) : "v"(acc[(k) >> 1][j][((k)&1) * 2]), "v"(acc[(k) >> 1][j][((k)&1) * 2 + 1]))
            auto UEP = [&](auto jc, auto rc) __attribute__((always_inline)) {
              constexpr int j = decltype(jc)::value, r = decltype(rc)::value;
              (void)acc, (void)c0, (void)c1, (void)uv, (void)lane, (void)ul, (void)up;
              if constexpr (r < 4) { UMUL(j, 2 * r); UMUL(j, 2 * r + 1); }
              else if constexpr (r < 6) { UCVT(j, 2 * r - 8); UCVT(j, 2 * r - 7); }
              else asm volatile("global_store_dwordx4 %0, %2, %3 offset:%4 sc1\n\tds_write_b128 %1, %2 offset:%5"
                                ::"v"(lane * 16), "v"(ul), "v"(uv), "s"(up + (j >> 2) * 8192), "n"((j & 3) * 2048 - 4096), "n"(j * 2048) : "memory");
            };
#define UGAP(G) do { constexpr int j_ = (G) < 82 ? ((G)-18) / 7 : 9; if constexpr ((G) >= 18 && (G) != 81) UEP(IC(j_), IC((G)-ug(j_))); } while (0)
            sfor<44>([&](auto nn) __attribute__((always_inline)) {
              constexpr int n = decltype(nn)::value, s = as(n / 2), p = ap(n / 2), i = n & 1, e = n + 6;
              (void)acc, (void)X, (void)Y, (void)ye, (void)yo, (void)xo0, (void)xpa, (void)UEP;
              if constexpr (n == 0 || n == 6) WAIT_V(8);
              if constexpr (!i) WAIT_L(e < 44 ? 4 : 42 - n);
              MFMAI(acc[i][p], X[s][0][i], Y[(n + 4) & 6]);
              if constexpr (e < 44) DSR(Y[(e + 4) & 7], i ? yo : ye, (as(e / 2) & 1) * SL + ap(e / 2) * 2048);
              UGAP(2 * n);
              if constexpr (s == 2) MFMAI(acc[i][p], X[s][1][i], Y[((n + 4) & 6) + 1]);
              else MFMA(acc[i][p], X[s][1][i], Y[((n + 4) & 6) + 1]);
              if constexpr (n < 4) XLD(X[1][n / 2][i], xpa, 2 + n / 2, i);
              UGAP(2 * n + 1);
            });
            TSC1(41);
            TS(0);
            SKEWPT();
            UEP(IC(9), IC(6));
            asm volatile("s_nop 1");
            sfor<7>([&](auto rc) __attribute__((always_inline)) { UEP(IC(10), rc); });
            ye = lds0 + lane * 16;
            yo = ye + 1024;
            so = 4 * SL;
            a0 = ye + 4 * SL;
            a1 = yo + 4 * SL;
            yb[0] = ye + 2 * SL;
            yb[1] = a0;
            asm volatile("" : "+v"(yb[0]), "+v"(yb[1]), "+v"(ye), "+v"(yo));
            LBAR();
            TS(44);
            sfor<6>([&](auto ee) __attribute__((always_inline)) { (void)Y, (void)yb, (void)ye; YRD(decltype(ee)::value); });
            sfor<44>(BP);
            u = 19;
            xpa = WP(8);
          }
          STEP2(2, xpa, xpb);
        } while (u != 24);
        TS(2);
        sfor<NE - 264>([&](auto nn) __attribute__((always_inline)) { BP(std::integral_constant<int, 264 + decltype(nn)::value>{}); });
        __builtin_amdgcn_s_setprio(0);
        TSC1(59);
        TS(13);
        SKEWPT();
      }
    } else if (wid < 6) {
      const int hw = wid - 4;
      __builtin_amdgcn_s_setprio(2);
      unsigned voy[11];
#pragma unroll
      for (int i = 0; i < 11; ++i) voy[i] = (unsigned)((PROW(i) + hw * 8 + (lane >> 3)) * 4096 + ((lane & 7) ^ (lane >> 3)) * 16);
      const unsigned lbase = lds0 + hw * 1024;
      if (zd) voy[5] = (lane & 7) * 16;
      auto stage = [&](int n, int slot) {
        const char* yp = Yx + (((n + qx * 4) & 31) << 7);
        const unsigned l = lbase + slot * SL;
#pragma unroll
        for (int i = 0; i < 11; ++i) GLDS(i == 5 && zd ? lbase + ZSC : l + i * 2048, voy[i], yp);
      };
      asm volatile("s_waitcnt vmcnt(%3)" ::"v"(voy[0]), "v"(voy[10]), "v"(tag0), "n"(0) : "memory");
      BARRIER();
#pragma unroll 1
      for (int s = 0; s <= A_LD; ++s) stage(s, s);
      WAIT_V(11 * A_LD);
      TS(38);
      BARRIER();
      int slot = A_LD + 1;
#pragma unroll 1
      for (int k = 1; k <= 31 - A_LD; ++k) {
        WAIT_V(11 * (A_LD - 1));
        BARRIER();
        if (hw == 1) TSD(k);
        stage(k + A_LD, slot);
        slot = slot == NS - 1 ? 0 : slot + 1;
        if (k == CPF_K && hw == 1 && lane < CPF_N) PFL(cpf);
      }
      if (A_LD > 2) { WAIT_V(22); BARRIER(); }
      WAIT_V(0); BARRIER();
      const unsigned* ufp = ufl + (KT(min(lane >> 2, 6) + 1) * 4 + (lane & 3)) * 32;
      unsigned miss = ~0u;
      const char* y1 = Ug + KT(1) * 2 * SL + hw * 11264;
      asm volatile("" : "+s"(y1), "+v"(ufp));
      BARRIER();
      TS(47);
      if (U_D) __builtin_amdgcn_s_sleep(U_D);
      auto UPOLL = [&](unsigned need, unsigned em0) __attribute__((always_inline)) {
        POLL { PDBG(it);
          if ((miss & need) == 0) break;
          if (it) __builtin_amdgcn_s_sleep(U_R);
          unsigned f;
          const u64 em = miss & em0;
          asm volatile("s_mov_b64 exec, %2\n\tglobal_load_dword %0, %1, off sc1\n\ts_mov_b64 exec, -1\n\ts_waitcnt vmcnt(0)" : "=&v"(f) : "v"(ufp), "s"(em) : "memory");
          miss &= ~(unsigned)em | (unsigned)__builtin_amdgcn_ballot_w64(f == SENT);
        }
      };
#define G1(o) "global_load_lds_dwordx4 %0, %1 offset:" #o " sc1\n\t"
#define ULD(yp, l)                                                                                                                          \
  do {                                                                                                                                      \
    asm volatile("s_mov_b32 m0, %2\n\ts_nop 0\n\t" G1(0) G1(1024) G1(2048) G1(3072)::"v"(lane * 16), "s"(yp), "s"(l) : "memory");            \
    asm volatile("s_mov_b32 m0, %2\n\ts_nop 0\n\t" G1(0) G1(1024) G1(2048) G1(3072)::"v"(lane * 16 + 4096), "s"(yp), "s"((l) + 4096) : "memory"); \
    asm volatile("s_mov_b32 m0, %2\n\ts_nop 0\n\t" G1(0) G1(1024) G1(2048)::"v"(lane * 16 + 8192), "s"(yp), "s"((l) + 8192) : "memory");     \
  } while (0)
      {
        UPOLL(3u, 15u);
        TS(48);
        ULD(y1, lds0 + 4 * SL + hw * 11264);
        UPOLL(15u, 15u);
        unsigned f;
        asm volatile("global_load_dword %0, %1, off sc1" : "=v"(f) : "v"(ufp) : "memory");
        ULD(y1 + SL, lds0 + hw * 11264);
        asm volatile("s_waitcnt vmcnt(11)" : "+v"(f)::"memory");
        BARRIER();
        miss = (unsigned)__builtin_amdgcn_ballot_w64(f == SENT);
      }
      slot = 1;
#pragma unroll 1
      for (int n = 4; n < 16; ++n) {
        UPOLL(3u << (2 * n - 4), ~0u);
        const char* yp = Ug + (KT(n >> 1) * 2 + (n & 1)) * SL + hw * 11264;
        unsigned l = lds0 + slot * SL + hw * 11264;
        slot = slot == NS - 1 ? 0 : slot + 1;
        if (n == CPF_B && hw == 1 && lane < CPF_N) PFL(cpf);
        asm volatile("" : "+s"(yp), "+s"(l));
        if (n >= 6) {
          WAIT_V(22);
          BARRIER();
          if (hw == 1) TSD(n + 44);
        }
        ULD(yp, l);
      }
      WAIT_V(22);
      BARRIER();
      WAIT_V(0);
      BARRIER();
      BARRIER();
      BARRIER();
      if (b < 56 && tid == 256) ST4(reinterpret_cast<unsigned*>(ws + WS_WAK) + b * 32, 1u);
      TSE();
      return;
    } else {
      const int aw = wid - 6, rp = min(mt * 2 + aw, 10);
      PBAR(0);
      XFLAG();
      if (zd) {
#pragma unroll
        for (int s = 0; s < NS; ++s) *reinterpret_cast<u32x4*>(shm + s * SL + 10240 + aw * 1024 + lane * 16) = u32x4{0u, 0u, 0u, 0u};
      }
      LBAR();
      float rs[4] = {};
      const unsigned ra = lds0 + rp * 2048 + lane * 16;
      unsigned so = 0;
#define RSUM()                                                                                                                        \
  do {                                                                                                                                \
    u32x4 R_[2];                                                                                                                      \
    asm volatile("ds_read_b128 %0, %2\n\tds_read_b128 %1, %2 offset:1024\n\ts_waitcnt lgkmcnt(0)" : "=&v"(R_[0]), "=&v"(R_[1]) : "v"(ra + so) : "memory"); \
    _Pragma("unroll") for (int j = 0; j < 8; ++j) {                                                                                   \
      asm volatile("v_add_f32 %0, %0, %1" : "+v"(rs[(j >> 2) * 2]) : "v"(R_[j >> 2][j & 3] << 16));                                   \
      asm volatile("v_add_f32 %0, %0, %1" : "+v"(rs[(j >> 2) * 2 + 1]) : "v"(R_[j >> 2][j & 3] & 0xffff0000u));                       \
    }                                                                                                                                 \
    so = so == (NS - 1) * SL ? 0 : so + SL;                                                                                           \
  } while (0)
      {
#pragma unroll 1
        for (int bi = 0; bi < F_AT; ++bi) {
          RSUM();
          BARRIER();
        }
        u32x4 f;
#define FLD() asm volatile("global_load_dwordx4 %0, %1, %2 sc1" : "=v"(f) : "v"(lane * 16), "s"(ws + WS_XF2 + aw * 1024) : "memory")
        FLD();
#pragma unroll 1
        for (int bi = F_AT; bi < 4; ++bi) {
          RSUM();
          BARRIER();
        }
        POLL { PDBG(it);
          asm volatile("s_waitcnt vmcnt(0)" : "+v"(f)::"memory");
          if (__all(mk64(f[1], f[0]) == tag && mk64(f[3], f[2]) == tag)) break;
          __builtin_amdgcn_s_sleep(1);
          FLD();
        }
        asm volatile("s_waitcnt vmcnt(0)" : "+v"(f)::"memory");
      }
      const unsigned xo = (unsigned)(((qx * 512 + aw * 4 + g4) * D + mt * 128) * 2 + ((l16 ^ (aw * 4 + g4)) << 4));
#pragma unroll 1
      for (int bi = 4; bi < H_AT; ++bi) {
        RSUM();
        const int i = bi - X_AT;
        if (i >= 0 && i < 12) GLDS(lds0 + XR + i * 2048 + aw * 1024, xo, x + (PROW(5 + i / 2) + (i & 1) * 8) * D);
        BARRIER();
      }
      {
        u64 f;
        const unsigned* fp = hfl + lane * 2;
        asm volatile("global_load_dwordx2 %0, %1, off sc1" : "=v"(f) : "v"(fp) : "memory");
        RSUM();
        BARRIER();
        RSUM();
        BARRIER();
        POLL { PDBG(it);
          asm volatile("s_waitcnt vmcnt(0)" : "+v"(f)::"memory");
          if (__all(f == tag)) break;
          __builtin_amdgcn_s_sleep(1);
          asm volatile("global_load_dwordx2 %0, %1, off sc1" : "=v"(f) : "v"(fp) : "memory");
        }
        asm volatile("s_waitcnt vmcnt(0)" : "+v"(f)::"memory");
        unsigned c;
        const unsigned* cp = modv + 1024 + mt * 128 + aw * 64 + lane;
        asm volatile("global_load_dword %0, %1, off sc1" : "=v"(c) : "v"(cp) : "memory");
        RSUM();
        BARRIER();
        RSUM();
        BARRIER();
        POLL { PDBG(it);
          asm volatile("s_waitcnt vmcnt(0)" : "+v"(c)::"memory");
          if (__all(c != SENT)) break;
          __builtin_amdgcn_s_sleep(1);
          asm volatile("global_load_dword %0, %1, off sc1" : "=v"(c) : "v"(cp) : "memory");
        }
        asm volatile("s_waitcnt vmcnt(0)" : "+v"(c)::"memory");
        *reinterpret_cast<unsigned*>(shm + SCA + (aw * 64 + lane) * 4) = c ^ (cc ? 0x80000000u : 0u);
      }
#pragma unroll 1
      for (int bi = H_AT + 4; bi < 32; ++bi) {
        RSUM();
        if (bi < 30) BARRIER();
      }
      unsigned* rsb = reinterpret_cast<unsigned*>(ws + WS_RS) + qx * 512;
      {
        const float v0 = dpp_add<0x141>(dpp_add<0x4E>(dpp_add<0xB1>(rs[0] + rs[1])));
        const float v1 = dpp_add<0x141>(dpp_add<0x4E>(dpp_add<0xB1>(rs[2] + rs[3])));
        if ((lane & 7) < 2 && mt * 2 + aw < 11 && !(nt == 2 && rp == 5)) ST4(rsb + cc * S + PROW(rp) + (lane & 1) * 8 + (lane >> 3), nsent((lane & 1) ? v1 : v0));
      }
      BARRIER();
#pragma unroll 1
      for (int k = 34; k <= (aw ? B_VAT : 46); ++k) BARRIER();
      u32x2 m[6];
      const unsigned lr = min(lane, 47);
      const unsigned o0 = aw ? (unsigned)(WS_Q - WS_MOD) + mt * 512 + lane * 8 : 8192u + mt * 512 + lane * 8;
      const unsigned o1 = aw ? (unsigned)(WS_RS - WS_MOD) + (qx * 512 + PROW(5 + (lr >> 3)) + (lr & 7) * 2) * 4 : o0 + 8192;
      const unsigned o2 = aw ? o1 + 8192 : o0 + 16384, o3 = aw ? o1 : 28672u + min(lane, 23) * 8;
#define VLD()                                                                                                                     \
  asm volatile("global_load_dwordx2 %0, %6, %10 sc1\n\tglobal_load_dwordx2 %1, %6, %11 sc1\n\tglobal_load_dwordx2 %2, %7, %10 sc1\n\t" \
               "global_load_dwordx2 %3, %7, %11 sc1\n\tglobal_load_dwordx2 %4, %8, %10 sc1\n\tglobal_load_dwordx2 %5, %9, %10 sc1"    \
               : "=&v"(m[0]), "=&v"(m[1]), "=&v"(m[2]), "=&v"(m[3]), "=&v"(m[4]), "=&v"(m[5])                                     \
               : "v"(o0), "v"(o1), "v"(o2), "v"(o3), "s"(modv), "s"(modv + 1024)                                                  \
               : "memory")
      VLD();
      if (aw) {
#pragma unroll
        for (int k = B_VAT + 1; k <= 45; ++k) BARRIER();
      }
      TS(30);
      POLL { PDBG(it);
        asm volatile("s_waitcnt vmcnt(0)" : W6(m)::"memory");
        if (__all(max(max(max(max2(m[0]), max2(m[1])), max(max2(m[2]), aw ? 0u : max2(m[3]))), max(max2(m[4]), max2(m[5]))) != SENT)) break;
        __builtin_amdgcn_s_sleep(2);
        VLD();
      }
      asm volatile("s_waitcnt vmcnt(0)" : W6(m)::"memory");
      if (aw == 0) {
        *reinterpret_cast<u32x2*>(shm + TGA + lane * 8) = m[0];
        *reinterpret_cast<unsigned*>(shm + TV + lane * 4) = hi2(m[2]);
        *reinterpret_cast<unsigned*>(shm + TV + 256 + lane * 4) = hi2(m[1]);
        *reinterpret_cast<unsigned*>(shm + TV + 512 + lane * 4) = hi2(m[3]);
        const float p0 = __uint_as_float(m[4][0]), p1 = __uint_as_float(m[4][1]);
        const unsigned hh = pack2(p0, p1);
        *reinterpret_cast<unsigned*>(shm + SCW + lane * 4) = hh;
        *reinterpret_cast<unsigned*>(shm + SCW + 256 + lane * 4) = pack2(p0 - bflo(hh), p1 - bfhi(hh));
        *reinterpret_cast<u32x2*>(shm + SCW + 512 + lane * 8) = u32x2{0x3f803f80u, 0x3f803f80u};
        const float u = __uint_as_float(m[5][0]) + __uint_as_float(m[5][1]), u2 = dpp_add<0xB1>(u), u8 = dpp_add<0x141>(dpp_add<0x4E>(u2));
        if (lane < 17 && !(lane & 1)) *reinterpret_cast<float*>(shm + TSUM + lane * 2) = lane < 16 ? u2 : u8;
      } else {
        BARRIER();
#pragma unroll
        for (int j = 0; j < 2; ++j) {
          const float r0 = __uint_as_float(m[0][j]), r1 = __uint_as_float(m[1][j]);
          const unsigned a0 = pack2(r0, r0), a1 = pack2(r1, r1);
          *reinterpret_cast<u32x4*>(shm + RKA + ((lane >> 4) * 32 + ((lane >> 1) & 1) * 16 + ((lane >> 2) & 3) * 4 + (lane & 1) * 2 + j) * 16) =
              u32x4{a0, pack2(r0 - bflo(a0), r0 - bflo(a0)), a1, pack2(r1 - bflo(a1), r1 - bflo(a1))};
          const float s0 = __uint_as_float(m[2][j]), s1 = -__uint_as_float(m[4][j]);
          const unsigned b0 = pack2(s0, s0 - rbf(s0)), b1 = pack2(s1, s1 - rbf(s1));
          *reinterpret_cast<u32x4*>(shm + RKB + (lr * 2 + j) * 16) = u32x4{b0, b0, b1, b1};
        }
        *reinterpret_cast<u32x2*>(shm + RKZ + lane * 8) = u32x2{0u, 0u};
        const unsigned one = g4 == (l16 >> 2) ? ((lane & 1) ? 0x3f800000u : 0x3f80u) : 0u, e0 = (lane & 2) ? 0u : one, e1 = (lane & 2) ? one : 0u;
        *reinterpret_cast<u32x4*>(shm + IDT + lane * 16) = u32x4{e0, e1, 0u, 0u};
        *reinterpret_cast<u32x4*>(shm + IDT + 1024 + lane * 16) = u32x4{0u, 0u, e0, e1};
      }
      TS(17);
      LBAR();
    }
    const int row0 = rg * 16 + l16, col1 = mt * 128 + g4 * 8;
    u32x4 xq[4], sa4[4];
#pragma unroll
    for (int q = 0; q < 4; ++q) DSR(sa4[q], saa, q * 64);
    f32x2 cj = *reinterpret_cast<const f32x2*>(shm + TSUM + g4 * 8);
    float sw = *reinterpret_cast<const float*>(shm + TSUM + 32), ce = *reinterpret_cast<const float*>(shm + TSUM + mt * 4);
    asm volatile("" : "+v"(cj), "+v"(sw), "+v"(ce));
    if (wid < 4) {
      auto X1A = [&](auto kc) __attribute__((always_inline)) {
        constexpr int k = decltype(kc)::value;
        (void)acc, (void)idt, (void)pv;
        MFMA(acc[0][5 + k], idt[0], pv[k]);
        MFMA(acc[1][5 + k], idt[1], pv[k]);
      };
      auto X1B = [&](auto kc) __attribute__((always_inline)) {
        constexpr int j = decltype(kc)::value;
        (void)acc, (void)ga, (void)x5, (void)xw1;
        asm volatile("" : "+v"(acc[0][5 + j]), "+v"(acc[1][5 + j]));
        const u32x4 v = pk8((j < 5 ? acc[0][j % 5] : x5[0]) + ga[0] * acc[0][5 + j], (j < 5 ? acc[1][j % 5] : x5[1]) + ga[1] * acc[1][5 + j]);
        asm volatile("ds_write_b128 %0, %1 offset:%2" ::"v"(xw1), "v"(v), "n"(j * 4096) : "memory");
      };
      asm volatile("s_waitcnt vmcnt(2)" : "+v"(pv[2]), "+v"(pv[3]), "+v"(pv[4]), "+v"(pv[5])::"memory");
      POLL { PDBG(it);
        if (__all(max(max(max4(pv[2]), max4(pv[3])), max(max4(pv[4]), max4(pv[5]))) != SENT)) break;
        WAIT_V(0);
        __builtin_amdgcn_s_sleep(X_R);
        XCHLD();
        asm volatile("s_waitcnt vmcnt(0)" : W6(pv)::"memory");
      }
      X1A(IC(5)); X1A(IC(4)); X1A(IC(3)); X1B(IC(5)); X1A(IC(2)); X1B(IC(4)); X1B(IC(3));
      asm volatile("s_waitcnt vmcnt(0)" : "+v"(pv[0]), "+v"(pv[1])::"memory");
      POLL { PDBG(it);
        if (__all(max(max4(pv[0]), max4(pv[1])) != SENT)) break;
        __builtin_amdgcn_s_sleep(X_R);
        asm volatile("global_load_dwordx4 %0, %2, %3 offset:2048 sc1\n\tglobal_load_dwordx4 %1, %2, %3 offset:1024 sc1\n\ts_waitcnt vmcnt(0)"
                     : "=&v"(pv[0]), "=&v"(pv[1]) : "v"(lane * 16), "s"(xr) : "memory");
      }
      TS(27);
      X1A(IC(1)); X1A(IC(0)); X1B(IC(2)); X1B(IC(1)); X1B(IC(0));
    }
    {
      TS(6);
      LBAR();
      asm volatile("ds_read_b128 %0, %8\n\tds_read_b128 %1, %8 offset:1024\n\tds_read_b128 %2, %8 offset:2048\n\tds_read_b128 %3, %8 offset:3072\n\ts_waitcnt lgkmcnt(0)"
                   : "=&v"(xq[0]), "=&v"(xq[1]), "=&v"(xq[2]), "=&v"(xq[3]), "+v"(sa4[0]), "+v"(sa4[1]), "+v"(sa4[2]), "+v"(sa4[3])
                   : "v"(xa)
                   : "memory");
      if (!act) {
        TSE();
        return;
      }
      f32x4 s4 = {0.f, 0.f, 0.f, 0.f}, gr = {0.f, 0.f, 0.f, 0.f};
#pragma unroll
      for (int q = 0; q < 4; ++q) {
        gr = MM(xq[q], xq[q], gr);
        s4 = MM(sa4[q], xq[q], s4);
      }
      const u32x4 gd = __builtin_bit_cast(u32x4, gr) & dk;
      const float m = s4[2] * (1.0f / 128);
      float q2 = ar4(__uint_as_float((gd[0] | gd[1]) | (gd[2] | gd[3]))) - s4[2] * m, lg = (s4[0] + s4[1]) - m * ce;
      if (__any(!(s4[2] * m <= 256.0f * q2))) {
        f32x4 a = {0.f, 0.f, 0.f, 0.f}, l = {0.f, 0.f, 0.f, 0.f};
#pragma unroll
        for (int q = 0; q < 4; ++q) {
          const u32x4 ch = *reinterpret_cast<const u32x4*>(shm + SCW + g4 * 16 + q * 64), cl = *reinterpret_cast<const u32x4*>(shm + SCW + 256 + g4 * 16 + q * 64);
          const f32x4 d0 = lo4(xq[q]) - m, d1 = hi4(xq[q]) - m;
          a += d0 * d0 + d1 * d1;
          l += d0 * (lo4(ch) + lo4(cl)) + d1 * (hi4(ch) + hi4(cl));
        }
        q2 = ar4((a[0] + a[1]) + (a[2] + a[3]));
        lg = ar4((l[0] + l[1]) + (l[2] + l[3]));
      }
      const u32x4 own = {nsent(m), nsent(q2), nsent(lg), 0u};
      if (g4 == 0) ST16(sto, own, ws + WS_STAT, 0);
      TS(7);
      SKEWPT();
      const bool k0 = g4 * 2 == mt, k1 = g4 * 2 + 1 == mt;
#define STOK() (sr[0] = k0 ? own : sr[0], sr[1] = k1 ? own : sr[1], __all(max(max(max(sr[0][0], sr[0][1]), sr[0][2]), max(max(sr[1][0], sr[1][1]), sr[1][2])) != SENT))
      const bool pub = mt == (rg & 7);
      if (OPT_SD) __builtin_amdgcn_s_sleep(OPT_SD);
      POLL { PDBG(it);
        STLD();
        asm volatile("s_waitcnt vmcnt(0)" : "+v"(sr[0]), "+v"(sr[1])::"memory");
        if (STOK()) break;
        __builtin_amdgcn_s_sleep(1);
      }
      TS(9);
      const f32x2 mk = {__uint_as_float(sr[0][0]), __uint_as_float(sr[1][0])};
      const f32x2 qk = {__uint_as_float(sr[0][1]), __uint_as_float(sr[1][1])};
      const f32x2 lk = {__uint_as_float(sr[0][2]), __uint_as_float(sr[1][2])};
      const float mean = ar4(mk[0] + mk[1]) * (1.0f / 8);
      const f32x2 dm = mk - mean;
      const f32x2 a = qk + 128.0f * (dm * dm), l = lk + dm * cj;
      const float vf = ar4(a[0] + a[1]) * (1.0f / D);
      const float rstd = __builtin_amdgcn_rsqf(vf + 1e-5f);
      const float rd = rstd * ar4(l[0] + l[1]) + sw;
      const unsigned rb = f2bf(rd);
      const unsigned ord = rb ^ ((rb & 0x8000u) ? 0xffffu : 0x8000u);
      const unsigned key = (ord << 16) | (0xffffu - (unsigned)row0);
      unsigned t1 = key, t2 = 0u;
      TOP2(0xB1); TOP2(0x4E); TOP2(0x141); TOP2(0x140);
      if (lane == 0 && pub) ST4(reinterpret_cast<u64*>(ws + WS_KEY) + rg * 16, mk64(~t2, ~t1));
      TS(1);
      SKEWPT();
      if (key >= t2) {
        const int sl = rg * 2 + (key == t2);
#pragma unroll
        for (int q = 0; q < 4; ++q) ST16(sl * 2048 + col1 * 2, nsent4(xq[q]), ws + WS_CAND, q * 64);
        if (g4 == 0 && pub) ST16(sl * 16, (u32x4{nsent(mean), nsent(rstd), 0u, 0u}), ws + WS_HDR, 0);
      }
      TS(8);
      if (OUT_D) __builtin_amdgcn_s_sleep(OUT_D);
#pragma unroll
      for (int q = 0; q < 4; ++q) {
        const u32x4 cq = *reinterpret_cast<const u32x4*>(shm + TV + q * 64 + g4 * 16), sbq = *reinterpret_cast<const u32x4*>(shm + TV + 256 + q * 64 + g4 * 16);
        const u32x4 gbq = *reinterpret_cast<const u32x4*>(shm + TV + 512 + q * 64 + g4 * 16);
        const f32x4 x0 = lo4(xq[q]), x1 = hi4(xq[q]);
        const f32x4 h0 = ((x0 - mean) * rstd) * lo4(cq) + lo4(sbq), h1 = ((x1 - mean) * rstd) * hi4(cq) + hi4(sbq);
        ST16((row0 * D + col1) * 2, pk8(x0 + lo4(gbq) * h0, x1 + hi4(gbq) * h1), out, q * 64);
      }
      asm volatile(".Lcpf_e2:");
      WAIT_V(0);
      if (lane == 0) ST4(reinterpret_cast<unsigned*>(ws + WS_DON) + (rg * 8 + mt) * 2, 1u);
      TS(16);
    }
    TSE();
    return;
  }
  {
    const int hid = b - 192, hi = j8 - 24;
    constexpr int RNG = 65536, SSI = MISC, GP = MISC + 2048, SSA = MISC + 3072, HS = MISC + 5120, SX1 = MISC + 9216, ACT = MISC + 13312,
                  HFLG = MISC + 13568, RR = MISC + 13824;
    unsigned* flg = reinterpret_cast<unsigned*>(shm + HFLG);
    const int ec = xcd * 7 + hi, hd = hi == 7 ? 7 : (hi + 7 - ((3 + xcd) & 7) % 7) % 7;
    const bool pon = wid < ((0x06555542 >> (4 * hd)) & 7);
    const char* pfh = PFG((hd < 2 ? 2 * hd : 5 * hd - 4) + wid, 8 - qx);
#define HPF(g) do { if (pon) PFL(pfh + ((((g) + qx) & 7) << 9)); } while (0)
    WHLD(4);
    const int cb0 = xcd * 8 + ((hd + 2) & 7);
    const unsigned om = (unsigned)((wid * 16 + (lane >> 3)) * 12288 + (lane & 7) * 16);
    const unsigned og = (unsigned)((wid * 8 + (lane >> 3)) * 7168 + (lane & 7) * 16);
    const unsigned od = (unsigned)((wid * 8 + (lane >> 3)) * 2048 + (lane & 7) * 16);
    const char* pm = reinterpret_cast<const char*>(wmod) + cb0 * 128;
    const char* pg = reinterpret_cast<const char*>(wg) + ec * 128;
    const char* pu = reinterpret_cast<const char*>(wu) + ec * 128;
    const char* pd = reinterpret_cast<const char*>(wd) + (size_t)ec * 131072;
    const unsigned ow = (unsigned)(((wid & 1) * 16 + (lane >> 3)) * 12288 + (wid >> 1) * 128 + (lane & 7) * 16);
    const char* pw = reinterpret_cast<const char*>(wmod) + 8192 + xcd * 512;
#define RQM(r)                                                                                   \
  do {                                                                                           \
    const char* p_ = pm + (size_t)(r)*1572864;                                                   \
    GLDSP(lds0 + RNG + ((r)&3) * 16384 + wid * 2048, om, p_, WPOL);                              \
    GLDSP(lds0 + RNG + ((r)&3) * 16384 + wid * 2048 + 1024, om, p_ + 98304, WPOL);               \
  } while (0)
#define RQE(r)                                                                                                                \
  do {                                                                                                                        \
    GLDSP(lds0 + RNG + ((r)&3) * 16384 + wid * 1024, (r) < 16 ? og : od, (r) < 16 ? pg + (r)*458752 : pd + ((r)-16) * 256, WPOL);   \
    GLDSP(lds0 + RNG + ((r)&3) * 16384 + 8192 + wid * 1024, (r) < 16 ? og : od, (r) < 16 ? pu + (r)*458752 : pd + ((r)-16) * 256 + 128, WPOL); \
  } while (0)
    PBAR(4);
    XFLAG();
    const u32x4 sv = {SENT, SENT, SENT, SENT};
    auto PUBS = [&](int cb, unsigned v, unsigned p, unsigned r) __attribute__((always_inline)) {
      const int vi = cb >> 4, col = (cb & 15) * 64 + lane;
      ST4(modv + cb * 64 + lane, v);
      if (vi == 4) ST4(modv + 6144 + col, p);
      if (vi == 4 ? (lane & 31) == 0 : vi == 3 && lane == 0) ST4(modv + 7168 + (vi == 4 ? col >> 5 : 32 + (cb & 15)), r);
    };
    if (wid == 4 || (hi == 7 && wid < 4)) PUBS(wid == 4 ? cb0 : 64 + xcd * 4 + wid, SENT, SENT, SENT);
    if (tid >= 480) ST4(reinterpret_cast<unsigned*>(ws + WS_Q) + hsl * 32 + tid - 480, SENT);
    unsigned cv, wv;
    asm volatile("global_load_dword %0, %2, %3\n\tglobal_load_dword %1, %2, %4" : "=&v"(cv), "=&v"(wv) : "v"(tid * 4), "s"(cvec), "s"(wr_) : "memory");
    if (hi < 7) {
      if (PF_N < 3) HPF(2);
      HPF(3);
      const char* ap = ws + WS_ARM + (size_t)(ec * 8 + wid) * 3328;
#pragma unroll
      for (int j = 0; j < 4; ++j) ST16(lane * 16, sv, ap, j < 3 ? j * 1024 : 2304);
      HPF(4);
      WAIT_V(4);
    } else {
      WAIT_V(0);
    }
    asm volatile("; AUDIT_OK %0 %1" : "+v"(cv), "+v"(wv)::"memory");
    *reinterpret_cast<unsigned*>(shm + SSI + tid * 4) = pack2(silu_bf(bflo(cv)), silu_bf(bfhi(cv)));
    *reinterpret_cast<unsigned*>(shm + SX1 + tid * 4) = wv;
    LBAR();
    TS(50);
    const unsigned ha = (unsigned)(((l16 >> 2) * 8 + (lane & 3)) * 2048 + wid * 256);
#define HFR(j) *reinterpret_cast<const u32x4*>(shm + ha + ((j)&1) * 8192 + ((j) >> 2) * 128 + (((((j) >> 1) & 1) * 4 + g4) ^ (lane & 7)) * 16)
    HPF(5);
#pragma unroll
    for (int j = 0; j < 8; ++j) ST16(lane * 16, HFR(j), ws + WS_WH + (size_t)hsl * 65536 + wid * 8192 + 4096, j * 1024 - 4096);
    HPF(6);
    RQM(0);
    RQM(1);
    RQM(2);
    const unsigned tm = lds0 + RNG + ((wid >> 2) * 64 + g4 * 8 + ((lane >> 2) & 3)) * 128 + (wid & 3) * 32 + (lane & 3) * 8;
    f32x4 ma = {0.f, 0.f, 0.f, 0.f};
#define GVR(i)                                                                                                                               \
  do {                                                                                                                                       \
    _Pragma("unroll") for (int f = 0; f < 2; ++f)                                                                                            \
        ma = MM(trd(tm + ((i)&3) * 16384 + f * 4096), *reinterpret_cast<const u32x4*>(shm + SSI + ((i)*128 + (wid >> 2) * 64 + f * 32 + g4 * 8) * 2), ma); \
    if ((i) == 7 && l16 == 0) *reinterpret_cast<f32x4*>(shm + GP + wid * 64 + g4 * 16) = ma;                                                 \
  } while (0)
    const float* gp = reinterpret_cast<const float*>(shm + GP);
    auto PUB = [&](int cb, float s) __attribute__((always_inline)) {
      const int vi = cb >> 4;
      const float v = rbf(((vi == 1 || vi == 4) ? 1.0f : 0.0f) + rbf(s));
      const float p = v * bf2f(*reinterpret_cast<const u16*>(shm + SX1 + ((cb & 15) * 64 + lane) * 2));
      const float r16 = row_sum(p), r32 = swap16_add(r16, r16);
      PUBS(cb, nsent(v), nsent(p), nsent(vi == 4 ? r32 : swap32_add(r32, r32)));
    };
    {
      sfor<8>([&](auto ii) __attribute__((always_inline)) {
        constexpr int i = decltype(ii)::value;
        (void)ow, (void)pw, (void)om, (void)pm, (void)og, (void)od, (void)pg, (void)pu, (void)pd, (void)lds0, (void)wid, (void)pfl, (void)Yx, (void)l16, (void)g4, (void)xcd, (void)hfl, (void)lane, (void)hid;
        TSCAP();
        WAIT_V(4);
        LBAR();
        if (wid == 2) TS(i);
        if constexpr (i == 0) { if (tid == 0) ST4(reinterpret_cast<u64*>(hfl + hid * 2), tag); }
        if constexpr (i == 0) HPF(7);
        if constexpr (i < 5) {
          RQM(i + 3);
        } else {
          const bool e = hid < 56;
          const char* p_ = pw + (i - 5) * 393216;
          GLDSP(lds0 + RNG + (i - 5) * 16384 + wid * (e ? 1024 : 2048), e ? og : ow, e ? pg + (i - 5) * 458752 : p_, WPOL);
          GLDSP(lds0 + RNG + (i - 5) * 16384 + wid * (e ? 1024 : 2048) + (e ? 8192 : 1024), e ? og : ow, e ? pu + (i - 5) * 458752 : p_ + 98304, WPOL);
        }
        GVR(i);
      });
    }
    TS(51);
#define PUB0() do { if (wid == 0) PUB(cb0, gp[lane] + gp[64 + lane]); } while (0)
    u64 sf, hf;
#define SFLD()                                                                                                                     \
  asm volatile("s_mov_b64 exec, 3\n\tglobal_load_dwordx2 %0, %2, %3 sc1\n\ts_mov_b64 exec, 0xff\n\tglobal_load_dwordx2 %1, %5, %4 sc1\n\ts_mov_b64 exec, -1" \
               : "=&v"(sf), "=&v"(hf) : "v"(lane * 128), "s"(ws + WS_SFL + wid * 256), "s"(ws + WS_HFL + wid * 64), "v"(lane * 8) : "memory")
    auto SAW = [&]() __attribute__((always_inline)) {
      POLL { PDBG(it);
        if (((__builtin_amdgcn_ballot_w64(sf != tag) & 3u) | (__builtin_amdgcn_ballot_w64(hf != tag) & 0xffu)) == 0) break;
        __builtin_amdgcn_s_sleep(4);
        SFLD();
        asm volatile("s_waitcnt vmcnt(0)" : "+v"(sf), "+v"(hf)::"memory");
      }
    };
    u32x2 s2;
#define SALD() asm volatile("global_load_dwordx2 %0, %1, %2 sc1" : "=v"(s2) : "v"(tid * 8), "s"(modv) : "memory")
    auto QMM = [&]() __attribute__((always_inline)) {
      *reinterpret_cast<unsigned*>(shm + SSA + tid * 4) = hi2(s2);
      f32x4 qa[2] = {};
#pragma unroll
      for (int j = 0; j < 8; ++j) qa[j & 1] = MM(HFR(j), *reinterpret_cast<const u32x4*>(shm + SSA + wid * 256 + (j >> 1) * 64 + g4 * 16), qa[j & 1]);
      if (l16 < 2) *reinterpret_cast<f32x4*>(shm + HS + wid * 128 + g4 * 32 + l16 * 16) = l16 ? qa[1] : qa[0];
    };
    auto QPUB = [&]() __attribute__((always_inline)) {
      if (tid >= 480) {
        float s = 0.f;
#pragma unroll
        for (int w = 0; w < 8; ++w) s += *reinterpret_cast<const float*>(shm + HS + w * 128 + (tid - 480) * 4);
        ST4(reinterpret_cast<unsigned*>(ws + WS_Q) + hsl * 32 + tid - 480, nsent(s));
      }
    };
    u32x8 GU[16], DN[8];
    if (hid < 56) {
      const unsigned tg = lds0 + RNG + (g4 * 8 + ((lane >> 2) & 3)) * 128 + ((lane >> 1) & 1) * 8192 + wid * 16 + (lane & 1) * 8;
      const unsigned td = lds0 + RNG + (g4 * 8 + ((lane >> 2) & 3)) * 128 + (wid >> 2) * 8192 + (wid & 3) * 32 + (lane & 3) * 8;
      sfor<24>([&](auto rr) __attribute__((always_inline)) {
        constexpr int r = decltype(rr)::value;
        (void)td, (void)tg, (void)GU, (void)DN, (void)og, (void)od, (void)pg, (void)pu, (void)pd, (void)lds0, (void)wid, (void)pfl, (void)Yx, (void)l16, (void)g4, (void)xcd;
        (void)s2, (void)modv, (void)tid;
        (void)hid, (void)lane, (void)sf, (void)hf;
        (void)sv, (void)hi, (void)ws;
#define EX(r) (((r) < 0 ? 0 : (r) < 2 ? 4 : (r) == 2 ? 2 : 0) + ((r) == Q_AT - 6 ? 2 : 0) + ((r) == Q_AT - 3))
        WAIT_V(r > 21 ? 2 * (23 - r) : 4 + EX(r - 2) + EX(r - 1));
        LBAR();
        TSCAP();
        if (wid == 3) TS(r);
        if constexpr (r == 0) PUB0();
        if constexpr (r == 1) {
          if (cb0 < 16 && wid == 0) {
            WAIT_V(2 + EX(0));
            if (lane == 0) ST4(reinterpret_cast<u64*>(ws + WS_SFL) + cb0 * 16, tag);
          }
        }
        if constexpr (r == Q_AT - 6) SFLD();
        if constexpr (r == Q_AT - 3) {
          asm volatile("" : "+v"(sf), "+v"(hf));
          SAW();
          SALD();
        }
        if constexpr (r < 3) {
          const char* ap = ws + WS_XCH + xcd * 589824 + (hi * 8 + wid) * 10240 + r * 4096;
#pragma unroll
          for (int j = 0; j < (r < 2 ? 4 : 2); ++j) ST16(lane * 16, sv, ap, j * 1024);
          if (r == 2 && wid < 3) ST16(lane * 16, sv, ws + WS_XCH + xcd * 589824 + min(560 + hi * 3 + wid, 575) * 1024, 0);
        }
        if constexpr (r < 21) RQE(r + 3);
        if constexpr (r < 16) {
          GU[r] = trd2(tg + (r & 3) * 16384);
          asm volatile("" : "+v"(GU[r]));
        } else {
          DN[r - 16] = trd2(td + (r & 3) * 16384);
          asm volatile("" : "+v"(DN[r - 16]));
        }
        if constexpr (r == Q_AT) {
          asm volatile("" : "+v"(s2));
          QMM();
        }
        if constexpr (r == Q_AT + 1) QPUB();
      });
      TS(52);
    } else {
      const unsigned tw = lds0 + RNG + (wid >> 1) * 4096 + (g4 * 8 + ((lane >> 2) & 3)) * 128 + (wid & 1) * 64 + (lane & 3) * 8;
      f32x4 mb[2] = {};
      auto WR = [&](int r0, int r1) __attribute__((always_inline)) {
#pragma unroll 1
        for (int r = r0; r < r1; ++r) {
          WAIT_V(4);
          LBAR();
          if (wid == 2 && r < 22) TSD(r + 8);
          const char* p_ = pw + min(r + 3, 31) * 393216;
          GLDSP(lds0 + RNG + ((r + 3) & 3) * 16384 + wid * 2048, ow, p_, WPOL);
          GLDSP(lds0 + RNG + ((r + 3) & 3) * 16384 + wid * 2048 + 1024, ow, p_ + 98304, WPOL);
          const u32x4 sf = *reinterpret_cast<const u32x4*>(shm + SSI + (r * 32 + g4 * 8) * 2);
          mb[0] = MM(trd(tw + (r & 3) * 16384), sf, mb[0]);
          mb[1] = MM(trd(tw + (r & 3) * 16384 + 32), sf, mb[1]);
        }
      };
      WR(0, 1);
      PUB0();
      WR(1, 4);
      if (cb0 < 16 && tid == 0) ST4(reinterpret_cast<u64*>(ws + WS_SFL) + cb0 * 16, tag);
      WR(4, MQ_AT - 6);
      SFLD();
      WR(MQ_AT - 6, MQ_AT - 3);
      asm volatile("; AUDIT_OK %0 %1" : "+v"(sf), "+v"(hf));
      SAW();
      SALD();
      WR(MQ_AT - 3, MQ_AT);
      asm volatile("; AUDIT_OK %0" : "+v"(s2));
      QMM();
      WR(MQ_AT, 32);
      if (l16 < 2) *reinterpret_cast<f32x4*>(shm + GP + wid * 128 + l16 * 64 + g4 * 16) = l16 ? mb[1] : mb[0];
      LBAR();
      if (wid < 4) PUB(64 + xcd * 4 + wid, gp[tid]);
      QPUB();
      TS(52);
      WAIT_V(0);
      TSE();
      return;
    }
    bool early = false;
    if (tid == 0) {
      POLL { PDBG(it);
        unsigned g;
        asm volatile("global_load_dword %0, %1, %2 sc1\n\ts_waitcnt vmcnt(0)" : "=&v"(g) : "v"(0u), "s"(ws + WS_WAK + hid * 128) : "memory");
        if (g != SENT) break;
        early = true;
        __builtin_amdgcn_s_sleep(OPT_KS);
      }
    }
    BARRIER();
    TS(21);
    const int tok = hid & 1, col = (hid & 16) * 32 + (lane & 3) * 128 + (hid & 14) * 8 + (lane >> 2);
    u32x4 c4, s4;
    unsigned gbu;
    POLL { PDBG(it);
      asm volatile("global_load_dwordx4 %0, %3, %5 sc1\n\tglobal_load_dwordx4 %1, %3, %6 sc1\n\tglobal_load_dword %2, %4, %7 sc1\n\ts_waitcnt vmcnt(0)"
                   : "=&v"(c4), "=&v"(s4), "=&v"(gbu) : "v"((tid & 255) * 16), "v"(col * 4), "s"(modv + 4096), "s"(modv + 3072), "s"(modv + 5120) : "memory");
      if (__all(max(max(max4(c4), max4(s4)), gbu) != SENT)) break;
      __builtin_amdgcn_s_sleep(8);
    }
    const f32x4 cb2 = __builtin_bit_cast(f32x4, c4), sb2 = __builtin_bit_cast(f32x4, s4);
    unsigned hra = lds0 + HS + g4 * 64 + (lane & 4) * 8 + (lane & 1) * 16, hwa = lds0 + HS + ((tid & 255) >> 2) * 64 + (wid >> 2) * 32 + (tid & 3) * 4;
    unsigned pto = (g4 & 1) * 4096 + ((g4 >> 1) * 8 + wid) * 256 + l16 * 16, six = (lane & 1) ? 0xEEEEu : 0x4444u;
    unsigned awa = lds0 + ACT + (wid >> 1) * 64 + g4 * 32 + (lane & 2) * 8 + (wid & 1) * 8 + (lane & 4) + (lane & 1) * 2;
    f32x4 d = {0.f, 0.f, 0.f, 0.f};
    asm volatile("" : "+v"(hra), "+v"(hwa), "+v"(pto), "+v"(awa), "+v"(six), "+v"(d));
    const float gb = __uint_as_float(gbu);
    if (wid == 0) {
      u32x2 g0 = {SENT, SENT}, g1 = {SENT, SENT};
      const char* gp = ws + WS_KEY + lane * 128;
      if (__any(early)) __builtin_amdgcn_s_sleep(OPT_KD);
      TS(20);
      POLL { PDBG(it);
        if (max2(g0) == SENT) asm volatile("global_load_dwordx2 %0, %1, off sc1" : "+v"(g0) : "v"(gp) : "memory");
        if (max2(g1) == SENT) asm volatile("global_load_dwordx2 %0, %1, off sc1" : "+v"(g1) : "v"(gp + 8192) : "memory");
        asm volatile("s_waitcnt vmcnt(0)" : "+v"(g0), "+v"(g1)::"memory");
        if (__all(max(max2(g0), max2(g1)) != SENT)) break;
        __builtin_amdgcn_s_sleep(1);
      }
      g0 = ~g0;
      g1 = ~g1;
      const unsigned a1 = max(g0[0], g1[0]), a2 = max(min(g0[0], g1[0]), max(g0[1], g1[1]));
      const unsigned k1 = wave_max_u(a1);
      const unsigned k2 = wave_max_u(a1 == k1 ? a2 : a1);
      if (lane == 0) *reinterpret_cast<u32x2*>(flg) = u32x2{k1, k2};
      TS(22);
    }
    LBAR();
    const unsigned k1 = flg[0], k2 = flg[1];
    {
      const int rg1 = key_row(k1) >> 4, rg2 = key_row(k2) >> 4;
      const int sl = wid >= 4 ? rg2 * 2 + (rg2 == rg1) : rg1 * 2;
      u32x2 v, hd;
      POLL { PDBG(it);
        asm volatile("global_load_dwordx2 %0, %2, %4 sc1\n\tglobal_load_dwordx2 %1, %3, %5 sc1\n\ts_waitcnt vmcnt(0)"
                     : "=&v"(v), "=&v"(hd) : "v"(sl * 2048 + (tid & 255) * 8), "v"(sl * 16), "s"(ws + WS_CAND), "s"(ws + WS_HDR) : "memory");
        if (__all(max(max2(v), max2(hd)) != SENT)) break;
        __builtin_amdgcn_s_sleep(1);
      }
      TS(23);
      const float mean = __uint_as_float(hd[0]), rstd = __uint_as_float(hd[1]);
      const f32x4 h = ((f32x4{bflo(v[0]), bfhi(v[0]), bflo(v[1]), bfhi(v[1])} - mean) * rstd) * cb2 + sb2;
      asm volatile("ds_write2_b32 %0, %1, %2 offset1:4" ::"v"(hwa), "v"(pack2(h[0], h[1])), "v"(pack2(h[2], h[3])) : "memory");
      *reinterpret_cast<u32x2*>(shm + SX1 + tid * 8) = v;
    }
    SKEWPT();
    TS(26);
    LBAR();
    TS(27);
    f32x4 dn[8];
    {
      u32x4 hb[8];
      sfor<23>([&](auto ff) __attribute__((always_inline)) {
        constexpr int f = decltype(ff)::value, c = f - 7;
        (void)d, (void)hb, (void)GU, (void)hra, (void)six;
        if constexpr (c >= 0) {
          WAIT_L(f <= 16 ? 6 : 22 - f);
          SMF(d, hb[c & 7], GU[c], six);
        }
        if constexpr (f < 16) DSR(hb[f & 7], hra, f * 256);
      });
      asm volatile("s_nop 7" : "+v"(d));
      TS(29);
      const float dv = rbf(d[0] + d[1]), uv = dpp_f<0x128>(dv);
      const unsigned av = f2bf(rbf(dv * __builtin_amdgcn_rcpf(1.0f + __expf(-dv))) * uv);
      asm volatile("s_mov_b64 exec, 0xff00ff\n\tds_write_b16 %0, %1\n\ts_mov_b64 exec, -1" ::"v"(awa), "v"(av) : "memory");
    }
#pragma unroll
    for (int c = 0; c < 8; ++c) asm volatile("v_mov_b64 %0, 0" : "=v"(dn[c].lo));
    asm volatile("" : W8(dn));
    TS(24);
    LBAR();
    {
      u32x4 a0;
      asm volatile("ds_read_b128 %0, %1 offset:%2\n\ts_waitcnt lgkmcnt(0)" : "=&v"(a0) : "v"(hra), "n"(ACT - HS) : "memory");
#pragma unroll
      for (int c = 0; c < 8; ++c) SMF(dn[(c >> 1) + (c & 1) * 4], a0, DN[(c >> 1) + (c & 1) * 4], six);
      asm volatile("s_nop 7" : W8(dn));
      u32x4 o;
#pragma unroll
      for (int i = 0; i < 4; ++i) o[i] = nsent((g4 & 2) ? dn[4 + i][0] + dn[4 + i][1] : dn[i][0] + dn[i][1]);
      ST16(pto, o, ws + WS_PART + ec * 8192, 0);
    }
    TS(25);
    SKEWPT();
    if (hid >= 32) {
      if (hi == 6 && wid == 1 && lane < 35) {
        char* fp = ws + (lane < 32 ? WS_XFL + (xcd * 32 + lane) * 128 : lane < 34 ? WS_SFL + (xcd * 2 + lane - 32) * 128 : WS_EPO);
        if (lane < 34 || xcd == 0) ST4(reinterpret_cast<u64*>(fp), lane < 34 ? ~tag : tag);
      }
      if (hid == 40 && tid < 320) ST4(reinterpret_cast<u64*>(ws + WS_HFL) + tid, ~tag);
      TSE();
      return;
    }
    const unsigned kk = tok ? k2 : k1;
    const int row = key_row(kk);
    float* rr = reinterpret_cast<float*>(shm + RR);
    float x1 = bf2f(*reinterpret_cast<const u16*>(shm + SX1 + tok * 2048 + col * 2));
    float h2 = bf2f(*reinterpret_cast<const u16*>(shm + HS + (col >> 4) * 64 + tok * 32 + (col & 2) * 8 + (col & 12) + (col & 1) * 2));
    float rsg = rbf(1.0f / (1.0f + __expf(-key_val(kk))));
    unsigned oo3 = (unsigned)(row * D + col) * 2, rra = lds0 + RR + lane * 32;
    asm volatile("" : "+v"(rsg), "+v"(x1), "+v"(h2), "+v"(oo3), "+v"(rra));
    if (wid == 7) {
      POLL { PDBG(it);
        unsigned g;
        asm volatile("global_load_dword %0, %1, %2 sc1\n\ts_waitcnt vmcnt(0)" : "=&v"(g) : "v"(((row >> 4) * 8 + (col >> 7)) * 8), "s"(ws + WS_DON) : "memory");
        if (__all(g != SENT)) break;
        __builtin_amdgcn_s_sleep(2);
      }
    } else {
      const unsigned po = (unsigned)((wid * 8 + g4) * 8192 + tok * 4096 + (hid >> 1) * 256 + l16 * 16);
      u32x4 v[2];
      if (OPT_RD) __builtin_amdgcn_s_sleep(OPT_RD);
      POLL { PDBG(it);
        asm volatile("global_load_dwordx4 %0, %2, %3 sc1\n\tglobal_load_dwordx4 %1, %2, %4 sc1\n\ts_waitcnt vmcnt(0)"
                     : "=&v"(v[0]), "=&v"(v[1])
                     : "v"(po), "s"(ws + WS_PART), "s"(ws + WS_PART + 32768)
                     : "memory");
        if (__all(max(max4(v[0]), max4(v[1])) != SENT)) break;
        __builtin_amdgcn_s_sleep(OPT_RR);
      }
      TS(28);
      const f32x4 t = __builtin_bit_cast(f32x4, v[0]) + __builtin_bit_cast(f32x4, v[1]);
      rr[(l16 * 4 + g4) * 8 + wid] = swap16_add(swap32_add(t[0], t[2]), swap32_add(t[1], t[3]));
    }
    LBAR();
    asm volatile(".Lcpf_e1:");
    TS(5);
    if (wid == 0) {
      f32x4 r0, r1;
      asm volatile("ds_read_b128 %0, %2\n\tds_read_b128 %1, %2 offset:16\n\ts_waitcnt lgkmcnt(0)" : "=&v"(r0), "=&v"(r1) : "v"(rra) : "memory");
      const float sum = ((r0[0] + r0[1]) + (r0[2] + r0[3])) + ((r1[0] + r1[1]) + r1[2]);
      asm volatile("global_store_short %0, %1, %2" ::"v"(oo3), "v"(f2bf(x1 + gb * rbf(h2 + rbf(rbf(sum) * rsg)))), "s"(out) : "memory");
    }
    TS(15);
    TSE();
  }
}

extern "C" void kernel_launch(void* const* d_in, const int* in_sizes, int n_in, void* d_out, int out_size, void* d_ws, size_t ws_size,
                              hipStream_t stream) {
  const u16* x = (const u16*)d_in[0];
  const u16* wsr = (const u16*)d_in[1];
  const u16* wsi = (const u16*)d_in[2];
  const u16* whr = (const u16*)d_in[3];
  const u16* whi = (const u16*)d_in[4];
  const u16* w_r = (const u16*)d_in[6];
  const u16* w_gate = (const u16*)d_in[7];
  const u16* w_up = (const u16*)d_in[8];
  const u16* w_down = (const u16*)d_in[9];
  const u16* c = (const u16*)d_in[10];
  const u16* w_mod = (const u16*)d_in[11];
  u16* out = (u16*)d_out;
  k_main<<<256, 512, 0, stream>>>(x, (char*)d_ws, wsr, wsi, c, w_mod, whr, whi, w_r, w_gate, w_up, w_down, out);
}
```

```cpp
#include <hip/hip_runtime.h>
#include <hip/hip_bf16.h>
#include <stdint.h>
#include <utility>

typedef unsigned short u16;
typedef unsigned long long u64;
using bf16x8 = __attribute__((ext_vector_type(8))) short;
using bf16v8 = __attribute__((ext_vector_type(8))) __bf16;
using f32x4 = __attribute__((ext_vector_type(4))) float;
using f32x2 = __attribute__((ext_vector_type(2))) float;
using u32x4 = __attribute__((ext_vector_type(4))) unsigned;
using u32x2 = __attribute__((ext_vector_type(2))) unsigned;

constexpr int S = 2048, D = 1024, F = 3584;

__device__ __forceinline__ float bf2f(unsigned v) { return __uint_as_float(v << 16); }
__device__ __forceinline__ float bflo(unsigned v) { return __uint_as_float(v << 16); }
__device__ __forceinline__ float bfhi(unsigned v) { return __uint_as_float(v & 0xffff0000u); }
__device__ __forceinline__ unsigned f2bf(float f) {
  __hip_bfloat16 b = __float2bfloat16(f);
  return (unsigned)*reinterpret_cast<u16*>(&b);
}
__device__ __forceinline__ float rbf(float f) { return bf2f(f2bf(f)); }
using bf16v2 = __attribute__((ext_vector_type(2))) __bf16;
__device__ __forceinline__ unsigned pack2(float lo, float hi) {
  return __builtin_bit_cast(unsigned, __builtin_convertvector(f32x2{lo, hi}, bf16v2));
}

template <int CTRL>
__device__ __forceinline__ float dpp_f(float v) {
  return __int_as_float(__builtin_amdgcn_mov_dpp(__float_as_int(v), CTRL, 0xf, 0xf, true));
}
template <int CTRL>
__device__ __forceinline__ float dpp_add(float v) { return v + dpp_f<CTRL>(v); }
__device__ __forceinline__ float row_sum(float v) { return dpp_add<0x140>(dpp_add<0x141>(dpp_add<0x4E>(dpp_add<0xB1>(v)))); }
template <int CTRL>
__device__ __forceinline__ unsigned dpp_u(unsigned v) { return (unsigned)__builtin_amdgcn_mov_dpp((int)v, CTRL, 0xf, 0xf, true); }
__device__ __forceinline__ unsigned wave_max_u(unsigned v) {
  v = max(v, dpp_u<0xB1>(v));
  v = max(v, dpp_u<0x4E>(v));
  v = max(v, dpp_u<0x141>(v));
  v = max(v, dpp_u<0x140>(v));
  const unsigned a = (unsigned)__builtin_amdgcn_readlane((int)v, 0), b = (unsigned)__builtin_amdgcn_readlane((int)v, 16);
  const unsigned c = (unsigned)__builtin_amdgcn_readlane((int)v, 32), d = (unsigned)__builtin_amdgcn_readlane((int)v, 48);
  return max(max(a, b), max(c, d));
}
__device__ __forceinline__ u64 mk64(unsigned hi, unsigned lo) { return ((u64)hi << 32) | lo; }
__device__ __forceinline__ float swap32_add(float a, float b) {
  auto r = __builtin_amdgcn_permlane32_swap(__float_as_uint(a), __float_as_uint(b), false, false);
  return __uint_as_float(r[0]) + __uint_as_float(r[1]);
}
__device__ __forceinline__ float swap16_add(float a, float b) {
  auto r = __builtin_amdgcn_permlane16_swap(__float_as_uint(a), __float_as_uint(b), false, false);
  return __uint_as_float(r[0]) + __uint_as_float(r[1]);
}
__device__ __forceinline__ float ar4(float v) {
  const float t = swap32_add(v, v);
  return swap16_add(t, t);
}
__device__ __forceinline__ float wave_sum_s(float v) {
  v = row_sum(v);
  v += __int_as_float(__builtin_amdgcn_update_dpp(0, __float_as_int(v), 0x142, 0xa, 0xf, false));
  v += __int_as_float(__builtin_amdgcn_update_dpp(0, __float_as_int(v), 0x143, 0xc, 0xf, false));
  return __int_as_float(__builtin_amdgcn_readlane(__float_as_int(v), 63));
}
__device__ __forceinline__ float silu_bf(float c) { return rbf(c * __builtin_amdgcn_rcpf(1.0f + __expf(-c))); }
__device__ __forceinline__ float dsum(unsigned v, float a) {
  return __builtin_amdgcn_fdot2_f32_bf16(__builtin_bit_cast(bf16v2, v), __builtin_bit_cast(bf16v2, 0x3f803f80u), a, false);
}
template <class Fn, int... I>
__device__ __forceinline__ void sfor_(Fn&& f, std::integer_sequence<int, I...>) { (f(std::integral_constant<int, I>{}), ...); }
template <int N, class Fn>
__device__ __forceinline__ void sfor(Fn&& f) { sfor_(f, std::make_integer_sequence<int, N>{}); }
constexpr unsigned SENT = 0xFFFFFFFFu;

constexpr size_t WS_EPO = 0;
constexpr size_t WS_XFL = 4096;
constexpr size_t WS_HFL = 40960;
constexpr size_t WS_XF2 = WS_HFL + 512;
constexpr size_t WS_SFL = WS_HFL + 8192;
constexpr size_t WS_MOD = 65536;
constexpr size_t WS_Q = WS_MOD + 32768;
constexpr size_t WS_ARM = WS_Q + 8192;
constexpr size_t WS_RS = WS_ARM;
constexpr size_t WS_KEY = WS_RS + 16384;
constexpr size_t WS_HDR = WS_KEY + 16384;
constexpr size_t WS_UFL = WS_HDR + 4096;
constexpr size_t WS_DON = WS_UFL + 98304;
constexpr size_t WS_STAT = WS_DON + 12288;
constexpr size_t WS_CAND = WS_STAT + 262144;
constexpr size_t WS_PART = WS_CAND + 524288;
constexpr size_t WS_WAK = WS_PART + 483328;
static_assert(WS_WAK + 8192 <= WS_ARM + 448 * 3328, "armed region");
constexpr size_t WS_XCH = 2u << 20;
constexpr size_t WS_H = 8u << 20;
constexpr size_t WS_WH = 12u << 20;
constexpr size_t WS_U = 16u << 20;
constexpr size_t WS_DBG = 64u << 20;
#define NEWDESIGN 1

#define PCAP (1 << 20)
#define PDBG(it)
#define TS(i)
#define TSCAP()
#define TSC0()
#define TSC1(i)
#define TSD(i)
#define TSE()
#define ST16(voff, v, base, off) asm volatile("global_store_dwordx4 %0, %1, %2 offset:%3 sc1\n\ts_nop 1" ::"v"(voff), "v"(v), "s"(base), "n"(off) : "memory")
#define ST4(p, v) __hip_atomic_store(p, v, __ATOMIC_RELAXED, __HIP_MEMORY_SCOPE_AGENT)
#define WAIT_V(n) asm volatile("s_waitcnt vmcnt(%0)" ::"n"(n) : "memory")
#define WAIT_L(n) asm volatile("s_waitcnt lgkmcnt(%0)" ::"n"(n) : "memory")
#define BARRIER() asm volatile("s_barrier" ::: "memory")
#define LBAR() asm volatile("s_waitcnt lgkmcnt(0)\n\ts_barrier" ::: "memory")
#define DSR(dst, addr, off) asm volatile("ds_read_b128 %0, %1 offset:%2" : "=v"(dst) : "v"(addr), "n"(off) : "memory")
#define MFMA(c, a, b) asm volatile("v_mfma_f32_16x16x32_bf16 %0, %1, %2, %0" : "+v"(c) : "v"(a), "v"(b))
#define MFMAI(c, a, b) asm volatile("v_mfma_f32_16x16x32_bf16 %0, %1, %2, %0" ::"v"(c), "v"(a), "v"(b))
#define MFMA0(c, a, b) asm volatile("v_mfma_f32_16x16x32_bf16 %0, %1, %2, 0" : "=&v"(c) : "v"(a), "v"(b))
#define GLDSP(m0v, voff, gbase, pol) \
  asm volatile("s_mov_b32 m0, %0\n\ts_nop 0\n\tglobal_load_lds_dwordx4 %1, %2" pol ::"s"(m0v), "v"(voff), "s"(gbase) : "memory")
#define GLDS(m0v, voff, gbase) GLDSP(m0v, voff, gbase, "")
#define WPOL " nt"
#define LDXS(dst, voff, base, off) asm volatile("global_load_dwordx4 %0, %1, %2 offset:%3 sc1" : "=v"(dst) : "v"(voff), "s"(base), "n"(off) : "memory")
#define BF8(v) __builtin_bit_cast(bf16v8, v)
#define MM(a, b, c) __builtin_amdgcn_mfma_f32_16x16x32_bf16(BF8(a), BF8(b), c, 0, 0, 0)
#define W6(a) "+v"(a[0]), "+v"(a[1]), "+v"(a[2]), "+v"(a[3]), "+v"(a[4]), "+v"(a[5])
#define W8(a) W6(a), "+v"(a[6]), "+v"(a[7])
#define PFL(ptr) asm volatile("s_mov_b32 m0, %0\n\ts_nop 0\n\tglobal_load_lds_dword %1, off" ::"s"(pfl), "v"(ptr) : "memory")
using s16x4 = __attribute__((ext_vector_type(4))) short;
__device__ __forceinline__ u32x4 trd(unsigned a) {
  const u32x2 lo = __builtin_bit_cast(u32x2, __builtin_amdgcn_ds_read_tr16_b64_v4i16((__attribute__((address_space(3))) s16x4*)(size_t)a));
  const u32x2 hi = __builtin_bit_cast(u32x2, __builtin_amdgcn_ds_read_tr16_b64_v4i16((__attribute__((address_space(3))) s16x4*)(size_t)(a + 512)));
  return u32x4{lo[0], lo[1], hi[0], hi[1]};
}
using u32x8 = __attribute__((ext_vector_type(8))) unsigned;
__device__ __forceinline__ u32x8 trd2(unsigned a) {
  const u32x4 lo = trd(a), hi = trd(a + 4096);
  return u32x8{lo[0], lo[1], lo[2], lo[3], hi[0], hi[1], hi[2], hi[3]};
}
#define SMF(c, a, b, i) asm volatile("v_smfmac_f32_16x16x64_bf16 %0, %1, %2, %3" ::"v"(c), "v"(a), "v"(b), "v"(i))
__device__ __forceinline__ int key_row(unsigned k) { return (int)((0xffffu - (k & 0xffffu)) & 2047u); }
__device__ __forceinline__ float key_val(unsigned k) {
  const unsigned ord = k >> 16;
  return bf2f(ord ^ ((ord & 0x8000u) ? 0x8000u : 0xffffu));
}
__device__ __forceinline__ unsigned nsent(float v) { return min(__float_as_uint(v), SENT - 1u); }
__device__ __forceinline__ f32x4 lo4(u32x4 q) { return f32x4{bflo(q[0]), bfhi(q[0]), bflo(q[1]), bfhi(q[1])}; }
__device__ __forceinline__ f32x4 hi4(u32x4 q) { return f32x4{bflo(q[2]), bfhi(q[2]), bflo(q[3]), bfhi(q[3])}; }
__device__ __forceinline__ u32x4 pk8(f32x4 a, f32x4 b) { return u32x4{pack2(a[0], a[1]), pack2(a[2], a[3]), pack2(b[0], b[1]), pack2(b[2], b[3])}; }
__device__ __forceinline__ u32x4 nsent4(u32x4 v) { return u32x4{min(v[0], SENT - 1u), min(v[1], SENT - 1u), min(v[2], SENT - 1u), min(v[3], SENT - 1u)}; }
__device__ __forceinline__ unsigned max4(u32x4 v) { return max(max(v[0], v[1]), max(v[2], v[3])); }
__device__ __forceinline__ unsigned max2(u32x2 v) { return max(v[0], v[1]); }
__device__ __forceinline__ unsigned hi2(u32x2 v) { return (v[0] >> 16) | (v[1] & 0xffff0000u); }
#define TOP2(CTRL)                                                  \
  do {                                                              \
    const unsigned o1 = dpp_u<CTRL>(t1), o2 = dpp_u<CTRL>(t2);      \
    t2 = max(min(t1, o1), max(t2, o2));                             \
    t1 = max(t1, o1);                                               \
  } while (0)
#define POLL _Pragma("unroll 1") for (int it = 0; it < PCAP; ++it)
#define SPIN(OK, PAUSE, LD)                                 \
  do {                                                      \
    int left_ = PCAP;                                       \
    do {                                                    \
      __builtin_amdgcn_s_sleep(PAUSE);                      \
      LD;                                                   \
      left_ = (OK) ? 0 : left_ - 1;                         \
      PDBG(PCAP - left_);                                   \
    } while (left_);                                        \
  } while (0)
#define UNTIL(OK, PAUSE, LD)                                \
  do {                                                      \
    if (__builtin_expect(!(OK), 0)) {                       \
      POLL { PDBG(it);                                      \
        __builtin_amdgcn_s_sleep(PAUSE);                    \
        LD;                                                 \
        if (OK) break;                                      \
      }                                                     \
    }                                                       \
  } while (0)
#define P_D3 16
#define P_D4 2
#define U_D 28
#define U_R 1
#define X_R 1
#define OPT_SD 15
#define OPT_KS 2
#define OPT_KD 32
#define OPT_RD 8
#define OPT_RR 2
#define OUT_D 20
#define CPF_N 10
#define CPF_K 24
#define CPF_B 10
#define PF_N 3
#define A_LD 3
#define F_AT 3
#define X_AT 12
#define H_AT 24
#define B_VAT 42
#define UF_AT 8
#define B46 339
#define TP_XL 340
#define TP_X2 350
#define Q_AT 9
#define MQ_AT 31
#define XQ0 2
#define XQ1 4
#define XQ2 18
#define XQ3 20
#define HX0 9
#define HXS 2
static_assert(HX0 + 11 * HXS < 44 && UF_AT <= 22 && A_LD <= 3 && H_AT <= 24 && X_AT + 12 <= H_AT && Q_AT >= 7, "");
constexpr int SL = 22528, NS = 5, XR = NS * SL, MISC = XR + 24576;
constexpr int as(int c) { return c < 3 ? 2 : c < 6 ? 3 : 2 + (c & 1); }
constexpr int ap(int c) { return c < 3 ? c : c < 6 ? c - 3 : c / 2; }
constexpr int ug(int j) { return j < 9 ? 18 + 7 * j : 82; }
constexpr int E0 = 264, E3 = E0 + 88, NE = E3 + 6;
constexpr int es(int c) { return c < 132 ? c / 11 : c < 144 ? 12 + (c - 132) / 6 : c < 156 ? 14 + (c & 1) : 12 + (c & 3); }
constexpr int ep(int c) { return c < 132 ? c % 11 : c < 144 ? (c - 132) % 6 : c < 156 ? (c - 144) / 2 : 6 + (c - 156) / 4; }

__global__ __launch_bounds__(512, 2) void k_main(const u16* __restrict__ x, char* ws, const u16* __restrict__ Y0, const u16* __restrict__ Y1,
                                              const u16* __restrict__ cvec, const u16* __restrict__ wmod, const u16* __restrict__ X0,
                                              const u16* __restrict__ X1, const u16* __restrict__ wr_, const u16* __restrict__ wg,
                                              const u16* __restrict__ wu, const u16* __restrict__ wd, u16* __restrict__ out) {
  constexpr int STG = 114688;
  __shared__ __attribute__((aligned(1024))) char shm[MISC + 16384];
  const int b = blockIdx.x, xcd = b & 7, j8 = b >> 3;
  const int cc = xcd >> 2, qx = xcd & 3;
  const int tid = threadIdx.x, lane = tid & 63;
  const int wid = __builtin_amdgcn_readfirstlane(tid >> 6);
  const unsigned lds0 = (unsigned)(size_t)(__attribute__((address_space(3))) char*)shm;
  const char* cpf;
  {
    unsigned long long pc;
    unsigned len;
    asm volatile("s_getpc_b64 %0\n.Lcpf_here:\n\ts_mov_b32 %1, .Lcpf_e1-.Lcpf_here\n\ts_max_u32 %1, %1, .Lcpf_e2-.Lcpf_here" : "=s"(pc), "=s"(len)::"scc");
    cpf = reinterpret_cast<const char*>(pc) + min((unsigned)((lane * 32 + j8) * 128), len);
  }
  const unsigned pfl = lds0 + MISC + 16128;
  if (wid == 4 && lane < CPF_N) PFL(cpf);
  u32x4 xv[2];
  {
    const unsigned xo = (unsigned)((b * 8 + wid) * D + lane * 8) * 2;
    asm volatile("global_load_dwordx4 %0, %2, %3\n\tglobal_load_dwordx4 %1, %2, %3 offset:1024" : "=&v"(xv[0]), "=&v"(xv[1]) : "v"(xo), "s"(x) : "memory");
  }
  asm volatile("" ::"s"(ws), "s"(Y0), "s"(Y1), "s"(X0), "s"(X1));
  unsigned* xfl = reinterpret_cast<unsigned*>(ws + WS_XFL);
  unsigned* hfl = reinterpret_cast<unsigned*>(ws + WS_HFL);
  unsigned* modv = reinterpret_cast<unsigned*>(ws + WS_MOD);
#define SKEWPT()
  const int g4 = lane >> 4, l16 = lane & 15;
  u64 tag0, tag;
  asm volatile("global_load_dwordx2 %0, %1, %2 sc1" : "=v"(tag0) : "v"(0u), "s"(ws) : "memory");
  const char* Yx = reinterpret_cast<const char*>(cc ? Y1 : Y0) + (size_t)qx * 2097152;
#define PFG(r16, g) (Yx + (size_t)(((r16) * 16 + l16) * 4096 + g4 * 128 + ((((g) + qx) & 7) << 9)))
  unsigned ta = lds0 + STG + ((lane >> 2) & 3) * 2368 + (wid * 128 + (lane >> 5) * 32 + (lane & 3) * 8 + (g4 & 1) * 4) * 2;
  unsigned oo = (unsigned)((wid * 4 + (lane >> 5)) * 131072 + (b >> 2) * 2048 + (g4 & 1) * 1024 + ((b & 3) * 16 + l16) * 16), oo2 = oo + 262144;
  unsigned wa = lds0 + STG + wid * 2368 + lane * 16;
  asm volatile("" : "+v"(ta), "+v"(oo), "+v"(oo2), "+v"(wa));
  const int hsl = (b & 32) + xcd * 4 + (j8 & 3);
  const char* hp = reinterpret_cast<const char*>((b & 32) ? X1 : X0) + (size_t)(hsl & 31) * 65536 + wid * 8192;
#define WHLD(i0)                                                                                                                  \
  _Pragma("unroll") for (int i = i0; i < i0 + 4; ++i)                                                                             \
      GLDS(lds0 + wid * 8192 + i * 1024, (unsigned)((lane ^ ((i >> 1) | ((wid & 2) << 1))) * 16), hp + i * 1024)
  if (wid >= 8 - PF_N) PFL(PFG(j8, 7 - wid));
  if (j8 >= 24) {
    WHLD(0);
    WAIT_V(5);
  } else if (wid >= 8 - PF_N) {
    WAIT_V(2);
  } else {
    WAIT_V(1);
  }
  {
    asm volatile("; AUDIT_OK %0 %1" : "+v"(xv[0]), "+v"(xv[1])::"memory");
    TS(32);
    const float s0 = dsum(xv[0][3], dsum(xv[0][2], dsum(xv[0][1], dsum(xv[0][0], 0.f))));
    const float s1 = dsum(xv[1][3], dsum(xv[1][2], dsum(xv[1][1], dsum(xv[1][0], 0.f))));
    const float mf = wave_sum_s(s0 + s1) * (1.0f / D), m = rbf(mf);
    const f32x4 d0 = lo4(xv[0]) - m, d1 = hi4(xv[0]) - m, d2 = lo4(xv[1]) - m, d3 = hi4(xv[1]) - m;
    const f32x4 q = (d0 * d0 + d1 * d1) + (d2 * d2 + d3 * d3);
    const float vf = wave_sum_s((q[0] + q[1]) + (q[2] + q[3])) * (1.0f / D) - (mf - m) * (mf - m);
    const float rden = __builtin_amdgcn_rcpf(rbf(__builtin_amdgcn_sqrtf(rbf(rbf(vf) + 1e-5f))));
    asm volatile("ds_write_b128 %0, %1" ::"v"(wa), "v"(pk8(d0 * rden, d1 * rden)) : "memory");
    asm volatile("ds_write_b128 %0, %1 offset:1024" ::"v"(wa), "v"(pk8(d2 * rden, d3 * rden)) : "memory");
    LBAR();
    TS(34);
    u32x2 t[4];
    asm volatile(
        "ds_read_b64_tr_b16 %0, %4\n\tds_read_b64_tr_b16 %1, %4 offset:%5\n\t"
        "ds_read_b64_tr_b16 %2, %4 offset:128\n\tds_read_b64_tr_b16 %3, %4 offset:%6\n\ts_waitcnt lgkmcnt(0)"
        : "=&v"(t[0]), "=&v"(t[1]), "=&v"(t[2]), "=&v"(t[3])
        : "v"(ta), "n"(4 * 2368), "n"(4 * 2368 + 128)
        : "memory");
    asm volatile("global_store_dwordx4 %0, %2, %4 sc1\n\tglobal_store_dwordx4 %1, %3, %4 sc1\n\ts_nop 1" ::"v"(oo), "v"(oo2),
                 "v"(u32x4{t[0][0], t[0][1], t[1][0], t[1][1]}), "v"(u32x4{t[2][0], t[2][1], t[3][0], t[3][1]}), "s"(ws + WS_H)
                 : "memory");
  }
#define PBAR(n)                                                                                                          \
  do {                                                                                                                   \
    asm volatile("s_waitcnt vmcnt(%2)\n\tv_lshl_add_u64 %0, %1, 0, 1" : "=v"(tag) : "v"(tag0), "n"(n) : "memory");       \
    TS(36);                                                                                                              \
    BARRIER();                                                                                                           \
    TS(37);                                                                                                              \
  } while (0)
#define XFLAG() do { if ((tid | 1) == 385) ST4(reinterpret_cast<u64*>(ws + ((tid & 1) ? WS_XF2 + b * 8 : WS_XFL + b * 128)), tag); } while (0)
  if (j8 < 24) {
    const int mt = (j8 * 11) >> 5, nt = j8 - mt * 3, grp = xcd * 3 + nt;
#define FROW(f) (nt * 176 + (f) * 16 - (nt == 2 && (f) > 5 ? 16 : 0))
#define PROW(p) FROW(cc ? 10 - (p) : (p))
    constexpr int SCA = MISC, TGA = MISC + 512, TV = MISC + 1024, TSUM = MISC + 1792, SCW = MISC + 2048, RKA = MISC + 3072,
                  RKB = MISC + 5120, RKZ = MISC + 6656, IDT = MISC + 7168, ZSC = MISC + 9216;
    const bool zd = nt == 2;
    unsigned* ufl = reinterpret_cast<unsigned*>(ws + WS_UFL) + grp * 1024;
    const char* Ug = ws + WS_U + (size_t)grp * (16 * SL + 1024) + 1024;
    const unsigned perm = (unsigned)mt | (((0x76543210u & ((1u << (4 * mt)) - 1u)) << 4) | (0x76543210u & (0xfffffff0u << (4 * mt))));
#define KT(i) ((perm >> (4 * (i))) & 7u)
    const int kk = wid < 4 ? wid : wid - 2;
    const bool act = (wid < 4 || wid >= 6) && (kk || (cc == 0 && nt < 2));
    const int rg = qx * 32 + (PROW(5 + kk) >> 4);
    u32x4 pv[6];
    f32x4 acc[2][11], ga[2], x5[2];
    bf16x8 idt[2];
    unsigned spo = (unsigned)(((rg * 8 + g4 * 2) * 16 + l16) * 16);
    u32x4 sr[2], dk;
#pragma unroll
    for (int q = 0; q < 4; ++q) dk[q] = l16 == g4 * 4 + q ? ~0u : 0u;
    unsigned xw1 = lds0 + XR + tid * 16, xa = lds0 + XR + kk * 4096 + lane * 16, sto = (unsigned)(((rg * 8 + mt) * 16 + lane) * 16);
    unsigned saa = lds0 + SCW + (lane & 3) * 256 + g4 * 16, cja = lds0 + TSUM + g4 * 8;
    asm volatile("" : "+v"(cja), "+v"(dk), "+v"(xa), "+v"(sto), "+v"(xw1), "+v"(spo), "+v"(saa));
#define STLD() asm volatile("global_load_dwordx4 %0, %2, %3 sc1\n\tglobal_load_dwordx4 %1, %2, %3 offset:256 sc1\n\ts_waitcnt vmcnt(0)" : "=&v"(sr[0]), "=&v"(sr[1]) : "v"(spo), "s"(ws + WS_STAT) : "memory")
    const char* xr = ws + WS_XCH + (size_t)(b * 4 + (wid & 3)) * 6144 + 3072;
#define XCHLD()                                                                                                       \
  asm volatile(                                                                                                       \
      "global_load_dwordx4 %0, %6, %7 offset:2048 sc1\n\tglobal_load_dwordx4 %1, %6, %7 offset:1024 sc1\n\t"          \
      "global_load_dwordx4 %2, %6, %7 sc1\n\tglobal_load_dwordx4 %3, %6, %7 offset:-1024 sc1\n\t"                     \
      "global_load_dwordx4 %4, %6, %7 offset:-2048 sc1\n\tglobal_load_dwordx4 %5, %6, %7 offset:-3072 sc1"            \
      : "=&v"(pv[0]), "=&v"(pv[1]), "=&v"(pv[2]), "=&v"(pv[3]), "=&v"(pv[4]), "=&v"(pv[5])                            \
      : "v"(lane * 16), "s"(xr)                                                                                       \
      : "memory")
    if (wid < 4) {
      bf16x8 X[4][2][2], Y[8];
      u32x4 xq[6];
      const unsigned xo0 = lane * 16 + 4096;

#define XLD(dst, XP, XH, i) LDXS(dst, xo0, XP, ((XH)&3) * 2048 + (i)*1024 - 4096)
      int xt = lane < 16 ? (int)WS_H + (mt * 4 + wid) * 131072 + (((qx * 2 + lane) & 15) << 13) : (int)WS_WH + (cc * 32 + mt * 4 + wid) * 65536 + (KT(lane & 7) << 13);
      asm volatile("" : "+v"(xt));
#define XT(j) (ws + (unsigned)__builtin_amdgcn_readlane(xt, j))
#define WP(h) XT(16 + ((h) >> 2))
#define XS(h) X[((h) >> 1) & 3][(h)&1]
      {
        unsigned ye = lds0 + l16 * 128 + (g4 ^ (lane & 7)) * 16, yo = ye ^ 64;
        const char* xpa = XT(0);
        const char* xpb = XT(1);
#pragma unroll
        for (int i = 0; i < 22; ++i) {
          acc[i / 11][i % 11] = f32x4{0.f, 0.f, 0.f, 0.f};
          asm volatile("" : "+v"(acc[i / 11][i % 11]));
        }
        const char* xw = ws + WS_XCH + (size_t)((b ^ 4) * 4 + wid) * 6144 + 3072;
        unsigned xra = lds0 + XR + l16 * 256 + (((wid * 4 + g4) ^ (lane & 7)) << 4), gaa = lds0 + TGA + wid * 128 + g4 * 32;
        unsigned rka = lds0 + (g4 ? RKZ : RKA + wid * 512 + l16 * 16), rkb = lds0 + RKB + l16 * 16;
        asm volatile("" : "+s"(xw), "+s"(xr), "+v"(xra), "+v"(gaa), "+v"(rka), "+v"(rkb));
        __builtin_amdgcn_s_setprio(1);
        PBAR(0);
        if (wid == 3) {
          const unsigned* fp = xfl + (qx * 32 + lane) * 32;
          __builtin_amdgcn_s_sleep(P_D3 - P_D4);
          u64 f;
#define GLD() asm volatile("global_load_dwordx2 %0, %1, off sc1\n\ts_waitcnt vmcnt(0)" : "=&v"(f) : "v"(fp) : "memory")
          SPIN(__all(f == tag), P_D4, GLD());
        }
        TS(38);
        BARRIER();
        TS(39);
#pragma unroll
        for (int h = 0; h < 6; ++h) {
          XLD(XS(h)[0], h < 4 ? xpa : xpb, h, 0);
          XLD(XS(h)[1], h < 4 ? xpa : xpb, h, 1);
        }
#pragma unroll
        for (int j = 0; j < 6; ++j) DSR(Y[j], (j & 1) ? yo : ye, (j >> 1) * 2048);
        unsigned so = 0, a0 = ye, a1 = yo, yb[2], tq, u = 2;
#define STEP2(S0, XA, XB)                                                                                                      \
  sfor<44>([&](auto nn) __attribute__((always_inline)) {                                                                       \
    constexpr int n = (S0)*22 + decltype(nn)::value, q = n % 22, p = q / 2, s = n / 22, i = n & 1;                             \
    constexpr int k = q == XQ0 ? 0 : q == XQ1 ? 1 : q == XQ2 ? 2 : q == XQ3 ? 3 : -1;                                          \
    (void)acc, (void)X, (void)Y, (void)a0, (void)a1, (void)xo0, (void)XA, (void)XB, (void)so, (void)ye, (void)yo, (void)u, (void)xt, (void)tq, (void)ws; \
    if constexpr (q == 11) BARRIER();                                                                                          \
    if constexpr (q == 0) asm volatile("s_waitcnt vmcnt(8) lgkmcnt(4)" ::: "memory");                                          \
    else if constexpr (!i) WAIT_L(4);                                                                                          \
    MFMAI(acc[i][p], X[s][0][i], Y[n & 6]);                                                                                    \
    DSR(Y[(n + 6) & 7], i ? a1 : a0, ((p + 3) % 11) * 2048);                                                                   \
    MFMA(acc[i][p], X[s][1][i], Y[(n & 6) + 1]);                                                                               \
    if constexpr (k >= 0) XLD(X[(s + 3) & 3][k / 2][k & 1], (s & 1) ? XB : XA, 2 * s + 6 + k / 2, k & 1);                      \
    if constexpr (q == 0 && !(s & 1)) asm volatile("v_readlane_b32 %0, %2, %1\n\ts_add_u32 %1, %1, 1" : "=&s"(tq), "+s"(u) : "v"(xt) : "scc"); \
    if constexpr (q == 6) asm volatile("s_add_u32 %0, %0, %1" : "+s"(so) : "n"(SL) : "scc");                                   \
    if constexpr (q == 8) asm volatile("s_cmp_lg_u32 %0, %1\n\ts_cselect_b32 %0, %0, 0" : "+s"(so) : "n"(NS * SL) : "scc");    \
    if constexpr (q == 12 && !(s & 1)) { asm volatile("" : "+s"(tq)); XB = ws + tq; asm volatile("" : "+s"(XB)); }             \
    if constexpr (q == 14) asm volatile("v_add_u32 %0, %1, %2" : "=v"(a0) : "s"(so), "v"(ye));                                 \
    if constexpr (q == 16) asm volatile("v_add_u32 %0, %1, %2" : "=v"(a1) : "s"(so), "v"(yo));                                 \
  })
#define YSL(e) ((32 + es((e) / 2)) % NS)
#define YRD(e) DSR(Y[(e)&7], YSL(e) < 2 ? ye : yb[YSL(e) / 4], (YSL(e) & 1) * SL + ep((e) / 2) * 2048 + ((e)&1) * 1024)

        u32x4 ov;
        bf16x8 qf[2];
        auto OPS = [&](auto kc) __attribute__((always_inline)) {
          constexpr int k = decltype(kc)::value, q = k / 10, r = k % 10, c = k - 60;
          (void)ov, (void)acc, (void)xw, (void)lane, (void)xq, (void)x5;
          if constexpr (k < 60) {
            if constexpr (r < 4) asm volatile("v_cvt_pk_bf16_f32 %0, %1, %2" : "=v"(ov[r]) : "v"(acc[r / 2][q][(r & 1) * 2]), "v"(acc[r / 2][q][(r & 1) * 2 + 1]));
            else if constexpr (r < 8) asm volatile("v_min_u32 %0, -2, %0" : "+v"(ov[r - 4]));
            else if constexpr (r == 9) asm volatile("global_store_dwordx4 %0, %1, %2 offset:%3 sc1" ::"v"(lane * 16), "v"(ov), "s"(xw), "n"(q * 1024 - 3072) : "memory");
          } else if constexpr (c >= 40 && c < 48) {
            if constexpr (c & 1) asm volatile("v_and_b32 %0, 0xffff0000, %1" : "=v"(x5[(c >> 2) & 1][c & 3]) : "v"(xq[5][(c >> 1) & 3]));
            else asm volatile("v_lshlrev_b32 %0, 16, %1" : "=v"(x5[(c >> 2) & 1][c & 3]) : "v"(xq[5][(c >> 1) & 3]));
          } else if constexpr (c < 40) {
            if constexpr (c & 1) asm volatile("v_and_b32 %0, 0xffff0000, %1" : "=v"(acc[(c >> 2) & 1][c >> 3][c & 3]) : "v"(xq[c >> 3][(c >> 1) & 3]));
            else asm volatile("v_lshlrev_b32 %0, 16, %1" : "=v"(acc[(c >> 2) & 1][c >> 3][c & 3]) : "v"(xq[c >> 3][(c >> 1) & 3]));
          }
        };
#define IC(v) std::integral_constant<int, (v)>{}
#define FILL(g)                                                                              \
  do {                                                                                       \
    if constexpr (m >= 0) {                                                                  \
      OPS(IC(m / 2 * 5 + (m & 1) * 3 + (g)));                                                \
      if constexpr ((g) && !(m & 1)) OPS(IC(m / 2 * 5 + 2));                                 \
    }                                                                                        \
  } while (0)
        auto BP = [&](auto nn) __attribute__((always_inline)) {
          constexpr int n = decltype(nn)::value, s = es(n / 2), p = n < E3 ? ep(n / 2) : 5 + n - E3, i = n & 1, e = n + 6, m = n - E0 - 30;
          (void)ga, (void)qf, (void)idt, (void)rka, (void)rkb, (void)gaa, (void)xq, (void)ov, (void)xw, (void)x, (void)pv, (void)xr, (void)ufl;
          (void)acc, (void)X, (void)Y, (void)yb, (void)ye, (void)xra, (void)xo0, (void)xt, (void)ws, (void)lane, (void)mt, (void)nt, (void)cc, (void)lds0, (void)wid, (void)g4, (void)perm;
          (void)OPS;
          TSCAP();
          if constexpr (n == E0 + 55) TS(10);
          if constexpr (n == UF_AT) {
            TS(45);
            WAIT_V(n > HX0 ? (n - HX0 + HXS - 1) / HXS : 0);
            if (lane == 0) ST4(ufl + (mt * 4 + wid) * 32, 1u);
            TS(46);
          }
          if constexpr (n == 37) TS(48);
          if constexpr (n == 37 || n == E0 + 5 || n == E0 + 17 || n == B46) BARRIER();
          if constexpr (n == 37) TS(4);
          if constexpr (n == E0 + 25) { TS(3); SKEWPT(); }
          if constexpr (p == 0 && !i && n != 22) WAIT_V(n == 0 ? 15 : s < 14 ? 8 : 4 * (15 - s));
          if constexpr (!i) WAIT_L(e < NE ? 4 : NE - 2 - n);
          if constexpr (n < 22) MFMA0(acc[i][p], X[0][0][i], Y[n & 6]);
          else if constexpr (n < E3) MFMAI(acc[i][p], X[s & 3][0][i], Y[n & 6]);
          else MFMA(acc[0][p], qf[0], Y[n & 7]);
          if constexpr (e < E3) YRD(e);
          else if constexpr (e < NE) DSR(Y[e & 7], rkb, (e - E3) * 256);
          FILL(0);
          if constexpr (n < E3 && (s == 1 || s == 15)) MFMA(acc[i][p], X[s & 3][1][i], Y[(n & 6) + 1]);
          else if constexpr (n < E3) MFMAI(acc[i][p], X[s & 3][1][i], Y[(n & 6) + 1]);
          else MFMA(acc[1][p], qf[1], Y[n & 7]);
          if constexpr (n >= HX0 && n < HX0 + 12 * HXS && (n - HX0) % HXS == 0) {
            constexpr int k = (n - HX0) / HXS;
            static_assert(k < 8 || n >= 22, "slot still in use");
            XLD(XS(4 + k / 2)[k & 1], WP(4 + k / 2), k / 2, k & 1);
          }
          if constexpr (n >= 272 && n < 294 && n % 4 == 0) DSR(xq[(n - 272) / 4], xra, (n - 272) / 4 * 4096);
          if constexpr (n >= E0 && n < E0 + 4) XLD(X[3][(n - E0) / 2][i], WP(31), 30 + (n - E0) / 2, i);
          FILL(1);
          if constexpr (n == B46 || n == B46 + 1) DSR(qf[n - B46], rka, (n - B46) * 256);
          if constexpr (n == B46 + 2 || n == B46 + 3) DSR(ga[n - B46 - 2], gaa, (n - B46 - 2) * 16);
          if constexpr (n == B46 + 4 || n == B46 + 5) DSR(idt[n - B46 - 4], yb[1], IDT - 4 * SL + (n - B46 - 4) * 1024);
          if constexpr (n >= TP_XL && n < TP_XL + 4) LDXS(pv[TP_XL + 5 - n], lane * 16, xr, (n - TP_XL) * 1024 - 3072);
          if constexpr (n == TP_X2 || n == TP_X2 + 1) LDXS(pv[TP_X2 + 1 - n], lane * 16, xr, (n - TP_X2) * 1024 + 1024);
        };
        do {
          STEP2(0, xpb, xpa);
          if (__builtin_expect(u == 17, 0)) {
            TS(57);
            TSC1(40);
            TSC0();
            f32x4 c0, c1;
            const char* up = Ug + (mt * 2 + (wid >> 1)) * SL + (wid & 1) * 1024 + 4096;
            const unsigned ul = lds0 + (2 + (wid >> 1)) * SL + (wid & 1) * 1024 + lane * 16;
            u32x4 uv;
            DSR(c0, lds0 + SCA + wid * 128 + g4 * 32, 0);
            DSR(c1, lds0 + SCA + wid * 128 + g4 * 32, 16);
#define UMUL(j, k) asm volatile("v_mul_f32 %0, %0, %1" : "+v"(acc[(k) >> 2][j][(k)&3]) : "v"(((k) >> 2 ? c1 : c0)[(k)&3]))
#define UCVT(j, k) asm volatile("v_cvt_pk_bf16_f32 %0, %1, %2" : "=v"(uv[k]) : "v"(acc[(k) >> 1][j][((k)&1) * 2]), "v"(acc[(k) >> 1][j][((k)&1) * 2 + 1]))
            auto UEP = [&](auto jc, auto rc) __attribute__((always_inline)) {
              constexpr int j = decltype(jc)::value, r = decltype(rc)::value;
              (void)acc, (void)c0, (void)c1, (void)uv, (void)lane, (void)ul, (void)up;
              if constexpr (r < 4) { UMUL(j, 2 * r); UMUL(j, 2 * r + 1); }
              else if constexpr (r < 6) { UCVT(j, 2 * r - 8); UCVT(j, 2 * r - 7); }
              else asm volatile("global_store_dwordx4 %0, %2, %3 offset:%4 sc1\n\tds_write_b128 %1, %2 offset:%5"
                                ::"v"(lane * 16), "v"(ul), "v"(uv), "s"(up + (j >> 2) * 8192), "n"((j & 3) * 2048 - 4096), "n"(j * 2048) : "memory");
            };
#define UGAP(G) do { constexpr int j_ = (G) < 82 ? ((G)-18) / 7 : 9; if constexpr ((G) >= 18 && (G) != 81) UEP(IC(j_), IC((G)-ug(j_))); } while (0)
            sfor<44>([&](auto nn) __attribute__((always_inline)) {
              constexpr int n = decltype(nn)::value, s = as(n / 2), p = ap(n / 2), i = n & 1, e = n + 6;
              (void)acc, (void)X, (void)Y, (void)ye, (void)yo, (void)xo0, (void)xpa, (void)UEP;
              if constexpr (n == 0 || n == 6) WAIT_V(8);
              if constexpr (!i) WAIT_L(e < 44 ? 4 : 42 - n);
              MFMAI(acc[i][p], X[s][0][i], Y[(n + 4) & 6]);
              if constexpr (e < 44) DSR(Y[(e + 4) & 7], i ? yo : ye, (as(e / 2) & 1) * SL + ap(e / 2) * 2048);
              UGAP(2 * n);
              if constexpr (s == 2) MFMAI(acc[i][p], X[s][1][i], Y[((n + 4) & 6) + 1]);
              else MFMA(acc[i][p], X[s][1][i], Y[((n + 4) & 6) + 1]);
              if constexpr (n < 4) XLD(X[1][n / 2][i], xpa, 2 + n / 2, i);
              UGAP(2 * n + 1);
            });
            TSC1(41);
            TS(0);
            SKEWPT();
            UEP(IC(9), IC(6));
            asm volatile("s_nop 1");
            sfor<7>([&](auto rc) __attribute__((always_inline)) { UEP(IC(10), rc); });
            ye = lds0 + lane * 16;
            yo = ye + 1024;
            so = 4 * SL;
            a0 = ye + 4 * SL;
            a1 = yo + 4 * SL;
            yb[0] = ye + 2 * SL;
            yb[1] = a0;
            asm volatile("" : "+v"(yb[0]), "+v"(yb[1]), "+v"(ye), "+v"(yo));
            LBAR();
            TS(44);
            sfor<6>([&](auto ee) __attribute__((always_inline)) { (void)Y, (void)yb, (void)ye; YRD(decltype(ee)::value); });
            sfor<44>(BP);
            u = 19;
            xpa = WP(8);
          }
          STEP2(2, xpa, xpb);
        } while (u != 24);
        TS(2);
        sfor<NE - 264>([&](auto nn) __attribute__((always_inline)) { BP(std::integral_constant<int, 264 + decltype(nn)::value>{}); });
        __builtin_amdgcn_s_setprio(0);
        TSC1(59);
        TS(13);
        SKEWPT();
      }
    } else if (wid < 6) {
      const int hw = wid - 4;
      __builtin_amdgcn_s_setprio(2);
      unsigned voy[11];
#pragma unroll
      for (int i = 0; i < 11; ++i) voy[i] = (unsigned)((PROW(i) + hw * 8 + (lane >> 3)) * 4096 + ((lane & 7) ^ (lane >> 3)) * 16);
      const unsigned lbase = lds0 + hw * 1024;
      if (zd) voy[5] = (lane & 7) * 16;
      auto stage = [&](int n, int slot) {
        const char* yp = Yx + (((n + qx * 4) & 31) << 7);
        const unsigned l = lbase + slot * SL;
#pragma unroll
        for (int i = 0; i < 11; ++i) GLDS(i == 5 && zd ? lbase + ZSC : l + i * 2048, voy[i], yp);
      };
      asm volatile("s_waitcnt vmcnt(%3)" ::"v"(voy[0]), "v"(voy[10]), "v"(tag0), "n"(0) : "memory");
      BARRIER();
#pragma unroll 1
      for (int s = 0; s <= A_LD; ++s) stage(s, s);
      WAIT_V(11 * A_LD);
      TS(38);
      BARRIER();
      int slot = A_LD + 1;
#pragma unroll 1
      for (int k = 1; k <= 31 - A_LD; ++k) {
        WAIT_V(11 * (A_LD - 1));
        BARRIER();
        if (hw == 1) TSD(k);
        stage(k + A_LD, slot);
        slot = slot == NS - 1 ? 0 : slot + 1;
        if (k == CPF_K && hw == 1 && lane < CPF_N) PFL(cpf);
      }
      if (A_LD > 2) { WAIT_V(22); BARRIER(); }
      WAIT_V(0); BARRIER();
      const unsigned* ufp = ufl + (KT(min(lane >> 2, 6) + 1) * 4 + (lane & 3)) * 32;
      unsigned miss = ~0u;
      const char* y1 = Ug + KT(1) * 2 * SL + hw * 11264;
      asm volatile("" : "+s"(y1), "+v"(ufp));
      BARRIER();
      TS(47);
      if (U_D) __builtin_amdgcn_s_sleep(U_D);
      auto ULDF = [&](unsigned em0) __attribute__((always_inline)) {
        unsigned f;
        const u64 em = miss & em0;
        asm volatile("s_mov_b64 exec, %2\n\tglobal_load_dword %0, %1, off sc1\n\ts_mov_b64 exec, -1\n\ts_waitcnt vmcnt(0)" : "=&v"(f) : "v"(ufp), "s"(em) : "memory");
        miss &= ~(unsigned)em | (unsigned)__builtin_amdgcn_ballot_w64(f == SENT);
      };
#define UPOLL(need, em0) UNTIL((miss & (need)) == 0, U_R, ULDF(em0))
#define G1(o) "global_load_lds_dwordx4 %0, %1 offset:" #o " sc1\n\t"
#define ULD(yp, l)                                                                                                                          \
  do {                                                                                                                                      \
    asm volatile("s_mov_b32 m0, %2\n\ts_nop 0\n\t" G1(0) G1(1024) G1(2048) G1(3072)::"v"(lane * 16), "s"(yp), "s"(l) : "memory");            \
    asm volatile("s_mov_b32 m0, %2\n\ts_nop 0\n\t" G1(0) G1(1024) G1(2048) G1(3072)::"v"(lane * 16 + 4096), "s"(yp), "s"((l) + 4096) : "memory"); \
    asm volatile("s_mov_b32 m0, %2\n\ts_nop 0\n\t" G1(0) G1(1024) G1(2048)::"v"(lane * 16 + 8192), "s"(yp), "s"((l) + 8192) : "memory");     \
  } while (0)
      {
        SPIN((miss & 3u) == 0, U_R, ULDF(15u));
        TS(48);
        ULD(y1, lds0 + 4 * SL + hw * 11264);
        UPOLL(15u, 15u);
        unsigned f;
        asm volatile("global_load_dword %0, %1, off sc1" : "=v"(f) : "v"(ufp) : "memory");
        ULD(y1 + SL, lds0 + hw * 11264);
        asm volatile("s_waitcnt vmcnt(11)" : "+v"(f)::"memory");
        BARRIER();
        miss = (unsigned)__builtin_amdgcn_ballot_w64(f == SENT);
      }
      slot = 1;
#pragma unroll 1
      for (int n = 4; n < 16; ++n) {
        UPOLL(3u << (2 * n - 4), ~0u);
        const char* yp = Ug + (KT(n >> 1) * 2 + (n & 1)) * SL + hw * 11264;
        unsigned l = lds0 + slot * SL + hw * 11264;
        slot = slot == NS - 1 ? 0 : slot + 1;
        if (n == CPF_B && hw == 1 && lane < CPF_N) PFL(cpf);
        asm volatile("" : "+s"(yp), "+s"(l));
        if (n >= 6) {
          WAIT_V(22);
          BARRIER();
          if (hw == 1) TSD(n + 44);
        }
        ULD(yp, l);
      }
      WAIT_V(22);
      BARRIER();
      WAIT_V(0);
      BARRIER();
      BARRIER();
      BARRIER();
      if (b < 56 && tid == 256) ST4(reinterpret_cast<unsigned*>(ws + WS_WAK) + b * 32, 1u);
      TSE();
      return;
    } else {
      const int aw = wid - 6, rp = min(mt * 2 + aw, 10);
      PBAR(0);
      XFLAG();
      if (zd) {
#pragma unroll
        for (int s = 0; s < NS; ++s) *reinterpret_cast<u32x4*>(shm + s * SL + 10240 + aw * 1024 + lane * 16) = u32x4{0u, 0u, 0u, 0u};
      }
      LBAR();
      float rs[4] = {};
      const unsigned ra = lds0 + rp * 2048 + lane * 16;
      unsigned so = 0;
#define RSUM()                                                                                                                        \
  do {                                                                                                                                \
    u32x4 R_[2];                                                                                                                      \
    asm volatile("ds_read_b128 %0, %2\n\tds_read_b128 %1, %2 offset:1024\n\ts_waitcnt lgkmcnt(0)" : "=&v"(R_[0]), "=&v"(R_[1]) : "v"(ra + so) : "memory"); \
    _Pragma("unroll") for (int j = 0; j < 8; ++j) {                                                                                   \
      asm volatile("v_add_f32 %0, %0, %1" : "+v"(rs[(j >> 2) * 2]) : "v"(R_[j >> 2][j & 3] << 16));                                   \
      asm volatile("v_add_f32 %0, %0, %1" : "+v"(rs[(j >> 2) * 2 + 1]) : "v"(R_[j >> 2][j & 3] & 0xffff0000u));                       \
    }                                                                                                                                 \
    so = so == (NS - 1) * SL ? 0 : so + SL;                                                                                           \
  } while (0)
      {
#pragma unroll 1
        for (int bi = 0; bi < F_AT; ++bi) {
          RSUM();
          BARRIER();
        }
        u32x4 f;
#define FLD() asm volatile("global_load_dwordx4 %0, %1, %2 sc1" : "=v"(f) : "v"(lane * 16), "s"(ws + WS_XF2 + aw * 1024) : "memory")
        FLD();
#pragma unroll 1
        for (int bi = F_AT; bi < 4; ++bi) {
          RSUM();
          BARRIER();
        }
        POLL { PDBG(it);
          asm volatile("s_waitcnt vmcnt(0)" : "+v"(f)::"memory");
          if (__all(mk64(f[1], f[0]) == tag && mk64(f[3], f[2]) == tag)) break;
          __builtin_amdgcn_s_sleep(1);
          FLD();
        }
        asm volatile("s_waitcnt vmcnt(0)" : "+v"(f)::"memory");
      }
      const unsigned xo = (unsigned)(((qx * 512 + aw * 4 + g4) * D + mt * 128) * 2 + ((l16 ^ (aw * 4 + g4)) << 4));
#pragma unroll 1
      for (int bi = 4; bi < H_AT; ++bi) {
        RSUM();
        const int i = bi - X_AT;
        if (i >= 0 && i < 12) GLDS(lds0 + XR + i * 2048 + aw * 1024, xo, x + (PROW(5 + i / 2) + (i & 1) * 8) * D);
        BARRIER();
      }
      {
        u64 f;
        const unsigned* fp = hfl + lane * 2;
        asm volatile("global_load_dwordx2 %0, %1, off sc1" : "=v"(f) : "v"(fp) : "memory");
        RSUM();
        BARRIER();
        RSUM();
        BARRIER();
        POLL { PDBG(it);
          asm volatile("s_waitcnt vmcnt(0)" : "+v"(f)::"memory");
          if (__all(f == tag)) break;
          __builtin_amdgcn_s_sleep(1);
          asm volatile("global_load_dwordx2 %0, %1, off sc1" : "=v"(f) : "v"(fp) : "memory");
        }
        asm volatile("s_waitcnt vmcnt(0)" : "+v"(f)::"memory");
        unsigned c;
        const unsigned* cp = modv + 1024 + mt * 128 + aw * 64 + lane;
        asm volatile("global_load_dword %0, %1, off sc1" : "=v"(c) : "v"(cp) : "memory");
        RSUM();
        BARRIER();
        RSUM();
        BARRIER();
        POLL { PDBG(it);
          asm volatile("s_waitcnt vmcnt(0)" : "+v"(c)::"memory");
          if (__all(c != SENT)) break;
          __builtin_amdgcn_s_sleep(1);
          asm volatile("global_load_dword %0, %1, off sc1" : "=v"(c) : "v"(cp) : "memory");
        }
        asm volatile("s_waitcnt vmcnt(0)" : "+v"(c)::"memory");
        *reinterpret_cast<unsigned*>(shm + SCA + (aw * 64 + lane) * 4) = c ^ (cc ? 0x80000000u : 0u);
      }
#pragma unroll 1
      for (int bi = H_AT + 4; bi < 32; ++bi) {
        RSUM();
        if (bi < 30) BARRIER();
      }
      unsigned* rsb = reinterpret_cast<unsigned*>(ws + WS_RS) + qx * 512;
      {
        const float v0 = dpp_add<0x141>(dpp_add<0x4E>(dpp_add<0xB1>(rs[0] + rs[1])));
        const float v1 = dpp_add<0x141>(dpp_add<0x4E>(dpp_add<0xB1>(rs[2] + rs[3])));
        if ((lane & 7) < 2 && mt * 2 + aw < 11 && !(nt == 2 && rp == 5)) ST4(rsb + cc * S + PROW(rp) + (lane & 1) * 8 + (lane >> 3), nsent((lane & 1) ? v1 : v0));
      }
      BARRIER();
#pragma unroll 1
      for (int k = 34; k <= (aw ? B_VAT : 46); ++k) BARRIER();
      u32x2 m[6];
      const unsigned lr = min(lane, 47);
      const unsigned o0 = aw ? (unsigned)(WS_Q - WS_MOD) + mt * 512 + lane * 8 : 8192u + mt * 512 + lane * 8;
      const unsigned o1 = aw ? (unsigned)(WS_RS - WS_MOD) + (qx * 512 + PROW(5 + (lr >> 3)) + (lr & 7) * 2) * 4 : o0 + 8192;
      const unsigned o2 = aw ? o1 + 8192 : o0 + 16384, o3 = aw ? o1 : 28672u + min(lane, 23) * 8;
#define VLD()                                                                                                                     \
  asm volatile("global_load_dwordx2 %0, %6, %10 sc1\n\tglobal_load_dwordx2 %1, %6, %11 sc1\n\tglobal_load_dwordx2 %2, %7, %10 sc1\n\t" \
               "global_load_dwordx2 %3, %7, %11 sc1\n\tglobal_load_dwordx2 %4, %8, %10 sc1\n\tglobal_load_dwordx2 %5, %9, %10 sc1"    \
               : "=&v"(m[0]), "=&v"(m[1]), "=&v"(m[2]), "=&v"(m[3]), "=&v"(m[4]), "=&v"(m[5])                                     \
               : "v"(o0), "v"(o1), "v"(o2), "v"(o3), "s"(modv), "s"(modv + 1024)                                                  \
               : "memory")
      VLD();
      if (aw) {
#pragma unroll
        for (int k = B_VAT + 1; k <= 45; ++k) BARRIER();
      }
      TS(30);
      POLL { PDBG(it);
        asm volatile("s_waitcnt vmcnt(0)" : W6(m)::"memory");
        if (__all(max(max(max(max2(m[0]), max2(m[1])), max(max2(m[2]), aw ? 0u : max2(m[3]))), max(max2(m[4]), max2(m[5]))) != SENT)) break;
        __builtin_amdgcn_s_sleep(2);
        VLD();
      }
      asm volatile("s_waitcnt vmcnt(0)" : W6(m)::"memory");
      if (aw == 0) {
        *reinterpret_cast<u32x2*>(shm + TGA + lane * 8) = m[0];
        *reinterpret_cast<unsigned*>(shm + TV + lane * 4) = hi2(m[2]);
        *reinterpret_cast<unsigned*>(shm + TV + 256 + lane * 4) = hi2(m[1]);
        *reinterpret_cast<unsigned*>(shm + TV + 512 + lane * 4) = hi2(m[3]);
        const float p0 = __uint_as_float(m[4][0]), p1 = __uint_as_float(m[4][1]);
        const unsigned hh = pack2(p0, p1);
        *reinterpret_cast<unsigned*>(shm + SCW + lane * 4) = hh;
        *reinterpret_cast<unsigned*>(shm + SCW + 256 + lane * 4) = pack2(p0 - bflo(hh), p1 - bfhi(hh));
        *reinterpret_cast<u32x2*>(shm + SCW + 512 + lane * 8) = u32x2{0x3f803f80u, 0x3f803f80u};
        const float u = __uint_as_float(m[5][0]) + __uint_as_float(m[5][1]), u2 = dpp_add<0xB1>(u), u8 = dpp_add<0x141>(dpp_add<0x4E>(u2));
        if (lane < 17 && !(lane & 1)) *reinterpret_cast<float*>(shm + TSUM + lane * 2) = lane < 16 ? u2 : u8;
      } else {
        BARRIER();
#pragma unroll
        for (int j = 0; j < 2; ++j) {
          const float r0 = __uint_as_float(m[0][j]), r1 = __uint_as_float(m[1][j]);
          const unsigned a0 = pack2(r0, r0), a1 = pack2(r1, r1);
          *reinterpret_cast<u32x4*>(shm + RKA + ((lane >> 4) * 32 + ((lane >> 1) & 1) * 16 + ((lane >> 2) & 3) * 4 + (lane & 1) * 2 + j) * 16) =
              u32x4{a0, pack2(r0 - bflo(a0), r0 - bflo(a0)), a1, pack2(r1 - bflo(a1), r1 - bflo(a1))};
          const float s0 = __uint_as_float(m[2][j]), s1 = -__uint_as_float(m[4][j]);
          const unsigned b0 = pack2(s0, s0 - rbf(s0)), b1 = pack2(s1, s1 - rbf(s1));
          *reinterpret_cast<u32x4*>(shm + RKB + (lr * 2 + j) * 16) = u32x4{b0, b0, b1, b1};
        }
        *reinterpret_cast<u32x2*>(shm + RKZ + lane * 8) = u32x2{0u, 0u};
        const unsigned one = g4 == (l16 >> 2) ? ((lane & 1) ? 0x3f800000u : 0x3f80u) : 0u, e0 = (lane & 2) ? 0u : one, e1 = (lane & 2) ? one : 0u;
        *reinterpret_cast<u32x4*>(shm + IDT + lane * 16) = u32x4{e0, e1, 0u, 0u};
        *reinterpret_cast<u32x4*>(shm + IDT + 1024 + lane * 16) = u32x4{0u, 0u, e0, e1};
      }
      TS(17);
      LBAR();
    }
    const int row0 = rg * 16 + l16, col1 = mt * 128 + g4 * 8;
    u32x4 xq[4], sa4[4];
#pragma unroll
    for (int q = 0; q < 4; ++q) DSR(sa4[q], saa, q * 64);
    f32x2 cj;
    float sw, ce;
    asm volatile("ds_read_b64 %0, %3\n\tds_read_b32 %1, %4\n\tds_read_b32 %2, %5" : "=&v"(cj), "=&v"(sw), "=&v"(ce) : "v"(cja), "v"(lds0 + TSUM + 32), "v"(lds0 + TSUM + mt * 4) : "memory");
    if (wid < 4) {
      auto X1A = [&](auto kc) __attribute__((always_inline)) {
        constexpr int k = decltype(kc)::value;
        (void)acc, (void)idt, (void)pv;
        MFMA(acc[0][5 + k], idt[0], pv[k]);
        MFMA(acc[1][5 + k], idt[1], pv[k]);
      };
      auto X1B = [&](auto kc) __attribute__((always_inline)) {
        constexpr int j = decltype(kc)::value;
        (void)acc, (void)ga, (void)x5, (void)xw1;
        asm volatile("" : "+v"(acc[0][5 + j]), "+v"(acc[1][5 + j]));
        const u32x4 v = pk8((j < 5 ? acc[0][j % 5] : x5[0]) + ga[0] * acc[0][5 + j], (j < 5 ? acc[1][j % 5] : x5[1]) + ga[1] * acc[1][5 + j]);
        asm volatile("ds_write_b128 %0, %1 offset:%2" ::"v"(xw1), "v"(v), "n"(j * 4096) : "memory");
      };
      asm volatile("s_waitcnt vmcnt(2)" : "+v"(pv[2]), "+v"(pv[3]), "+v"(pv[4]), "+v"(pv[5])::"memory");
      UNTIL(__all(max(max(max4(pv[2]), max4(pv[3])), max(max4(pv[4]), max4(pv[5]))) != SENT), X_R,
            { WAIT_V(0); XCHLD(); asm volatile("s_waitcnt vmcnt(0)" : W6(pv)::"memory"); });
      X1A(IC(5)); X1A(IC(4)); X1A(IC(3)); X1B(IC(5)); X1A(IC(2)); X1B(IC(4)); X1B(IC(3));
      asm volatile("s_waitcnt vmcnt(0)" : "+v"(pv[0]), "+v"(pv[1])::"memory");
#define XCHL2()                                                                                                                             \
  asm volatile("global_load_dwordx4 %0, %2, %3 offset:2048 sc1\n\tglobal_load_dwordx4 %1, %2, %3 offset:1024 sc1\n\ts_waitcnt vmcnt(0)" \
               : "=&v"(pv[0]), "=&v"(pv[1]) : "v"(lane * 16), "s"(xr) : "memory")
      UNTIL(__all(max(max4(pv[0]), max4(pv[1])) != SENT), X_R, { XCHL2(); asm volatile(".Lcpf_e2:"); });
      TS(27);
      X1A(IC(1)); X1A(IC(0)); X1B(IC(2)); X1B(IC(1)); X1B(IC(0));
    }
    {
      TS(6);
      LBAR();
      asm volatile("ds_read_b128 %0, %8\n\tds_read_b128 %1, %8 offset:1024\n\tds_read_b128 %2, %8 offset:2048\n\tds_read_b128 %3, %8 offset:3072\n\ts_waitcnt lgkmcnt(0)"
                   : "=&v"(xq[0]), "=&v"(xq[1]), "=&v"(xq[2]), "=&v"(xq[3]), "+v"(sa4[0]), "+v"(sa4[1]), "+v"(sa4[2]), "+v"(sa4[3]), "+v"(xa),
                     "+v"(cj), "+v"(sw), "+v"(ce)
                   :
                   : "memory");
      if (!act) {
        TSE();
        return;
      }
      f32x4 s4 = {0.f, 0.f, 0.f, 0.f}, gr = {0.f, 0.f, 0.f, 0.f};
#pragma unroll
      for (int q = 0; q < 4; ++q) {
        gr = MM(xq[q], xq[q], gr);
        s4 = MM(sa4[q], xq[q], s4);
      }
      const u32x4 gd = __builtin_bit_cast(u32x4, gr) & dk;
      const float m = s4[2] * (1.0f / 128);
      float q2 = ar4(__uint_as_float((gd[0] | gd[1]) | (gd[2] | gd[3]))) - s4[2] * m, lg = (s4[0] + s4[1]) - m * ce;
      if (__builtin_expect(__any(!(s4[2] * m <= 256.0f * q2)), 0)) {
        f32x4 a = {0.f, 0.f, 0.f, 0.f}, l = {0.f, 0.f, 0.f, 0.f};
#pragma unroll
        for (int q = 0; q < 4; ++q) {
          const u32x4 ch = *reinterpret_cast<const u32x4*>(shm + SCW + g4 * 16 + q * 64), cl = *reinterpret_cast<const u32x4*>(shm + SCW + 256 + g4 * 16 + q * 64);
          const f32x4 d0 = lo4(xq[q]) - m, d1 = hi4(xq[q]) - m;
          a += d0 * d0 + d1 * d1;
          l += d0 * (lo4(ch) + lo4(cl)) + d1 * (hi4(ch) + hi4(cl));
        }
        q2 = ar4((a[0] + a[1]) + (a[2] + a[3]));
        lg = ar4((l[0] + l[1]) + (l[2] + l[3]));
      }
      const u32x4 own = {nsent(m), nsent(q2), nsent(lg), 0u};
      if (g4 == 0) ST16(sto, own, ws + WS_STAT, 0);
      TS(7);
      SKEWPT();
      const bool k0 = g4 * 2 == mt, k1 = g4 * 2 + 1 == mt;
#define STOK() (sr[0] = k0 ? own : sr[0], sr[1] = k1 ? own : sr[1], __all(max(max(max(sr[0][0], sr[0][1]), sr[0][2]), max(max(sr[1][0], sr[1][1]), sr[1][2])) != SENT))
      const bool pub = mt == (rg & 7);
      if (OPT_SD) __builtin_amdgcn_s_sleep(OPT_SD);
      SPIN(STOK(), 1, STLD());
      TS(9);
      const f32x2 mk = {__uint_as_float(sr[0][0]), __uint_as_float(sr[1][0])};
      const f32x2 qk = {__uint_as_float(sr[0][1]), __uint_as_float(sr[1][1])};
      const f32x2 lk = {__uint_as_float(sr[0][2]), __uint_as_float(sr[1][2])};
      const float mean = ar4(mk[0] + mk[1]) * (1.0f / 8);
      const f32x2 dm = mk - mean;
      const f32x2 a = qk + 128.0f * (dm * dm), l = lk + dm * cj;
      const float vf = ar4(a[0] + a[1]) * (1.0f / D);
      const float rstd = __builtin_amdgcn_rsqf(vf + 1e-5f);
      const float rd = rstd * ar4(l[0] + l[1]) + sw;
      const unsigned rb = f2bf(rd);
      const unsigned ord = rb ^ ((rb & 0x8000u) ? 0xffffu : 0x8000u);
      const unsigned key = (ord << 16) | (0xffffu - (unsigned)row0);
      unsigned t1 = key, t2 = 0u;
      TOP2(0xB1); TOP2(0x4E); TOP2(0x141); TOP2(0x140);
      if (lane == 0 && pub) ST4(reinterpret_cast<u64*>(ws + WS_KEY) + rg * 16, mk64(~t2, ~t1));
      TS(1);
      SKEWPT();
      if (key >= t2) {
        const int sl = rg * 2 + (key == t2);
#pragma unroll
        for (int q = 0; q < 4; ++q) ST16(sl * 2048 + col1 * 2, nsent4(xq[q]), ws + WS_CAND, q * 64);
        if (g4 == 0 && pub) ST16(sl * 16, (u32x4{nsent(mean), nsent(rstd), 0u, 0u}), ws + WS_HDR, 0);
      }
      TS(8);
      if (OUT_D) __builtin_amdgcn_s_sleep(OUT_D);
#pragma unroll
      for (int q = 0; q < 4; ++q) {
        const u32x4 cq = *reinterpret_cast<const u32x4*>(shm + TV + q * 64 + g4 * 16), sbq = *reinterpret_cast<const u32x4*>(shm + TV + 256 + q * 64 + g4 * 16);
        const u32x4 gbq = *reinterpret_cast<const u32x4*>(shm + TV + 512 + q * 64 + g4 * 16);
        const f32x4 x0 = lo4(xq[q]), x1 = hi4(xq[q]);
        const f32x4 h0 = ((x0 - mean) * rstd) * lo4(cq) + lo4(sbq), h1 = ((x1 - mean) * rstd) * hi4(cq) + hi4(sbq);
        ST16((row0 * D + col1) * 2, pk8(x0 + lo4(gbq) * h0, x1 + hi4(gbq) * h1), out, q * 64);
      }
      WAIT_V(0);
      if (lane == 0) ST4(reinterpret_cast<unsigned*>(ws + WS_DON) + (rg * 8 + mt) * 2, 1u);
      TS(16);
    }
    TSE();
    return;
  }
  {
    const int hid = b - 192, hi = j8 - 24;
    constexpr int RNG = 65536, SSI = MISC, GP = MISC + 2048, SSA = MISC + 3072, HS = MISC + 5120, SX1 = MISC + 9216, ACT = MISC + 13312,
                  HFLG = MISC + 13568, RR = MISC + 13824;
    unsigned* flg = reinterpret_cast<unsigned*>(shm + HFLG);
    const int ec = xcd * 7 + hi, hd = hi == 7 ? 7 : (hi + 7 - ((3 + xcd) & 7) % 7) % 7;
    const bool pon = wid < ((0x06555542 >> (4 * hd)) & 7);
    const char* pfh = PFG((hd < 2 ? 2 * hd : 5 * hd - 4) + wid, 8 - qx);
#define HPF(g) do { if (pon) PFL(pfh + ((((g) + qx) & 7) << 9)); } while (0)
    WHLD(4);
    const int cb0 = xcd * 8 + ((hd + 2) & 7);
    const unsigned om = (unsigned)((wid * 16 + (lane >> 3)) * 12288 + (lane & 7) * 16);
    const unsigned og = (unsigned)((wid * 8 + (lane >> 3)) * 7168 + (lane & 7) * 16);
    const unsigned od = (unsigned)((wid * 8 + (lane >> 3)) * 2048 + (lane & 7) * 16);
    const char* pm = reinterpret_cast<const char*>(wmod) + cb0 * 128;
    const char* pg = reinterpret_cast<const char*>(wg) + ec * 128;
    const char* pu = reinterpret_cast<const char*>(wu) + ec * 128;
    const char* pd = reinterpret_cast<const char*>(wd) + (size_t)ec * 131072;
    const unsigned ow = (unsigned)(((wid & 1) * 16 + (lane >> 3)) * 12288 + (wid >> 1) * 128 + (lane & 7) * 16);
    const char* pw = reinterpret_cast<const char*>(wmod) + 8192 + xcd * 512;
#define RQM(r)                                                                                   \
  do {                                                                                           \
    const char* p_ = pm + (size_t)(r)*1572864;                                                   \
    GLDSP(lds0 + RNG + ((r)&3) * 16384 + wid * 2048, om, p_, WPOL);                              \
    GLDSP(lds0 + RNG + ((r)&3) * 16384 + wid * 2048 + 1024, om, p_ + 98304, WPOL);               \
  } while (0)
#define RQE(r)                                                                                                                \
  do {                                                                                                                        \
    GLDSP(lds0 + RNG + ((r)&3) * 16384 + wid * 1024, (r) < 16 ? og : od, (r) < 16 ? pg + (r)*458752 : pd + ((r)-16) * 256, WPOL);   \
    GLDSP(lds0 + RNG + ((r)&3) * 16384 + 8192 + wid * 1024, (r) < 16 ? og : od, (r) < 16 ? pu + (r)*458752 : pd + ((r)-16) * 256 + 128, WPOL); \
  } while (0)
    PBAR(4);
    XFLAG();
    const u32x4 sv = {SENT, SENT, SENT, SENT};
    auto PUBS = [&](int cb, unsigned v, unsigned p, unsigned r) __attribute__((always_inline)) {
      const int vi = cb >> 4, col = (cb & 15) * 64 + lane;
      ST4(modv + cb * 64 + lane, v);
      if (vi == 4) ST4(modv + 6144 + col, p);
      if (vi == 4 ? (lane & 31) == 0 : vi == 3 && lane == 0) ST4(modv + 7168 + (vi == 4 ? col >> 5 : 32 + (cb & 15)), r);
    };
    if (wid == 4 || (hi == 7 && wid < 4)) PUBS(wid == 4 ? cb0 : 64 + xcd * 4 + wid, SENT, SENT, SENT);
    if (tid >= 480) ST4(reinterpret_cast<unsigned*>(ws + WS_Q) + hsl * 32 + tid - 480, SENT);
    unsigned cv, wv;
    asm volatile("global_load_dword %0, %2, %3\n\tglobal_load_dword %1, %2, %4" : "=&v"(cv), "=&v"(wv) : "v"(tid * 4), "s"(cvec), "s"(wr_) : "memory");
    if (hi < 7) {
      if (PF_N < 3) HPF(2);
      HPF(3);
      const char* ap = ws + WS_ARM + (size_t)(ec * 8 + wid) * 3328;
#pragma unroll
      for (int j = 0; j < 4; ++j) ST16(lane * 16, sv, ap, j < 3 ? j * 1024 : 2304);
      HPF(4);
      WAIT_V(4);
    } else {
      WAIT_V(0);
    }
    asm volatile("; AUDIT_OK %0 %1" : "+v"(cv), "+v"(wv)::"memory");
    *reinterpret_cast<unsigned*>(shm + SSI + tid * 4) = pack2(silu_bf(bflo(cv)), silu_bf(bfhi(cv)));
    *reinterpret_cast<unsigned*>(shm + SX1 + tid * 4) = wv;
    LBAR();
    TS(50);
    const unsigned ha = (unsigned)(((l16 >> 2) * 8 + (lane & 3)) * 2048 + wid * 256);
#define HFR(j) *reinterpret_cast<const u32x4*>(shm + ha + ((j)&1) * 8192 + ((j) >> 2) * 128 + (((((j) >> 1) & 1) * 4 + g4) ^ (lane & 7)) * 16)
    HPF(5);
#pragma unroll
    for (int j = 0; j < 8; ++j) ST16(lane * 16, HFR(j), ws + WS_WH + (size_t)hsl * 65536 + wid * 8192 + 4096, j * 1024 - 4096);
    HPF(6);
    RQM(0);
    RQM(1);
    RQM(2);
    const unsigned tm = lds0 + RNG + ((wid >> 2) * 64 + g4 * 8 + ((lane >> 2) & 3)) * 128 + (wid & 3) * 32 + (lane & 3) * 8;
    f32x4 ma = {0.f, 0.f, 0.f, 0.f};
#define GVR(i)                                                                                                                               \
  do {                                                                                                                                       \
    _Pragma("unroll") for (int f = 0; f < 2; ++f)                                                                                            \
        ma = MM(trd(tm + ((i)&3) * 16384 + f * 4096), *reinterpret_cast<const u32x4*>(shm + SSI + ((i)*128 + (wid >> 2) * 64 + f * 32 + g4 * 8) * 2), ma); \
    if ((i) == 7 && l16 == 0) *reinterpret_cast<f32x4*>(shm + GP + wid * 64 + g4 * 16) = ma;                                                 \
  } while (0)
    const float* gp = reinterpret_cast<const float*>(shm + GP);
    auto PUB = [&](int cb, float s) __attribute__((always_inline)) {
      const int vi = cb >> 4;
      const float v = rbf(((vi == 1 || vi == 4) ? 1.0f : 0.0f) + rbf(s));
      const float p = v * bf2f(*reinterpret_cast<const u16*>(shm + SX1 + ((cb & 15) * 64 + lane) * 2));
      const float r16 = row_sum(p), r32 = swap16_add(r16, r16);
      PUBS(cb, nsent(v), nsent(p), nsent(vi == 4 ? r32 : swap32_add(r32, r32)));
    };
    {
      sfor<8>([&](auto ii) __attribute__((always_inline)) {
        constexpr int i = decltype(ii)::value;
        (void)ow, (void)pw, (void)om, (void)pm, (void)og, (void)od, (void)pg, (void)pu, (void)pd, (void)lds0, (void)wid, (void)pfl, (void)Yx, (void)l16, (void)g4, (void)xcd, (void)hfl, (void)lane, (void)hid;
        TSCAP();
        WAIT_V(4);
        LBAR();
        if (wid == 2) TS(i);
        if constexpr (i == 0) { if (tid == 0) ST4(reinterpret_cast<u64*>(hfl + hid * 2), tag); }
        if constexpr (i == 0) HPF(7);
        if constexpr (i < 5) {
          RQM(i + 3);
        } else {
          const bool e = hid < 56;
          const char* p_ = pw + (i - 5) * 393216;
          GLDSP(lds0 + RNG + (i - 5) * 16384 + wid * (e ? 1024 : 2048), e ? og : ow, e ? pg + (i - 5) * 458752 : p_, WPOL);
          GLDSP(lds0 + RNG + (i - 5) * 16384 + wid * (e ? 1024 : 2048) + (e ? 8192 : 1024), e ? og : ow, e ? pu + (i - 5) * 458752 : p_ + 98304, WPOL);
        }
        GVR(i);
      });
    }
    TS(51);
#define PUB0() do { if (wid == 0) PUB(cb0, gp[lane] + gp[64 + lane]); } while (0)
    u64 sf, hf;
#define SFLD()                                                                                                                     \
  asm volatile("s_mov_b64 exec, 3\n\tglobal_load_dwordx2 %0, %2, %3 sc1\n\ts_mov_b64 exec, 0xff\n\tglobal_load_dwordx2 %1, %5, %4 sc1\n\ts_mov_b64 exec, -1" \
               : "=&v"(sf), "=&v"(hf) : "v"(lane * 128), "s"(ws + WS_SFL + wid * 256), "s"(ws + WS_HFL + wid * 64), "v"(lane * 8) : "memory")
    auto SAW = [&]() __attribute__((always_inline)) {
      POLL { PDBG(it);
        if (((__builtin_amdgcn_ballot_w64(sf != tag) & 3u) | (__builtin_amdgcn_ballot_w64(hf != tag) & 0xffu)) == 0) break;
        __builtin_amdgcn_s_sleep(4);
        SFLD();
        asm volatile("s_waitcnt vmcnt(0)" : "+v"(sf), "+v"(hf)::"memory");
      }
    };
    u32x2 s2;
#define SALD() asm volatile("global_load_dwordx2 %0, %1, %2 sc1" : "=v"(s2) : "v"(tid * 8), "s"(modv) : "memory")
    auto QMM = [&]() __attribute__((always_inline)) {
      *reinterpret_cast<unsigned*>(shm + SSA + tid * 4) = hi2(s2);
      f32x4 qa[2] = {};
#pragma unroll
      for (int j = 0; j < 8; ++j) qa[j & 1] = MM(HFR(j), *reinterpret_cast<const u32x4*>(shm + SSA + wid * 256 + (j >> 1) * 64 + g4 * 16), qa[j & 1]);
      if (l16 < 2) *reinterpret_cast<f32x4*>(shm + HS + wid * 128 + g4 * 32 + l16 * 16) = l16 ? qa[1] : qa[0];
    };
    auto QPUB = [&]() __attribute__((always_inline)) {
      if (tid >= 480) {
        float s = 0.f;
#pragma unroll
        for (int w = 0; w < 8; ++w) s += *reinterpret_cast<const float*>(shm + HS + w * 128 + (tid - 480) * 4);
        ST4(reinterpret_cast<unsigned*>(ws + WS_Q) + hsl * 32 + tid - 480, nsent(s));
      }
    };
    u32x8 GU[16], DN[8];
    if (hid < 56) {
      const unsigned tg = lds0 + RNG + (g4 * 8 + ((lane >> 2) & 3)) * 128 + ((lane >> 1) & 1) * 8192 + wid * 16 + (lane & 1) * 8;
      const unsigned td = lds0 + RNG + (g4 * 8 + ((lane >> 2) & 3)) * 128 + (wid >> 2) * 8192 + (wid & 3) * 32 + (lane & 3) * 8;
      sfor<24>([&](auto rr) __attribute__((always_inline)) {
        constexpr int r = decltype(rr)::value;
        (void)td, (void)tg, (void)GU, (void)DN, (void)og, (void)od, (void)pg, (void)pu, (void)pd, (void)lds0, (void)wid, (void)pfl, (void)Yx, (void)l16, (void)g4, (void)xcd;
        (void)s2, (void)modv, (void)tid;
        (void)hid, (void)lane, (void)sf, (void)hf;
        (void)sv, (void)hi, (void)ws;
#define EX(r) (((r) < 0 ? 0 : (r) < 2 ? 4 : (r) == 2 ? 2 : 0) + ((r) == Q_AT - 6 ? 2 : 0) + ((r) == Q_AT - 3))
        WAIT_V(r > 21 ? 2 * (23 - r) : 4 + EX(r - 2) + EX(r - 1));
        LBAR();
        TSCAP();
        if (wid == 3) TS(r);
        if constexpr (r == 0) PUB0();
        if constexpr (r == 1) {
          if (cb0 < 16 && wid == 0) {
            WAIT_V(2 + EX(0));
            if (lane == 0) ST4(reinterpret_cast<u64*>(ws + WS_SFL) + cb0 * 16, tag);
          }
        }
        if constexpr (r == Q_AT - 6) SFLD();
        if constexpr (r == Q_AT - 3) {
          asm volatile("" : "+v"(sf), "+v"(hf));
          SAW();
          SALD();
        }
        if constexpr (r < 3) {
          const char* ap = ws + WS_XCH + xcd * 589824 + (hi * 8 + wid) * 10240 + r * 4096;
#pragma unroll
          for (int j = 0; j < (r < 2 ? 4 : 2); ++j) ST16(lane * 16, sv, ap, j * 1024);
          if (r == 2 && wid < 3) ST16(lane * 16, sv, ws + WS_XCH + xcd * 589824 + min(560 + hi * 3 + wid, 575) * 1024, 0);
        }
        if constexpr (r < 21) RQE(r + 3);
        if constexpr (r < 16) {
          GU[r] = trd2(tg + (r & 3) * 16384);
          asm volatile("" : "+v"(GU[r]));
        } else {
          DN[r - 16] = trd2(td + (r & 3) * 16384);
          asm volatile("" : "+v"(DN[r - 16]));
        }
        if constexpr (r == Q_AT) {
          asm volatile("" : "+v"(s2));
          QMM();
        }
        if constexpr (r == Q_AT + 1) QPUB();
      });
      TS(52);
    } else {
      const unsigned tw = lds0 + RNG + (wid >> 1) * 4096 + (g4 * 8 + ((lane >> 2) & 3)) * 128 + (wid & 1) * 64 + (lane & 3) * 8;
      f32x4 mb[2] = {};
      auto WR = [&](int r0, int r1) __attribute__((always_inline)) {
#pragma unroll 1
        for (int r = r0; r < r1; ++r) {
          WAIT_V(4);
          LBAR();
          if (wid == 2 && r < 22) TSD(r + 8);
          const char* p_ = pw + min(r + 3, 31) * 393216;
          GLDSP(lds0 + RNG + ((r + 3) & 3) * 16384 + wid * 2048, ow, p_, WPOL);
          GLDSP(lds0 + RNG + ((r + 3) & 3) * 16384 + wid * 2048 + 1024, ow, p_ + 98304, WPOL);
          const u32x4 sf = *reinterpret_cast<const u32x4*>(shm + SSI + (r * 32 + g4 * 8) * 2);
          mb[0] = MM(trd(tw + (r & 3) * 16384), sf, mb[0]);
          mb[1] = MM(trd(tw + (r & 3) * 16384 + 32), sf, mb[1]);
        }
      };
      WR(0, 1);
      PUB0();
      WR(1, 4);
      if (cb0 < 16 && tid == 0) ST4(reinterpret_cast<u64*>(ws + WS_SFL) + cb0 * 16, tag);
      WR(4, MQ_AT - 6);
      SFLD();
      WR(MQ_AT - 6, MQ_AT - 3);
      asm volatile("; AUDIT_OK %0 %1" : "+v"(sf), "+v"(hf));
      SAW();
      SALD();
      WR(MQ_AT - 3, MQ_AT);
      asm volatile("; AUDIT_OK %0" : "+v"(s2));
      QMM();
      WR(MQ_AT, 32);
      if (l16 < 2) *reinterpret_cast<f32x4*>(shm + GP + wid * 128 + l16 * 64 + g4 * 16) = l16 ? mb[1] : mb[0];
      LBAR();
      if (wid < 4) PUB(64 + xcd * 4 + wid, gp[tid]);
      QPUB();
      TS(52);
      WAIT_V(0);
      TSE();
      return;
    }
    bool early = false;
    if (tid == 0) {
      POLL { PDBG(it);
        unsigned g;
        asm volatile("global_load_dword %0, %1, %2 sc1\n\ts_waitcnt vmcnt(0)" : "=&v"(g) : "v"(0u), "s"(ws + WS_WAK + hid * 128) : "memory");
        if (g != SENT) break;
        early = true;
        __builtin_amdgcn_s_sleep(OPT_KS);
      }
    }
    BARRIER();
    TS(21);
    const int tok = hid & 1, col = (hid & 16) * 32 + (lane & 3) * 128 + (hid & 14) * 8 + (lane >> 2);
    u32x4 c4, s4;
    unsigned gbu;
    POLL { PDBG(it);
      asm volatile("global_load_dwordx4 %0, %3, %5 sc1\n\tglobal_load_dwordx4 %1, %3, %6 sc1\n\tglobal_load_dword %2, %4, %7 sc1\n\ts_waitcnt vmcnt(0)"
                   : "=&v"(c4), "=&v"(s4), "=&v"(gbu) : "v"((tid & 255) * 16), "v"(col * 4), "s"(modv + 4096), "s"(modv + 3072), "s"(modv + 5120) : "memory");
      if (__all(max(max(max4(c4), max4(s4)), gbu) != SENT)) break;
      __builtin_amdgcn_s_sleep(8);
    }
    const f32x4 cb2 = __builtin_bit_cast(f32x4, c4), sb2 = __builtin_bit_cast(f32x4, s4);
    unsigned hra = lds0 + HS + g4 * 64 + (lane & 4) * 8 + (lane & 1) * 16, hwa = lds0 + HS + ((tid & 255) >> 2) * 64 + (wid >> 2) * 32 + (tid & 3) * 4;
    unsigned pto = (g4 & 1) * 4096 + ((g4 >> 1) * 8 + wid) * 256 + l16 * 16, six = (lane & 1) ? 0xEEEEu : 0x4444u;
    unsigned awa = lds0 + ACT + (wid >> 1) * 64 + g4 * 32 + (lane & 2) * 8 + (wid & 1) * 8 + (lane & 4) + (lane & 1) * 2;
    f32x4 d = {0.f, 0.f, 0.f, 0.f};
    unsigned sla = lds0 + HFLG + 8 + (wid >> 2) * 4;
    asm volatile("" : "+v"(sla), "+v"(hra), "+v"(hwa), "+v"(pto), "+v"(awa), "+v"(six), "+v"(d));
    const float gb = __uint_as_float(gbu);
    if (wid == 0) {
      u32x2 g0 = {SENT, SENT}, g1 = {SENT, SENT};
      const char* gp = ws + WS_KEY + lane * 128;
      if (__any(early)) __builtin_amdgcn_s_sleep(OPT_KD);
      TS(20);
      u64 m0 = ~0ull, m1 = ~0ull;
      int left = PCAP;
      do {
        asm volatile("s_sleep 1\n\ts_mov_b64 exec, %4\n\tglobal_load_dwordx2 %0, %2, off sc1\n\ts_mov_b64 exec, %5\n\tglobal_load_dwordx2 %1, %3, off sc1\n\t"
                     "s_mov_b64 exec, -1\n\ts_waitcnt vmcnt(0)"
                     : "+v"(g0), "+v"(g1) : "v"(gp), "v"(gp + 8192), "s"(m0), "s"(m1) : "memory");
        m0 = __builtin_amdgcn_ballot_w64(max2(g0) == SENT);
        m1 = __builtin_amdgcn_ballot_w64(max2(g1) == SENT);
        left = (m0 | m1) ? left - 1 : 0;
        PDBG(PCAP - left);
      } while (left);
      g0 = ~g0;
      g1 = ~g1;
      const unsigned a1 = max(g0[0], g1[0]), a2 = max(min(g0[0], g1[0]), max(g0[1], g1[1]));
      const unsigned k1 = wave_max_u(a1);
      const unsigned k2 = wave_max_u(a1 == k1 ? a2 : a1);
      const int rg1 = key_row(k1) >> 4, rg2 = key_row(k2) >> 4;
      asm volatile("ds_write_b128 %0, %1" ::"v"(lds0 + HFLG), "v"(u32x4{k1, k2, rg1 * 32u, rg2 * 32u + (rg2 == rg1) * 16u}) : "memory");
      TS(22);
    }
    LBAR();
    {
      unsigned sl;
      asm volatile("ds_read_b32 %0, %1\n\ts_waitcnt lgkmcnt(0)" : "=&v"(sl) : "v"(sla) : "memory");
      u32x2 v, hd;
#define CLD()                                                                                                          \
  asm volatile("global_load_dwordx2 %0, %2, %4 sc1\n\tglobal_load_dwordx2 %1, %3, %5 sc1\n\ts_waitcnt vmcnt(0)"        \
               : "=&v"(v), "=&v"(hd) : "v"(sl * 128 + (tid & 255) * 8), "v"(sl), "s"(ws + WS_CAND), "s"(ws + WS_HDR) : "memory")
      CLD();
      UNTIL(__all(max(max2(v), max2(hd)) != SENT), 1, { CLD(); asm volatile(".Lcpf_e1:"); });
      TS(23);
      const float mean = __uint_as_float(hd[0]), rstd = __uint_as_float(hd[1]);
      const f32x4 h = ((f32x4{bflo(v[0]), bfhi(v[0]), bflo(v[1]), bfhi(v[1])} - mean) * rstd) * cb2 + sb2;
      asm volatile("ds_write2_b32 %0, %1, %2 offset1:4" ::"v"(hwa), "v"(pack2(h[0], h[1])), "v"(pack2(h[2], h[3])) : "memory");
      *reinterpret_cast<u32x2*>(shm + SX1 + tid * 8) = v;
    }
    SKEWPT();
    TS(26);
    LBAR();
    TS(27);
    f32x4 dn[8];
    {
      u32x4 hb[8];
      sfor<23>([&](auto ff) __attribute__((always_inline)) {
        constexpr int f = decltype(ff)::value, c = f - 7;
        (void)d, (void)hb, (void)GU, (void)hra, (void)six;
        if constexpr (c >= 0) {
          WAIT_L(f <= 16 ? 6 : 22 - f);
          SMF(d, hb[c & 7], GU[c], six);
        }
        if constexpr (f < 16) DSR(hb[f & 7], hra, f * 256);
      });
      asm volatile("s_nop 7" : "+v"(d));
      TS(29);
      const float dv = rbf(d[0] + d[1]), uv = dpp_f<0x128>(dv);
      const unsigned av = f2bf(rbf(dv * __builtin_amdgcn_rcpf(1.0f + __expf(-dv))) * uv);
      asm volatile("s_mov_b64 exec, 0xff00ff\n\tds_write_b16 %0, %1\n\ts_mov_b64 exec, -1" ::"v"(awa), "v"(av) : "memory");
    }
#pragma unroll
    for (int c = 0; c < 8; ++c) asm volatile("v_mov_b64 %0, 0" : "=v"(dn[c].lo));
    asm volatile("" : W8(dn));
    TS(24);
    LBAR();
    {
      u32x4 a0;
      asm volatile("ds_read_b128 %0, %1 offset:%2\n\ts_waitcnt lgkmcnt(0)" : "=&v"(a0) : "v"(hra), "n"(ACT - HS) : "memory");
#pragma unroll
      for (int c = 0; c < 8; ++c) SMF(dn[(c >> 1) + (c & 1) * 4], a0, DN[(c >> 1) + (c & 1) * 4], six);
      asm volatile("s_nop 7" : W8(dn));
      u32x4 o;
#pragma unroll
      for (int i = 0; i < 4; ++i) o[i] = nsent((g4 & 2) ? dn[4 + i][0] + dn[4 + i][1] : dn[i][0] + dn[i][1]);
      ST16(pto, o, ws + WS_PART + ec * 8192, 0);
    }
    TS(25);
    SKEWPT();
    if (hid >= 32) {
      if (hi == 6 && wid == 1 && lane < 35) {
        char* fp = ws + (lane < 32 ? WS_XFL + (xcd * 32 + lane) * 128 : lane < 34 ? WS_SFL + (xcd * 2 + lane - 32) * 128 : WS_EPO);
        if (lane < 34 || xcd == 0) ST4(reinterpret_cast<u64*>(fp), lane < 34 ? ~tag : tag);
      }
      if (hid == 40 && tid < 320) ST4(reinterpret_cast<u64*>(ws + WS_HFL) + tid, ~tag);
      TSE();
      return;
    }
    const unsigned kk = flg[tok];
    const int row = key_row(kk);
    float* rr = reinterpret_cast<float*>(shm + RR);
    float x1 = bf2f(*reinterpret_cast<const u16*>(shm + SX1 + tok * 2048 + col * 2));
    float h2 = bf2f(*reinterpret_cast<const u16*>(shm + HS + (col >> 4) * 64 + tok * 32 + (col & 2) * 8 + (col & 12) + (col & 1) * 2));
    float rsg = rbf(1.0f / (1.0f + __expf(-key_val(kk))));
    unsigned oo3 = (unsigned)(row * D + col) * 2, rra = lds0 + RR + lane * 32;
    asm volatile("" : "+v"(rsg), "+v"(x1), "+v"(h2), "+v"(oo3), "+v"(rra));
    if (wid == 7) {
      POLL { PDBG(it);
        unsigned g;
        asm volatile("global_load_dword %0, %1, %2 sc1\n\ts_waitcnt vmcnt(0)" : "=&v"(g) : "v"(((row >> 4) * 8 + (col >> 7)) * 8), "s"(ws + WS_DON) : "memory");
        if (__all(g != SENT)) break;
        __builtin_amdgcn_s_sleep(2);
      }
    } else {
      const unsigned po = (unsigned)((wid * 8 + g4) * 8192 + tok * 4096 + (hid >> 1) * 256 + l16 * 16);
      u32x4 v[2];
      if (OPT_RD) __builtin_amdgcn_s_sleep(OPT_RD);
#define PLD()                                                                                                          \
  asm volatile("global_load_dwordx4 %0, %2, %3 sc1\n\tglobal_load_dwordx4 %1, %2, %4 sc1\n\ts_waitcnt vmcnt(0)"        \
               : "=&v"(v[0]), "=&v"(v[1]) : "v"(po), "s"(ws + WS_PART), "s"(ws + WS_PART + 32768) : "memory")
      SPIN(__all(max(max4(v[0]), max4(v[1])) != SENT), OPT_RR, PLD());
      TS(28);
      const f32x4 t = __builtin_bit_cast(f32x4, v[0]) + __builtin_bit_cast(f32x4, v[1]);
      rr[(l16 * 4 + g4) * 8 + wid] = swap16_add(swap32_add(t[0], t[2]), swap32_add(t[1], t[3]));
    }
    LBAR();
    TS(5);
    if (wid == 0) {
      f32x4 r0, r1;
      asm volatile("ds_read_b128 %0, %2\n\tds_read_b128 %1, %2 offset:16\n\ts_waitcnt lgkmcnt(0)" : "=&v"(r0), "=&v"(r1) : "v"(rra) : "memory");
      const float sum = ((r0[0] + r0[1]) + (r0[2] + r0[3])) + ((r1[0] + r1[1]) + r1[2]);
      asm volatile("global_store_short %0, %1, %2" ::"v"(oo3), "v"(f2bf(x1 + gb * rbf(h2 + rbf(rbf(sum) * rsg)))), "s"(out) : "memory");
    }
    TS(15);
    TSE();
  }
}

extern "C" void kernel_launch(void* const* d_in, const int* in_sizes, int n_in, void* d_out, int out_size, void* d_ws, size_t ws_size,
                              hipStream_t stream) {
  const u16* x = (const u16*)d_in[0];
  const u16* wsr = (const u16*)d_in[1];
  const u16* wsi = (const u16*)d_in[2];
  const u16* whr = (const u16*)d_in[3];
  const u16* whi = (const u16*)d_in[4];
  const u16* w_r = (const u16*)d_in[6];
  const u16* w_gate = (const u16*)d_in[7];
  const u16* w_up = (const u16*)d_in[8];
  const u16* w_down = (const u16*)d_in[9];
  const u16* c = (const u16*)d_in[10];
  const u16* w_mod = (const u16*)d_in[11];
  u16* out = (u16*)d_out;
  k_main<<<256, 512, 0, stream>>>(x, (char*)d_ws, wsr, wsi, c, w_mod, whr, whi, w_r, w_gate, w_up, w_down, out);
}
```

```cpp
#include <hip/hip_runtime.h>
#include <hip/hip_bf16.h>
#include <stdint.h>
#include <utility>

typedef unsigned short u16;
typedef unsigned long long u64;
using bf16x8 = __attribute__((ext_vector_type(8))) short;
using bf16v8 = __attribute__((ext_vector_type(8))) __bf16;
using f32x4 = __attribute__((ext_vector_type(4))) float;
using f32x2 = __attribute__((ext_vector_type(2))) float;
using u32x4 = __attribute__((ext_vector_type(4))) unsigned;
using u32x2 = __attribute__((ext_vector_type(2))) unsigned;

constexpr int S = 2048, D = 1024, F = 3584;

__device__ __forceinline__ float bf2f(unsigned v) { return __uint_as_float(v << 16); }
__device__ __forceinline__ float bflo(unsigned v) { return __uint_as_float(v << 16); }
__device__ __forceinline__ float bfhi(unsigned v) { return __uint_as_float(v & 0xffff0000u); }
__device__ __forceinline__ unsigned f2bf(float f) {
  __hip_bfloat16 b = __float2bfloat16(f);
  return (unsigned)*reinterpret_cast<u16*>(&b);
}
__device__ __forceinline__ float rbf(float f) { return bf2f(f2bf(f)); }
using bf16v2 = __attribute__((ext_vector_type(2))) __bf16;
__device__ __forceinline__ unsigned pack2(float lo, float hi) {
  return __builtin_bit_cast(unsigned, __builtin_convertvector(f32x2{lo, hi}, bf16v2));
}

template <int CTRL>
__device__ __forceinline__ float dpp_f(float v) {
  return __int_as_float(__builtin_amdgcn_mov_dpp(__float_as_int(v), CTRL, 0xf, 0xf, true));
}
template <int CTRL>
__device__ __forceinline__ float dpp_add(float v) { return v + dpp_f<CTRL>(v); }
__device__ __forceinline__ float row_sum(float v) { return dpp_add<0x140>(dpp_add<0x141>(dpp_add<0x4E>(dpp_add<0xB1>(v)))); }
template <int CTRL>
__device__ __forceinline__ unsigned dpp_u(unsigned v) { return (unsigned)__builtin_amdgcn_mov_dpp((int)v, CTRL, 0xf, 0xf, true); }
__device__ __forceinline__ unsigned wave_max_u(unsigned v) {
  v = max(v, dpp_u<0xB1>(v));
  v = max(v, dpp_u<0x4E>(v));
  v = max(v, dpp_u<0x141>(v));
  v = max(v, dpp_u<0x140>(v));
  const unsigned a = (unsigned)__builtin_amdgcn_readlane((int)v, 0), b = (unsigned)__builtin_amdgcn_readlane((int)v, 16);
  const unsigned c = (unsigned)__builtin_amdgcn_readlane((int)v, 32), d = (unsigned)__builtin_amdgcn_readlane((int)v, 48);
  return max(max(a, b), max(c, d));
}
__device__ __forceinline__ u64 mk64(unsigned hi, unsigned lo) { return ((u64)hi << 32) | lo; }
__device__ __forceinline__ float swap32_add(float a, float b) {
  auto r = __builtin_amdgcn_permlane32_swap(__float_as_uint(a), __float_as_uint(b), false, false);
  return __uint_as_float(r[0]) + __uint_as_float(r[1]);
}
__device__ __forceinline__ float swap16_add(float a, float b) {
  auto r = __builtin_amdgcn_permlane16_swap(__float_as_uint(a), __float_as_uint(b), false, false);
  return __uint_as_float(r[0]) + __uint_as_float(r[1]);
}
__device__ __forceinline__ float ar4(float v) {
  const float t = swap32_add(v, v);
  return swap16_add(t, t);
}
__device__ __forceinline__ float wave_sum_s(float v) {
  v = row_sum(v);
  v += __int_as_float(__builtin_amdgcn_update_dpp(0, __float_as_int(v), 0x142, 0xa, 0xf, false));
  v += __int_as_float(__builtin_amdgcn_update_dpp(0, __float_as_int(v), 0x143, 0xc, 0xf, false));
  return __int_as_float(__builtin_amdgcn_readlane(__float_as_int(v), 63));
}
__device__ __forceinline__ float silu_bf(float c) { return rbf(c * __builtin_amdgcn_rcpf(1.0f + __expf(-c))); }
__device__ __forceinline__ float dsum(unsigned v, float a) {
  return __builtin_amdgcn_fdot2_f32_bf16(__builtin_bit_cast(bf16v2, v), __builtin_bit_cast(bf16v2, 0x3f803f80u), a, false);
}
template <class Fn, int... I>
__device__ __forceinline__ void sfor_(Fn&& f, std::integer_sequence<int, I...>) { (f(std::integral_constant<int, I>{}), ...); }
template <int N, class Fn>
__device__ __forceinline__ void sfor(Fn&& f) { sfor_(f, std::make_integer_sequence<int, N>{}); }
constexpr unsigned SENT = 0xFFFFFFFFu;

constexpr size_t WS_EPO = 0;
constexpr size_t WS_XFL = 4096;
constexpr size_t WS_HFL = 40960;
constexpr size_t WS_XF2 = WS_HFL + 512;
constexpr size_t WS_SFL = WS_HFL + 8192;
constexpr size_t WS_MOD = 65536;
constexpr size_t WS_Q = WS_MOD + 32768;
constexpr size_t WS_ARM = WS_Q + 8192;
constexpr size_t WS_RS = WS_ARM;
constexpr size_t WS_KEY = WS_RS + 16384;
constexpr size_t WS_HDR = WS_KEY + 16384;
constexpr size_t WS_UFL = WS_HDR + 4096;
constexpr size_t WS_DON = WS_UFL + 98304;
constexpr size_t WS_STAT = WS_DON + 12288;
constexpr size_t WS_CAND = WS_STAT + 262144;
constexpr size_t WS_PART = WS_CAND + 524288;
constexpr size_t WS_WAK = WS_PART + 483328;
static_assert(WS_WAK + 8192 <= WS_ARM + 448 * 3328, "armed region");
constexpr size_t WS_XCH = 2u << 20;
constexpr size_t WS_H = 8u << 20;
constexpr size_t WS_WH = 12u << 20;
constexpr size_t WS_U = 16u << 20;
constexpr size_t WS_DBG = 64u << 20;
#define NEWDESIGN 1

#define PCAP (1 << 20)
#define PDBG(it)
#define TS(i)
#define TSCAP()
#define TSC0()
#define TSC1(i)
#define TSD(i)
#define TSE()
#define ST16(voff, v, base, off) asm volatile("global_store_dwordx4 %0, %1, %2 offset:%3 sc1\n\ts_nop 1" ::"v"(voff), "v"(v), "s"(base), "n"(off) : "memory")
#define ST4(p, v) __hip_atomic_store(p, v, __ATOMIC_RELAXED, __HIP_MEMORY_SCOPE_AGENT)
#define WAIT_V(n) asm volatile("s_waitcnt vmcnt(%0)" ::"n"(n) : "memory")
#define WAIT_L(n) asm volatile("s_waitcnt lgkmcnt(%0)" ::"n"(n) : "memory")
#define BARRIER() asm volatile("s_barrier" ::: "memory")
#define LBAR() asm volatile("s_waitcnt lgkmcnt(0)\n\ts_barrier" ::: "memory")
#define DSR(dst, addr, off) asm volatile("ds_read_b128 %0, %1 offset:%2" : "=v"(dst) : "v"(addr), "n"(off) : "memory")
#define MFMA(c, a, b) asm volatile("v_mfma_f32_16x16x32_bf16 %0, %1, %2, %0" : "+v"(c) : "v"(a), "v"(b))
#define MFMAI(c, a, b) asm volatile("v_mfma_f32_16x16x32_bf16 %0, %1, %2, %0" ::"v"(c), "v"(a), "v"(b))
#define MFMA0(c, a, b) asm volatile("v_mfma_f32_16x16x32_bf16 %0, %1, %2, 0" : "=&v"(c) : "v"(a), "v"(b))
#define GLDSP(m0v, voff, gbase, pol) \
  asm volatile("s_mov_b32 m0, %0\n\ts_nop 0\n\tglobal_load_lds_dwordx4 %1, %2" pol ::"s"(m0v), "v"(voff), "s"(gbase) : "memory")
#define GLDS(m0v, voff, gbase) GLDSP(m0v, voff, gbase, "")
#define WPOL " nt"
#define LDXS(dst, voff, base, off) asm volatile("global_load_dwordx4 %0, %1, %2 offset:%3 sc1" : "=v"(dst) : "v"(voff), "s"(base), "n"(off) : "memory")
#define BF8(v) __builtin_bit_cast(bf16v8, v)
#define MM(a, b, c) __builtin_amdgcn_mfma_f32_16x16x32_bf16(BF8(a), BF8(b), c, 0, 0, 0)
#define W6(a) "+v"(a[0]), "+v"(a[1]), "+v"(a[2]), "+v"(a[3]), "+v"(a[4]), "+v"(a[5])
#define W8(a) W6(a), "+v"(a[6]), "+v"(a[7])
#define PFL(ptr) asm volatile("s_mov_b32 m0, %0\n\ts_nop 0\n\tglobal_load_lds_dword %1, off" ::"s"(pfl), "v"(ptr) : "memory")
using s16x4 = __attribute__((ext_vector_type(4))) short;
__device__ __forceinline__ u32x4 trd(unsigned a) {
  const u32x2 lo = __builtin_bit_cast(u32x2, __builtin_amdgcn_ds_read_tr16_b64_v4i16((__attribute__((address_space(3))) s16x4*)(size_t)a));
  const u32x2 hi = __builtin_bit_cast(u32x2, __builtin_amdgcn_ds_read_tr16_b64_v4i16((__attribute__((address_space(3))) s16x4*)(size_t)(a + 512)));
  return u32x4{lo[0], lo[1], hi[0], hi[1]};
}
using u32x8 = __attribute__((ext_vector_type(8))) unsigned;
__device__ __forceinline__ u32x8 trd2(unsigned a) {
  const u32x4 lo = trd(a), hi = trd(a + 4096);
  return u32x8{lo[0], lo[1], lo[2], lo[3], hi[0], hi[1], hi[2], hi[3]};
}
#define SMF(c, a, b, i) asm volatile("v_smfmac_f32_16x16x64_bf16 %0, %1, %2, %3" ::"v"(c), "v"(a), "v"(b), "v"(i))
__device__ __forceinline__ int key_row(unsigned k) { return (int)((0xffffu - (k & 0xffffu)) & 2047u); }
__device__ __forceinline__ float key_val(unsigned k) {
  const unsigned ord = k >> 16;
  return bf2f(ord ^ ((ord & 0x8000u) ? 0x8000u : 0xffffu));
}
__device__ __forceinline__ unsigned nsent(float v) { return min(__float_as_uint(v), SENT - 1u); }
__device__ __forceinline__ f32x4 lo4(u32x4 q) { return f32x4{bflo(q[0]), bfhi(q[0]), bflo(q[1]), bfhi(q[1])}; }
__device__ __forceinline__ f32x4 hi4(u32x4 q) { return f32x4{bflo(q[2]), bfhi(q[2]), bflo(q[3]), bfhi(q[3])}; }
__device__ __forceinline__ u32x4 pk8(f32x4 a, f32x4 b) { return u32x4{pack2(a[0], a[1]), pack2(a[2], a[3]), pack2(b[0], b[1]), pack2(b[2], b[3])}; }
__device__ __forceinline__ u32x4 nsent4(u32x4 v) { return u32x4{min(v[0], SENT - 1u), min(v[1], SENT - 1u), min(v[2], SENT - 1u), min(v[3], SENT - 1u)}; }
__device__ __forceinline__ unsigned max4(u32x4 v) { return max(max(v[0], v[1]), max(v[2], v[3])); }
__device__ __forceinline__ unsigned max2(u32x2 v) { return max(v[0], v[1]); }
__device__ __forceinline__ unsigned hi2(u32x2 v) { return (v[0] >> 16) | (v[1] & 0xffff0000u); }
#define TOP2(CTRL)                                                  \
  do {                                                              \
    const unsigned o1 = dpp_u<CTRL>(t1), o2 = dpp_u<CTRL>(t2);      \
    t2 = max(min(t1, o1), max(t2, o2));                             \
    t1 = max(t1, o1);                                               \
  } while (0)
#define POLL _Pragma("unroll 1") for (int it = 0; it < PCAP; ++it)
#define SPIN(OK, PAUSE, LD)                                 \
  do {                                                      \
    int left_ = PCAP;                                       \
    do {                                                    \
      __builtin_amdgcn_s_sleep(PAUSE);                      \
      LD;                                                   \
      left_ = (OK) ? 0 : left_ - 1;                         \
      PDBG(PCAP - left_);                                   \
    } while (left_);                                        \
  } while (0)
#define UNTIL(OK, PAUSE, LD)                                \
  do {                                                      \
    if (__builtin_expect(!(OK), 0)) {                       \
      POLL { PDBG(it);                                      \
        __builtin_amdgcn_s_sleep(PAUSE);                    \
        LD;                                                 \
        if (OK) break;                                      \
      }                                                     \
    }                                                       \
  } while (0)
#define P_D3 16
#define P_D4 2
#define U_D 28
#define U_R 1
#define X_R 1
#define OPT_SD 15
#define OPT_KS 2
#define OPT_KD 32
#define OPT_RD 8
#define OPT_RR 2
#define OUT_D 20
#define CPF_N 10
#define CPF_K 24
#define CPF_B 10
#define PF_N 3
#define A_LD 3
#define F_AT 3
#define X_AT 12
#define H_AT 24
#define B_VAT 43
#define UF_AT 8
#define B46 339
#define TP_XL 340
#define TP_X2 350
#define Q_AT 10
#define MQ_AT 31
#define XQ0 2
#define XQ1 4
#define XQ2 18
#define XQ3 20
#define HX0 9
#define HXS 2
static_assert(HX0 + 11 * HXS < 44 && UF_AT <= 22 && A_LD <= 3 && H_AT <= 24 && X_AT + 12 <= H_AT && Q_AT >= 7, "");
constexpr int SL = 22528, NS = 5, XR = NS * SL, MISC = XR + 24576;
constexpr int as(int c) { return c < 3 ? 2 : c < 6 ? 3 : 2 + (c & 1); }
constexpr int ap(int c) { return c < 3 ? c : c < 6 ? c - 3 : c / 2; }
constexpr int ug(int j) { return j < 9 ? 18 + 7 * j : 82; }
constexpr int E0 = 264, E3 = E0 + 88, NE = E3 + 6;
constexpr int es(int c) { return c < 132 ? c / 11 : c < 144 ? 12 + (c - 132) / 6 : c < 156 ? 14 + (c & 1) : 12 + (c & 3); }
constexpr int ep(int c) { return c < 132 ? c % 11 : c < 144 ? (c - 132) % 6 : c < 156 ? (c - 144) / 2 : 6 + (c - 156) / 4; }

__global__ __launch_bounds__(512, 2) void k_main(const u16* __restrict__ x, char* ws, const u16* __restrict__ Y0, const u16* __restrict__ Y1,
                                              const u16* __restrict__ cvec, const u16* __restrict__ wmod, const u16* __restrict__ X0,
                                              const u16* __restrict__ X1, const u16* __restrict__ wr_, const u16* __restrict__ wg,
                                              const u16* __restrict__ wu, const u16* __restrict__ wd, u16* __restrict__ out) {
  constexpr int STG = 114688;
  __shared__ __attribute__((aligned(1024))) char shm[MISC + 16384];
  const int b = blockIdx.x, xcd = b & 7, j8 = b >> 3;
  const int cc = xcd >> 2, qx = xcd & 3;
  const int tid = threadIdx.x, lane = tid & 63;
  const int wid = __builtin_amdgcn_readfirstlane(tid >> 6);
  const unsigned lds0 = (unsigned)(size_t)(__attribute__((address_space(3))) char*)shm;
  const char* cpf;
  {
    unsigned long long pc;
    unsigned len;
    asm volatile("s_getpc_b64 %0\n.Lcpf_here:\n\ts_mov_b32 %1, .Lcpf_e1-.Lcpf_here\n\ts_max_u32 %1, %1, .Lcpf_e2-.Lcpf_here" : "=s"(pc), "=s"(len)::"scc");
    cpf = reinterpret_cast<const char*>(pc) + min((unsigned)((lane * 32 + j8) * 128), len);
  }
  const unsigned pfl = lds0 + MISC + 16128;
  if (wid == 4 && lane < CPF_N) PFL(cpf);
  u32x4 xv[2];
  {
    const unsigned xo = (unsigned)((b * 8 + wid) * D + lane * 8) * 2;
    asm volatile("global_load_dwordx4 %0, %2, %3\n\tglobal_load_dwordx4 %1, %2, %3 offset:1024" : "=&v"(xv[0]), "=&v"(xv[1]) : "v"(xo), "s"(x) : "memory");
  }
  asm volatile("" ::"s"(ws), "s"(Y0), "s"(Y1), "s"(X0), "s"(X1));
  unsigned* xfl = reinterpret_cast<unsigned*>(ws + WS_XFL);
  unsigned* hfl = reinterpret_cast<unsigned*>(ws + WS_HFL);
  unsigned* modv = reinterpret_cast<unsigned*>(ws + WS_MOD);
#define SKEWPT()
  const int g4 = lane >> 4, l16 = lane & 15;
  u64 tag0, tag;
  asm volatile("global_load_dwordx2 %0, %1, %2 sc1" : "=v"(tag0) : "v"(0u), "s"(ws) : "memory");
  const char* Yx = reinterpret_cast<const char*>(cc ? Y1 : Y0) + (size_t)qx * 2097152;
#define PFG(r16, g) (Yx + (size_t)(((r16) * 16 + l16) * 4096 + g4 * 128 + ((((g) + qx) & 7) << 9)))
  unsigned ta = lds0 + STG + ((lane >> 2) & 3) * 2368 + (wid * 128 + (lane >> 5) * 32 + (lane & 3) * 8 + (g4 & 1) * 4) * 2;
  unsigned oo = (unsigned)((wid * 4 + (lane >> 5)) * 131072 + (b >> 2) * 2048 + (g4 & 1) * 1024 + ((b & 3) * 16 + l16) * 16), oo2 = oo + 262144;
  unsigned wa = lds0 + STG + wid * 2368 + lane * 16;
  asm volatile("" : "+v"(ta), "+v"(oo), "+v"(oo2), "+v"(wa));
  const int hsl = (b & 32) + xcd * 4 + (j8 & 3);
  const char* hp = reinterpret_cast<const char*>((b & 32) ? X1 : X0) + (size_t)(hsl & 31) * 65536 + wid * 8192;
#define WHLD(i0)                                                                                                                  \
  _Pragma("unroll") for (int i = i0; i < i0 + 4; ++i)                                                                             \
      GLDS(lds0 + wid * 8192 + i * 1024, (unsigned)((lane ^ ((i >> 1) | ((wid & 2) << 1))) * 16), hp + i * 1024)
  if (wid >= 8 - PF_N) PFL(PFG(j8, 7 - wid));
  if (j8 >= 24) {
    WHLD(0);
    WAIT_V(5);
  } else if (wid >= 8 - PF_N) {
    WAIT_V(2);
  } else {
    WAIT_V(1);
  }
  {
    asm volatile("; AUDIT_OK %0 %1" : "+v"(xv[0]), "+v"(xv[1])::"memory");
    TS(32);
    const float s0 = dsum(xv[0][3], dsum(xv[0][2], dsum(xv[0][1], dsum(xv[0][0], 0.f))));
    const float s1 = dsum(xv[1][3], dsum(xv[1][2], dsum(xv[1][1], dsum(xv[1][0], 0.f))));
    const float mf = wave_sum_s(s0 + s1) * (1.0f / D), m = rbf(mf);
    const f32x4 d0 = lo4(xv[0]) - m, d1 = hi4(xv[0]) - m, d2 = lo4(xv[1]) - m, d3 = hi4(xv[1]) - m;
    const f32x4 q = (d0 * d0 + d1 * d1) + (d2 * d2 + d3 * d3);
    const float vf = wave_sum_s((q[0] + q[1]) + (q[2] + q[3])) * (1.0f / D) - (mf - m) * (mf - m);
    const float rden = __builtin_amdgcn_rcpf(rbf(__builtin_amdgcn_sqrtf(rbf(rbf(vf) + 1e-5f))));
    asm volatile("ds_write_b128 %0, %1" ::"v"(wa), "v"(pk8(d0 * rden, d1 * rden)) : "memory");
    asm volatile("ds_write_b128 %0, %1 offset:1024" ::"v"(wa), "v"(pk8(d2 * rden, d3 * rden)) : "memory");
    LBAR();
    TS(34);
    u32x2 t[4];
    asm volatile(
        "ds_read_b64_tr_b16 %0, %4\n\tds_read_b64_tr_b16 %1, %4 offset:%5\n\t"
        "ds_read_b64_tr_b16 %2, %4 offset:128\n\tds_read_b64_tr_b16 %3, %4 offset:%6\n\ts_waitcnt lgkmcnt(0)"
        : "=&v"(t[0]), "=&v"(t[1]), "=&v"(t[2]), "=&v"(t[3])
        : "v"(ta), "n"(4 * 2368), "n"(4 * 2368 + 128)
        : "memory");
    asm volatile("global_store_dwordx4 %0, %2, %4 sc1\n\tglobal_store_dwordx4 %1, %3, %4 sc1\n\ts_nop 1" ::"v"(oo), "v"(oo2),
                 "v"(u32x4{t[0][0], t[0][1], t[1][0], t[1][1]}), "v"(u32x4{t[2][0], t[2][1], t[3][0], t[3][1]}), "s"(ws + WS_H)
                 : "memory");
  }
#define PBAR(n)                                                                                                          \
  do {                                                                                                                   \
    asm volatile("s_waitcnt vmcnt(%2)\n\tv_lshl_add_u64 %0, %1, 0, 1" : "=v"(tag) : "v"(tag0), "n"(n) : "memory");       \
    TS(36);                                                                                                              \
    BARRIER();                                                                                                           \
    TS(37);                                                                                                              \
  } while (0)
#define XFLAG() do { if ((tid | 1) == 385) ST4(reinterpret_cast<u64*>(ws + ((tid & 1) ? WS_XF2 + b * 8 : WS_XFL + b * 128)), tag); } while (0)
  if (j8 < 24) {
    const int mt = (j8 * 11) >> 5, nt = j8 - mt * 3, grp = xcd * 3 + nt;
#define FROW(f) (nt * 176 + (f) * 16 - (nt == 2 && (f) > 5 ? 16 : 0))
#define PROW(p) FROW(cc ? 10 - (p) : (p))
    constexpr int SCA = MISC, TGA = MISC + 512, TV = MISC + 1024, TSUM = MISC + 1792, SCW = MISC + 2048, RKA = MISC + 3072,
                  RKB = MISC + 5120, RKZ = MISC + 6656, IDT = MISC + 7168, ZSC = MISC + 9216;
    const bool zd = nt == 2;
    unsigned* ufl = reinterpret_cast<unsigned*>(ws + WS_UFL) + grp * 1024;
    const char* Ug = ws + WS_U + (size_t)grp * (16 * SL + 1024) + 1024;
    const unsigned perm = (unsigned)mt | (((0x76543210u & ((1u << (4 * mt)) - 1u)) << 4) | (0x76543210u & (0xfffffff0u << (4 * mt))));
#define KT(i) ((perm >> (4 * (i))) & 7u)
    const int kk = wid < 4 ? wid : wid - 2;
    const bool act = (wid < 4 || wid >= 6) && (kk || (cc == 0 && nt < 2));
    const int rg = qx * 32 + (PROW(5 + kk) >> 4);
    u32x4 pv[6];
    f32x4 acc[2][11], ga[2], x5[2];
    bf16x8 idt[2];
    unsigned spo = (unsigned)(((rg * 8 + g4 * 2) * 16 + l16) * 16);
    u32x4 sr[2], dk;
#pragma unroll
    for (int q = 0; q < 4; ++q) dk[q] = l16 == g4 * 4 + q ? ~0u : 0u;
    unsigned xw1 = lds0 + XR + tid * 16, xa = lds0 + XR + kk * 4096 + lane * 16, sto = (unsigned)(((rg * 8 + mt) * 16 + lane) * 16);
    unsigned saa = lds0 + SCW + (lane & 3) * 256 + g4 * 16, cja = lds0 + TSUM + g4 * 8;
    asm volatile("" : "+v"(cja), "+v"(dk), "+v"(xa), "+v"(sto), "+v"(xw1), "+v"(spo), "+v"(saa));
#define STLD() asm volatile("global_load_dwordx4 %0, %2, %3 sc1\n\tglobal_load_dwordx4 %1, %2, %3 offset:256 sc1\n\ts_waitcnt vmcnt(0)" : "=&v"(sr[0]), "=&v"(sr[1]) : "v"(spo), "s"(ws + WS_STAT) : "memory")
    const char* xr = ws + WS_XCH + (size_t)(b * 4 + (wid & 3)) * 6144 + 3072;
#define XCHLD()                                                                                                       \
  asm volatile(                                                                                                       \
      "global_load_dwordx4 %0, %6, %7 offset:2048 sc1\n\tglobal_load_dwordx4 %1, %6, %7 offset:1024 sc1\n\t"          \
      "global_load_dwordx4 %2, %6, %7 sc1\n\tglobal_load_dwordx4 %3, %6, %7 offset:-1024 sc1\n\t"                     \
      "global_load_dwordx4 %4, %6, %7 offset:-2048 sc1\n\tglobal_load_dwordx4 %5, %6, %7 offset:-3072 sc1"            \
      : "=&v"(pv[0]), "=&v"(pv[1]), "=&v"(pv[2]), "=&v"(pv[3]), "=&v"(pv[4]), "=&v"(pv[5])                            \
      : "v"(lane * 16), "s"(xr)                                                                                       \
      : "memory")
    if (wid < 4) {
      bf16x8 X[4][2][2], Y[8];
      u32x4 xq[6];
      const unsigned xo0 = lane * 16 + 4096;

#define XLD(dst, XP, XH, i) LDXS(dst, xo0, XP, ((XH)&3) * 2048 + (i)*1024 - 4096)
      int xt = lane < 16 ? (int)WS_H + (mt * 4 + wid) * 131072 + (((qx * 2 + lane) & 15) << 13) : (int)WS_WH + (cc * 32 + mt * 4 + wid) * 65536 + (KT(lane & 7) << 13);
      asm volatile("" : "+v"(xt));
#define XT(j) (ws + (unsigned)__builtin_amdgcn_readlane(xt, j))
#define WP(h) XT(16 + ((h) >> 2))
#define XS(h) X[((h) >> 1) & 3][(h)&1]
      {
        unsigned ye = lds0 + l16 * 128 + (g4 ^ (lane & 7)) * 16, yo = ye ^ 64;
        const char* xpa = XT(0);
        const char* xpb = XT(1);
#pragma unroll
        for (int i = 0; i < 22; ++i) {
          acc[i / 11][i % 11] = f32x4{0.f, 0.f, 0.f, 0.f};
          asm volatile("" : "+v"(acc[i / 11][i % 11]));
        }
        const char* xw = ws + WS_XCH + (size_t)((b ^ 4) * 4 + wid) * 6144 + 3072;
        unsigned xra = lds0 + XR + l16 * 256 + (((wid * 4 + g4) ^ (lane & 7)) << 4), gaa = lds0 + TGA + wid * 128 + g4 * 32;
        unsigned rka = lds0 + (g4 ? RKZ : RKA + wid * 512 + l16 * 16), rkb = lds0 + RKB + l16 * 16;
        asm volatile("" : "+s"(xw), "+s"(xr), "+v"(xra), "+v"(gaa), "+v"(rka), "+v"(rkb));
        __builtin_amdgcn_s_setprio(1);
        PBAR(0);
        if (wid == 3) {
          const unsigned* fp = xfl + (qx * 32 + lane) * 32;
          __builtin_amdgcn_s_sleep(P_D3 - P_D4);
          u64 f;
#define GLD() asm volatile("global_load_dwordx2 %0, %1, off sc1\n\ts_waitcnt vmcnt(0)" : "=&v"(f) : "v"(fp) : "memory")
          SPIN(__all(f == tag), P_D4, GLD());
        }
        TS(38);
        BARRIER();
        TS(39);
#pragma unroll
        for (int h = 0; h < 6; ++h) {
          XLD(XS(h)[0], h < 4 ? xpa : xpb, h, 0);
          XLD(XS(h)[1], h < 4 ? xpa : xpb, h, 1);
        }
#pragma unroll
        for (int j = 0; j < 6; ++j) DSR(Y[j], (j & 1) ? yo : ye, (j >> 1) * 2048);
        unsigned so = 0, a0 = ye, a1 = yo, yb[2], tq, u = 2;
#define STEP2(S0, XA, XB)                                                                                                      \
  sfor<44>([&](auto nn) __attribute__((always_inline)) {                                                                       \
    constexpr int n = (S0)*22 + decltype(nn)::value, q = n % 22, p = q / 2, s = n / 22, i = n & 1;                             \
    constexpr int k = q == XQ0 ? 0 : q == XQ1 ? 1 : q == XQ2 ? 2 : q == XQ3 ? 3 : -1;                                          \
    (void)acc, (void)X, (void)Y, (void)a0, (void)a1, (void)xo0, (void)XA, (void)XB, (void)so, (void)ye, (void)yo, (void)u, (void)xt, (void)tq, (void)ws; \
    if constexpr (q == 11) BARRIER();                                                                                          \
    if constexpr (q == 0) asm volatile("s_waitcnt vmcnt(8) lgkmcnt(4)" ::: "memory");                                          \
    else if constexpr (!i) WAIT_L(4);                                                                                          \
    MFMAI(acc[i][p], X[s][0][i], Y[n & 6]);                                                                                    \
    DSR(Y[(n + 6) & 7], i ? a1 : a0, ((p + 3) % 11) * 2048);                                                                   \
    MFMA(acc[i][p], X[s][1][i], Y[(n & 6) + 1]);                                                                               \
    if constexpr (k >= 0) XLD(X[(s + 3) & 3][k / 2][k & 1], (s & 1) ? XB : XA, 2 * s + 6 + k / 2, k & 1);                      \
    if constexpr (q == 0 && !(s & 1)) asm volatile("v_readlane_b32 %0, %2, %1\n\ts_add_u32 %1, %1, 1" : "=&s"(tq), "+s"(u) : "v"(xt) : "scc"); \
    if constexpr (q == 6) asm volatile("s_add_u32 %0, %0, %1" : "+s"(so) : "n"(SL) : "scc");                                   \
    if constexpr (q == 8) asm volatile("s_cmp_lg_u32 %0, %1\n\ts_cselect_b32 %0, %0, 0" : "+s"(so) : "n"(NS * SL) : "scc");    \
    if constexpr (q == 12 && !(s & 1)) { asm volatile("" : "+s"(tq)); XB = ws + tq; asm volatile("" : "+s"(XB)); }             \
    if constexpr (q == 14) asm volatile("v_add_u32 %0, %1, %2" : "=v"(a0) : "s"(so), "v"(ye));                                 \
    if constexpr (q == 16) asm volatile("v_add_u32 %0, %1, %2" : "=v"(a1) : "s"(so), "v"(yo));                                 \
  })
#define YSL(e) ((32 + es((e) / 2)) % NS)
#define YRD(e) DSR(Y[(e)&7], YSL(e) < 2 ? ye : yb[YSL(e) / 4], (YSL(e) & 1) * SL + ep((e) / 2) * 2048 + ((e)&1) * 1024)

        u32x4 ov;
        bf16x8 qf[2];
        auto OPS = [&](auto kc) __attribute__((always_inline)) {
          constexpr int k = decltype(kc)::value, q = k / 10, r = k % 10, c = k - 60;
          (void)ov, (void)acc, (void)xw, (void)lane, (void)xq, (void)x5;
          if constexpr (k < 60) {
            if constexpr (r < 4) asm volatile("v_cvt_pk_bf16_f32 %0, %1, %2" : "=v"(ov[r]) : "v"(acc[r / 2][q][(r & 1) * 2]), "v"(acc[r / 2][q][(r & 1) * 2 + 1]));
            else if constexpr (r < 8) asm volatile("v_min_u32 %0, -2, %0" : "+v"(ov[r - 4]));
            else if constexpr (r == 9) asm volatile("global_store_dwordx4 %0, %1, %2 offset:%3 sc1" ::"v"(lane * 16), "v"(ov), "s"(xw), "n"(q * 1024 - 3072) : "memory");
          } else if constexpr (c >= 40 && c < 48) {
            if constexpr (c & 1) asm volatile("v_and_b32 %0, 0xffff0000, %1" : "=v"(x5[(c >> 2) & 1][c & 3]) : "v"(xq[5][(c >> 1) & 3]));
            else asm volatile("v_lshlrev_b32 %0, 16, %1" : "=v"(x5[(c >> 2) & 1][c & 3]) : "v"(xq[5][(c >> 1) & 3]));
          } else if constexpr (c < 40) {
            if constexpr (c & 1) asm volatile("v_and_b32 %0, 0xffff0000, %1" : "=v"(acc[(c >> 2) & 1][c >> 3][c & 3]) : "v"(xq[c >> 3][(c >> 1) & 3]));
            else asm volatile("v_lshlrev_b32 %0, 16, %1" : "=v"(acc[(c >> 2) & 1][c >> 3][c & 3]) : "v"(xq[c >> 3][(c >> 1) & 3]));
          }
        };
#define IC(v) std::integral_constant<int, (v)>{}
#define FILL(g)                                                                              \
  do {                                                                                       \
    if constexpr (m >= 0) {                                                                  \
      OPS(IC(m / 2 * 5 + (m & 1) * 3 + (g)));                                                \
      if constexpr ((g) && !(m & 1)) OPS(IC(m / 2 * 5 + 2));                                 \
    }                                                                                        \
  } while (0)
        auto BP = [&](auto nn) __attribute__((always_inline)) {
          constexpr int n = decltype(nn)::value, s = es(n / 2), p = n < E3 ? ep(n / 2) : 5 + n - E3, i = n & 1, e = n + 6, m = n - E0 - 30;
          (void)ga, (void)qf, (void)idt, (void)rka, (void)rkb, (void)gaa, (void)xq, (void)ov, (void)xw, (void)x, (void)pv, (void)xr, (void)ufl;
          (void)acc, (void)X, (void)Y, (void)yb, (void)ye, (void)xra, (void)xo0, (void)xt, (void)ws, (void)lane, (void)mt, (void)nt, (void)cc, (void)lds0, (void)wid, (void)g4, (void)perm;
          (void)OPS;
          TSCAP();
          if constexpr (n == E0 + 55) TS(10);
          if constexpr (n == UF_AT) {
            TS(45);
            WAIT_V(n > HX0 ? (n - HX0 + HXS - 1) / HXS : 0);
            if (lane == 0) ST4(ufl + (mt * 4 + wid) * 32, 1u);
            TS(46);
          }
          if constexpr (n == 37) TS(48);
          if constexpr (n == 37 || n == E0 + 5 || n == E0 + 17 || n == B46) BARRIER();
          if constexpr (n == 37) TS(4);
          if constexpr (n == E0 + 25) { TS(3); SKEWPT(); }
          if constexpr (p == 0 && !i && n != 22) WAIT_V(n == 0 ? 15 : s < 14 ? 8 : 4 * (15 - s));
          if constexpr (!i) WAIT_L(e < NE ? 4 : NE - 2 - n);
          if constexpr (n < 22) MFMA0(acc[i][p], X[0][0][i], Y[n & 6]);
          else if constexpr (n < E3) MFMAI(acc[i][p], X[s & 3][0][i], Y[n & 6]);
          else MFMA(acc[0][p], qf[0], Y[n & 7]);
          if constexpr (e < E3) YRD(e);
          else if constexpr (e < NE) DSR(Y[e & 7], rkb, (e - E3) * 256);
          FILL(0);
          if constexpr (n < E3 && (s == 1 || s == 15)) MFMA(acc[i][p], X[s & 3][1][i], Y[(n & 6) + 1]);
          else if constexpr (n < E3) MFMAI(acc[i][p], X[s & 3][1][i], Y[(n & 6) + 1]);
          else MFMA(acc[1][p], qf[1], Y[n & 7]);
          if constexpr (n >= HX0 && n < HX0 + 12 * HXS && (n - HX0) % HXS == 0) {
            constexpr int k = (n - HX0) / HXS;
            static_assert(k < 8 || n >= 22, "slot still in use");
            XLD(XS(4 + k / 2)[k & 1], WP(4 + k / 2), k / 2, k & 1);
          }
          if constexpr (n >= 272 && n < 294 && n % 4 == 0) DSR(xq[(n - 272) / 4], xra, (n - 272) / 4 * 4096);
          if constexpr (n >= E0 && n < E0 + 4) XLD(X[3][(n - E0) / 2][i], WP(31), 30 + (n - E0) / 2, i);
          FILL(1);
          if constexpr (n == B46 || n == B46 + 1) DSR(qf[n - B46], rka, (n - B46) * 256);
          if constexpr (n == B46 + 2 || n == B46 + 3) DSR(ga[n - B46 - 2], gaa, (n - B46 - 2) * 16);
          if constexpr (n == B46 + 4 || n == B46 + 5) DSR(idt[n - B46 - 4], yb[1], IDT - 4 * SL + (n - B46 - 4) * 1024);
          if constexpr (n >= TP_XL && n < TP_XL + 4) LDXS(pv[TP_XL + 5 - n], lane * 16, xr, (n - TP_XL) * 1024 - 3072);
          if constexpr (n == TP_X2 || n == TP_X2 + 1) LDXS(pv[TP_X2 + 1 - n], lane * 16, xr, (n - TP_X2) * 1024 + 1024);
        };
        do {
          STEP2(0, xpb, xpa);
          if (__builtin_expect(u == 17, 0)) {
            TS(57);
            TSC1(40);
            TSC0();
            f32x4 c0, c1;
            const char* up = Ug + (mt * 2 + (wid >> 1)) * SL + (wid & 1) * 1024 + 4096;
            const unsigned ul = lds0 + (2 + (wid >> 1)) * SL + (wid & 1) * 1024 + lane * 16;
            u32x4 uv;
            DSR(c0, lds0 + SCA + wid * 128 + g4 * 32, 0);
            DSR(c1, lds0 + SCA + wid * 128 + g4 * 32, 16);
#define UMUL(j, k) asm volatile("v_mul_f32 %0, %0, %1" : "+v"(acc[(k) >> 2][j][(k)&3]) : "v"(((k) >> 2 ? c1 : c0)[(k)&3]))
#define UCVT(j, k) asm volatile("v_cvt_pk_bf16_f32 %0, %1, %2" : "=v"(uv[k]) : "v"(acc[(k) >> 1][j][((k)&1) * 2]), "v"(acc[(k) >> 1][j][((k)&1) * 2 + 1]))
            auto UEP = [&](auto jc, auto rc) __attribute__((always_inline)) {
              constexpr int j = decltype(jc)::value, r = decltype(rc)::value;
              (void)acc, (void)c0, (void)c1, (void)uv, (void)lane, (void)ul, (void)up;
              if constexpr (r < 4) { UMUL(j, 2 * r); UMUL(j, 2 * r + 1); }
              else if constexpr (r < 6) { UCVT(j, 2 * r - 8); UCVT(j, 2 * r - 7); }
              else asm volatile("global_store_dwordx4 %0, %2, %3 offset:%4 sc1\n\tds_write_b128 %1, %2 offset:%5"
                                ::"v"(lane * 16), "v"(ul), "v"(uv), "s"(up + (j >> 2) * 8192), "n"((j & 3) * 2048 - 4096), "n"(j * 2048) : "memory");
            };
#define UGAP(G) do { constexpr int j_ = (G) < 82 ? ((G)-18) / 7 : 9; if constexpr ((G) >= 18 && (G) != 81) UEP(IC(j_), IC((G)-ug(j_))); } while (0)
            sfor<44>([&](auto nn) __attribute__((always_inline)) {
              constexpr int n = decltype(nn)::value, s = as(n / 2), p = ap(n / 2), i = n & 1, e = n + 6;
              (void)acc, (void)X, (void)Y, (void)ye, (void)yo, (void)xo0, (void)xpa, (void)UEP;
              if constexpr (n == 0 || n == 6) WAIT_V(8);
              if constexpr (!i) WAIT_L(e < 44 ? 4 : 42 - n);
              MFMAI(acc[i][p], X[s][0][i], Y[(n + 4) & 6]);
              if constexpr (e < 44) DSR(Y[(e + 4) & 7], i ? yo : ye, (as(e / 2) & 1) * SL + ap(e / 2) * 2048);
              UGAP(2 * n);
              if constexpr (s == 2) MFMAI(acc[i][p], X[s][1][i], Y[((n + 4) & 6) + 1]);
              else MFMA(acc[i][p], X[s][1][i], Y[((n + 4) & 6) + 1]);
              if constexpr (n < 4) XLD(X[1][n / 2][i], xpa, 2 + n / 2, i);
              UGAP(2 * n + 1);
            });
            TSC1(41);
            TS(0);
            SKEWPT();
            UEP(IC(9), IC(6));
            asm volatile("s_nop 1");
            sfor<7>([&](auto rc) __attribute__((always_inline)) { UEP(IC(10), rc); });
            ye = lds0 + lane * 16;
            yo = ye + 1024;
            so = 4 * SL;
            a0 = ye + 4 * SL;
            a1 = yo + 4 * SL;
            yb[0] = ye + 2 * SL;
            yb[1] = a0;
            asm volatile("" : "+v"(yb[0]), "+v"(yb[1]), "+v"(ye), "+v"(yo));
            LBAR();
            TS(44);
            sfor<6>([&](auto ee) __attribute__((always_inline)) { (void)Y, (void)yb, (void)ye; YRD(decltype(ee)::value); });
            sfor<44>(BP);
            u = 19;
            xpa = WP(8);
          }
          STEP2(2, xpa, xpb);
        } while (u != 24);
        TS(2);
        sfor<NE - 264>([&](auto nn) __attribute__((always_inline)) { BP(std::integral_constant<int, 264 + decltype(nn)::value>{}); });
        __builtin_amdgcn_s_setprio(0);
        TSC1(59);
        TS(13);
        SKEWPT();
      }
    } else if (wid < 6) {
      const int hw = wid - 4;
      __builtin_amdgcn_s_setprio(2);
      unsigned voy[11];
#pragma unroll
      for (int i = 0; i < 11; ++i) voy[i] = (unsigned)((PROW(i) + hw * 8 + (lane >> 3)) * 4096 + ((lane & 7) ^ (lane >> 3)) * 16);
      const unsigned lbase = lds0 + hw * 1024;
      if (zd) voy[5] = (lane & 7) * 16;
      auto stage = [&](int n, int slot) {
        const char* yp = Yx + (((n + qx * 4) & 31) << 7);
        const unsigned l = lbase + slot * SL;
#pragma unroll
        for (int i = 0; i < 11; ++i) GLDS(i == 5 && zd ? lbase + ZSC : l + i * 2048, voy[i], yp);
      };
      asm volatile("s_waitcnt vmcnt(%3)" ::"v"(voy[0]), "v"(voy[10]), "v"(tag0), "n"(0) : "memory");
      BARRIER();
#pragma unroll 1
      for (int s = 0; s <= A_LD; ++s) stage(s, s);
      WAIT_V(11 * A_LD);
      TS(38);
      BARRIER();
      int slot = A_LD + 1;
#pragma unroll 1
      for (int k = 1; k <= 31 - A_LD; ++k) {
        WAIT_V(11 * (A_LD - 1));
        BARRIER();
        if (hw == 1) TSD(k);
        stage(k + A_LD, slot);
        slot = slot == NS - 1 ? 0 : slot + 1;
        if (k == CPF_K && hw == 1 && lane < CPF_N) PFL(cpf);
      }
      if (A_LD > 2) { WAIT_V(22); BARRIER(); }
      WAIT_V(0); BARRIER();
      const unsigned* ufp = ufl + (KT(min(lane >> 2, 6) + 1) * 4 + (lane & 3)) * 32;
      unsigned miss = ~0u;
      const char* y1 = Ug + KT(1) * 2 * SL + hw * 11264;
      asm volatile("" : "+s"(y1), "+v"(ufp));
      BARRIER();
      TS(47);
      if (U_D) __builtin_amdgcn_s_sleep(U_D);
      auto ULDF = [&](unsigned em0) __attribute__((always_inline)) {
        unsigned f;
        const u64 em = miss & em0;
        asm volatile("s_mov_b64 exec, %2\n\tglobal_load_dword %0, %1, off sc1\n\ts_mov_b64 exec, -1\n\ts_waitcnt vmcnt(0)" : "=&v"(f) : "v"(ufp), "s"(em) : "memory");
        miss &= ~(unsigned)em | (unsigned)__builtin_amdgcn_ballot_w64(f == SENT);
      };
#define UPOLL(need, em0) UNTIL((miss & (need)) == 0, U_R, ULDF(em0))
#define G1(o) "global_load_lds_dwordx4 %0, %1 offset:" #o " sc1\n\t"
#define ULD(yp, l)                                                                                                                          \
  do {                                                                                                                                      \
    asm volatile("s_mov_b32 m0, %2\n\ts_nop 0\n\t" G1(0) G1(1024) G1(2048) G1(3072)::"v"(lane * 16), "s"(yp), "s"(l) : "memory");            \
    asm volatile("s_mov_b32 m0, %2\n\ts_nop 0\n\t" G1(0) G1(1024) G1(2048) G1(3072)::"v"(lane * 16 + 4096), "s"(yp), "s"((l) + 4096) : "memory"); \
    asm volatile("s_mov_b32 m0, %2\n\ts_nop 0\n\t" G1(0) G1(1024) G1(2048)::"v"(lane * 16 + 8192), "s"(yp), "s"((l) + 8192) : "memory");     \
  } while (0)
      {
        SPIN((miss & 3u) == 0, U_R, ULDF(15u));
        TS(48);
        ULD(y1, lds0 + 4 * SL + hw * 11264);
        UPOLL(15u, 15u);
        unsigned f;
        asm volatile("global_load_dword %0, %1, off sc1" : "=v"(f) : "v"(ufp) : "memory");
        ULD(y1 + SL, lds0 + hw * 11264);
        asm volatile("s_waitcnt vmcnt(11)" : "+v"(f)::"memory");
        BARRIER();
        miss = (unsigned)__builtin_amdgcn_ballot_w64(f == SENT);
      }
      slot = 1;
#pragma unroll 1
      for (int n = 4; n < 16; ++n) {
        UPOLL(3u << (2 * n - 4), ~0u);
        const char* yp = Ug + (KT(n >> 1) * 2 + (n & 1)) * SL + hw * 11264;
        unsigned l = lds0 + slot * SL + hw * 11264;
        slot = slot == NS - 1 ? 0 : slot + 1;
        if (n == CPF_B && hw == 1 && lane < CPF_N) PFL(cpf);
        asm volatile("" : "+s"(yp), "+s"(l));
        if (n >= 6) {
          WAIT_V(22);
          BARRIER();
          if (hw == 1) TSD(n + 44);
        }
        ULD(yp, l);
      }
      WAIT_V(22);
      BARRIER();
      WAIT_V(0);
      BARRIER();
      BARRIER();
      BARRIER();
      if (b < 56 && tid == 256) ST4(reinterpret_cast<unsigned*>(ws + WS_WAK) + b * 32, 1u);
      TSE();
      return;
    } else {
      const int aw = wid - 6, rp = min(mt * 2 + aw, 10);
      PBAR(0);
      XFLAG();
      if (zd) {
#pragma unroll
        for (int s = 0; s < NS; ++s) *reinterpret_cast<u32x4*>(shm + s * SL + 10240 + aw * 1024 + lane * 16) = u32x4{0u, 0u, 0u, 0u};
      }
      LBAR();
      float rs[4] = {};
      const unsigned ra = lds0 + rp * 2048 + lane * 16;
      unsigned so = 0;
#define RSUM()                                                                                                                        \
  do {                                                                                                                                \
    u32x4 R_[2];                                                                                                                      \
    asm volatile("ds_read_b128 %0, %2\n\tds_read_b128 %1, %2 offset:1024\n\ts_waitcnt lgkmcnt(0)" : "=&v"(R_[0]), "=&v"(R_[1]) : "v"(ra + so) : "memory"); \
    _Pragma("unroll") for (int j = 0; j < 8; ++j) {                                                                                   \
      asm volatile("v_add_f32 %0, %0, %1" : "+v"(rs[(j >> 2) * 2]) : "v"(R_[j >> 2][j & 3] << 16));                                   \
      asm volatile("v_add_f32 %0, %0, %1" : "+v"(rs[(j >> 2) * 2 + 1]) : "v"(R_[j >> 2][j & 3] & 0xffff0000u));                       \
    }                                                                                                                                 \
    so = so == (NS - 1) * SL ? 0 : so + SL;                                                                                           \
  } while (0)
      {
#pragma unroll 1
        for (int bi = 0; bi < F_AT; ++bi) {
          RSUM();
          BARRIER();
        }
        u32x4 f;
#define FLD() asm volatile("global_load_dwordx4 %0, %1, %2 sc1" : "=v"(f) : "v"(lane * 16), "s"(ws + WS_XF2 + aw * 1024) : "memory")
        FLD();
#pragma unroll 1
        for (int bi = F_AT; bi < 4; ++bi) {
          RSUM();
          BARRIER();
        }
        POLL { PDBG(it);
          asm volatile("s_waitcnt vmcnt(0)" : "+v"(f)::"memory");
          if (__all(mk64(f[1], f[0]) == tag && mk64(f[3], f[2]) == tag)) break;
          __builtin_amdgcn_s_sleep(1);
          FLD();
        }
        asm volatile("s_waitcnt vmcnt(0)" : "+v"(f)::"memory");
      }
      const unsigned xo = (unsigned)(((qx * 512 + aw * 4 + g4) * D + mt * 128) * 2 + ((l16 ^ (aw * 4 + g4)) << 4));
#pragma unroll 1
      for (int bi = 4; bi < H_AT; ++bi) {
        RSUM();
        const int i = bi - X_AT;
        if (i >= 0 && i < 12) GLDS(lds0 + XR + i * 2048 + aw * 1024, xo, x + (PROW(5 + i / 2) + (i & 1) * 8) * D);
        BARRIER();
      }
      {
        u64 f;
        const unsigned* fp = hfl + lane * 2;
        asm volatile("global_load_dwordx2 %0, %1, off sc1" : "=v"(f) : "v"(fp) : "memory");
        RSUM();
        BARRIER();
        RSUM();
        BARRIER();
        POLL { PDBG(it);
          asm volatile("s_waitcnt vmcnt(0)" : "+v"(f)::"memory");
          if (__all(f == tag)) break;
          __builtin_amdgcn_s_sleep(1);
          asm volatile("global_load_dwordx2 %0, %1, off sc1" : "=v"(f) : "v"(fp) : "memory");
        }
        asm volatile("s_waitcnt vmcnt(0)" : "+v"(f)::"memory");
        unsigned c;
        const unsigned* cp = modv + 1024 + mt * 128 + aw * 64 + lane;
        asm volatile("global_load_dword %0, %1, off sc1" : "=v"(c) : "v"(cp) : "memory");
        RSUM();
        BARRIER();
        RSUM();
        BARRIER();
        POLL { PDBG(it);
          asm volatile("s_waitcnt vmcnt(0)" : "+v"(c)::"memory");
          if (__all(c != SENT)) break;
          __builtin_amdgcn_s_sleep(1);
          asm volatile("global_load_dword %0, %1, off sc1" : "=v"(c) : "v"(cp) : "memory");
        }
        asm volatile("s_waitcnt vmcnt(0)" : "+v"(c)::"memory");
        *reinterpret_cast<unsigned*>(shm + SCA + (aw * 64 + lane) * 4) = c ^ (cc ? 0x80000000u : 0u);
      }
#pragma unroll 1
      for (int bi = H_AT + 4; bi < 32; ++bi) {
        RSUM();
        if (bi < 30) BARRIER();
      }
      unsigned* rsb = reinterpret_cast<unsigned*>(ws + WS_RS) + qx * 512;
      {
        const float v0 = dpp_add<0x141>(dpp_add<0x4E>(dpp_add<0xB1>(rs[0] + rs[1])));
        const float v1 = dpp_add<0x141>(dpp_add<0x4E>(dpp_add<0xB1>(rs[2] + rs[3])));
        if ((lane & 7) < 2 && mt * 2 + aw < 11 && !(nt == 2 && rp == 5)) ST4(rsb + cc * S + PROW(rp) + (lane & 1) * 8 + (lane >> 3), nsent((lane & 1) ? v1 : v0));
      }
      BARRIER();
#pragma unroll 1
      for (int k = 34; k <= (aw ? B_VAT : 46); ++k) BARRIER();
      u32x2 m[6];
      const unsigned lr = min(lane, 47);
      const unsigned o0 = aw ? (unsigned)(WS_Q - WS_MOD) + mt * 512 + lane * 8 : 8192u + mt * 512 + lane * 8;
      const unsigned o1 = aw ? (unsigned)(WS_RS - WS_MOD) + (qx * 512 + PROW(5 + (lr >> 3)) + (lr & 7) * 2) * 4 : o0 + 8192;
      const unsigned o2 = aw ? o1 + 8192 : o0 + 16384, o3 = aw ? o1 : 28672u + min(lane, 23) * 8;
#define VLD()                                                                                                                     \
  asm volatile("global_load_dwordx2 %0, %6, %10 sc1\n\tglobal_load_dwordx2 %1, %6, %11 sc1\n\tglobal_load_dwordx2 %2, %7, %10 sc1\n\t" \
               "global_load_dwordx2 %3, %7, %11 sc1\n\tglobal_load_dwordx2 %4, %8, %10 sc1\n\tglobal_load_dwordx2 %5, %9, %10 sc1"    \
               : "=&v"(m[0]), "=&v"(m[1]), "=&v"(m[2]), "=&v"(m[3]), "=&v"(m[4]), "=&v"(m[5])                                     \
               : "v"(o0), "v"(o1), "v"(o2), "v"(o3), "s"(modv), "s"(modv + 1024)                                                  \
               : "memory")
      VLD();
      if (aw) {
#pragma unroll
        for (int k = B_VAT + 1; k <= 45; ++k) BARRIER();
      }
      TS(30);
      POLL { PDBG(it);
        asm volatile("s_waitcnt vmcnt(0)" : W6(m)::"memory");
        if (__all(max(max(max(max2(m[0]), max2(m[1])), max(max2(m[2]), aw ? 0u : max2(m[3]))), max(max2(m[4]), max2(m[5]))) != SENT)) break;
        __builtin_amdgcn_s_sleep(2);
        VLD();
      }
      asm volatile("s_waitcnt vmcnt(0)" : W6(m)::"memory");
      if (aw == 0) {
        *reinterpret_cast<u32x2*>(shm + TGA + lane * 8) = m[0];
        *reinterpret_cast<unsigned*>(shm + TV + lane * 4) = hi2(m[2]);
        *reinterpret_cast<unsigned*>(shm + TV + 256 + lane * 4) = hi2(m[1]);
        *reinterpret_cast<unsigned*>(shm + TV + 512 + lane * 4) = hi2(m[3]);
        const float p0 = __uint_as_float(m[4][0]), p1 = __uint_as_float(m[4][1]);
        const unsigned hh = pack2(p0, p1);
        *reinterpret_cast<unsigned*>(shm + SCW + lane * 4) = hh;
        *reinterpret_cast<unsigned*>(shm + SCW + 256 + lane * 4) = pack2(p0 - bflo(hh), p1 - bfhi(hh));
        *reinterpret_cast<u32x2*>(shm + SCW + 512 + lane * 8) = u32x2{0x3f803f80u, 0x3f803f80u};
        const float u = __uint_as_float(m[5][0]) + __uint_as_float(m[5][1]), u2 = dpp_add<0xB1>(u), u8 = dpp_add<0x141>(dpp_add<0x4E>(u2));
        if (lane < 17 && !(lane & 1)) *reinterpret_cast<float*>(shm + TSUM + lane * 2) = lane < 16 ? u2 : u8;
      } else {
        BARRIER();
#pragma unroll
        for (int j = 0; j < 2; ++j) {
          const float r0 = __uint_as_float(m[0][j]), r1 = __uint_as_float(m[1][j]);
          const unsigned a0 = pack2(r0, r0), a1 = pack2(r1, r1);
          *reinterpret_cast<u32x4*>(shm + RKA + ((lane >> 4) * 32 + ((lane >> 1) & 1) * 16 + ((lane >> 2) & 3) * 4 + (lane & 1) * 2 + j) * 16) =
              u32x4{a0, pack2(r0 - bflo(a0), r0 - bflo(a0)), a1, pack2(r1 - bflo(a1), r1 - bflo(a1))};
          const float s0 = __uint_as_float(m[2][j]), s1 = -__uint_as_float(m[4][j]);
          const unsigned b0 = pack2(s0, s0 - rbf(s0)), b1 = pack2(s1, s1 - rbf(s1));
          *reinterpret_cast<u32x4*>(shm + RKB + (lr * 2 + j) * 16) = u32x4{b0, b0, b1, b1};
        }
        *reinterpret_cast<u32x2*>(shm + RKZ + lane * 8) = u32x2{0u, 0u};
        const unsigned one = g4 == (l16 >> 2) ? ((lane & 1) ? 0x3f800000u : 0x3f80u) : 0u, e0 = (lane & 2) ? 0u : one, e1 = (lane & 2) ? one : 0u;
        *reinterpret_cast<u32x4*>(shm + IDT + lane * 16) = u32x4{e0, e1, 0u, 0u};
        *reinterpret_cast<u32x4*>(shm + IDT + 1024 + lane * 16) = u32x4{0u, 0u, e0, e1};
      }
      TS(17);
      LBAR();
    }
    const int row0 = rg * 16 + l16, col1 = mt * 128 + g4 * 8;
    u32x4 xq[4], sa4[4];
#pragma unroll
    for (int q = 0; q < 4; ++q) DSR(sa4[q], saa, q * 64);
    f32x2 cj;
    float sw, ce;
    asm volatile("ds_read_b64 %0, %3\n\tds_read_b32 %1, %4\n\tds_read_b32 %2, %5" : "=&v"(cj), "=&v"(sw), "=&v"(ce) : "v"(cja), "v"(lds0 + TSUM + 32), "v"(lds0 + TSUM + mt * 4) : "memory");
    if (wid < 4) {
      auto X1A = [&](auto kc) __attribute__((always_inline)) {
        constexpr int k = decltype(kc)::value;
        (void)acc, (void)idt, (void)pv;
        MFMA(acc[0][5 + k], idt[0], pv[k]);
        MFMA(acc[1][5 + k], idt[1], pv[k]);
      };
      auto X1B = [&](auto kc) __attribute__((always_inline)) {
        constexpr int j = decltype(kc)::value;
        (void)acc, (void)ga, (void)x5, (void)xw1;
        asm volatile("" : "+v"(acc[0][5 + j]), "+v"(acc[1][5 + j]));
        const u32x4 v = pk8((j < 5 ? acc[0][j % 5] : x5[0]) + ga[0] * acc[0][5 + j], (j < 5 ? acc[1][j % 5] : x5[1]) + ga[1] * acc[1][5 + j]);
        asm volatile("ds_write_b128 %0, %1 offset:%2" ::"v"(xw1), "v"(v), "n"(j * 4096) : "memory");
      };
      asm volatile("s_waitcnt vmcnt(2)" : "+v"(pv[2]), "+v"(pv[3]), "+v"(pv[4]), "+v"(pv[5])::"memory");
      UNTIL(__all(max(max(max4(pv[2]), max4(pv[3])), max(max4(pv[4]), max4(pv[5]))) != SENT), X_R,
            { WAIT_V(0); XCHLD(); asm volatile("s_waitcnt vmcnt(0)" : W6(pv)::"memory"); });
      X1A(IC(5)); X1A(IC(4)); X1A(IC(3)); X1B(IC(5)); X1A(IC(2)); X1B(IC(4)); X1B(IC(3));
      asm volatile("s_waitcnt vmcnt(0)" : "+v"(pv[0]), "+v"(pv[1])::"memory");
#define XCHL2()                                                                                                                             \
  asm volatile("global_load_dwordx4 %0, %2, %3 offset:2048 sc1\n\tglobal_load_dwordx4 %1, %2, %3 offset:1024 sc1\n\ts_waitcnt vmcnt(0)" \
               : "=&v"(pv[0]), "=&v"(pv[1]) : "v"(lane * 16), "s"(xr) : "memory")
      UNTIL(__all(max(max4(pv[0]), max4(pv[1])) != SENT), X_R, { XCHL2(); asm volatile(".Lcpf_e2:"); });
      TS(27);
      X1A(IC(1)); X1A(IC(0)); X1B(IC(2)); X1B(IC(1)); X1B(IC(0));
    }
    {
      TS(6);
      LBAR();
      asm volatile("ds_read_b128 %0, %8\n\tds_read_b128 %1, %8 offset:1024\n\tds_read_b128 %2, %8 offset:2048\n\tds_read_b128 %3, %8 offset:3072\n\ts_waitcnt lgkmcnt(0)"
                   : "=&v"(xq[0]), "=&v"(xq[1]), "=&v"(xq[2]), "=&v"(xq[3]), "+v"(sa4[0]), "+v"(sa4[1]), "+v"(sa4[2]), "+v"(sa4[3]), "+v"(xa),
                     "+v"(cj), "+v"(sw), "+v"(ce)
                   :
                   : "memory");
      if (!act) {
        TSE();
        return;
      }
      f32x4 s4 = {0.f, 0.f, 0.f, 0.f}, gr = {0.f, 0.f, 0.f, 0.f};
#pragma unroll
      for (int q = 0; q < 4; ++q) {
        gr = MM(xq[q], xq[q], gr);
        s4 = MM(sa4[q], xq[q], s4);
      }
      const u32x4 gd = __builtin_bit_cast(u32x4, gr) & dk;
      const float m = s4[2] * (1.0f / 128);
      float q2 = ar4(__uint_as_float((gd[0] | gd[1]) | (gd[2] | gd[3]))) - s4[2] * m, lg = (s4[0] + s4[1]) - m * ce;
      if (__builtin_expect(__any(!(s4[2] * m <= 256.0f * q2)), 0)) {
        f32x4 a = {0.f, 0.f, 0.f, 0.f}, l = {0.f, 0.f, 0.f, 0.f};
#pragma unroll
        for (int q = 0; q < 4; ++q) {
          const u32x4 ch = *reinterpret_cast<const u32x4*>(shm + SCW + g4 * 16 + q * 64), cl = *reinterpret_cast<const u32x4*>(shm + SCW + 256 + g4 * 16 + q * 64);
          const f32x4 d0 = lo4(xq[q]) - m, d1 = hi4(xq[q]) - m;
          a += d0 * d0 + d1 * d1;
          l += d0 * (lo4(ch) + lo4(cl)) + d1 * (hi4(ch) + hi4(cl));
        }
        q2 = ar4((a[0] + a[1]) + (a[2] + a[3]));
        lg = ar4((l[0] + l[1]) + (l[2] + l[3]));
      }
      const u32x4 own = {nsent(m), nsent(q2), nsent(lg), 0u};
      if (g4 == 0) ST16(sto, own, ws + WS_STAT, 0);
      TS(7);
      SKEWPT();
      const bool k0 = g4 * 2 == mt, k1 = g4 * 2 + 1 == mt;
#define STOK() (sr[0] = k0 ? own : sr[0], sr[1] = k1 ? own : sr[1], __all(max(max(max(sr[0][0], sr[0][1]), sr[0][2]), max(max(sr[1][0], sr[1][1]), sr[1][2])) != SENT))
      const bool pub = mt == (rg & 7);
      if (OPT_SD) __builtin_amdgcn_s_sleep(OPT_SD);
      SPIN(STOK(), 1, STLD());
      TS(9);
      const f32x2 mk = {__uint_as_float(sr[0][0]), __uint_as_float(sr[1][0])};
      const f32x2 qk = {__uint_as_float(sr[0][1]), __uint_as_float(sr[1][1])};
      const f32x2 lk = {__uint_as_float(sr[0][2]), __uint_as_float(sr[1][2])};
      const float mean = ar4(mk[0] + mk[1]) * (1.0f / 8);
      const f32x2 dm = mk - mean;
      const f32x2 a = qk + 128.0f * (dm * dm), l = lk + dm * cj;
      const float vf = ar4(a[0] + a[1]) * (1.0f / D);
      const float rstd = __builtin_amdgcn_rsqf(vf + 1e-5f);
      const float rd = rstd * ar4(l[0] + l[1]) + sw;
      const unsigned rb = f2bf(rd);
      const unsigned ord = rb ^ ((rb & 0x8000u) ? 0xffffu : 0x8000u);
      const unsigned key = (ord << 16) | (0xffffu - (unsigned)row0);
      unsigned t1 = key, t2 = 0u;
      TOP2(0xB1); TOP2(0x4E); TOP2(0x141); TOP2(0x140);
      if (lane == 0 && pub) ST4(reinterpret_cast<u64*>(ws + WS_KEY) + rg * 16, mk64(~t2, ~t1));
      TS(1);
      SKEWPT();
      if (key >= t2) {
        const int sl = rg * 2 + (key == t2);
#pragma unroll
        for (int q = 0; q < 4; ++q) ST16(sl * 2048 + col1 * 2, nsent4(xq[q]), ws + WS_CAND, q * 64);
        if (g4 == 0 && pub) ST16(sl * 16, (u32x4{nsent(mean), nsent(rstd), 0u, 0u}), ws + WS_HDR, 0);
      }
      TS(8);
      if (OUT_D) __builtin_amdgcn_s_sleep(OUT_D);
#pragma unroll
      for (int q = 0; q < 4; ++q) {
        const u32x4 cq = *reinterpret_cast<const u32x4*>(shm + TV + q * 64 + g4 * 16), sbq = *reinterpret_cast<const u32x4*>(shm + TV + 256 + q * 64 + g4 * 16);
        const u32x4 gbq = *reinterpret_cast<const u32x4*>(shm + TV + 512 + q * 64 + g4 * 16);
        const f32x4 x0 = lo4(xq[q]), x1 = hi4(xq[q]);
        const f32x4 h0 = ((x0 - mean) * rstd) * lo4(cq) + lo4(sbq), h1 = ((x1 - mean) * rstd) * hi4(cq) + hi4(sbq);
        ST16((row0 * D + col1) * 2, pk8(x0 + lo4(gbq) * h0, x1 + hi4(gbq) * h1), out, q * 64);
      }
      WAIT_V(0);
      if (lane == 0) ST4(reinterpret_cast<unsigned*>(ws + WS_DON) + (rg * 8 + mt) * 2, 1u);
      TS(16);
    }
    TSE();
    return;
  }
  {
    const int hid = b - 192, hi = j8 - 24;
    constexpr int RNG = 65536, SSI = MISC, GP = MISC + 2048, SSA = MISC + 3072, HS = MISC + 5120, SX1 = MISC + 9216, ACT = MISC + 13312,
                  HFLG = MISC + 13568, RR = MISC + 13824;
    unsigned* flg = reinterpret_cast<unsigned*>(shm + HFLG);
    const int ec = xcd * 7 + hi, hd = hi == 7 ? 7 : (hi + 7 - ((3 + xcd) & 7) % 7) % 7;
    const bool pon = wid < ((0x06555542 >> (4 * hd)) & 7);
    const char* pfh = PFG((hd < 2 ? 2 * hd : 5 * hd - 4) + wid, 8 - qx);
#define HPF(g) do { if (pon) PFL(pfh + ((((g) + qx) & 7) << 9)); } while (0)
    WHLD(4);
    const int cj = hd == 1 ? 34 + xcd : hd * 8 - 16 + xcd, co = hd == 1 && xcd < 6 ? xcd : cj / 7, ce = hd == 1 && xcd < 6 ? 3 : cj % 7 + (cj % 7 >= 3);
    const int cb0 = hd % 7 == 0 ? 16 + (hd & 1) * 8 + xcd : (co < 2 ? co : co + 2) * 8 + ce;
    const unsigned om = (unsigned)((wid * 16 + (lane >> 3)) * 12288 + (lane & 7) * 16);
    const unsigned og = (unsigned)((wid * 8 + (lane >> 3)) * 7168 + (lane & 7) * 16);
    const unsigned od = (unsigned)((wid * 8 + (lane >> 3)) * 2048 + (lane & 7) * 16);
    const char* pm = reinterpret_cast<const char*>(wmod) + cb0 * 128;
    const char* pg = reinterpret_cast<const char*>(wg) + ec * 128;
    const char* pu = reinterpret_cast<const char*>(wu) + ec * 128;
    const char* pd = reinterpret_cast<const char*>(wd) + (size_t)ec * 131072;
    const unsigned ow = (unsigned)(((wid & 1) * 16 + (lane >> 3)) * 12288 + (wid >> 1) * 128 + (lane & 7) * 16);
    const char* pw = reinterpret_cast<const char*>(wmod) + 8192 + xcd * 512;
#define RQM(r)                                                                                   \
  do {                                                                                           \
    const char* p_ = pm + (size_t)(r)*1572864;                                                   \
    GLDSP(lds0 + RNG + ((r)&3) * 16384 + wid * 2048, om, p_, WPOL);                              \
    GLDSP(lds0 + RNG + ((r)&3) * 16384 + wid * 2048 + 1024, om, p_ + 98304, WPOL);               \
  } while (0)
#define RQE(r)                                                                                                                \
  do {                                                                                                                        \
    GLDSP(lds0 + RNG + ((r)&3) * 16384 + wid * 1024, (r) < 16 ? og : od, (r) < 16 ? pg + (r)*458752 : pd + ((r)-16) * 256, WPOL);   \
    GLDSP(lds0 + RNG + ((r)&3) * 16384 + 8192 + wid * 1024, (r) < 16 ? og : od, (r) < 16 ? pu + (r)*458752 : pd + ((r)-16) * 256 + 128, WPOL); \
  } while (0)
    PBAR(4);
    XFLAG();
    const u32x4 sv = {SENT, SENT, SENT, SENT};
    auto PUBS = [&](int cb, unsigned v, unsigned p, unsigned r) __attribute__((always_inline)) {
      const int vi = cb >> 4, col = (cb & 15) * 64 + lane;
      ST4(modv + cb * 64 + lane, v);
      if (vi == 4) ST4(modv + 6144 + col, p);
      if (vi == 4 ? (lane & 31) == 0 : vi == 3 && lane == 0) ST4(modv + 7168 + (vi == 4 ? col >> 5 : 32 + (cb & 15)), r);
    };
    if (wid == 4 || (hi == 7 && wid < 4)) PUBS(wid == 4 ? cb0 : 64 + xcd * 4 + wid, SENT, SENT, SENT);
    if (tid >= 480) ST4(reinterpret_cast<unsigned*>(ws + WS_Q) + hsl * 32 + tid - 480, SENT);
    unsigned cv, wv;
    asm volatile("global_load_dword %0, %2, %3\n\tglobal_load_dword %1, %2, %4" : "=&v"(cv), "=&v"(wv) : "v"(tid * 4), "s"(cvec), "s"(wr_) : "memory");
    if (hi < 7) {
      if (PF_N < 3) HPF(2);
      HPF(3);
      const char* ap = ws + WS_ARM + (size_t)(ec * 8 + wid) * 3328;
#pragma unroll
      for (int j = 0; j < 4; ++j) ST16(lane * 16, sv, ap, j < 3 ? j * 1024 : 2304);
      HPF(4);
      WAIT_V(4);
    } else {
      WAIT_V(0);
    }
    asm volatile("; AUDIT_OK %0 %1" : "+v"(cv), "+v"(wv)::"memory");
    *reinterpret_cast<unsigned*>(shm + SSI + tid * 4) = pack2(silu_bf(bflo(cv)), silu_bf(bfhi(cv)));
    *reinterpret_cast<unsigned*>(shm + SX1 + tid * 4) = wv;
    LBAR();
    TS(50);
    const unsigned ha = (unsigned)(((l16 >> 2) * 8 + (lane & 3)) * 2048 + wid * 256);
#define HFR(j) *reinterpret_cast<const u32x4*>(shm + ha + ((j)&1) * 8192 + ((j) >> 2) * 128 + (((((j) >> 1) & 1) * 4 + g4) ^ (lane & 7)) * 16)
    HPF(5);
#pragma unroll
    for (int j = 0; j < 8; ++j) ST16(lane * 16, HFR(j), ws + WS_WH + (size_t)hsl * 65536 + wid * 8192 + 4096, j * 1024 - 4096);
    HPF(6);
    RQM(0);
    RQM(1);
    RQM(2);
    const unsigned tm = lds0 + RNG + ((wid >> 2) * 64 + g4 * 8 + ((lane >> 2) & 3)) * 128 + (wid & 3) * 32 + (lane & 3) * 8;
    f32x4 ma = {0.f, 0.f, 0.f, 0.f};
#define GVR(i)                                                                                                                               \
  do {                                                                                                                                       \
    _Pragma("unroll") for (int f = 0; f < 2; ++f)                                                                                            \
        ma = MM(trd(tm + ((i)&3) * 16384 + f * 4096), *reinterpret_cast<const u32x4*>(shm + SSI + ((i)*128 + (wid >> 2) * 64 + f * 32 + g4 * 8) * 2), ma); \
    if ((i) == 7 && l16 == 0) *reinterpret_cast<f32x4*>(shm + GP + wid * 64 + g4 * 16) = ma;                                                 \
  } while (0)
    const float* gp = reinterpret_cast<const float*>(shm + GP);
    auto PUB = [&](int cb, float s) __attribute__((always_inline)) {
      const int vi = cb >> 4;
      const float v = rbf(((vi == 1 || vi == 4) ? 1.0f : 0.0f) + rbf(s));
      const float p = v * bf2f(*reinterpret_cast<const u16*>(shm + SX1 + ((cb & 15) * 64 + lane) * 2));
      const float r16 = row_sum(p), r32 = swap16_add(r16, r16);
      PUBS(cb, nsent(v), nsent(p), nsent(vi == 4 ? r32 : swap32_add(r32, r32)));
    };
    {
      sfor<8>([&](auto ii) __attribute__((always_inline)) {
        constexpr int i = decltype(ii)::value;
        (void)ow, (void)pw, (void)om, (void)pm, (void)og, (void)od, (void)pg, (void)pu, (void)pd, (void)lds0, (void)wid, (void)pfl, (void)Yx, (void)l16, (void)g4, (void)xcd, (void)hfl, (void)lane, (void)hid;
        TSCAP();
        WAIT_V(4);
        LBAR();
        if (wid == 2) TS(i);
        if constexpr (i == 0) { if (tid == 0) ST4(reinterpret_cast<u64*>(hfl + hid * 2), tag); }
        if constexpr (i == 0) HPF(7);
        if constexpr (i < 5) {
          RQM(i + 3);
        } else {
          const bool e = hid < 56;
          const char* p_ = pw + (i - 5) * 393216;
          GLDSP(lds0 + RNG + (i - 5) * 16384 + wid * (e ? 1024 : 2048), e ? og : ow, e ? pg + (i - 5) * 458752 : p_, WPOL);
          GLDSP(lds0 + RNG + (i - 5) * 16384 + wid * (e ? 1024 : 2048) + (e ? 8192 : 1024), e ? og : ow, e ? pu + (i - 5) * 458752 : p_ + 98304, WPOL);
        }
        GVR(i);
      });
    }
    TS(51);
#define PUB0() do { if (wid == 0) PUB(cb0, gp[lane] + gp[64 + lane]); } while (0)
    u64 sf, hf;
#define SFLD()                                                                                                                     \
  asm volatile("s_mov_b64 exec, 3\n\tglobal_load_dwordx2 %0, %2, %3 sc1\n\ts_mov_b64 exec, 0xff\n\tglobal_load_dwordx2 %1, %5, %4 sc1\n\ts_mov_b64 exec, -1" \
               : "=&v"(sf), "=&v"(hf) : "v"(lane * 128), "s"(ws + WS_SFL + wid * 256), "s"(ws + WS_HFL + wid * 64), "v"(lane * 8) : "memory")
    auto SAW = [&]() __attribute__((always_inline)) {
      POLL { PDBG(it);
        if (((__builtin_amdgcn_ballot_w64(sf != tag) & 3u) | (__builtin_amdgcn_ballot_w64(hf != tag) & 0xffu)) == 0) break;
        __builtin_amdgcn_s_sleep(4);
        SFLD();
        asm volatile("s_waitcnt vmcnt(0)" : "+v"(sf), "+v"(hf)::"memory");
      }
    };
    u32x2 s2;
#define SALD() asm volatile("global_load_dwordx2 %0, %1, %2 sc1" : "=v"(s2) : "v"(tid * 8), "s"(modv) : "memory")
    auto QMM = [&]() __attribute__((always_inline)) {
      *reinterpret_cast<unsigned*>(shm + SSA + tid * 4) = hi2(s2);
      f32x4 qa[2] = {};
#pragma unroll
      for (int j = 0; j < 8; ++j) qa[j & 1] = MM(HFR(j), *reinterpret_cast<const u32x4*>(shm + SSA + wid * 256 + (j >> 1) * 64 + g4 * 16), qa[j & 1]);
      if (l16 < 2) *reinterpret_cast<f32x4*>(shm + HS + wid * 128 + g4 * 32 + l16 * 16) = l16 ? qa[1] : qa[0];
    };
    auto QPUB = [&]() __attribute__((always_inline)) {
      if (tid >= 480) {
        float s = 0.f;
#pragma unroll
        for (int w = 0; w < 8; ++w) s += *reinterpret_cast<const float*>(shm + HS + w * 128 + (tid - 480) * 4);
        ST4(reinterpret_cast<unsigned*>(ws + WS_Q) + hsl * 32 + tid - 480, nsent(s));
      }
    };
    u32x8 GU[16], DN[8];
    if (hid < 56) {
      const unsigned tg = lds0 + RNG + (g4 * 8 + ((lane >> 2) & 3)) * 128 + ((lane >> 1) & 1) * 8192 + wid * 16 + (lane & 1) * 8;
      const unsigned td = lds0 + RNG + (g4 * 8 + ((lane >> 2) & 3)) * 128 + (wid >> 2) * 8192 + (wid & 3) * 32 + (lane & 3) * 8;
      sfor<24>([&](auto rr) __attribute__((always_inline)) {
        constexpr int r = decltype(rr)::value;
        (void)td, (void)tg, (void)GU, (void)DN, (void)og, (void)od, (void)pg, (void)pu, (void)pd, (void)lds0, (void)wid, (void)pfl, (void)Yx, (void)l16, (void)g4, (void)xcd;
        (void)s2, (void)modv, (void)tid;
        (void)hid, (void)lane, (void)sf, (void)hf;
        (void)sv, (void)hi, (void)ws;
#define EX(r) (((r) < 0 ? 0 : (r) < 2 ? 4 : (r) == 2 ? 2 : 0) + ((r) == Q_AT - 6 ? 2 : 0) + ((r) == Q_AT - 3))
        WAIT_V(r > 21 ? 2 * (23 - r) : 4 + EX(r - 2) + EX(r - 1));
        LBAR();
        TSCAP();
        if (wid == 3) TS(r);
        if constexpr (r == 0) PUB0();
        if constexpr (r == 1) {
          if (cb0 < 16 && wid == 0) {
            WAIT_V(2 + EX(0));
            if (lane == 0) ST4(reinterpret_cast<u64*>(ws + WS_SFL) + cb0 * 16, tag);
          }
        }
        if constexpr (r == Q_AT - 6) SFLD();
        if constexpr (r == Q_AT - 3) {
          asm volatile("" : "+v"(sf), "+v"(hf));
          SAW();
          SALD();
        }
        if constexpr (r < 3) {
          const char* ap = ws + WS_XCH + xcd * 589824 + (hi * 8 + wid) * 10240 + r * 4096;
#pragma unroll
          for (int j = 0; j < (r < 2 ? 4 : 2); ++j) ST16(lane * 16, sv, ap, j * 1024);
          if (r == 2 && wid < 3) ST16(lane * 16, sv, ws + WS_XCH + xcd * 589824 + min(560 + hi * 3 + wid, 575) * 1024, 0);
        }
        if constexpr (r < 21) RQE(r + 3);
        if constexpr (r < 16) {
          GU[r] = trd2(tg + (r & 3) * 16384);
          asm volatile("" : "+v"(GU[r]));
        } else {
          DN[r - 16] = trd2(td + (r & 3) * 16384);
          asm volatile("" : "+v"(DN[r - 16]));
        }
        if constexpr (r == Q_AT) {
          asm volatile("" : "+v"(s2));
          QMM();
        }
        if constexpr (r == Q_AT + 1) QPUB();
      });
      TS(52);
    } else {
      const unsigned tw = lds0 + RNG + (wid >> 1) * 4096 + (g4 * 8 + ((lane >> 2) & 3)) * 128 + (wid & 1) * 64 + (lane & 3) * 8;
      f32x4 mb[2] = {};
      auto WR = [&](int r0, int r1) __attribute__((always_inline)) {
#pragma unroll 1
        for (int r = r0; r < r1; ++r) {
          WAIT_V(4);
          LBAR();
          if (wid == 2 && r < 22) TSD(r + 8);
          const char* p_ = pw + min(r + 3, 31) * 393216;
          GLDSP(lds0 + RNG + ((r + 3) & 3) * 16384 + wid * 2048, ow, p_, WPOL);
          GLDSP(lds0 + RNG + ((r + 3) & 3) * 16384 + wid * 2048 + 1024, ow, p_ + 98304, WPOL);
          const u32x4 sf = *reinterpret_cast<const u32x4*>(shm + SSI + (r * 32 + g4 * 8) * 2);
          mb[0] = MM(trd(tw + (r & 3) * 16384), sf, mb[0]);
          mb[1] = MM(trd(tw + (r & 3) * 16384 + 32), sf, mb[1]);
        }
      };
      WR(0, 1);
      PUB0();
      WR(1, MQ_AT - 6);
      SFLD();
      WR(MQ_AT - 6, MQ_AT - 3);
      asm volatile("; AUDIT_OK %0 %1" : "+v"(sf), "+v"(hf));
      SAW();
      SALD();
      WR(MQ_AT - 3, MQ_AT);
      asm volatile("; AUDIT_OK %0" : "+v"(s2));
      QMM();
      WR(MQ_AT, 32);
      if (l16 < 2) *reinterpret_cast<f32x4*>(shm + GP + wid * 128 + l16 * 64 + g4 * 16) = l16 ? mb[1] : mb[0];
      LBAR();
      if (wid < 4) PUB(64 + xcd * 4 + wid, gp[tid]);
      QPUB();
      TS(52);
      WAIT_V(0);
      TSE();
      return;
    }
    bool early = false;
    if (tid == 0) {
      POLL { PDBG(it);
        unsigned g;
        asm volatile("global_load_dword %0, %1, %2 sc1\n\ts_waitcnt vmcnt(0)" : "=&v"(g) : "v"(0u), "s"(ws + WS_WAK + hid * 128) : "memory");
        if (g != SENT) break;
        early = true;
        __builtin_amdgcn_s_sleep(OPT_KS);
      }
    }
    BARRIER();
    TS(21);
    const int tok = hid & 1, col = (hid & 16) * 32 + (lane & 3) * 128 + (hid & 14) * 8 + (lane >> 2);
    u32x4 c4, s4;
    unsigned gbu;
    POLL { PDBG(it);
      asm volatile("global_load_dwordx4 %0, %3, %5 sc1\n\tglobal_load_dwordx4 %1, %3, %6 sc1\n\tglobal_load_dword %2, %4, %7 sc1\n\ts_waitcnt vmcnt(0)"
                   : "=&v"(c4), "=&v"(s4), "=&v"(gbu) : "v"((tid & 255) * 16), "v"(col * 4), "s"(modv + 4096), "s"(modv + 3072), "s"(modv + 5120) : "memory");
      if (__all(max(max(max4(c4), max4(s4)), gbu) != SENT)) break;
      __builtin_amdgcn_s_sleep(8);
    }
    const f32x4 cb2 = __builtin_bit_cast(f32x4, c4), sb2 = __builtin_bit_cast(f32x4, s4);
    unsigned hra = lds0 + HS + g4 * 64 + (lane & 4) * 8 + (lane & 1) * 16, hwa = lds0 + HS + ((tid & 255) >> 2) * 64 + (wid >> 2) * 32 + (tid & 3) * 4;
    unsigned pto = (g4 & 1) * 4096 + ((g4 >> 1) * 8 + wid) * 256 + l16 * 16, six = (lane & 1) ? 0xEEEEu : 0x4444u;
    unsigned awa = lds0 + ACT + (wid >> 1) * 64 + g4 * 32 + (lane & 2) * 8 + (wid & 1) * 8 + (lane & 4) + (lane & 1) * 2;
    f32x4 d = {0.f, 0.f, 0.f, 0.f};
    unsigned sla = lds0 + HFLG + 8 + (wid >> 2) * 4;
    asm volatile("" : "+v"(sla), "+v"(hra), "+v"(hwa), "+v"(pto), "+v"(awa), "+v"(six), "+v"(d));
    const float gb = __uint_as_float(gbu);
    if (wid == 0) {
      u32x2 g0 = {SENT, SENT}, g1 = {SENT, SENT};
      const char* gp = ws + WS_KEY + lane * 128;
      if (__any(early)) __builtin_amdgcn_s_sleep(OPT_KD);
      TS(20);
      u64 m0 = ~0ull, m1 = ~0ull;
      int left = PCAP;
      do {
        asm volatile("s_sleep 1\n\ts_mov_b64 exec, %4\n\tglobal_load_dwordx2 %0, %2, off sc1\n\ts_mov_b64 exec, %5\n\tglobal_load_dwordx2 %1, %3, off sc1\n\t"
                     "s_mov_b64 exec, -1\n\ts_waitcnt vmcnt(0)"
                     : "+v"(g0), "+v"(g1) : "v"(gp), "v"(gp + 8192), "s"(m0), "s"(m1) : "memory");
        m0 = __builtin_amdgcn_ballot_w64(max2(g0) == SENT);
        m1 = __builtin_amdgcn_ballot_w64(max2(g1) == SENT);
        left = (m0 | m1) ? left - 1 : 0;
        PDBG(PCAP - left);
      } while (left);
      g0 = ~g0;
      g1 = ~g1;
      const unsigned a1 = max(g0[0], g1[0]), a2 = max(min(g0[0], g1[0]), max(g0[1], g1[1]));
      const unsigned k1 = wave_max_u(a1);
      const unsigned k2 = wave_max_u(a1 == k1 ? a2 : a1);
      const int rg1 = key_row(k1) >> 4, rg2 = key_row(k2) >> 4;
      asm volatile("ds_write_b128 %0, %1" ::"v"(lds0 + HFLG), "v"(u32x4{k1, k2, rg1 * 32u, rg2 * 32u + (rg2 == rg1) * 16u}) : "memory");
      TS(22);
    }
    LBAR();
    {
      unsigned sl;
      asm volatile("ds_read_b32 %0, %1\n\ts_waitcnt lgkmcnt(0)" : "=&v"(sl) : "v"(sla) : "memory");
      u32x2 v, hd;
#define CLD()                                                                                                          \
  asm volatile("global_load_dwordx2 %0, %2, %4 sc1\n\tglobal_load_dwordx2 %1, %3, %5 sc1\n\ts_waitcnt vmcnt(0)"        \
               : "=&v"(v), "=&v"(hd) : "v"(sl * 128 + (tid & 255) * 8), "v"(sl), "s"(ws + WS_CAND), "s"(ws + WS_HDR) : "memory")
      CLD();
      UNTIL(__all(max(max2(v), max2(hd)) != SENT), 1, { CLD(); asm volatile(".Lcpf_e1:"); });
      TS(23);
      const float mean = __uint_as_float(hd[0]), rstd = __uint_as_float(hd[1]);
      const f32x4 h = ((f32x4{bflo(v[0]), bfhi(v[0]), bflo(v[1]), bfhi(v[1])} - mean) * rstd) * cb2 + sb2;
      asm volatile("ds_write2_b32 %0, %1, %2 offset1:4" ::"v"(hwa), "v"(pack2(h[0], h[1])), "v"(pack2(h[2], h[3])) : "memory");
      *reinterpret_cast<u32x2*>(shm + SX1 + tid * 8) = v;
    }
    SKEWPT();
    TS(26);
    LBAR();
    TS(27);
    f32x4 dn[8];
    {
      u32x4 hb[8];
      sfor<23>([&](auto ff) __attribute__((always_inline)) {
        constexpr int f = decltype(ff)::value, c = f - 7;
        (void)d, (void)hb, (void)GU, (void)hra, (void)six;
        if constexpr (c >= 0) {
          WAIT_L(f <= 16 ? 6 : 22 - f);
          SMF(d, hb[c & 7], GU[c], six);
        }
        if constexpr (f < 16) DSR(hb[f & 7], hra, f * 256);
      });
      asm volatile("s_nop 7" : "+v"(d));
      TS(29);
      const float dv = rbf(d[0] + d[1]), uv = dpp_f<0x128>(dv);
      const unsigned av = f2bf(rbf(dv * __builtin_amdgcn_rcpf(1.0f + __expf(-dv))) * uv);
      asm volatile("s_mov_b64 exec, 0xff00ff\n\tds_write_b16 %0, %1\n\ts_mov_b64 exec, -1" ::"v"(awa), "v"(av) : "memory");
    }
#pragma unroll
    for (int c = 0; c < 8; ++c) asm volatile("v_mov_b64 %0, 0" : "=v"(dn[c].lo));
    asm volatile("" : W8(dn));
    TS(24);
    LBAR();
    {
      u32x4 a0;
      asm volatile("ds_read_b128 %0, %1 offset:%2\n\ts_waitcnt lgkmcnt(0)" : "=&v"(a0) : "v"(hra), "n"(ACT - HS) : "memory");
#pragma unroll
      for (int c = 0; c < 8; ++c) SMF(dn[(c >> 1) + (c & 1) * 4], a0, DN[(c >> 1) + (c & 1) * 4], six);
      asm volatile("s_nop 7" : W8(dn));
      u32x4 o;
#pragma unroll
      for (int i = 0; i < 4; ++i) o[i] = nsent((g4 & 2) ? dn[4 + i][0] + dn[4 + i][1] : dn[i][0] + dn[i][1]);
      ST16(pto, o, ws + WS_PART + ec * 8192, 0);
    }
    TS(25);
    SKEWPT();
    if (hid >= 32) {
      if (hi == 6 && wid == 1 && lane < 35) {
        char* fp = ws + (lane < 32 ? WS_XFL + (xcd * 32 + lane) * 128 : lane < 34 ? WS_SFL + (xcd * 2 + lane - 32) * 128 : WS_EPO);
        if (lane < 34 || xcd == 0) ST4(reinterpret_cast<u64*>(fp), lane < 34 ? ~tag : tag);
      }
      if (hid == 40 && tid < 320) ST4(reinterpret_cast<u64*>(ws + WS_HFL) + tid, ~tag);
      TSE();
      return;
    }
    const unsigned kk = flg[tok];
    const int row = key_row(kk);
    float* rr = reinterpret_cast<float*>(shm + RR);
    float x1 = bf2f(*reinterpret_cast<const u16*>(shm + SX1 + tok * 2048 + col * 2));
    float h2 = bf2f(*reinterpret_cast<const u16*>(shm + HS + (col >> 4) * 64 + tok * 32 + (col & 2) * 8 + (col & 12) + (col & 1) * 2));
    float rsg = rbf(1.0f / (1.0f + __expf(-key_val(kk))));
    unsigned oo3 = (unsigned)(row * D + col) * 2, rra = lds0 + RR + lane * 32;
    asm volatile("" : "+v"(rsg), "+v"(x1), "+v"(h2), "+v"(oo3), "+v"(rra));
    if (wid == 7) {
      POLL { PDBG(it);
        unsigned g;
        asm volatile("global_load_dword %0, %1, %2 sc1\n\ts_waitcnt vmcnt(0)" : "=&v"(g) : "v"(((row >> 4) * 8 + (col >> 7)) * 8), "s"(ws + WS_DON) : "memory");
        if (__all(g != SENT)) break;
        __builtin_amdgcn_s_sleep(2);
      }
    } else {
      const unsigned po = (unsigned)((wid * 8 + g4) * 8192 + tok * 4096 + (hid >> 1) * 256 + l16 * 16);
      u32x4 v[2];
      if (OPT_RD) __builtin_amdgcn_s_sleep(OPT_RD);
#define PLD()                                                                                                          \
  asm volatile("global_load_dwordx4 %0, %2, %3 sc1\n\tglobal_load_dwordx4 %1, %2, %4 sc1\n\ts_waitcnt vmcnt(0)"        \
               : "=&v"(v[0]), "=&v"(v[1]) : "v"(po), "s"(ws + WS_PART), "s"(ws + WS_PART + 32768) : "memory")
      SPIN(__all(max(max4(v[0]), max4(v[1])) != SENT), OPT_RR, PLD());
      TS(28);
      const f32x4 t = __builtin_bit_cast(f32x4, v[0]) + __builtin_bit_cast(f32x4, v[1]);
      rr[(l16 * 4 + g4) * 8 + wid] = swap16_add(swap32_add(t[0], t[2]), swap32_add(t[1], t[3]));
    }
    LBAR();
    TS(5);
    if (wid == 0) {
      f32x4 r0, r1;
      asm volatile("ds_read_b128 %0, %2\n\tds_read_b128 %1, %2 offset:16\n\ts_waitcnt lgkmcnt(0)" : "=&v"(r0), "=&v"(r1) : "v"(rra) : "memory");
      const float sum = ((r0[0] + r0[1]) + (r0[2] + r0[3])) + ((r1[0] + r1[1]) + r1[2]);
      asm volatile("global_store_short %0, %1, %2" ::"v"(oo3), "v"(f2bf(x1 + gb * rbf(h2 + rbf(rbf(sum) * rsg)))), "s"(out) : "memory");
    }
    TS(15);
    TSE();
  }
}

extern "C" void kernel_launch(void* const* d_in, const int* in_sizes, int n_in, void* d_out, int out_size, void* d_ws, size_t ws_size,
                              hipStream_t stream) {
  const u16* x = (const u16*)d_in[0];
  const u16* wsr = (const u16*)d_in[1];
  const u16* wsi = (const u16*)d_in[2];
  const u16* whr = (const u16*)d_in[3];
  const u16* whi = (const u16*)d_in[4];
  const u16* w_r = (const u16*)d_in[6];
  const u16* w_gate = (const u16*)d_in[7];
  const u16* w_up = (const u16*)d_in[8];
  const u16* w_down = (const u16*)d_in[9];
  const u16* c = (const u16*)d_in[10];
  const u16* w_mod = (const u16*)d_in[11];
  u16* out = (u16*)d_out;
  k_main<<<256, 512, 0, stream>>>(x, (char*)d_ws, wsr, wsi, c, w_mod, whr, whi, w_r, w_gate, w_up, w_down, out);
}
```

```cpp
#include <hip/hip_runtime.h>
#include <hip/hip_bf16.h>
#include <stdint.h>
#include <utility>

typedef unsigned short u16;
typedef unsigned long long u64;
using bf16x8 = __attribute__((ext_vector_type(8))) short;
using bf16v8 = __attribute__((ext_vector_type(8))) __bf16;
using f32x4 = __attribute__((ext_vector_type(4))) float;
using f32x2 = __attribute__((ext_vector_type(2))) float;
using u32x4 = __attribute__((ext_vector_type(4))) unsigned;
using u32x2 = __attribute__((ext_vector_type(2))) unsigned;

constexpr int S = 2048, D = 1024, F = 3584;

__device__ __forceinline__ float bf2f(unsigned v) { return __uint_as_float(v << 16); }
__device__ __forceinline__ float bflo(unsigned v) { return __uint_as_float(v << 16); }
__device__ __forceinline__ float bfhi(unsigned v) { return __uint_as_float(v & 0xffff0000u); }
__device__ __forceinline__ unsigned f2bf(float f) {
  __hip_bfloat16 b = __float2bfloat16(f);
  return (unsigned)*reinterpret_cast<u16*>(&b);
}
__device__ __forceinline__ float rbf(float f) { return bf2f(f2bf(f)); }
using bf16v2 = __attribute__((ext_vector_type(2))) __bf16;
__device__ __forceinline__ unsigned pack2(float lo, float hi) {
  return __builtin_bit_cast(unsigned, __builtin_convertvector(f32x2{lo, hi}, bf16v2));
}

template <int CTRL>
__device__ __forceinline__ float dpp_f(float v) {
  return __int_as_float(__builtin_amdgcn_mov_dpp(__float_as_int(v), CTRL, 0xf, 0xf, true));
}
template <int CTRL>
__device__ __forceinline__ float dpp_add(float v) { return v + dpp_f<CTRL>(v); }
__device__ __forceinline__ float row_sum(float v) { return dpp_add<0x140>(dpp_add<0x141>(dpp_add<0x4E>(dpp_add<0xB1>(v)))); }
template <int CTRL>
__device__ __forceinline__ unsigned dpp_u(unsigned v) { return (unsigned)__builtin_amdgcn_mov_dpp((int)v, CTRL, 0xf, 0xf, true); }
__device__ __forceinline__ unsigned wave_max_u(unsigned v) {
  v = max(v, dpp_u<0xB1>(v));
  v = max(v, dpp_u<0x4E>(v));
  v = max(v, dpp_u<0x141>(v));
  v = max(v, dpp_u<0x140>(v));
  const unsigned a = (unsigned)__builtin_amdgcn_readlane((int)v, 0), b = (unsigned)__builtin_amdgcn_readlane((int)v, 16);
  const unsigned c = (unsigned)__builtin_amdgcn_readlane((int)v, 32), d = (unsigned)__builtin_amdgcn_readlane((int)v, 48);
  return max(max(a, b), max(c, d));
}
__device__ __forceinline__ u64 mk64(unsigned hi, unsigned lo) { return ((u64)hi << 32) | lo; }
__device__ __forceinline__ float swap32_add(float a, float b) {
  auto r = __builtin_amdgcn_permlane32_swap(__float_as_uint(a), __float_as_uint(b), false, false);
  return __uint_as_float(r[0]) + __uint_as_float(r[1]);
}
__device__ __forceinline__ float swap16_add(float a, float b) {
  auto r = __builtin_amdgcn_permlane16_swap(__float_as_uint(a), __float_as_uint(b), false, false);
  return __uint_as_float(r[0]) + __uint_as_float(r[1]);
}
__device__ __forceinline__ float ar4(float v) {
  const float t = swap32_add(v, v);
  return swap16_add(t, t);
}
__device__ __forceinline__ float wave_sum_s(float v) {
  v = row_sum(v);
  v += __int_as_float(__builtin_amdgcn_update_dpp(0, __float_as_int(v), 0x142, 0xa, 0xf, false));
  v += __int_as_float(__builtin_amdgcn_update_dpp(0, __float_as_int(v), 0x143, 0xc, 0xf, false));
  return __int_as_float(__builtin_amdgcn_readlane(__float_as_int(v), 63));
}
__device__ __forceinline__ float silu_bf(float c) { return rbf(c * __builtin_amdgcn_rcpf(1.0f + __expf(-c))); }
__device__ __forceinline__ float dsum(unsigned v, float a) {
  return __builtin_amdgcn_fdot2_f32_bf16(__builtin_bit_cast(bf16v2, v), __builtin_bit_cast(bf16v2, 0x3f803f80u), a, false);
}
template <class Fn, int... I>
__device__ __forceinline__ void sfor_(Fn&& f, std::integer_sequence<int, I...>) { (f(std::integral_constant<int, I>{}), ...); }
template <int N, class Fn>
__device__ __forceinline__ void sfor(Fn&& f) { sfor_(f, std::make_integer_sequence<int, N>{}); }
constexpr unsigned SENT = 0xFFFFFFFFu;

constexpr size_t WS_EPO = 0;
constexpr size_t WS_XFL = 4096;
constexpr size_t WS_HFL = 40960;
constexpr size_t WS_XF2 = WS_HFL + 512;
constexpr size_t WS_SFL = WS_HFL + 8192;
constexpr size_t WS_MOD = 65536;
constexpr size_t WS_Q = WS_MOD + 32768;
constexpr size_t WS_ARM = WS_Q + 8192;
constexpr size_t WS_RS = WS_ARM;
constexpr size_t WS_KEY = WS_RS + 16384;
constexpr size_t WS_HDR = WS_KEY + 16384;
constexpr size_t WS_UFL = WS_HDR + 4096;
constexpr size_t WS_DON = WS_UFL + 98304;
constexpr size_t WS_STAT = WS_DON + 12288;
constexpr size_t WS_CAND = WS_STAT + 262144;
constexpr size_t WS_PART = WS_CAND + 524288;
constexpr size_t WS_WAK = WS_PART + 483328;
static_assert(WS_WAK + 8192 <= WS_ARM + 448 * 3328, "armed region");
constexpr size_t WS_XCH = 2u << 20;
constexpr size_t WS_H = 8u << 20;
constexpr size_t WS_WH = 12u << 20;
constexpr size_t WS_U = 16u << 20;
constexpr size_t WS_DBG = 64u << 20;
#define NEWDESIGN 1

#define PCAP (1 << 20)
#define PDBG(it)
#define TS(i)
#define TSCAP()
#define TSC0()
#define TSC1(i)
#define TSD(i)
#define TSE()
#define ST16(voff, v, base, off) asm volatile("global_store_dwordx4 %0, %1, %2 offset:%3 sc1\n\ts_nop 1" ::"v"(voff), "v"(v), "s"(base), "n"(off) : "memory")
#define ST4(p, v) __hip_atomic_store(p, v, __ATOMIC_RELAXED, __HIP_MEMORY_SCOPE_AGENT)
#define WAIT_V(n) asm volatile("s_waitcnt vmcnt(%0)" ::"n"(n) : "memory")
#define WAIT_L(n) asm volatile("s_waitcnt lgkmcnt(%0)" ::"n"(n) : "memory")
#define BARRIER() asm volatile("s_barrier" ::: "memory")
#define LBAR() asm volatile("s_waitcnt lgkmcnt(0)\n\ts_barrier" ::: "memory")
#define DSR(dst, addr, off) asm volatile("ds_read_b128 %0, %1 offset:%2" : "=v"(dst) : "v"(addr), "n"(off) : "memory")
#define MFMA(c, a, b) asm volatile("v_mfma_f32_16x16x32_bf16 %0, %1, %2, %0" : "+v"(c) : "v"(a), "v"(b))
#define MFMAI(c, a, b) asm volatile("v_mfma_f32_16x16x32_bf16 %0, %1, %2, %0" ::"v"(c), "v"(a), "v"(b))
#define MFMA0(c, a, b) asm volatile("v_mfma_f32_16x16x32_bf16 %0, %1, %2, 0" : "=&v"(c) : "v"(a), "v"(b))
#define GLDSP(m0v, voff, gbase, pol) \
  asm volatile("s_mov_b32 m0, %0\n\ts_nop 0\n\tglobal_load_lds_dwordx4 %1, %2" pol ::"s"(m0v), "v"(voff), "s"(gbase) : "memory")
#define GLDS(m0v, voff, gbase) GLDSP(m0v, voff, gbase, "")
#define WPOL " nt"
#define LDXS(dst, voff, base, off) asm volatile("global_load_dwordx4 %0, %1, %2 offset:%3 sc1" : "=v"(dst) : "v"(voff), "s"(base), "n"(off) : "memory")
#define BF8(v) __builtin_bit_cast(bf16v8, v)
#define MM(a, b, c) __builtin_amdgcn_mfma_f32_16x16x32_bf16(BF8(a), BF8(b), c, 0, 0, 0)
#define W6(a) "+v"(a[0]), "+v"(a[1]), "+v"(a[2]), "+v"(a[3]), "+v"(a[4]), "+v"(a[5])
#define W8(a) W6(a), "+v"(a[6]), "+v"(a[7])
#define PFL(ptr) asm volatile("s_mov_b32 m0, %0\n\ts_nop 0\n\tglobal_load_lds_dword %1, off" ::"s"(pfl), "v"(ptr) : "memory")
using s16x4 = __attribute__((ext_vector_type(4))) short;
__device__ __forceinline__ u32x4 trd(unsigned a) {
  const u32x2 lo = __builtin_bit_cast(u32x2, __builtin_amdgcn_ds_read_tr16_b64_v4i16((__attribute__((address_space(3))) s16x4*)(size_t)a));
  const u32x2 hi = __builtin_bit_cast(u32x2, __builtin_amdgcn_ds_read_tr16_b64_v4i16((__attribute__((address_space(3))) s16x4*)(size_t)(a + 512)));
  return u32x4{lo[0], lo[1], hi[0], hi[1]};
}
using u32x8 = __attribute__((ext_vector_type(8))) unsigned;
__device__ __forceinline__ u32x8 trd2(unsigned a) {
  const u32x4 lo = trd(a), hi = trd(a + 4096);
  return u32x8{lo[0], lo[1], lo[2], lo[3], hi[0], hi[1], hi[2], hi[3]};
}
#define SMF(c, a, b, i) asm volatile("v_smfmac_f32_16x16x64_bf16 %0, %1, %2, %3" ::"v"(c), "v"(a), "v"(b), "v"(i))
__device__ __forceinline__ int key_row(unsigned k) { return (int)((0xffffu - (k & 0xffffu)) & 2047u); }
__device__ __forceinline__ float key_val(unsigned k) {
  const unsigned ord = k >> 16;
  return bf2f(ord ^ ((ord & 0x8000u) ? 0x8000u : 0xffffu));
}
__device__ __forceinline__ unsigned nsent(float v) { return min(__float_as_uint(v), SENT - 1u); }
__device__ __forceinline__ f32x4 lo4(u32x4 q) { return f32x4{bflo(q[0]), bfhi(q[0]), bflo(q[1]), bfhi(q[1])}; }
__device__ __forceinline__ f32x4 hi4(u32x4 q) { return f32x4{bflo(q[2]), bfhi(q[2]), bflo(q[3]), bfhi(q[3])}; }
__device__ __forceinline__ u32x4 pk8(f32x4 a, f32x4 b) { return u32x4{pack2(a[0], a[1]), pack2(a[2], a[3]), pack2(b[0], b[1]), pack2(b[2], b[3])}; }
__device__ __forceinline__ u32x4 nsent4(u32x4 v) { return u32x4{min(v[0], SENT - 1u), min(v[1], SENT - 1u), min(v[2], SENT - 1u), min(v[3], SENT - 1u)}; }
__device__ __forceinline__ unsigned max4(u32x4 v) { return max(max(v[0], v[1]), max(v[2], v[3])); }
__device__ __forceinline__ unsigned max2(u32x2 v) { return max(v[0], v[1]); }
__device__ __forceinline__ unsigned hi2(u32x2 v) { return (v[0] >> 16) | (v[1] & 0xffff0000u); }
#define TOP2(CTRL)                                                  \
  do {                                                              \
    const unsigned o1 = dpp_u<CTRL>(t1), o2 = dpp_u<CTRL>(t2);      \
    t2 = max(min(t1, o1), max(t2, o2));                             \
    t1 = max(t1, o1);                                               \
  } while (0)
#define POLL _Pragma("unroll 1") for (int it = 0; it < PCAP; ++it)
#define SPIN(OK, PAUSE, LD)                                 \
  do {                                                      \
    int left_ = PCAP;                                       \
    do {                                                    \
      __builtin_amdgcn_s_sleep(PAUSE);                      \
      LD;                                                   \
      left_ = (OK) ? 0 : left_ - 1;                         \
      PDBG(PCAP - left_);                                   \
    } while (left_);                                        \
  } while (0)
#define UNTIL(OK, PAUSE, LD)                                \
  do {                                                      \
    if (__builtin_expect(!(OK), 0)) {                       \
      POLL { PDBG(it);                                      \
        __builtin_amdgcn_s_sleep(PAUSE);                    \
        LD;                                                 \
        if (OK) break;                                      \
      }                                                     \
    }                                                       \
  } while (0)
#define P_D3 16
#define P_D4 2
#define U_D 28
#define U_R 1
#define X_R 1
#define OPT_SD 16
#define OPT_KS 2
#define OPT_KD 32
#define OPT_RD 8
#define OPT_RR 2
#define OUT_D 20
#define CPF_N 10
#define CPF_K 24
#define CPF_B 10
#define PF_N 3
#define A_LD 3
#define F_AT 3
#define X_AT 12
#define H_AT 24
#define B_VAT 43
#define UF_AT 8
#define B46 339
#define TP_XL 340
#define TP_X2 350
#define Q_AT 10
#define MQ_AT 28
#define XQ0 2
#define XQ1 4
#define XQ2 18
#define XQ3 20
#define HX0 9
#define HXS 2
static_assert(HX0 + 11 * HXS < 44 && UF_AT <= 22 && A_LD <= 3 && H_AT <= 24 && X_AT + 12 <= H_AT && Q_AT >= 7, "");
#define P_L 2
#define V_D 24
#define PF_SH 0x06555542
#define PF_BS 0x001a15100b060200ull
constexpr int SL = 22528, NS = 5, XR = NS * SL, MISC = XR + 24576;
constexpr int as(int c) { return c < 3 ? 2 : c < 6 ? 3 : 2 + (c & 1); }
constexpr int ap(int c) { return c < 3 ? c : c < 6 ? c - 3 : c / 2; }
constexpr int ug(int j) { return j < 9 ? 18 + 7 * j : 82; }
constexpr int E0 = 264, E3 = E0 + 88, NE = E3 + 6;
constexpr int es(int c) { return c < 132 ? c / 11 : c < 144 ? 12 + (c - 132) / 6 : c < 156 ? 14 + (c & 1) : 12 + (c & 3); }
constexpr int ep(int c) { return c < 132 ? c % 11 : c < 144 ? (c - 132) % 6 : c < 156 ? (c - 144) / 2 : 6 + (c - 156) / 4; }

__global__ __launch_bounds__(512, 2) void k_main(const u16* __restrict__ x, char* ws, const u16* __restrict__ Y0, const u16* __restrict__ Y1,
                                              const u16* __restrict__ cvec, const u16* __restrict__ wmod, const u16* __restrict__ X0,
                                              const u16* __restrict__ X1, const u16* __restrict__ wr_, const u16* __restrict__ wg,
                                              const u16* __restrict__ wu, const u16* __restrict__ wd, u16* __restrict__ out) {
  constexpr int STG = 114688;
  __shared__ __attribute__((aligned(1024))) char shm[MISC + 16384];
  const int b = blockIdx.x, xcd = b & 7, j8 = b >> 3;
  const int cc = xcd >> 2, qx = xcd & 3;
  const int tid = threadIdx.x, lane = tid & 63;
  const int wid = __builtin_amdgcn_readfirstlane(tid >> 6);
  const unsigned lds0 = (unsigned)(size_t)(__attribute__((address_space(3))) char*)shm;
  const char* cpf;
  {
    unsigned long long pc;
    unsigned len;
    asm volatile("s_getpc_b64 %0\n.Lcpf_here:\n\ts_mov_b32 %1, .Lcpf_e1-.Lcpf_here\n\ts_max_u32 %1, %1, .Lcpf_e2-.Lcpf_here" : "=s"(pc), "=s"(len)::"scc");
    cpf = reinterpret_cast<const char*>(pc) + min((unsigned)((lane * 32 + j8) * 128), len);
  }
  const unsigned pfl = lds0 + MISC + 16128;
  if (wid == 4 && lane < CPF_N) PFL(cpf);
  u32x4 xv[2];
  {
    const unsigned xo = (unsigned)((b * 8 + wid) * D + lane * 8) * 2;
    asm volatile("global_load_dwordx4 %0, %2, %3\n\tglobal_load_dwordx4 %1, %2, %3 offset:1024" : "=&v"(xv[0]), "=&v"(xv[1]) : "v"(xo), "s"(x) : "memory");
  }
  asm volatile("" ::"s"(ws), "s"(Y0), "s"(Y1), "s"(X0), "s"(X1));
  unsigned* xfl = reinterpret_cast<unsigned*>(ws + WS_XFL);
  unsigned* hfl = reinterpret_cast<unsigned*>(ws + WS_HFL);
  unsigned* modv = reinterpret_cast<unsigned*>(ws + WS_MOD);
#define SKEWPT()
  const int g4 = lane >> 4, l16 = lane & 15;
  u64 tag0, tag;
  asm volatile("global_load_dwordx2 %0, %1, %2 sc1" : "=v"(tag0) : "v"(0u), "s"(ws) : "memory");
  const char* Yx = reinterpret_cast<const char*>(cc ? Y1 : Y0) + (size_t)qx * 2097152;
#define PFG(r16, g) (Yx + (size_t)(((r16) * 16 + l16) * 4096 + g4 * 128 + ((((g) + qx) & 7) << 9)))
  unsigned ta = lds0 + STG + ((lane >> 2) & 3) * 2368 + (wid * 128 + (lane >> 5) * 32 + (lane & 3) * 8 + (g4 & 1) * 4) * 2;
  unsigned oo = (unsigned)((wid * 4 + (lane >> 5)) * 131072 + (b >> 2) * 2048 + (g4 & 1) * 1024 + ((b & 3) * 16 + l16) * 16), oo2 = oo + 262144;
  unsigned wa = lds0 + STG + wid * 2368 + lane * 16;
  asm volatile("" : "+v"(ta), "+v"(oo), "+v"(oo2), "+v"(wa));
  const int hsl = (b & 32) + xcd * 4 + (j8 & 3);
  const char* hp = reinterpret_cast<const char*>((b & 32) ? X1 : X0) + (size_t)(hsl & 31) * 65536 + wid * 8192;
#define WHLD(i0)                                                                                                                  \
  _Pragma("unroll") for (int i = i0; i < i0 + 4; ++i)                                                                             \
      GLDS(lds0 + wid * 8192 + i * 1024, (unsigned)((lane ^ ((i >> 1) | ((wid & 2) << 1))) * 16), hp + i * 1024)
  if (wid >= 8 - PF_N) PFL(PFG(j8, 7 - wid));
  if (j8 >= 24) {
    WHLD(0);
    WAIT_V(5);
  } else if (wid >= 8 - PF_N) {
    WAIT_V(2);
  } else {
    WAIT_V(1);
  }
  {
    asm volatile("; AUDIT_OK %0 %1" : "+v"(xv[0]), "+v"(xv[1])::"memory");
    TS(32);
    const float s0 = dsum(xv[0][3], dsum(xv[0][2], dsum(xv[0][1], dsum(xv[0][0], 0.f))));
    const float s1 = dsum(xv[1][3], dsum(xv[1][2], dsum(xv[1][1], dsum(xv[1][0], 0.f))));
    const float mf = wave_sum_s(s0 + s1) * (1.0f / D), m = rbf(mf);
    const f32x4 d0 = lo4(xv[0]) - m, d1 = hi4(xv[0]) - m, d2 = lo4(xv[1]) - m, d3 = hi4(xv[1]) - m;
    const f32x4 q = (d0 * d0 + d1 * d1) + (d2 * d2 + d3 * d3);
    const float vf = wave_sum_s((q[0] + q[1]) + (q[2] + q[3])) * (1.0f / D) - (mf - m) * (mf - m);
    const float rden = __builtin_amdgcn_rcpf(rbf(__builtin_amdgcn_sqrtf(rbf(rbf(vf) + 1e-5f))));
    asm volatile("ds_write_b128 %0, %1" ::"v"(wa), "v"(pk8(d0 * rden, d1 * rden)) : "memory");
    asm volatile("ds_write_b128 %0, %1 offset:1024" ::"v"(wa), "v"(pk8(d2 * rden, d3 * rden)) : "memory");
    LBAR();
    TS(34);
    u32x2 t[4];
    asm volatile(
        "ds_read_b64_tr_b16 %0, %4\n\tds_read_b64_tr_b16 %1, %4 offset:%5\n\t"
        "ds_read_b64_tr_b16 %2, %4 offset:128\n\tds_read_b64_tr_b16 %3, %4 offset:%6\n\ts_waitcnt lgkmcnt(0)"
        : "=&v"(t[0]), "=&v"(t[1]), "=&v"(t[2]), "=&v"(t[3])
        : "v"(ta), "n"(4 * 2368), "n"(4 * 2368 + 128)
        : "memory");
    asm volatile("global_store_dwordx4 %0, %2, %4 sc1\n\tglobal_store_dwordx4 %1, %3, %4 sc1\n\ts_nop 1" ::"v"(oo), "v"(oo2),
                 "v"(u32x4{t[0][0], t[0][1], t[1][0], t[1][1]}), "v"(u32x4{t[2][0], t[2][1], t[3][0], t[3][1]}), "s"(ws + WS_H)
                 : "memory");
  }
#define PBAR(n)                                                                                                          \
  do {                                                                                                                   \
    asm volatile("s_waitcnt vmcnt(%2)\n\tv_lshl_add_u64 %0, %1, 0, 1" : "=v"(tag) : "v"(tag0), "n"(n) : "memory");       \
    TS(36);                                                                                                              \
    BARRIER();                                                                                                           \
    TS(37);                                                                                                              \
  } while (0)
#define XFLAG() do { if ((tid | 1) == 385) ST4(reinterpret_cast<u64*>(ws + ((tid & 1) ? WS_XF2 + b * 8 : WS_XFL + b * 128)), tag); } while (0)
  if (j8 < 24) {
    const int mt = (j8 * 11) >> 5, nt = j8 - mt * 3, grp = xcd * 3 + nt;
#define FROW(f) (nt * 176 + (f) * 16 - (nt == 2 && (f) > 5 ? 16 : 0))
#define PROW(p) FROW(cc ? 10 - (p) : (p))
    constexpr int SCA = MISC, TGA = MISC + 512, TV = MISC + 1024, TSUM = MISC + 1792, SCW = MISC + 2048, RKA = MISC + 3072,
                  RKB = MISC + 5120, RKZ = MISC + 6656, IDT = MISC + 7168, ZSC = MISC + 9216;
    const bool zd = nt == 2;
    unsigned* ufl = reinterpret_cast<unsigned*>(ws + WS_UFL) + grp * 1024;
    const char* Ug = ws + WS_U + (size_t)grp * (16 * SL + 1024) + 1024;
    const unsigned perm = (unsigned)mt | (((0x76543210u & ((1u << (4 * mt)) - 1u)) << 4) | (0x76543210u & (0xfffffff0u << (4 * mt))));
#define KT(i) ((perm >> (4 * (i))) & 7u)
    const int kk = wid < 4 ? wid : wid - 2;
    const bool act = (wid < 4 || wid >= 6) && (kk || (cc == 0 && nt < 2));
    const int rg = qx * 32 + (PROW(5 + kk) >> 4);
    u32x4 pv[6];
    f32x4 acc[2][11], ga[2], x5[2];
    bf16x8 idt[2];
    unsigned spo = (unsigned)(((rg * 8 + g4 * 2) * 16 + l16) * 16);
    u32x4 sr[2], dk;
#pragma unroll
    for (int q = 0; q < 4; ++q) dk[q] = l16 == g4 * 4 + q ? ~0u : 0u;
    unsigned xw1 = lds0 + XR + tid * 16, xa = lds0 + XR + kk * 4096 + lane * 16, sto = (unsigned)(((rg * 8 + mt) * 16 + lane) * 16);
    unsigned saa = lds0 + SCW + (lane & 3) * 256 + g4 * 16, cja = lds0 + TSUM + g4 * 8, swa = lds0 + TSUM + 32, cea = lds0 + TSUM + mt * 4;
    const char* stp = ws + WS_STAT;
    asm volatile("" : "+s"(stp), "+v"(cja), "+v"(swa), "+v"(cea), "+v"(dk), "+v"(xa), "+v"(sto), "+v"(xw1), "+v"(spo), "+v"(saa));
#define STLD() asm volatile("global_load_dwordx4 %0, %2, %3 sc1\n\tglobal_load_dwordx4 %1, %2, %3 offset:256 sc1\n\ts_waitcnt vmcnt(0)" : "=&v"(sr[0]), "=&v"(sr[1]) : "v"(spo), "s"(stp) : "memory")
    const char* xr = ws + WS_XCH + (size_t)(b * 4 + (wid & 3)) * 6144 + 3072;
#define XCHLD()                                                                                                       \
  asm volatile(                                                                                                       \
      "global_load_dwordx4 %0, %6, %7 offset:2048 sc1\n\tglobal_load_dwordx4 %1, %6, %7 offset:1024 sc1\n\t"          \
      "global_load_dwordx4 %2, %6, %7 sc1\n\tglobal_load_dwordx4 %3, %6, %7 offset:-1024 sc1\n\t"                     \
      "global_load_dwordx4 %4, %6, %7 offset:-2048 sc1\n\tglobal_load_dwordx4 %5, %6, %7 offset:-3072 sc1"            \
      : "=&v"(pv[0]), "=&v"(pv[1]), "=&v"(pv[2]), "=&v"(pv[3]), "=&v"(pv[4]), "=&v"(pv[5])                            \
      : "v"(lane * 16), "s"(xr)                                                                                       \
      : "memory")
    if (wid < 4) {
      bf16x8 X[4][2][2], Y[8];
      u32x4 xq[6];
      const unsigned xo0 = lane * 16 + 4096;

#define XLD(dst, XP, XH, i) LDXS(dst, xo0, XP, ((XH)&3) * 2048 + (i)*1024 - 4096)
      int xt = lane < 16 ? (int)WS_H + (mt * 4 + wid) * 131072 + (((qx * 2 + lane) & 15) << 13) : (int)WS_WH + (cc * 32 + mt * 4 + wid) * 65536 + (KT(lane & 7) << 13);
      asm volatile("" : "+v"(xt));
#define XT(j) (ws + (unsigned)__builtin_amdgcn_readlane(xt, j))
#define WP(h) XT(16 + ((h) >> 2))
#define XS(h) X[((h) >> 1) & 3][(h)&1]
      {
        unsigned ye = lds0 + l16 * 128 + (g4 ^ (lane & 7)) * 16, yo = ye ^ 64;
        const char* xpa = XT(0);
        const char* xpb = XT(1);
#pragma unroll
        for (int i = 0; i < 22; ++i) {
          acc[i / 11][i % 11] = f32x4{0.f, 0.f, 0.f, 0.f};
          asm volatile("" : "+v"(acc[i / 11][i % 11]));
        }
        const char* xw = ws + WS_XCH + (size_t)((b ^ 4) * 4 + wid) * 6144 + 3072;
        unsigned xra = lds0 + XR + l16 * 256 + (((wid * 4 + g4) ^ (lane & 7)) << 4), gaa = lds0 + TGA + wid * 128 + g4 * 32;
        unsigned rka = lds0 + (g4 ? RKZ : RKA + wid * 512 + l16 * 16), rkb = lds0 + RKB + l16 * 16;
        asm volatile("" : "+s"(xw), "+s"(xr), "+v"(xra), "+v"(gaa), "+v"(rka), "+v"(rkb));
        __builtin_amdgcn_s_setprio(1);
        PBAR(0);
        if (wid == 3) {
          const unsigned* fp = xfl + (qx * 32 + lane) * 32;
          __builtin_amdgcn_s_sleep(P_D3 - P_D4);
          u64 f;
#define GLD() asm volatile("global_load_dwordx2 %0, %1, off sc1\n\ts_waitcnt vmcnt(0)" : "=&v"(f) : "v"(fp) : "memory")
          SPIN(__all(f == tag), P_D4, GLD());
        }
        TS(38);
        BARRIER();
        TS(39);
#pragma unroll
        for (int h = 0; h < 6; ++h) {
          XLD(XS(h)[0], h < 4 ? xpa : xpb, h, 0);
          XLD(XS(h)[1], h < 4 ? xpa : xpb, h, 1);
        }
#pragma unroll
        for (int j = 0; j < 6; ++j) DSR(Y[j], (j & 1) ? yo : ye, (j >> 1) * 2048);
        unsigned so = 0, a0 = ye, a1 = yo, yb[2], tq, u = 2;
#define STEP2(S0, XA, XB)                                                                                                      \
  sfor<44>([&](auto nn) __attribute__((always_inline)) {                                                                       \
    constexpr int n = (S0)*22 + decltype(nn)::value, q = n % 22, p = q / 2, s = n / 22, i = n & 1;                             \
    constexpr int k = q == XQ0 ? 0 : q == XQ1 ? 1 : q == XQ2 ? 2 : q == XQ3 ? 3 : -1;                                          \
    (void)acc, (void)X, (void)Y, (void)a0, (void)a1, (void)xo0, (void)XA, (void)XB, (void)so, (void)ye, (void)yo, (void)u, (void)xt, (void)tq, (void)ws; \
    if constexpr (q == 11) BARRIER();                                                                                          \
    if constexpr (q == 0) asm volatile("s_waitcnt vmcnt(8) lgkmcnt(4)" ::: "memory");                                          \
    else if constexpr (!i) WAIT_L(4);                                                                                          \
    MFMAI(acc[i][p], X[s][0][i], Y[n & 6]);                                                                                    \
    DSR(Y[(n + 6) & 7], i ? a1 : a0, ((p + 3) % 11) * 2048);                                                                   \
    MFMA(acc[i][p], X[s][1][i], Y[(n & 6) + 1]);                                                                               \
    if constexpr (k >= 0) XLD(X[(s + 3) & 3][k / 2][k & 1], (s & 1) ? XB : XA, 2 * s + 6 + k / 2, k & 1);                      \
    if constexpr (q == 0 && !(s & 1)) asm volatile("v_readlane_b32 %0, %2, %1\n\ts_add_u32 %1, %1, 1" : "=&s"(tq), "+s"(u) : "v"(xt) : "scc"); \
    if constexpr (q == 6) asm volatile("s_add_u32 %0, %0, %1" : "+s"(so) : "n"(SL) : "scc");                                   \
    if constexpr (q == 8) asm volatile("s_cmp_lg_u32 %0, %1\n\ts_cselect_b32 %0, %0, 0" : "+s"(so) : "n"(NS * SL) : "scc");    \
    if constexpr (q == 12 && !(s & 1)) { asm volatile("" : "+s"(tq)); XB = ws + tq; asm volatile("" : "+s"(XB)); }             \
    if constexpr (q == 14) asm volatile("v_add_u32 %0, %1, %2" : "=v"(a0) : "s"(so), "v"(ye));                                 \
    if constexpr (q == 16) asm volatile("v_add_u32 %0, %1, %2" : "=v"(a1) : "s"(so), "v"(yo));                                 \
  })
#define YSL(e) ((32 + es((e) / 2)) % NS)
#define YRD(e) DSR(Y[(e)&7], YSL(e) < 2 ? ye : yb[YSL(e) / 4], (YSL(e) & 1) * SL + ep((e) / 2) * 2048 + ((e)&1) * 1024)

        u32x4 ov;
        bf16x8 qf[2];
        auto OPS = [&](auto kc) __attribute__((always_inline)) {
          constexpr int k = decltype(kc)::value, q = k / 10, r = k % 10, c = k - 60;
          (void)ov, (void)acc, (void)xw, (void)lane, (void)xq, (void)x5;
          if constexpr (k < 60) {
            if constexpr (r < 4) asm volatile("v_cvt_pk_bf16_f32 %0, %1, %2" : "=v"(ov[r]) : "v"(acc[r / 2][q][(r & 1) * 2]), "v"(acc[r / 2][q][(r & 1) * 2 + 1]));
            else if constexpr (r < 8) asm volatile("v_min_u32 %0, -2, %0" : "+v"(ov[r - 4]));
            else if constexpr (r == 9) asm volatile("global_store_dwordx4 %0, %1, %2 offset:%3 sc1" ::"v"(lane * 16), "v"(ov), "s"(xw), "n"(q * 1024 - 3072) : "memory");
          } else if constexpr (c >= 40 && c < 48) {
            if constexpr (c & 1) asm volatile("v_and_b32 %0, 0xffff0000, %1" : "=v"(x5[(c >> 2) & 1][c & 3]) : "v"(xq[5][(c >> 1) & 3]));
            else asm volatile("v_lshlrev_b32 %0, 16, %1" : "=v"(x5[(c >> 2) & 1][c & 3]) : "v"(xq[5][(c >> 1) & 3]));
          } else if constexpr (c < 40) {
            if constexpr (c & 1) asm volatile("v_and_b32 %0, 0xffff0000, %1" : "=v"(acc[(c >> 2) & 1][c >> 3][c & 3]) : "v"(xq[c >> 3][(c >> 1) & 3]));
            else asm volatile("v_lshlrev_b32 %0, 16, %1" : "=v"(acc[(c >> 2) & 1][c >> 3][c & 3]) : "v"(xq[c >> 3][(c >> 1) & 3]));
          }
        };
#define IC(v) std::integral_constant<int, (v)>{}
#define FILL(g)                                                                              \
  do {                                                                                       \
    if constexpr (m >= 0) {                                                                  \
      OPS(IC(m / 2 * 5 + (m & 1) * 3 + (g)));                                                \
      if constexpr ((g) && !(m & 1)) OPS(IC(m / 2 * 5 + 2));                                 \
    }                                                                                        \
  } while (0)
        auto BP = [&](auto nn) __attribute__((always_inline)) {
          constexpr int n = decltype(nn)::value, s = es(n / 2), p = n < E3 ? ep(n / 2) : 5 + n - E3, i = n & 1, e = n + 6, m = n - E0 - 30;
          (void)ga, (void)qf, (void)idt, (void)rka, (void)rkb, (void)gaa, (void)xq, (void)ov, (void)xw, (void)x, (void)pv, (void)xr, (void)ufl;
          (void)acc, (void)X, (void)Y, (void)yb, (void)ye, (void)xra, (void)xo0, (void)xt, (void)ws, (void)lane, (void)mt, (void)nt, (void)cc, (void)lds0, (void)wid, (void)g4, (void)perm;
          (void)OPS;
          TSCAP();
          if constexpr (n == E0 + 55) TS(10);
          if constexpr (n == UF_AT) {
            TS(45);
            WAIT_V(n > HX0 ? (n - HX0 + HXS - 1) / HXS : 0);
            if (lane == 0) ST4(ufl + (mt * 4 + wid) * 32, 1u);
            TS(46);
          }
          if constexpr (n == 37) TS(48);
          if constexpr (n == 37 || n == E0 + 5 || n == E0 + 17 || n == B46) BARRIER();
          if constexpr (n == 37) TS(4);
          if constexpr (n == E0 + 25) { TS(3); SKEWPT(); }
          if constexpr (p == 0 && !i && n != 22) WAIT_V(n == 0 ? 15 : s < 14 ? 8 : 4 * (15 - s));
          if constexpr (!i) WAIT_L(e < NE ? 4 : NE - 2 - n);
          if constexpr (n < 22) MFMA0(acc[i][p], X[0][0][i], Y[n & 6]);
          else if constexpr (n < E3) MFMAI(acc[i][p], X[s & 3][0][i], Y[n & 6]);
          else MFMA(acc[0][p], qf[0], Y[n & 7]);
          if constexpr (e < E3) YRD(e);
          else if constexpr (e < NE) DSR(Y[e & 7], rkb, (e - E3) * 256);
          FILL(0);
          if constexpr (n < E3 && (s == 1 || s == 15)) MFMA(acc[i][p], X[s & 3][1][i], Y[(n & 6) + 1]);
          else if constexpr (n < E3) MFMAI(acc[i][p], X[s & 3][1][i], Y[(n & 6) + 1]);
          else MFMA(acc[1][p], qf[1], Y[n & 7]);
          if constexpr (n >= HX0 && n < HX0 + 12 * HXS && (n - HX0) % HXS == 0) {
            constexpr int k = (n - HX0) / HXS;
            static_assert(k < 8 || n >= 22, "slot still in use");
            XLD(XS(4 + k / 2)[k & 1], WP(4 + k / 2), k / 2, k & 1);
          }
          if constexpr (n >= 272 && n < 294 && n % 4 == 0) DSR(xq[(n - 272) / 4], xra, (n - 272) / 4 * 4096);
          if constexpr (n >= E0 && n < E0 + 4) XLD(X[3][(n - E0) / 2][i], WP(31), 30 + (n - E0) / 2, i);
          FILL(1);
          if constexpr (n == B46 || n == B46 + 1) DSR(qf[n - B46], rka, (n - B46) * 256);
          if constexpr (n == B46 + 2 || n == B46 + 3) DSR(ga[n - B46 - 2], gaa, (n - B46 - 2) * 16);
          if constexpr (n == B46 + 4 || n == B46 + 5) DSR(idt[n - B46 - 4], yb[1], IDT - 4 * SL + (n - B46 - 4) * 1024);
          if constexpr (n >= TP_XL && n < TP_XL + 4) LDXS(pv[TP_XL + 5 - n], lane * 16, xr, (n - TP_XL) * 1024 - 3072);
          if constexpr (n == TP_X2 || n == TP_X2 + 1) LDXS(pv[TP_X2 + 1 - n], lane * 16, xr, (n - TP_X2) * 1024 + 1024);
        };
        do {
          STEP2(0, xpb, xpa);
          if (__builtin_expect(u == 17, 0)) {
            TS(57);
            TSC1(40);
            TSC0();
            f32x4 c0, c1;
            const char* up = Ug + (mt * 2 + (wid >> 1)) * SL + (wid & 1) * 1024 + 4096;
            const unsigned ul = lds0 + (2 + (wid >> 1)) * SL + (wid & 1) * 1024 + lane * 16;
            u32x4 uv;
            DSR(c0, lds0 + SCA + wid * 128 + g4 * 32, 0);
            DSR(c1, lds0 + SCA + wid * 128 + g4 * 32, 16);
#define UMUL(j, k) asm volatile("v_mul_f32 %0, %0, %1" : "+v"(acc[(k) >> 2][j][(k)&3]) : "v"(((k) >> 2 ? c1 : c0)[(k)&3]))
#define UCVT(j, k) asm volatile("v_cvt_pk_bf16_f32 %0, %1, %2" : "=v"(uv[k]) : "v"(acc[(k) >> 1][j][((k)&1) * 2]), "v"(acc[(k) >> 1][j][((k)&1) * 2 + 1]))
            auto UEP = [&](auto jc, auto rc) __attribute__((always_inline)) {
              constexpr int j = decltype(jc)::value, r = decltype(rc)::value;
              (void)acc, (void)c0, (void)c1, (void)uv, (void)lane, (void)ul, (void)up;
              if constexpr (r < 4) { UMUL(j, 2 * r); UMUL(j, 2 * r + 1); }
              else if constexpr (r < 6) { UCVT(j, 2 * r - 8); UCVT(j, 2 * r - 7); }
              else asm volatile("global_store_dwordx4 %0, %2, %3 offset:%4 sc1\n\tds_write_b128 %1, %2 offset:%5"
                                ::"v"(lane * 16), "v"(ul), "v"(uv), "s"(up + (j >> 2) * 8192), "n"((j & 3) * 2048 - 4096), "n"(j * 2048) : "memory");
            };
#define UGAP(G) do { constexpr int j_ = (G) < 82 ? ((G)-18) / 7 : 9; if constexpr ((G) >= 18 && (G) != 81) UEP(IC(j_), IC((G)-ug(j_))); } while (0)
            sfor<44>([&](auto nn) __attribute__((always_inline)) {
              constexpr int n = decltype(nn)::value, s = as(n / 2), p = ap(n / 2), i = n & 1, e = n + 6;
              (void)acc, (void)X, (void)Y, (void)ye, (void)yo, (void)xo0, (void)xpa, (void)UEP;
              if constexpr (n == 0 || n == 6) WAIT_V(8);
              if constexpr (!i) WAIT_L(e < 44 ? 4 : 42 - n);
              MFMAI(acc[i][p], X[s][0][i], Y[(n + 4) & 6]);
              if constexpr (e < 44) DSR(Y[(e + 4) & 7], i ? yo : ye, (as(e / 2) & 1) * SL + ap(e / 2) * 2048);
              UGAP(2 * n);
              if constexpr (s == 2) MFMAI(acc[i][p], X[s][1][i], Y[((n + 4) & 6) + 1]);
              else MFMA(acc[i][p], X[s][1][i], Y[((n + 4) & 6) + 1]);
              if constexpr (n < 4) XLD(X[1][n / 2][i], xpa, 2 + n / 2, i);
              UGAP(2 * n + 1);
            });
            TSC1(41);
            TS(0);
            SKEWPT();
            UEP(IC(9), IC(6));
            asm volatile("s_nop 1");
            sfor<7>([&](auto rc) __attribute__((always_inline)) { UEP(IC(10), rc); });
            asm volatile("s_nop 1" ::"v"(uv));
            ye = lds0 + lane * 16;
            yo = ye + 1024;
            so = 4 * SL;
            a0 = ye + 4 * SL;
            a1 = yo + 4 * SL;
            yb[0] = ye + 2 * SL;
            yb[1] = a0;
            asm volatile("" : "+v"(yb[0]), "+v"(yb[1]), "+v"(ye), "+v"(yo));
            LBAR();
            TS(44);
            sfor<6>([&](auto ee) __attribute__((always_inline)) { (void)Y, (void)yb, (void)ye; YRD(decltype(ee)::value); });
            sfor<44>(BP);
            u = 19;
            xpa = WP(8);
          }
          STEP2(2, xpa, xpb);
        } while (u != 24);
        TS(2);
        sfor<NE - 264>([&](auto nn) __attribute__((always_inline)) { BP(std::integral_constant<int, 264 + decltype(nn)::value>{}); });
        __builtin_amdgcn_s_setprio(0);
        TSC1(59);
        TS(13);
        SKEWPT();
      }
    } else if (wid < 6) {
      const int hw = wid - 4;
      __builtin_amdgcn_s_setprio(2);
      unsigned voy[11];
#pragma unroll
      for (int i = 0; i < 11; ++i) voy[i] = (unsigned)((PROW(i) + hw * 8 + (lane >> 3)) * 4096 + ((lane & 7) ^ (lane >> 3)) * 16);
      const unsigned lbase = lds0 + hw * 1024;
      if (zd) voy[5] = (lane & 7) * 16;
      auto stage = [&](int n, int slot) {
        const char* yp = Yx + (((n + qx * 4) & 31) << 7);
        const unsigned l = lbase + slot * SL;
#pragma unroll
        for (int i = 0; i < 11; ++i) GLDS(i == 5 && zd ? lbase + ZSC : l + i * 2048, voy[i], yp);
      };
      asm volatile("s_waitcnt vmcnt(%3)" ::"v"(voy[0]), "v"(voy[10]), "v"(tag0), "n"(0) : "memory");
      BARRIER();
      if (P_L) __builtin_amdgcn_s_sleep(P_L);
#pragma unroll 1
      for (int s = 0; s <= A_LD; ++s) stage(s, s);
      WAIT_V(11 * A_LD);
      TS(38);
      BARRIER();
      int slot = A_LD + 1;
#pragma unroll 1
      for (int k = 1; k <= 31 - A_LD; ++k) {
        WAIT_V(11 * (A_LD - 1));
        BARRIER();
        if (hw == 1) TSD(k);
        stage(k + A_LD, slot);
        slot = slot == NS - 1 ? 0 : slot + 1;
        if (k == CPF_K && hw == 1 && lane < CPF_N) PFL(cpf);
      }
      if (A_LD > 2) { WAIT_V(22); BARRIER(); }
      WAIT_V(0); BARRIER();
      const unsigned* ufp = ufl + (KT(min(lane >> 2, 6) + 1) * 4 + (lane & 3)) * 32;
      unsigned miss = ~0u;
      const char* y1 = Ug + KT(1) * 2 * SL + hw * 11264;
      asm volatile("" : "+s"(y1), "+v"(ufp));
      BARRIER();
      TS(47);
      if (U_D) __builtin_amdgcn_s_sleep(U_D);
      auto ULDF = [&](unsigned em0) __attribute__((always_inline)) {
        unsigned f;
        const u64 em = miss & em0;
        asm volatile("s_mov_b64 exec, %2\n\tglobal_load_dword %0, %1, off sc1\n\ts_mov_b64 exec, -1\n\ts_waitcnt vmcnt(0)" : "=&v"(f) : "v"(ufp), "s"(em) : "memory");
        miss &= ~(unsigned)em | (unsigned)__builtin_amdgcn_ballot_w64(f == SENT);
      };
#define UPOLL(need, em0) UNTIL((miss & (need)) == 0, U_R, ULDF(em0))
#define G1(o) "global_load_lds_dwordx4 %0, %1 offset:" #o " sc1\n\t"
#define ULD(yp, l)                                                                                                                          \
  do {                                                                                                                                      \
    asm volatile("s_mov_b32 m0, %2\n\ts_nop 0\n\t" G1(0) G1(1024) G1(2048) G1(3072)::"v"(lane * 16), "s"(yp), "s"(l) : "memory");            \
    asm volatile("s_mov_b32 m0, %2\n\ts_nop 0\n\t" G1(0) G1(1024) G1(2048) G1(3072)::"v"(lane * 16 + 4096), "s"(yp), "s"((l) + 4096) : "memory"); \
    asm volatile("s_mov_b32 m0, %2\n\ts_nop 0\n\t" G1(0) G1(1024) G1(2048)::"v"(lane * 16 + 8192), "s"(yp), "s"((l) + 8192) : "memory");     \
  } while (0)
      {
        SPIN((miss & 3u) == 0, U_R, ULDF(15u));
        TS(48);
        ULD(y1, lds0 + 4 * SL + hw * 11264);
        UPOLL(15u, 15u);
        unsigned f;
        asm volatile("global_load_dword %0, %1, off sc1" : "=v"(f) : "v"(ufp) : "memory");
        ULD(y1 + SL, lds0 + hw * 11264);
        asm volatile("s_waitcnt vmcnt(11)" : "+v"(f)::"memory");
        BARRIER();
        miss = (unsigned)__builtin_amdgcn_ballot_w64(f == SENT);
      }
      slot = 1;
#pragma unroll 1
      for (int n = 4; n < 16; ++n) {
        UPOLL(3u << (2 * n - 4), ~0u);
        const char* yp = Ug + (KT(n >> 1) * 2 + (n & 1)) * SL + hw * 11264;
        unsigned l = lds0 + slot * SL + hw * 11264;
        slot = slot == NS - 1 ? 0 : slot + 1;
        if (n == CPF_B && hw == 1 && lane < CPF_N) PFL(cpf);
        asm volatile("" : "+s"(yp), "+s"(l));
        if (n >= 6) {
          WAIT_V(22);
          BARRIER();
          if (hw == 1) TSD(n + 44);
        }
        ULD(yp, l);
      }
      WAIT_V(22);
      BARRIER();
      WAIT_V(0);
      BARRIER();
      BARRIER();
      BARRIER();
      if (b < 56 && tid == 256) ST4(reinterpret_cast<unsigned*>(ws + WS_WAK) + b * 32, 1u);
      TSE();
      return;
    } else {
      const int aw = wid - 6, rp = min(mt * 2 + aw, 10);
      unsigned xfo = aw ? WS_XF2 + b * 8 : WS_XFL + b * 128;
      asm volatile("" : "+v"(xfo));
      __builtin_amdgcn_s_setprio(3);
      PBAR(0);
      asm volatile("s_mov_b64 exec, 1\n\tglobal_store_dwordx2 %0, %1, %2 sc1\n\ts_mov_b64 exec, -1" ::"v"(xfo), "v"(tag), "s"(ws) : "memory");
      __builtin_amdgcn_s_setprio(0);
      if (zd) {
#pragma unroll
        for (int s = 0; s < NS; ++s) *reinterpret_cast<u32x4*>(shm + s * SL + 10240 + aw * 1024 + lane * 16) = u32x4{0u, 0u, 0u, 0u};
      }
      LBAR();
      float rs[4] = {};
      const unsigned ra = lds0 + rp * 2048 + lane * 16;
      unsigned so = 0;
#define RSUM()                                                                                                                        \
  do {                                                                                                                                \
    u32x4 R_[2];                                                                                                                      \
    asm volatile("ds_read_b128 %0, %2\n\tds_read_b128 %1, %2 offset:1024\n\ts_waitcnt lgkmcnt(0)" : "=&v"(R_[0]), "=&v"(R_[1]) : "v"(ra + so) : "memory"); \
    _Pragma("unroll") for (int j = 0; j < 8; ++j) {                                                                                   \
      asm volatile("v_add_f32 %0, %0, %1" : "+v"(rs[(j >> 2) * 2]) : "v"(R_[j >> 2][j & 3] << 16));                                   \
      asm volatile("v_add_f32 %0, %0, %1" : "+v"(rs[(j >> 2) * 2 + 1]) : "v"(R_[j >> 2][j & 3] & 0xffff0000u));                       \
    }                                                                                                                                 \
    so = so == (NS - 1) * SL ? 0 : so + SL;                                                                                           \
  } while (0)
      {
#pragma unroll 1
        for (int bi = 0; bi < F_AT; ++bi) {
          RSUM();
          BARRIER();
        }
        u32x4 f;
#define FLD() asm volatile("global_load_dwordx4 %0, %1, %2 sc1" : "=v"(f) : "v"(lane * 16), "s"(ws + WS_XF2 + aw * 1024) : "memory")
        FLD();
#pragma unroll 1
        for (int bi = F_AT; bi < 4; ++bi) {
          RSUM();
          BARRIER();
        }
        POLL { PDBG(it);
          asm volatile("s_waitcnt vmcnt(0)" : "+v"(f)::"memory");
          if (__all(mk64(f[1], f[0]) == tag && mk64(f[3], f[2]) == tag)) break;
          __builtin_amdgcn_s_sleep(1);
          FLD();
        }
        asm volatile("s_waitcnt vmcnt(0)" : "+v"(f)::"memory");
      }
      const unsigned xo = (unsigned)(((qx * 512 + aw * 4 + g4) * D + mt * 128) * 2 + ((l16 ^ (aw * 4 + g4)) << 4));
#pragma unroll 1
      for (int bi = 4; bi < H_AT; ++bi) {
        RSUM();
        const int i = bi - X_AT;
        if (i >= 0 && i < 12) GLDS(lds0 + XR + i * 2048 + aw * 1024, xo, x + (PROW(5 + i / 2) + (i & 1) * 8) * D);
        BARRIER();
      }
      {
        u64 f;
        const unsigned* fp = hfl + lane * 2;
        asm volatile("global_load_dwordx2 %0, %1, off sc1" : "=v"(f) : "v"(fp) : "memory");
        RSUM();
        BARRIER();
        RSUM();
        BARRIER();
        POLL { PDBG(it);
          asm volatile("s_waitcnt vmcnt(0)" : "+v"(f)::"memory");
          if (__all(f == tag)) break;
          __builtin_amdgcn_s_sleep(1);
          asm volatile("global_load_dwordx2 %0, %1, off sc1" : "=v"(f) : "v"(fp) : "memory");
        }
        asm volatile("s_waitcnt vmcnt(0)" : "+v"(f)::"memory");
        unsigned c;
        const unsigned* cp = modv + 1024 + mt * 128 + aw * 64 + lane;
        asm volatile("global_load_dword %0, %1, off sc1" : "=v"(c) : "v"(cp) : "memory");
        RSUM();
        BARRIER();
        RSUM();
        BARRIER();
        POLL { PDBG(it);
          asm volatile("s_waitcnt vmcnt(0)" : "+v"(c)::"memory");
          if (__all(c != SENT)) break;
          __builtin_amdgcn_s_sleep(1);
          asm volatile("global_load_dword %0, %1, off sc1" : "=v"(c) : "v"(cp) : "memory");
        }
        asm volatile("s_waitcnt vmcnt(0)" : "+v"(c)::"memory");
        *reinterpret_cast<unsigned*>(shm + SCA + (aw * 64 + lane) * 4) = c ^ (cc ? 0x80000000u : 0u);
      }
#pragma unroll 1
      for (int bi = H_AT + 4; bi < 32; ++bi) {
        RSUM();
        if (bi < 30) BARRIER();
      }
      unsigned* rsb = reinterpret_cast<unsigned*>(ws + WS_RS) + qx * 512;
      {
        const float v0 = dpp_add<0x141>(dpp_add<0x4E>(dpp_add<0xB1>(rs[0] + rs[1])));
        const float v1 = dpp_add<0x141>(dpp_add<0x4E>(dpp_add<0xB1>(rs[2] + rs[3])));
        if ((lane & 7) < 2 && mt * 2 + aw < 11 && !(nt == 2 && rp == 5)) ST4(rsb + cc * S + PROW(rp) + (lane & 1) * 8 + (lane >> 3), nsent((lane & 1) ? v1 : v0));
      }
      BARRIER();
#pragma unroll 1
      for (int k = 34; k <= (aw ? B_VAT : 46); ++k) BARRIER();
      u32x2 m[6];
      const unsigned lr = min(lane, 47);
      const unsigned o0 = aw ? (unsigned)(WS_Q - WS_MOD) + mt * 512 + lane * 8 : 8192u + mt * 512 + lane * 8;
      const unsigned o1 = aw ? (unsigned)(WS_RS - WS_MOD) + (qx * 512 + PROW(5 + (lr >> 3)) + (lr & 7) * 2) * 4 : o0 + 8192;
      const unsigned o2 = aw ? o1 + 8192 : o0 + 16384, o3 = aw ? 8192u + mt * 512 + lane * 8 : 28672u + min(lane, 23) * 8;
#define VLD()                                                                                                                     \
  asm volatile("global_load_dwordx2 %0, %6, %10 sc1\n\tglobal_load_dwordx2 %1, %6, %11 sc1\n\tglobal_load_dwordx2 %2, %7, %10 sc1\n\t" \
               "global_load_dwordx2 %3, %7, %11 sc1\n\tglobal_load_dwordx2 %4, %8, %10 sc1\n\tglobal_load_dwordx2 %5, %9, %10 sc1"    \
               : "=&v"(m[0]), "=&v"(m[1]), "=&v"(m[2]), "=&v"(m[3]), "=&v"(m[4]), "=&v"(m[5])                                     \
               : "v"(o0), "v"(o1), "v"(o2), "v"(o3), "s"(modv), "s"(modv + 1024)                                                  \
               : "memory")
      if (!aw) __builtin_amdgcn_s_sleep(V_D);
      VLD();
      BARRIER();
      if (aw) {
#pragma unroll
        for (int k = B_VAT + 2; k <= 45; ++k) BARRIER();
      }
      TS(30);
      POLL { PDBG(it);
        asm volatile("s_waitcnt vmcnt(0)" : W6(m)::"memory");
        if (__all(max(max(max(max2(m[0]), max2(m[1])), max(max2(m[2]), aw ? 0u : max2(m[3]))), max(max2(m[4]), max2(m[5]))) != SENT)) break;
        __builtin_amdgcn_s_sleep(2);
        VLD();
      }
      asm volatile("s_waitcnt vmcnt(0)" : W6(m)::"memory");
      if (aw == 0) {
        *reinterpret_cast<unsigned*>(shm + TV + lane * 4) = hi2(m[2]);
        *reinterpret_cast<unsigned*>(shm + TV + 256 + lane * 4) = hi2(m[1]);
        *reinterpret_cast<unsigned*>(shm + TV + 512 + lane * 4) = hi2(m[3]);
        const float p0 = __uint_as_float(m[4][0]), p1 = __uint_as_float(m[4][1]);
        const unsigned hh = pack2(p0, p1);
        *reinterpret_cast<unsigned*>(shm + SCW + lane * 4) = hh;
        *reinterpret_cast<unsigned*>(shm + SCW + 256 + lane * 4) = pack2(p0 - bflo(hh), p1 - bfhi(hh));
        *reinterpret_cast<u32x2*>(shm + SCW + 512 + lane * 8) = u32x2{0x3f803f80u, 0x3f803f80u};
        const float u = __uint_as_float(m[5][0]) + __uint_as_float(m[5][1]), u2 = dpp_add<0xB1>(u), u8 = dpp_add<0x141>(dpp_add<0x4E>(u2));
        if (lane < 17 && !(lane & 1)) *reinterpret_cast<float*>(shm + TSUM + lane * 2) = lane < 16 ? u2 : u8;
      } else {
        BARRIER();
#pragma unroll
        for (int j = 0; j < 2; ++j) {
          const float r0 = __uint_as_float(m[0][j]), r1 = __uint_as_float(m[1][j]);
          const unsigned a0 = pack2(r0, r0), a1 = pack2(r1, r1);
          *reinterpret_cast<u32x4*>(shm + RKA + ((lane >> 4) * 32 + ((lane >> 1) & 1) * 16 + ((lane >> 2) & 3) * 4 + (lane & 1) * 2 + j) * 16) =
              u32x4{a0, pack2(r0 - bflo(a0), r0 - bflo(a0)), a1, pack2(r1 - bflo(a1), r1 - bflo(a1))};
          const float s0 = __uint_as_float(m[2][j]), s1 = -__uint_as_float(m[4][j]);
          const unsigned b0 = pack2(s0, s0 - rbf(s0)), b1 = pack2(s1, s1 - rbf(s1));
          *reinterpret_cast<u32x4*>(shm + RKB + (lr * 2 + j) * 16) = u32x4{b0, b0, b1, b1};
        }
        *reinterpret_cast<u32x2*>(shm + RKZ + lane * 8) = u32x2{0u, 0u};
        const unsigned one = g4 == (l16 >> 2) ? ((lane & 1) ? 0x3f800000u : 0x3f80u) : 0u, e0 = (lane & 2) ? 0u : one, e1 = (lane & 2) ? one : 0u;
        *reinterpret_cast<u32x4*>(shm + IDT + lane * 16) = u32x4{e0, e1, 0u, 0u};
        *reinterpret_cast<u32x4*>(shm + IDT + 1024 + lane * 16) = u32x4{0u, 0u, e0, e1};
        *reinterpret_cast<u32x2*>(shm + TGA + lane * 8) = m[5];
        LBAR();
      }
      TS(17);
    }
    const int row0 = rg * 16 + l16, col1 = mt * 128 + g4 * 8;
    u32x4 xq[4], sa4[4];
    if (wid < 4) {
      auto X1A = [&](auto kc) __attribute__((always_inline)) {
        constexpr int k = decltype(kc)::value;
        (void)acc, (void)idt, (void)pv;
        MFMA(acc[0][5 + k], idt[0], pv[k]);
        MFMA(acc[1][5 + k], idt[1], pv[k]);
      };
      auto X1B = [&](auto kc) __attribute__((always_inline)) {
        constexpr int j = decltype(kc)::value;
        (void)acc, (void)ga, (void)x5, (void)xw1;
        asm volatile("" : "+v"(acc[0][5 + j]), "+v"(acc[1][5 + j]));
        const u32x4 v = pk8((j < 5 ? acc[0][j % 5] : x5[0]) + ga[0] * acc[0][5 + j], (j < 5 ? acc[1][j % 5] : x5[1]) + ga[1] * acc[1][5 + j]);
        asm volatile("ds_write_b128 %0, %1 offset:%2" ::"v"(xw1), "v"(v), "n"(j * 4096) : "memory");
      };
      asm volatile("s_waitcnt vmcnt(2)" : "+v"(pv[2]), "+v"(pv[3]), "+v"(pv[4]), "+v"(pv[5])::"memory");
      UNTIL(__all(max(max(max4(pv[2]), max4(pv[3])), max(max4(pv[4]), max4(pv[5]))) != SENT), X_R,
            { WAIT_V(0); XCHLD(); asm volatile("s_waitcnt vmcnt(0)" : W6(pv)::"memory"); });
      X1A(IC(5)); X1A(IC(4)); X1A(IC(3)); X1B(IC(5)); X1A(IC(2)); X1B(IC(4)); X1B(IC(3));
      asm volatile("s_waitcnt vmcnt(0)" : "+v"(pv[0]), "+v"(pv[1])::"memory");
#define XCHL2()                                                                                                                             \
  asm volatile("global_load_dwordx4 %0, %2, %3 offset:2048 sc1\n\tglobal_load_dwordx4 %1, %2, %3 offset:1024 sc1\n\ts_waitcnt vmcnt(0)" \
               : "=&v"(pv[0]), "=&v"(pv[1]) : "v"(lane * 16), "s"(xr) : "memory")
      UNTIL(__all(max(max4(pv[0]), max4(pv[1])) != SENT), X_R, XCHL2());
      TS(27);
      X1A(IC(1)); X1A(IC(0)); X1B(IC(2)); X1B(IC(1)); X1B(IC(0));
    }
    {
      TS(6);
      LBAR();
#pragma unroll
      for (int q = 0; q < 4; ++q) {
        DSR(xq[q], xa, q * 1024);
        DSR(sa4[q], saa, q * 64);
      }
      f32x2 cj;
      float sw, ce;
      asm volatile("ds_read_b32 %2, %5\n\tds_read_b64 %0, %3\n\tds_read_b32 %1, %4\n\ts_waitcnt lgkmcnt(0)"
                   : "=&v"(cj), "=&v"(sw), "=&v"(ce), "+v"(cja), "+v"(swa), "+v"(cea), "+v"(xq[0]), "+v"(xq[1]), "+v"(xq[2]), "+v"(xq[3]), "+v"(sa4[0]),
                     "+v"(sa4[1]), "+v"(sa4[2]), "+v"(sa4[3])::"memory");
      if (!act) {
        TSE();
        return;
      }
      f32x4 s4 = {0.f, 0.f, 0.f, 0.f}, gr = {0.f, 0.f, 0.f, 0.f};
#pragma unroll
      for (int q = 0; q < 4; ++q) {
        gr = MM(xq[q], xq[q], gr);
        s4 = MM(sa4[q], xq[q], s4);
      }
      const u32x4 gd = __builtin_bit_cast(u32x4, gr) & dk;
      const float m = s4[2] * (1.0f / 128);
      float q2 = ar4(__uint_as_float((gd[0] | gd[1]) | (gd[2] | gd[3]))) - s4[2] * m, lg = (s4[0] + s4[1]) - m * ce;
      if (__builtin_expect(__any(!(s4[2] * m <= 256.0f * q2)), 0)) {
        f32x4 a = {0.f, 0.f, 0.f, 0.f}, l = {0.f, 0.f, 0.f, 0.f};
#pragma unroll
        for (int q = 0; q < 4; ++q) {
          const u32x4 ch = *reinterpret_cast<const u32x4*>(shm + SCW + g4 * 16 + q * 64), cl = *reinterpret_cast<const u32x4*>(shm + SCW + 256 + g4 * 16 + q * 64);
          const f32x4 d0 = lo4(xq[q]) - m, d1 = hi4(xq[q]) - m;
          a += d0 * d0 + d1 * d1;
          l += d0 * (lo4(ch) + lo4(cl)) + d1 * (hi4(ch) + hi4(cl));
        }
        q2 = ar4((a[0] + a[1]) + (a[2] + a[3]));
        lg = ar4((l[0] + l[1]) + (l[2] + l[3]));
      }
      const u32x4 own = {nsent(m), nsent(q2), nsent(lg), 0u};
      asm volatile("s_mov_b64 exec, 0xffff\n\tglobal_store_dwordx4 %0, %1, %2 sc1\n\ts_mov_b64 exec, -1\n\ts_nop 1" ::"v"(sto), "v"(own), "s"(stp) : "memory");
      TS(7);
      SKEWPT();
#define STOK() __all(max(max(max(sr[0][0], sr[0][1]), sr[0][2]), max(max(sr[1][0], sr[1][1]), sr[1][2])) != SENT)
      const bool pub = mt == (rg & 7);
      u64 pm = (unsigned)__builtin_amdgcn_readfirstlane(pub);
      unsigned klo = 0xffffu - (unsigned)row0, kof = WS_KEY + rg * 128;
      asm volatile("" : "+s"(pm), "+v"(klo), "+v"(kof));
      if (OPT_SD) __builtin_amdgcn_s_sleep(OPT_SD);
      STLD();
      UNTIL(STOK(), 1, { STLD(); asm volatile(".Lcpf_e2:"); });
      TS(9);
      const f32x2 mk = {__uint_as_float(sr[0][0]), __uint_as_float(sr[1][0])};
      const f32x2 qk = {__uint_as_float(sr[0][1]), __uint_as_float(sr[1][1])};
      const f32x2 lk = {__uint_as_float(sr[0][2]), __uint_as_float(sr[1][2])};
      const float mean = ar4(mk[0] + mk[1]) * (1.0f / 8);
      const f32x2 dm = mk - mean;
      const f32x2 a = qk + 128.0f * (dm * dm), l = lk + dm * cj;
      const float al = swap32_add(a[0] + a[1], l[0] + l[1]), al2 = swap16_add(al, al);
      const auto alx = __builtin_amdgcn_permlane32_swap(__float_as_uint(al2), __float_as_uint(al2), false, false);
      const float rstd = __builtin_amdgcn_rsqf(__uint_as_float(alx[0]) * (1.0f / D) + 1e-5f);
      const float rd = rstd * __uint_as_float(alx[1]) + sw;
      const unsigned rb = f2bf(rd);
      const unsigned ord = rb ^ ((rb & 0x8000u) ? 0xffffu : 0x8000u);
      const unsigned key = (ord << 16) | klo;
      unsigned t1 = key, t2 = 0u;
      TOP2(0xB1); TOP2(0x4E); TOP2(0x141); TOP2(0x140);
      asm volatile("s_mov_b64 exec, %3\n\tglobal_store_dwordx2 %0, %1, %2 sc1\n\ts_mov_b64 exec, -1" ::"v"(kof), "v"(u32x2{~t1, ~t2}), "s"(ws), "s"(pm) : "memory");
      TS(1);
      SKEWPT();
      if (key >= t2) {
        const int sl = rg * 2 + (key == t2);
#pragma unroll
        for (int q = 0; q < 4; ++q) ST16(sl * 2048 + col1 * 2, nsent4(xq[q]), ws + WS_CAND, q * 64);
        if (g4 == 0 && pub) ST16(sl * 16, (u32x4{nsent(mean), nsent(rstd), 0u, 0u}), ws + WS_HDR, 0);
      }
      TS(8);
      if (OUT_D) __builtin_amdgcn_s_sleep(OUT_D);
#pragma unroll
      for (int q = 0; q < 4; ++q) {
        const u32x4 cq = *reinterpret_cast<const u32x4*>(shm + TV + q * 64 + g4 * 16), sbq = *reinterpret_cast<const u32x4*>(shm + TV + 256 + q * 64 + g4 * 16);
        const u32x4 gbq = *reinterpret_cast<const u32x4*>(shm + TV + 512 + q * 64 + g4 * 16);
        const f32x4 x0 = lo4(xq[q]), x1 = hi4(xq[q]);
        const f32x4 h0 = ((x0 - mean) * rstd) * lo4(cq) + lo4(sbq), h1 = ((x1 - mean) * rstd) * hi4(cq) + hi4(sbq);
        ST16((row0 * D + col1) * 2, pk8(x0 + lo4(gbq) * h0, x1 + hi4(gbq) * h1), out, q * 64);
      }
      WAIT_V(0);
      if (lane == 0) ST4(reinterpret_cast<unsigned*>(ws + WS_DON) + (rg * 8 + mt) * 2, 1u);
      TS(16);
    }
    TSE();
    return;
  }
  {
    const int hid = b - 192, hi = j8 - 24;
    constexpr int RNG = 65536, SSI = MISC, GP = MISC + 2048, SSA = MISC + 3072, HS = MISC + 5120, SX1 = MISC + 9216, ACT = MISC + 13312,
                  HFLG = MISC + 13568, RR = MISC + 13824;
    unsigned* flg = reinterpret_cast<unsigned*>(shm + HFLG);
    const int ec = xcd * 7 + hi, hd = hi == 7 ? 7 : (hi + 7 - ((3 + xcd) & 7) % 7) % 7;
    const bool pon = wid < ((PF_SH >> (4 * hd)) & 7);
    const char* pfh = PFG((int)((PF_BS >> (8 * hd)) & 31) + wid, 8 - qx);
#define HPF(g) do { if (pon) PFL(pfh + ((((g) + qx) & 7) << 9)); } while (0)
    WHLD(4);
    const int cj = hd == 1 ? 34 + xcd : hd * 8 - 16 + xcd, co = hd == 1 && xcd < 6 ? xcd : cj / 7, ce = hd == 1 && xcd < 6 ? 3 : cj % 7 + (cj % 7 >= 3);
    const int cb0 = hd % 7 == 0 ? (hd ? 24 + xcd : 16 + ((xcd + 7) & 7)) : (co < 2 ? co : co + 2) * 8 + ce;
    const unsigned om = (unsigned)((wid * 16 + (lane >> 3)) * 12288 + (lane & 7) * 16);
    const unsigned og = (unsigned)((wid * 8 + (lane >> 3)) * 7168 + (lane & 7) * 16);
    const unsigned od = (unsigned)((wid * 8 + (lane >> 3)) * 2048 + (lane & 7) * 16);
    const char* pm = reinterpret_cast<const char*>(wmod) + cb0 * 128;
    const char* pg = reinterpret_cast<const char*>(wg) + ec * 128;
    const char* pu = reinterpret_cast<const char*>(wu) + ec * 128;
    const char* pd = reinterpret_cast<const char*>(wd) + (size_t)ec * 131072;
    const unsigned ow = (unsigned)(((wid & 1) * 16 + (lane >> 3)) * 12288 + (wid >> 1) * 128 + (lane & 7) * 16);
    const char* pw = reinterpret_cast<const char*>(wmod) + 8192 + xcd * 512;
#define RQM(r)                                                                                   \
  do {                                                                                           \
    const char* p_ = pm + (size_t)(r)*1572864;                                                   \
    GLDSP(lds0 + RNG + ((r)&3) * 16384 + wid * 2048, om, p_, WPOL);                              \
    GLDSP(lds0 + RNG + ((r)&3) * 16384 + wid * 2048 + 1024, om, p_ + 98304, WPOL);               \
  } while (0)
#define RQE(r)                                                                                                                \
  do {                                                                                                                        \
    GLDSP(lds0 + RNG + ((r)&3) * 16384 + wid * 1024, (r) < 16 ? og : od, (r) < 16 ? pg + (r)*458752 : pd + ((r)-16) * 256, WPOL);   \
    GLDSP(lds0 + RNG + ((r)&3) * 16384 + 8192 + wid * 1024, (r) < 16 ? og : od, (r) < 16 ? pu + (r)*458752 : pd + ((r)-16) * 256 + 128, WPOL); \
  } while (0)
    PBAR(4);
    XFLAG();
    const u32x4 sv = {SENT, SENT, SENT, SENT};
    auto PUBS = [&](int cb, unsigned v, unsigned p, unsigned r) __attribute__((always_inline)) {
      const int vi = cb >> 4, col = (cb & 15) * 64 + lane;
      ST4(modv + cb * 64 + lane, v);
      if (vi == 4) ST4(modv + 6144 + col, p);
      if (vi == 4 ? (lane & 31) == 0 : vi == 3 && lane == 0) ST4(modv + 7168 + (vi == 4 ? col >> 5 : 32 + (cb & 15)), r);
    };
    if (wid == 4 || (hi == 7 && wid < 4)) PUBS(wid == 4 ? cb0 : 64 + xcd * 4 + wid, SENT, SENT, SENT);
    if (tid >= 480) ST4(reinterpret_cast<unsigned*>(ws + WS_Q) + hsl * 32 + tid - 480, SENT);
    unsigned cv, wv;
    asm volatile("global_load_dword %0, %2, %3\n\tglobal_load_dword %1, %2, %4" : "=&v"(cv), "=&v"(wv) : "v"(tid * 4), "s"(cvec), "s"(wr_) : "memory");
    if (hi < 7) {
      if (PF_N < 3) HPF(2);
      HPF(3);
      const char* ap = ws + WS_ARM + (size_t)(ec * 8 + wid) * 3328;
#pragma unroll
      for (int j = 0; j < 4; ++j) ST16(lane * 16, sv, ap, j < 3 ? j * 1024 : 2304);
      HPF(4);
      WAIT_V(4);
    } else {
      WAIT_V(0);
    }
    asm volatile("; AUDIT_OK %0 %1" : "+v"(cv), "+v"(wv)::"memory");
    *reinterpret_cast<unsigned*>(shm + SSI + tid * 4) = pack2(silu_bf(bflo(cv)), silu_bf(bfhi(cv)));
    *reinterpret_cast<unsigned*>(shm + SX1 + tid * 4) = wv;
    LBAR();
    TS(50);
    const unsigned ha = (unsigned)(((l16 >> 2) * 8 + (lane & 3)) * 2048 + wid * 256);
#define HFR(j) *reinterpret_cast<const u32x4*>(shm + ha + ((j)&1) * 8192 + ((j) >> 2) * 128 + (((((j) >> 1) & 1) * 4 + g4) ^ (lane & 7)) * 16)
    HPF(5);
#pragma unroll
    for (int j = 0; j < 8; ++j) ST16(lane * 16, HFR(j), ws + WS_WH + (size_t)hsl * 65536 + wid * 8192 + 4096, j * 1024 - 4096);
    HPF(6);
    RQM(0);
    RQM(1);
    RQM(2);
    const unsigned tm = lds0 + RNG + ((wid >> 2) * 64 + g4 * 8 + ((lane >> 2) & 3)) * 128 + (wid & 3) * 32 + (lane & 3) * 8;
    f32x4 ma = {0.f, 0.f, 0.f, 0.f};
#define GVR(i)                                                                                                                               \
  do {                                                                                                                                       \
    _Pragma("unroll") for (int f = 0; f < 2; ++f)                                                                                            \
        ma = MM(trd(tm + ((i)&3) * 16384 + f * 4096), *reinterpret_cast<const u32x4*>(shm + SSI + ((i)*128 + (wid >> 2) * 64 + f * 32 + g4 * 8) * 2), ma); \
    if ((i) == 7 && l16 == 0) *reinterpret_cast<f32x4*>(shm + GP + wid * 64 + g4 * 16) = ma;                                                 \
  } while (0)
    const float* gp = reinterpret_cast<const float*>(shm + GP);
    auto PUB = [&](int cb, float s) __attribute__((always_inline)) {
      const int vi = cb >> 4;
      const float v = rbf(((vi == 1 || vi == 4) ? 1.0f : 0.0f) + rbf(s));
      const float p = v * bf2f(*reinterpret_cast<const u16*>(shm + SX1 + ((cb & 15) * 64 + lane) * 2));
      const float r16 = row_sum(p), r32 = swap16_add(r16, r16);
      PUBS(cb, nsent(v), nsent(p), nsent(vi == 4 ? r32 : swap32_add(r32, r32)));
    };
    {
      sfor<8>([&](auto ii) __attribute__((always_inline)) {
        constexpr int i = decltype(ii)::value;
        (void)ow, (void)pw, (void)om, (void)pm, (void)og, (void)od, (void)pg, (void)pu, (void)pd, (void)lds0, (void)wid, (void)pfl, (void)Yx, (void)l16, (void)g4, (void)xcd, (void)hfl, (void)lane, (void)hid;
        TSCAP();
        WAIT_V(4);
        LBAR();
        if (wid == 2) TS(i);
        if constexpr (i == 0) { if (tid == 0) ST4(reinterpret_cast<u64*>(hfl + hid * 2), tag); }
        if constexpr (i == 0) HPF(7);
        if constexpr (i < 5) {
          RQM(i + 3);
        } else {
          const bool e = hid < 56;
          const char* p_ = pw + (i - 5) * 393216;
          GLDSP(lds0 + RNG + (i - 5) * 16384 + wid * (e ? 1024 : 2048), e ? og : ow, e ? pg + (i - 5) * 458752 : p_, WPOL);
          GLDSP(lds0 + RNG + (i - 5) * 16384 + wid * (e ? 1024 : 2048) + (e ? 8192 : 1024), e ? og : ow, e ? pu + (i - 5) * 458752 : p_ + 98304, WPOL);
        }
        GVR(i);
      });
    }
    TS(51);
#define PUB0() do { if (wid == 0) PUB(cb0, gp[lane] + gp[64 + lane]); } while (0)
    u64 sf, hf;
#define SFLD()                                                                                                                     \
  asm volatile("s_mov_b64 exec, 3\n\tglobal_load_dwordx2 %0, %2, %3 sc1\n\ts_mov_b64 exec, 0xff\n\tglobal_load_dwordx2 %1, %5, %4 sc1\n\ts_mov_b64 exec, -1" \
               : "=&v"(sf), "=&v"(hf) : "v"(lane * 128), "s"(ws + WS_SFL + wid * 256), "s"(ws + WS_HFL + wid * 64), "v"(lane * 8) : "memory")
    auto SAW = [&]() __attribute__((always_inline)) {
      POLL { PDBG(it);
        if (((__builtin_amdgcn_ballot_w64(sf != tag) & 3u) | (__builtin_amdgcn_ballot_w64(hf != tag) & 0xffu)) == 0) break;
        __builtin_amdgcn_s_sleep(4);
        SFLD();
        asm volatile("s_waitcnt vmcnt(0)" : "+v"(sf), "+v"(hf)::"memory");
      }
    };
    u32x2 s2;
#define SALD() asm volatile("global_load_dwordx2 %0, %1, %2 sc1" : "=v"(s2) : "v"(tid * 8), "s"(modv) : "memory")
    auto QMM = [&]() __attribute__((always_inline)) {
      *reinterpret_cast<unsigned*>(shm + SSA + tid * 4) = hi2(s2);
      f32x4 qa[2] = {};
#pragma unroll
      for (int j = 0; j < 8; ++j) qa[j & 1] = MM(HFR(j), *reinterpret_cast<const u32x4*>(shm + SSA + wid * 256 + (j >> 1) * 64 + g4 * 16), qa[j & 1]);
      if (l16 < 2) *reinterpret_cast<f32x4*>(shm + HS + wid * 128 + g4 * 32 + l16 * 16) = l16 ? qa[1] : qa[0];
    };
    auto QPUB = [&]() __attribute__((always_inline)) {
      if (tid >= 480) {
        float s = 0.f;
#pragma unroll
        for (int w = 0; w < 8; ++w) s += *reinterpret_cast<const float*>(shm + HS + w * 128 + (tid - 480) * 4);
        ST4(reinterpret_cast<unsigned*>(ws + WS_Q) + hsl * 32 + tid - 480, nsent(s));
      }
    };
    u32x8 GU[16], DN[8];
    if (hid < 56) {
      const unsigned tg = lds0 + RNG + (g4 * 8 + ((lane >> 2) & 3)) * 128 + ((lane >> 1) & 1) * 8192 + wid * 16 + (lane & 1) * 8;
      const unsigned td = lds0 + RNG + (g4 * 8 + ((lane >> 2) & 3)) * 128 + (wid >> 2) * 8192 + (wid & 3) * 32 + (lane & 3) * 8;
      sfor<24>([&](auto rr) __attribute__((always_inline)) {
        constexpr int r = decltype(rr)::value;
        (void)td, (void)tg, (void)GU, (void)DN, (void)og, (void)od, (void)pg, (void)pu, (void)pd, (void)lds0, (void)wid, (void)pfl, (void)Yx, (void)l16, (void)g4, (void)xcd;
        (void)s2, (void)modv, (void)tid;
        (void)hid, (void)lane, (void)sf, (void)hf;
        (void)sv, (void)hi, (void)ws;
#define EX(r) (((r) < 0 ? 0 : (r) < 2 ? 4 : (r) == 2 ? 2 : 0) + ((r) == Q_AT - 6 ? 2 : 0) + ((r) == Q_AT - 3))
        WAIT_V(r > 21 ? 2 * (23 - r) : 4 + EX(r - 2) + EX(r - 1));
        LBAR();
        TSCAP();
        if (wid == 3) TS(r);
        if constexpr (r == 0) PUB0();
        if constexpr (r == 1) {
          if (cb0 < 16 && wid == 0) {
            WAIT_V(2 + EX(0));
            if (lane == 0) ST4(reinterpret_cast<u64*>(ws + WS_SFL) + cb0 * 16, tag);
          }
        }
        if constexpr (r == Q_AT - 6) SFLD();
        if constexpr (r == Q_AT - 3) {
          asm volatile("" : "+v"(sf), "+v"(hf));
          SAW();
          SALD();
        }
        if constexpr (r < 3) {
          const char* ap = ws + WS_XCH + xcd * 589824 + (hi * 8 + wid) * 10240 + r * 4096;
#pragma unroll
          for (int j = 0; j < (r < 2 ? 4 : 2); ++j) ST16(lane * 16, sv, ap, j * 1024);
          if (r == 2 && wid < 3) ST16(lane * 16, sv, ws + WS_XCH + xcd * 589824 + min(560 + hi * 3 + wid, 575) * 1024, 0);
        }
        if constexpr (r < 21) RQE(r + 3);
        if constexpr (r < 16) {
          GU[r] = trd2(tg + (r & 3) * 16384);
          asm volatile("" : "+v"(GU[r]));
        } else {
          DN[r - 16] = trd2(td + (r & 3) * 16384);
          asm volatile("" : "+v"(DN[r - 16]));
        }
        if constexpr (r == Q_AT) {
          asm volatile("" : "+v"(s2));
          QMM();
        }
        if constexpr (r == Q_AT + 1) QPUB();
      });
      TS(52);
    } else {
      const unsigned tw = lds0 + RNG + (wid >> 1) * 4096 + (g4 * 8 + ((lane >> 2) & 3)) * 128 + (wid & 1) * 64 + (lane & 3) * 8;
      f32x4 mb[2] = {};
      auto WR = [&](int r0, int r1) __attribute__((always_inline)) {
#pragma unroll 1
        for (int r = r0; r < r1; ++r) {
          WAIT_V(4);
          LBAR();
          if (wid == 2 && r < 22) TSD(r + 8);
          const char* p_ = pw + min(r + 3, 31) * 393216;
          GLDSP(lds0 + RNG + ((r + 3) & 3) * 16384 + wid * 2048, ow, p_, WPOL);
          GLDSP(lds0 + RNG + ((r + 3) & 3) * 16384 + wid * 2048 + 1024, ow, p_ + 98304, WPOL);
          const u32x4 sf = *reinterpret_cast<const u32x4*>(shm + SSI + (r * 32 + g4 * 8) * 2);
          mb[0] = MM(trd(tw + (r & 3) * 16384), sf, mb[0]);
          mb[1] = MM(trd(tw + (r & 3) * 16384 + 32), sf, mb[1]);
        }
      };
      WR(0, 1);
      PUB0();
      WR(1, MQ_AT - 6);
      SFLD();
      WR(MQ_AT - 6, MQ_AT - 3);
      asm volatile("; AUDIT_OK %0 %1" : "+v"(sf), "+v"(hf));
      SAW();
      SALD();
      WR(MQ_AT - 3, MQ_AT);
      asm volatile("; AUDIT_OK %0" : "+v"(s2));
      QMM();
      WR(MQ_AT, MQ_AT + 1);
      QPUB();
      WR(MQ_AT + 1, 32);
      if (l16 < 2) *reinterpret_cast<f32x4*>(shm + GP + wid * 128 + l16 * 64 + g4 * 16) = l16 ? mb[1] : mb[0];
      LBAR();
      if (wid < 4) PUB(64 + xcd * 4 + wid, gp[tid]);
      TS(52);
      WAIT_V(0);
      TSE();
      return;
    }
    bool early = false;
    if (tid == 0) {
      POLL { PDBG(it);
        unsigned g;
        asm volatile("global_load_dword %0, %1, %2 sc1\n\ts_waitcnt vmcnt(0)" : "=&v"(g) : "v"(0u), "s"(ws + WS_WAK + hid * 128) : "memory");
        if (g != SENT) break;
        early = true;
        __builtin_amdgcn_s_sleep(OPT_KS);
      }
    }
    BARRIER();
    TS(21);
    const int tok = hid & 1, col = (hid & 16) * 32 + (lane & 3) * 128 + (hid & 14) * 8 + (lane >> 2);
    u32x4 c4, s4;
    unsigned gbu;
    POLL { PDBG(it);
      asm volatile("global_load_dwordx4 %0, %3, %5 sc1\n\tglobal_load_dwordx4 %1, %3, %6 sc1\n\tglobal_load_dword %2, %4, %7 sc1\n\ts_waitcnt vmcnt(0)"
                   : "=&v"(c4), "=&v"(s4), "=&v"(gbu) : "v"((tid & 255) * 16), "v"(col * 4), "s"(modv + 4096), "s"(modv + 3072), "s"(modv + 5120) : "memory");
      if (__all(max(max(max4(c4), max4(s4)), gbu) != SENT)) break;
      __builtin_amdgcn_s_sleep(8);
    }
    const f32x4 cb2 = __builtin_bit_cast(f32x4, c4), sb2 = __builtin_bit_cast(f32x4, s4);
    unsigned hra = lds0 + HS + g4 * 64 + (lane & 4) * 8 + (lane & 1) * 16, hwa = lds0 + HS + ((tid & 255) >> 2) * 64 + (wid >> 2) * 32 + (tid & 3) * 4;
    unsigned pto = (g4 & 1) * 4096 + ((g4 >> 1) * 8 + wid) * 256 + l16 * 16, six = (lane & 1) ? 0xEEEEu : 0x4444u;
    unsigned awa = lds0 + ACT + (wid >> 1) * 64 + g4 * 32 + (lane & 2) * 8 + (wid & 1) * 8 + (lane & 4) + (lane & 1) * 2;
    f32x4 d = {0.f, 0.f, 0.f, 0.f};
    unsigned sla = lds0 + HFLG + 8 + (wid >> 2) * 4;
    asm volatile("" : "+v"(sla), "+v"(hra), "+v"(hwa), "+v"(pto), "+v"(awa), "+v"(six), "+v"(d));
    const float gb = __uint_as_float(gbu);
    if (wid == 0) {
      u32x2 g0 = {SENT, SENT}, g1 = {SENT, SENT};
      const char* gp = ws + WS_KEY + lane * 128;
      if (__any(early)) __builtin_amdgcn_s_sleep(OPT_KD);
      TS(20);
      u64 m0 = ~0ull, m1 = ~0ull;
      int left = PCAP;
      do {
        asm volatile("s_sleep 1\n\ts_mov_b64 exec, %4\n\tglobal_load_dwordx2 %0, %2, off sc1\n\ts_mov_b64 exec, %5\n\tglobal_load_dwordx2 %1, %3, off sc1\n\t"
                     "s_mov_b64 exec, -1\n\ts_waitcnt vmcnt(0)"
                     : "+v"(g0), "+v"(g1) : "v"(gp), "v"(gp + 8192), "s"(m0), "s"(m1) : "memory");
        m0 = __builtin_amdgcn_ballot_w64(max2(g0) == SENT);
        m1 = __builtin_amdgcn_ballot_w64(max2(g1) == SENT);
        left = (m0 | m1) ? left - 1 : 0;
        PDBG(PCAP - left);
      } while (left);
      g0 = ~g0;
      g1 = ~g1;
      const unsigned a1 = max(g0[0], g1[0]), a2 = max(min(g0[0], g1[0]), max(g0[1], g1[1]));
      const unsigned k1 = wave_max_u(a1);
      const unsigned k2 = wave_max_u(a1 == k1 ? a2 : a1);
      const int rg1 = key_row(k1) >> 4, rg2 = key_row(k2) >> 4;
      asm volatile("ds_write_b128 %0, %1" ::"v"(lds0 + HFLG), "v"(u32x4{k1, k2, rg1 * 32u, rg2 * 32u + (rg2 == rg1) * 16u}) : "memory");
      TS(22);
    }
    LBAR();
    {
      unsigned sl;
      asm volatile("ds_read_b32 %0, %1\n\ts_waitcnt lgkmcnt(0)" : "=&v"(sl) : "v"(sla) : "memory");
      u32x2 v, hd;
#define CLD()                                                                                                          \
  asm volatile("global_load_dwordx2 %0, %2, %4 sc1\n\tglobal_load_dwordx2 %1, %3, %5 sc1\n\ts_waitcnt vmcnt(0)"        \
               : "=&v"(v), "=&v"(hd) : "v"(sl * 128 + (tid & 255) * 8), "v"(sl), "s"(ws + WS_CAND), "s"(ws + WS_HDR) : "memory")
      CLD();
      UNTIL(__all(max(max2(v), max2(hd)) != SENT), 1, { CLD(); asm volatile(".Lcpf_e1:"); });
      TS(23);
      const float mean = __uint_as_float(hd[0]), rstd = __uint_as_float(hd[1]);
      const f32x4 h = ((f32x4{bflo(v[0]), bfhi(v[0]), bflo(v[1]), bfhi(v[1])} - mean) * rstd) * cb2 + sb2;
      asm volatile("ds_write2_b32 %0, %1, %2 offset1:4" ::"v"(hwa), "v"(pack2(h[0], h[1])), "v"(pack2(h[2], h[3])) : "memory");
      *reinterpret_cast<u32x2*>(shm + SX1 + tid * 8) = v;
    }
    SKEWPT();
    TS(26);
    LBAR();
    TS(27);
    f32x4 dn[8];
    {
      u32x4 hb[8];
      sfor<23>([&](auto ff) __attribute__((always_inline)) {
        constexpr int f = decltype(ff)::value, c = f - 7;
        (void)d, (void)hb, (void)GU, (void)hra, (void)six;
        if constexpr (c >= 0) {
          WAIT_L(f <= 16 ? 6 : 22 - f);
          SMF(d, hb[c & 7], GU[c], six);
        }
        if constexpr (f < 16) DSR(hb[f & 7], hra, f * 256);
      });
      asm volatile("s_nop 7" : "+v"(d));
      TS(29);
      const float dv = rbf(d[0] + d[1]), uv = dpp_f<0x128>(dv);
      const unsigned av = f2bf(rbf(dv * __builtin_amdgcn_rcpf(1.0f + __expf(-dv))) * uv);
      asm volatile("s_mov_b64 exec, 0xff00ff\n\tds_write_b16 %0, %1\n\ts_mov_b64 exec, -1" ::"v"(awa), "v"(av) : "memory");
    }
#pragma unroll
    for (int c = 0; c < 8; ++c) asm volatile("v_mov_b64 %0, 0" : "=v"(dn[c].lo));
    asm volatile("" : W8(dn));
    TS(24);
    LBAR();
    {
      u32x4 a0;
      asm volatile("ds_read_b128 %0, %1 offset:%2\n\ts_waitcnt lgkmcnt(0)" : "=&v"(a0) : "v"(hra), "n"(ACT - HS) : "memory");
#pragma unroll
      for (int c = 0; c < 8; ++c) SMF(dn[(c >> 1) + (c & 1) * 4], a0, DN[(c >> 1) + (c & 1) * 4], six);
      asm volatile("s_nop 7" : W8(dn));
      u32x4 o;
#pragma unroll
      for (int i = 0; i < 4; ++i) o[i] = nsent((g4 & 2) ? dn[4 + i][0] + dn[4 + i][1] : dn[i][0] + dn[i][1]);
      ST16(pto, o, ws + WS_PART + ec * 8192, 0);
    }
    TS(25);
    SKEWPT();
    if (hid >= 32) {
      if (hi == 6 && wid == 1 && lane < 35) {
        char* fp = ws + (lane < 32 ? WS_XFL + (xcd * 32 + lane) * 128 : lane < 34 ? WS_SFL + (xcd * 2 + lane - 32) * 128 : WS_EPO);
        if (lane < 34 || xcd == 0) ST4(reinterpret_cast<u64*>(fp), lane < 34 ? ~tag : tag);
      }
      if (hid == 40 && tid < 320) ST4(reinterpret_cast<u64*>(ws + WS_HFL) + tid, ~tag);
      TSE();
      return;
    }
    const unsigned kk = flg[tok];
    const int row = key_row(kk);
    float* rr = reinterpret_cast<float*>(shm + RR);
    float x1 = bf2f(*reinterpret_cast<const u16*>(shm + SX1 + tok * 2048 + col * 2));
    float h2 = bf2f(*reinterpret_cast<const u16*>(shm + HS + (col >> 4) * 64 + tok * 32 + (col & 2) * 8 + (col & 12) + (col & 1) * 2));
    float rsg = rbf(1.0f / (1.0f + __expf(-key_val(kk))));
    unsigned oo3 = (unsigned)(row * D + col) * 2, rra = lds0 + RR + lane * 32;
    asm volatile("" : "+v"(rsg), "+v"(x1), "+v"(h2), "+v"(oo3), "+v"(rra));
    if (wid == 7) {
      POLL { PDBG(it);
        unsigned g;
        asm volatile("global_load_dword %0, %1, %2 sc1\n\ts_waitcnt vmcnt(0)" : "=&v"(g) : "v"(((row >> 4) * 8 + (col >> 7)) * 8), "s"(ws + WS_DON) : "memory");
        if (__all(g != SENT)) break;
        __builtin_amdgcn_s_sleep(2);
      }
    } else {
      const unsigned po = (unsigned)((wid * 8 + g4) * 8192 + tok * 4096 + (hid >> 1) * 256 + l16 * 16);
      u32x4 v[2];
      if (OPT_RD) __builtin_amdgcn_s_sleep(OPT_RD);
#define PLD()                                                                                                          \
  asm volatile("global_load_dwordx4 %0, %2, %3 sc1\n\tglobal_load_dwordx4 %1, %2, %4 sc1\n\ts_waitcnt vmcnt(0)"        \
               : "=&v"(v[0]), "=&v"(v[1]) : "v"(po), "s"(ws + WS_PART), "s"(ws + WS_PART + 32768) : "memory")
      SPIN(__all(max(max4(v[0]), max4(v[1])) != SENT), OPT_RR, PLD());
      TS(28);
      const f32x4 t = __builtin_bit_cast(f32x4, v[0]) + __builtin_bit_cast(f32x4, v[1]);
      rr[(l16 * 4 + g4) * 8 + wid] = swap16_add(swap32_add(t[0], t[2]), swap32_add(t[1], t[3]));
    }
    LBAR();
    TS(5);
    if (wid == 0) {
      f32x4 r0, r1;
      asm volatile("ds_read_b128 %0, %2\n\tds_read_b128 %1, %2 offset:16\n\ts_waitcnt lgkmcnt(0)" : "=&v"(r0), "=&v"(r1) : "v"(rra) : "memory");
      const float sum = ((r0[0] + r0[1]) + (r0[2] + r0[3])) + ((r1[0] + r1[1]) + r1[2]);
      asm volatile("global_store_short %0, %1, %2" ::"v"(oo3), "v"(f2bf(x1 + gb * rbf(h2 + rbf(rbf(sum) * rsg)))), "s"(out) : "memory");
    }
    TS(15);
    TSE();
  }
}

extern "C" void kernel_launch(void* const* d_in, const int* in_sizes, int n_in, void* d_out, int out_size, void* d_ws, size_t ws_size,
                              hipStream_t stream) {
  const u16* x = (const u16*)d_in[0];
  const u16* wsr = (const u16*)d_in[1];
  const u16* wsi = (const u16*)d_in[2];
  const u16* whr = (const u16*)d_in[3];
  const u16* whi = (const u16*)d_in[4];
  const u16* w_r = (const u16*)d_in[6];
  const u16* w_gate = (const u16*)d_in[7];
  const u16* w_up = (const u16*)d_in[8];
  const u16* w_down = (const u16*)d_in[9];
  const u16* c = (const u16*)d_in[10];
  const u16* w_mod = (const u16*)d_in[11];
  u16* out = (u16*)d_out;
  k_main<<<256, 512, 0, stream>>>(x, (char*)d_ws, wsr, wsi, c, w_mod, whr, whi, w_r, w_gate, w_up, w_down, out);
}
```

```cpp
#include <hip/hip_runtime.h>
#include <hip/hip_bf16.h>
#include <stdint.h>
#include <utility>

typedef unsigned short u16;
typedef unsigned long long u64;
using bf16x8 = __attribute__((ext_vector_type(8))) short;
using bf16v8 = __attribute__((ext_vector_type(8))) __bf16;
using f32x4 = __attribute__((ext_vector_type(4))) float;
using f32x2 = __attribute__((ext_vector_type(2))) float;
using u32x4 = __attribute__((ext_vector_type(4))) unsigned;
using u32x2 = __attribute__((ext_vector_type(2))) unsigned;

constexpr int S = 2048, D = 1024, F = 3584;

__device__ __forceinline__ float bf2f(unsigned v) { return __uint_as_float(v << 16); }
__device__ __forceinline__ float bflo(unsigned v) { return __uint_as_float(v << 16); }
__device__ __forceinline__ float bfhi(unsigned v) { return __uint_as_float(v & 0xffff0000u); }
__device__ __forceinline__ unsigned f2bf(float f) {
  __hip_bfloat16 b = __float2bfloat16(f);
  return (unsigned)*reinterpret_cast<u16*>(&b);
}
__device__ __forceinline__ float rbf(float f) { return bf2f(f2bf(f)); }
using bf16v2 = __attribute__((ext_vector_type(2))) __bf16;
__device__ __forceinline__ unsigned pack2(float lo, float hi) {
  return __builtin_bit_cast(unsigned, __builtin_convertvector(f32x2{lo, hi}, bf16v2));
}

template <int CTRL>
__device__ __forceinline__ float dpp_f(float v) {
  return __int_as_float(__builtin_amdgcn_mov_dpp(__float_as_int(v), CTRL, 0xf, 0xf, true));
}
template <int CTRL>
__device__ __forceinline__ float dpp_add(float v) { return v + dpp_f<CTRL>(v); }
__device__ __forceinline__ float row_sum(float v) { return dpp_add<0x140>(dpp_add<0x141>(dpp_add<0x4E>(dpp_add<0xB1>(v)))); }
template <int CTRL>
__device__ __forceinline__ unsigned dpp_u(unsigned v) { return (unsigned)__builtin_amdgcn_mov_dpp((int)v, CTRL, 0xf, 0xf, true); }
__device__ __forceinline__ unsigned wave_max_u(unsigned v) {
  v = max(v, dpp_u<0xB1>(v));
  v = max(v, dpp_u<0x4E>(v));
  v = max(v, dpp_u<0x141>(v));
  v = max(v, dpp_u<0x140>(v));
  const unsigned a = (unsigned)__builtin_amdgcn_readlane((int)v, 0), b = (unsigned)__builtin_amdgcn_readlane((int)v, 16);
  const unsigned c = (unsigned)__builtin_amdgcn_readlane((int)v, 32), d = (unsigned)__builtin_amdgcn_readlane((int)v, 48);
  return max(max(a, b), max(c, d));
}
__device__ __forceinline__ u64 mk64(unsigned hi, unsigned lo) { return ((u64)hi << 32) | lo; }
__device__ __forceinline__ float swap32_add(float a, float b) {
  auto r = __builtin_amdgcn_permlane32_swap(__float_as_uint(a), __float_as_uint(b), false, false);
  return __uint_as_float(r[0]) + __uint_as_float(r[1]);
}
__device__ __forceinline__ float swap16_add(float a, float b) {
  auto r = __builtin_amdgcn_permlane16_swap(__float_as_uint(a), __float_as_uint(b), false, false);
  return __uint_as_float(r[0]) + __uint_as_float(r[1]);
}
__device__ __forceinline__ float ar4(float v) {
  const float t = swap32_add(v, v);
  return swap16_add(t, t);
}
__device__ __forceinline__ float wave_sum_s(float v) {
  v = row_sum(v);
  v += __int_as_float(__builtin_amdgcn_update_dpp(0, __float_as_int(v), 0x142, 0xa, 0xf, false));
  v += __int_as_float(__builtin_amdgcn_update_dpp(0, __float_as_int(v), 0x143, 0xc, 0xf, false));
  return __int_as_float(__builtin_amdgcn_readlane(__float_as_int(v), 63));
}
__device__ __forceinline__ float silu_bf(float c) { return rbf(c * __builtin_amdgcn_rcpf(1.0f + __expf(-c))); }
__device__ __forceinline__ float dsum(unsigned v, float a) {
  return __builtin_amdgcn_fdot2_f32_bf16(__builtin_bit_cast(bf16v2, v), __builtin_bit_cast(bf16v2, 0x3f803f80u), a, false);
}
template <class Fn, int... I>
__device__ __forceinline__ void sfor_(Fn&& f, std::integer_sequence<int, I...>) { (f(std::integral_constant<int, I>{}), ...); }
template <int N, class Fn>
__device__ __forceinline__ void sfor(Fn&& f) { sfor_(f, std::make_integer_sequence<int, N>{}); }
constexpr unsigned SENT = 0xFFFFFFFFu;

constexpr size_t WS_EPO = 0;
constexpr size_t WS_XFL = 4096;
constexpr size_t WS_HFL = 40960;
constexpr size_t WS_XF2 = WS_HFL + 512;
constexpr size_t WS_SFL = WS_HFL + 8192;
constexpr size_t WS_MOD = 65536;
constexpr size_t WS_Q = WS_MOD + 32768;
constexpr size_t WS_ARM = WS_Q + 8192;
constexpr size_t WS_RS = WS_ARM;
constexpr size_t WS_KEY = WS_RS + 16384;
constexpr size_t WS_HDR = WS_KEY + 16384;
constexpr size_t WS_UFL = WS_HDR + 4096;
constexpr size_t WS_DON = WS_UFL + 98304;
constexpr size_t WS_STAT = WS_DON + 12288;
constexpr size_t WS_CAND = WS_STAT + 262144;
constexpr size_t WS_PART = WS_CAND + 524288;
constexpr size_t WS_WAK = WS_PART + 483328;
static_assert(WS_WAK + 8192 <= WS_ARM + 448 * 3328, "armed region");
constexpr size_t WS_XCH = 2u << 20;
constexpr size_t WS_H = 8u << 20;
constexpr size_t WS_WH = 12u << 20;
constexpr size_t WS_U = 16u << 20;
constexpr size_t WS_DBG = 64u << 20;
#define NEWDESIGN 1

#define PCAP (1 << 20)
#define PDBG(it)
#define TS(i)
#define TSCAP()
#define TSC0()
#define TSC1(i)
#define TSD(i)
#define TSE()
#define ST16(voff, v, base, off) asm volatile("global_store_dwordx4 %0, %1, %2 offset:%3 sc1\n\ts_nop 1" ::"v"(voff), "v"(v), "s"(base), "n"(off) : "memory")
#define ST4(p, v) __hip_atomic_store(p, v, __ATOMIC_RELAXED, __HIP_MEMORY_SCOPE_AGENT)
#define WAIT_V(n) asm volatile("s_waitcnt vmcnt(%0)" ::"n"(n) : "memory")
#define WAIT_L(n) asm volatile("s_waitcnt lgkmcnt(%0)" ::"n"(n) : "memory")
#define BARRIER() asm volatile("s_barrier" ::: "memory")
#define LBAR() asm volatile("s_waitcnt lgkmcnt(0)\n\ts_barrier" ::: "memory")
#define DSR(dst, addr, off) asm volatile("ds_read_b128 %0, %1 offset:%2" : "=v"(dst) : "v"(addr), "n"(off) : "memory")
#define MFMA(c, a, b) asm volatile("v_mfma_f32_16x16x32_bf16 %0, %1, %2, %0" : "+v"(c) : "v"(a), "v"(b))
#define MFMAI(c, a, b) asm volatile("v_mfma_f32_16x16x32_bf16 %0, %1, %2, %0" ::"v"(c), "v"(a), "v"(b))
#define MFMA0(c, a, b) asm volatile("v_mfma_f32_16x16x32_bf16 %0, %1, %2, 0" : "=&v"(c) : "v"(a), "v"(b))
#define GLDSP(m0v, voff, gbase, pol) \
  asm volatile("s_mov_b32 m0, %0\n\ts_nop 0\n\tglobal_load_lds_dwordx4 %1, %2" pol ::"s"(m0v), "v"(voff), "s"(gbase) : "memory")
#define GLDS(m0v, voff, gbase) GLDSP(m0v, voff, gbase, "")
#define WPOL " nt"
#define LDXS(dst, voff, base, off) asm volatile("global_load_dwordx4 %0, %1, %2 offset:%3 sc1" : "=v"(dst) : "v"(voff), "s"(base), "n"(off) : "memory")
#define BF8(v) __builtin_bit_cast(bf16v8, v)
#define MM(a, b, c) __builtin_amdgcn_mfma_f32_16x16x32_bf16(BF8(a), BF8(b), c, 0, 0, 0)
#define W6(a) "+v"(a[0]), "+v"(a[1]), "+v"(a[2]), "+v"(a[3]), "+v"(a[4]), "+v"(a[5])
#define W8(a) W6(a), "+v"(a[6]), "+v"(a[7])
#define PFL(ptr) asm volatile("s_mov_b32 m0, %0\n\ts_nop 0\n\tglobal_load_lds_dword %1, off" ::"s"(pfl), "v"(ptr) : "memory")
using s16x4 = __attribute__((ext_vector_type(4))) short;
__device__ __forceinline__ u32x4 trd(unsigned a) {
  const u32x2 lo = __builtin_bit_cast(u32x2, __builtin_amdgcn_ds_read_tr16_b64_v4i16((__attribute__((address_space(3))) s16x4*)(size_t)a));
  const u32x2 hi = __builtin_bit_cast(u32x2, __builtin_amdgcn_ds_read_tr16_b64_v4i16((__attribute__((address_space(3))) s16x4*)(size_t)(a + 512)));
  return u32x4{lo[0], lo[1], hi[0], hi[1]};
}
using u32x8 = __attribute__((ext_vector_type(8))) unsigned;
__device__ __forceinline__ u32x8 trd2(unsigned a) {
  const u32x4 lo = trd(a), hi = trd(a + 4096);
  return u32x8{lo[0], lo[1], lo[2], lo[3], hi[0], hi[1], hi[2], hi[3]};
}
#define SMF(c, a, b, i) asm volatile("v_smfmac_f32_16x16x64_bf16 %0, %1, %2, %3" ::"v"(c), "v"(a), "v"(b), "v"(i))
__device__ __forceinline__ int key_row(unsigned k) { return (int)((0xffffu - (k & 0xffffu)) & 2047u); }
__device__ __forceinline__ float key_val(unsigned k) {
  const unsigned ord = k >> 16;
  return bf2f(ord ^ ((ord & 0x8000u) ? 0x8000u : 0xffffu));
}
__device__ __forceinline__ unsigned nsent(float v) { return min(__float_as_uint(v), SENT - 1u); }
__device__ __forceinline__ f32x4 lo4(u32x4 q) { return f32x4{bflo(q[0]), bfhi(q[0]), bflo(q[1]), bfhi(q[1])}; }
__device__ __forceinline__ f32x4 hi4(u32x4 q) { return f32x4{bflo(q[2]), bfhi(q[2]), bflo(q[3]), bfhi(q[3])}; }
__device__ __forceinline__ u32x4 pk8(f32x4 a, f32x4 b) { return u32x4{pack2(a[0], a[1]), pack2(a[2], a[3]), pack2(b[0], b[1]), pack2(b[2], b[3])}; }
__device__ __forceinline__ u32x4 nsent4(u32x4 v) { return u32x4{min(v[0], SENT - 1u), min(v[1], SENT - 1u), min(v[2], SENT - 1u), min(v[3], SENT - 1u)}; }
__device__ __forceinline__ unsigned max4(u32x4 v) { return max(max(v[0], v[1]), max(v[2], v[3])); }
__device__ __forceinline__ unsigned max2(u32x2 v) { return max(v[0], v[1]); }
__device__ __forceinline__ unsigned hi2(u32x2 v) { return (v[0] >> 16) | (v[1] & 0xffff0000u); }
#define TOP2(CTRL)                                                  \
  do {                                                              \
    const unsigned o1 = dpp_u<CTRL>(t1), o2 = dpp_u<CTRL>(t2);      \
    t2 = max(min(t1, o1), max(t2, o2));                             \
    t1 = max(t1, o1);                                               \
  } while (0)
#define POLL _Pragma("unroll 1") for (int it = 0; it < PCAP; ++it)
#define SPIN(OK, PAUSE, LD)                                 \
  do {                                                      \
    int left_ = PCAP;                                       \
    do {                                                    \
      __builtin_amdgcn_s_sleep(PAUSE);                      \
      LD;                                                   \
      left_ = (OK) ? 0 : left_ - 1;                         \
      asm volatile("" : "+s"(left_));                       \
      PDBG(PCAP - left_);                                   \
    } while (left_);                                        \
  } while (0)
#define UNTIL(OK, PAUSE, LD)                                \
  do {                                                      \
    if (__builtin_expect(!(OK), 0)) {                       \
      POLL { PDBG(it);                                      \
        __builtin_amdgcn_s_sleep(PAUSE);                    \
        LD;                                                 \
        if (OK) break;                                      \
      }                                                     \
    }                                                       \
  } while (0)
#define P_D3 16
#define P_D4 2
#define U_D 28
#define U_R 1
#define X_R 1
#define OPT_SD 16
#define OPT_KS 2
#define OPT_KD 32
#define OPT_RD 6
#define OPT_RR 2
#define OUT_D 20
#define CPF_N 10
#define CPF_K 24
#define CPF_B 10
#define PF_N 3
#define A_LD 3
#define F_AT 3
#define X_AT 12
#define H_AT 24
#define B_VAT 43
#define UF_AT 8
#define B46 339
#define TP_XL 340
#define TP_X2 350
#define Q_AT 10
#define MQ_AT 28
#define XQ0 2
#define XQ1 4
#define XQ2 18
#define XQ3 20
#define HX0 9
#define HXS 2
static_assert(HX0 + 11 * HXS < 44 && UF_AT <= 22 && A_LD <= 3 && H_AT <= 24 && X_AT + 12 <= H_AT && Q_AT >= 7, "");
#define P_L 2
#define V_D 24
#define PF_SH 0x06555542
#define PF_BS 0x001a15100b060200ull
#define NPD 4
constexpr int SL = 22528, NS = 5, XR = NS * SL, MISC = XR + 24576;
constexpr int as(int c) { return c < 3 ? 2 : c < 6 ? 3 : 2 + (c & 1); }
constexpr int ap(int c) { return c < 3 ? c : c < 6 ? c - 3 : c / 2; }
constexpr int ug(int j) { return j < 9 ? 18 + 7 * j : 82; }
constexpr int E0 = 264, E3 = E0 + 88, NE = E3 + 6;
constexpr int es(int c) { return c < 132 ? c / 11 : c < 144 ? 12 + (c - 132) / 6 : c < 156 ? 14 + (c & 1) : 12 + (c & 3); }
constexpr int ep(int c) { return c < 132 ? c % 11 : c < 144 ? (c - 132) % 6 : c < 156 ? (c - 144) / 2 : 6 + (c - 156) / 4; }

__global__ __launch_bounds__(512, 2) void k_main(const u16* __restrict__ x, char* ws, const u16* __restrict__ Y0, const u16* __restrict__ Y1,
                                              const u16* __restrict__ cvec, const u16* __restrict__ wmod, const u16* __restrict__ X0,
                                              const u16* __restrict__ X1, const u16* __restrict__ wr_, const u16* __restrict__ wg,
                                              const u16* __restrict__ wu, const u16* __restrict__ wd, u16* __restrict__ out) {
  constexpr int STG = 114688;
  __shared__ __attribute__((aligned(1024))) char shm[MISC + 16384];
  const int b = blockIdx.x, xcd = b & 7, j8 = b >> 3;
  const int cc = xcd >> 2, qx = xcd & 3;
  const int tid = threadIdx.x, lane = tid & 63;
  const int wid = __builtin_amdgcn_readfirstlane(tid >> 6);
  const unsigned lds0 = (unsigned)(size_t)(__attribute__((address_space(3))) char*)shm;
  const char* cpf;
  {
    unsigned long long pc;
    unsigned len;
    asm volatile("s_getpc_b64 %0\n.Lcpf_here:\n\ts_mov_b32 %1, .Lcpf_e1-.Lcpf_here\n\ts_max_u32 %1, %1, .Lcpf_e2-.Lcpf_here" : "=s"(pc), "=s"(len)::"scc");
    cpf = reinterpret_cast<const char*>(pc) + min((unsigned)((lane * 32 + j8) * 128), len);
  }
  const unsigned pfl = lds0 + MISC + 16128;
  if (wid == 4 && lane < CPF_N) PFL(cpf);
  u32x4 xv[2];
  {
    const unsigned xo = (unsigned)((b * 8 + wid) * D + lane * 8) * 2;
    asm volatile("global_load_dwordx4 %0, %2, %3\n\tglobal_load_dwordx4 %1, %2, %3 offset:1024" : "=&v"(xv[0]), "=&v"(xv[1]) : "v"(xo), "s"(x) : "memory");
  }
  asm volatile("" ::"s"(ws), "s"(Y0), "s"(Y1), "s"(X0), "s"(X1));
  unsigned* xfl = reinterpret_cast<unsigned*>(ws + WS_XFL);
  unsigned* hfl = reinterpret_cast<unsigned*>(ws + WS_HFL);
  unsigned* modv = reinterpret_cast<unsigned*>(ws + WS_MOD);
#define SKEWPT()
  const int g4 = lane >> 4, l16 = lane & 15;
  u64 tag0, tag;
  asm volatile("global_load_dwordx2 %0, %1, %2 sc1" : "=v"(tag0) : "v"(0u), "s"(ws) : "memory");
  const char* Yx = reinterpret_cast<const char*>(cc ? Y1 : Y0) + (size_t)qx * 2097152;
#define PFG(r16, g) (Yx + (size_t)(((r16) * 16 + l16) * 4096 + g4 * 128 + ((((g) + qx) & 7) << 9)))
  unsigned ta = lds0 + STG + ((lane >> 2) & 3) * 2368 + (wid * 128 + (lane >> 5) * 32 + (lane & 3) * 8 + (g4 & 1) * 4) * 2;
  unsigned oo = (unsigned)((wid * 4 + (lane >> 5)) * 131072 + (b >> 2) * 2048 + (g4 & 1) * 1024 + ((b & 3) * 16 + l16) * 16), oo2 = oo + 262144;
  unsigned wa = lds0 + STG + wid * 2368 + lane * 16;
  asm volatile("" : "+v"(ta), "+v"(oo), "+v"(oo2), "+v"(wa));
  const int hsl = (b & 32) + xcd * 4 + (j8 & 3);
  const char* hp = reinterpret_cast<const char*>((b & 32) ? X1 : X0) + (size_t)(hsl & 31) * 65536 + wid * 8192;
#define WHLD(i0)                                                                                                                  \
  _Pragma("unroll") for (int i = i0; i < i0 + 4; ++i)                                                                             \
      GLDS(lds0 + wid * 8192 + i * 1024, (unsigned)((lane ^ ((i >> 1) | ((wid & 2) << 1))) * 16), hp + i * 1024)
  if (wid >= 8 - PF_N) PFL(PFG(j8, 7 - wid));
  if (j8 >= 24) {
    WHLD(0);
    WAIT_V(5);
  } else if (wid >= 8 - PF_N) {
    WAIT_V(2);
  } else {
    WAIT_V(1);
  }
  {
    asm volatile("; AUDIT_OK %0 %1" : "+v"(xv[0]), "+v"(xv[1])::"memory");
    TS(32);
    const float s0 = dsum(xv[0][3], dsum(xv[0][2], dsum(xv[0][1], dsum(xv[0][0], 0.f))));
    const float s1 = dsum(xv[1][3], dsum(xv[1][2], dsum(xv[1][1], dsum(xv[1][0], 0.f))));
    const float mf = wave_sum_s(s0 + s1) * (1.0f / D), m = rbf(mf);
    const f32x4 d0 = lo4(xv[0]) - m, d1 = hi4(xv[0]) - m, d2 = lo4(xv[1]) - m, d3 = hi4(xv[1]) - m;
    const f32x4 q = (d0 * d0 + d1 * d1) + (d2 * d2 + d3 * d3);
    const float vf = wave_sum_s((q[0] + q[1]) + (q[2] + q[3])) * (1.0f / D) - (mf - m) * (mf - m);
    const float rden = __builtin_amdgcn_rcpf(rbf(__builtin_amdgcn_sqrtf(rbf(rbf(vf) + 1e-5f))));
    asm volatile("ds_write_b128 %0, %1" ::"v"(wa), "v"(pk8(d0 * rden, d1 * rden)) : "memory");
    asm volatile("ds_write_b128 %0, %1 offset:1024" ::"v"(wa), "v"(pk8(d2 * rden, d3 * rden)) : "memory");
    LBAR();
    TS(34);
    u32x2 t[4];
    asm volatile(
        "ds_read_b64_tr_b16 %0, %4\n\tds_read_b64_tr_b16 %1, %4 offset:%5\n\t"
        "ds_read_b64_tr_b16 %2, %4 offset:128\n\tds_read_b64_tr_b16 %3, %4 offset:%6\n\ts_waitcnt lgkmcnt(0)"
        : "=&v"(t[0]), "=&v"(t[1]), "=&v"(t[2]), "=&v"(t[3])
        : "v"(ta), "n"(4 * 2368), "n"(4 * 2368 + 128)
        : "memory");
    asm volatile("global_store_dwordx4 %0, %2, %4 sc1\n\tglobal_store_dwordx4 %1, %3, %4 sc1\n\ts_nop 1" ::"v"(oo), "v"(oo2),
                 "v"(u32x4{t[0][0], t[0][1], t[1][0], t[1][1]}), "v"(u32x4{t[2][0], t[2][1], t[3][0], t[3][1]}), "s"(ws + WS_H)
                 : "memory");
  }
#define PBAR(n)                                                                                                          \
  do {                                                                                                                   \
    asm volatile("s_waitcnt vmcnt(%2)\n\tv_lshl_add_u64 %0, %1, 0, 1" : "=v"(tag) : "v"(tag0), "n"(n) : "memory");       \
    TS(36);                                                                                                              \
    BARRIER();                                                                                                           \
    TS(37);                                                                                                              \
  } while (0)
#define XFLAG() do { if ((tid | 1) == 385) ST4(reinterpret_cast<u64*>(ws + ((tid & 1) ? WS_XF2 + b * 8 : WS_XFL + b * 128)), tag); } while (0)
  if (j8 < 24) {
    const int mt = (j8 * 11) >> 5, nt = j8 - mt * 3, grp = xcd * 3 + nt;
#define FROW(f) (nt * 176 + (f) * 16 - (nt == 2 && (f) > 5 ? 16 : 0))
#define PROW(p) FROW(cc ? 10 - (p) : (p))
    constexpr int SCA = MISC, TGA = MISC + 512, TV = MISC + 1024, TSUM = MISC + 1792, SCW = MISC + 2048, RKA = MISC + 3072,
                  RKB = MISC + 5120, RKZ = MISC + 6656, IDT = MISC + 7168, ZSC = MISC + 9216;
    const bool zd = nt == 2;
    unsigned* ufl = reinterpret_cast<unsigned*>(ws + WS_UFL) + grp * 1024;
    const char* Ug = ws + WS_U + (size_t)grp * (16 * SL + 1024) + 1024;
    const unsigned perm = (unsigned)mt | (((0x76543210u & ((1u << (4 * mt)) - 1u)) << 4) | (0x76543210u & (0xfffffff0u << (4 * mt))));
#define KT(i) ((perm >> (4 * (i))) & 7u)
    const int kk = wid < 4 ? wid : wid - 2;
    const bool act = (wid < 4 || wid >= 6) && (kk || (cc == 0 && nt < 2));
    const int rg = qx * 32 + (PROW(5 + kk) >> 4);
    u32x4 pv[6];
    f32x4 acc[2][11], ga[2], x5[2];
    bf16x8 idt[2];
    unsigned spo = (unsigned)(((rg * 8 + g4 * 2) * 16 + l16) * 16);
    u32x4 sr[2], dk;
#pragma unroll
    for (int q = 0; q < 4; ++q) dk[q] = l16 == g4 * 4 + q ? ~0u : 0u;
    unsigned xw1 = lds0 + XR + tid * 16, xa = lds0 + XR + kk * 4096 + lane * 16, sto = (unsigned)(((rg * 8 + mt) * 16 + lane) * 16);
    unsigned saa = lds0 + SCW + (lane & 3) * 256 + g4 * 16, cja = lds0 + TSUM + g4 * 8, swa = lds0 + TSUM + 32, cea = lds0 + TSUM + mt * 4;
    const char* stp = ws + WS_STAT;
    asm volatile("" : "+s"(stp), "+v"(cja), "+v"(swa), "+v"(cea), "+v"(dk), "+v"(xa), "+v"(sto), "+v"(xw1), "+v"(spo), "+v"(saa));
#define STLD() asm volatile("global_load_dwordx4 %0, %2, %3 sc1\n\tglobal_load_dwordx4 %1, %2, %3 offset:256 sc1\n\ts_waitcnt vmcnt(0)" : "=&v"(sr[0]), "=&v"(sr[1]) : "v"(spo), "s"(stp) : "memory")
    const char* xr = ws + WS_XCH + (size_t)(b * 4 + (wid & 3)) * 6144 + 3072;
#define XCHLD()                                                                                                       \
  asm volatile(                                                                                                       \
      "global_load_dwordx4 %0, %6, %7 offset:2048 sc1\n\tglobal_load_dwordx4 %1, %6, %7 offset:1024 sc1\n\t"          \
      "global_load_dwordx4 %2, %6, %7 sc1\n\tglobal_load_dwordx4 %3, %6, %7 offset:-1024 sc1\n\t"                     \
      "global_load_dwordx4 %4, %6, %7 offset:-2048 sc1\n\tglobal_load_dwordx4 %5, %6, %7 offset:-3072 sc1"            \
      : "=&v"(pv[0]), "=&v"(pv[1]), "=&v"(pv[2]), "=&v"(pv[3]), "=&v"(pv[4]), "=&v"(pv[5])                            \
      : "v"(lane * 16), "s"(xr)                                                                                       \
      : "memory")
    if (wid < 4) {
      bf16x8 X[4][2][2], Y[8];
      u32x4 xq[6];
      const unsigned xo0 = lane * 16 + 4096;

#define XLD(dst, XP, XH, i) LDXS(dst, xo0, XP, ((XH)&3) * 2048 + (i)*1024 - 4096)
      int xt = lane < 16 ? (int)WS_H + (mt * 4 + wid) * 131072 + (((qx * 2 + lane) & 15) << 13) : (int)WS_WH + (cc * 32 + mt * 4 + wid) * 65536 + (KT(lane & 7) << 13);
      asm volatile("" : "+v"(xt));
#define XT(j) (ws + (unsigned)__builtin_amdgcn_readlane(xt, j))
#define WP(h) XT(16 + ((h) >> 2))
#define XS(h) X[((h) >> 1) & 3][(h)&1]
      {
        unsigned ye = lds0 + l16 * 128 + (g4 ^ (lane & 7)) * 16, yo = ye ^ 64;
        const char* xpa = XT(0);
        const char* xpb = XT(1);
#pragma unroll
        for (int i = 0; i < 22; ++i) {
          acc[i / 11][i % 11] = f32x4{0.f, 0.f, 0.f, 0.f};
          asm volatile("" : "+v"(acc[i / 11][i % 11]));
        }
        const char* xw = ws + WS_XCH + (size_t)((b ^ 4) * 4 + wid) * 6144 + 3072;
        unsigned xra = lds0 + XR + l16 * 256 + (((wid * 4 + g4) ^ (lane & 7)) << 4), gaa = lds0 + TGA + wid * 128 + g4 * 32;
        unsigned rka = lds0 + (g4 ? RKZ : RKA + wid * 512 + l16 * 16), rkb = lds0 + RKB + l16 * 16;
        asm volatile("" : "+s"(xw), "+s"(xr), "+v"(xra), "+v"(gaa), "+v"(rka), "+v"(rkb));
        __builtin_amdgcn_s_setprio(1);
        PBAR(0);
        if (wid == 3) {
          const unsigned* fp = xfl + (qx * 32 + lane) * 32;
          __builtin_amdgcn_s_sleep(P_D3 - P_D4);
          u64 f;
#define GLD() asm volatile("global_load_dwordx2 %0, %1, off sc1\n\ts_waitcnt vmcnt(0)" : "=&v"(f) : "v"(fp) : "memory")
          SPIN(__all(f == tag), P_D4, GLD());
        }
        TS(38);
        BARRIER();
        TS(39);
#pragma unroll
        for (int h = 0; h < 6; ++h) {
          XLD(XS(h)[0], h < 4 ? xpa : xpb, h, 0);
          XLD(XS(h)[1], h < 4 ? xpa : xpb, h, 1);
        }
#pragma unroll
        for (int j = 0; j < 6; ++j) DSR(Y[j], (j & 1) ? yo : ye, (j >> 1) * 2048);
        unsigned so = 0, a0 = ye, a1 = yo, yb[2], tq, u = 2;
#define STEP2(S0, XA, XB)                                                                                                      \
  sfor<44>([&](auto nn) __attribute__((always_inline)) {                                                                       \
    constexpr int n = (S0)*22 + decltype(nn)::value, q = n % 22, p = q / 2, s = n / 22, i = n & 1;                             \
    constexpr int k = q == XQ0 ? 0 : q == XQ1 ? 1 : q == XQ2 ? 2 : q == XQ3 ? 3 : -1;                                          \
    (void)acc, (void)X, (void)Y, (void)a0, (void)a1, (void)xo0, (void)XA, (void)XB, (void)so, (void)ye, (void)yo, (void)u, (void)xt, (void)tq, (void)ws; \
    if constexpr (q == 11) BARRIER();                                                                                          \
    if constexpr (q == 0) asm volatile("s_waitcnt vmcnt(8) lgkmcnt(4)" ::: "memory");                                          \
    else if constexpr (!i) WAIT_L(4);                                                                                          \
    MFMAI(acc[i][p], X[s][0][i], Y[n & 6]);                                                                                    \
    DSR(Y[(n + 6) & 7], i ? a1 : a0, ((p + 3) % 11) * 2048);                                                                   \
    MFMA(acc[i][p], X[s][1][i], Y[(n & 6) + 1]);                                                                               \
    if constexpr (k >= 0) XLD(X[(s + 3) & 3][k / 2][k & 1], (s & 1) ? XB : XA, 2 * s + 6 + k / 2, k & 1);                      \
    if constexpr (q == 0 && !(s & 1)) asm volatile("v_readlane_b32 %0, %2, %1\n\ts_add_u32 %1, %1, 1" : "=&s"(tq), "+s"(u) : "v"(xt) : "scc"); \
    if constexpr (q == 6) asm volatile("s_add_u32 %0, %0, %1" : "+s"(so) : "n"(SL) : "scc");                                   \
    if constexpr (q == 8) asm volatile("s_cmp_lg_u32 %0, %1\n\ts_cselect_b32 %0, %0, 0" : "+s"(so) : "n"(NS * SL) : "scc");    \
    if constexpr (q == 12 && !(s & 1)) { asm volatile("" : "+s"(tq)); XB = ws + tq; asm volatile("" : "+s"(XB)); }             \
    if constexpr (q == 14) asm volatile("v_add_u32 %0, %1, %2" : "=v"(a0) : "s"(so), "v"(ye));                                 \
    if constexpr (q == 16) asm volatile("v_add_u32 %0, %1, %2" : "=v"(a1) : "s"(so), "v"(yo));                                 \
  })
#define YSL(e) ((32 + es((e) / 2)) % NS)
#define YRD(e) DSR(Y[(e)&7], YSL(e) < 2 ? ye : yb[YSL(e) / 4], (YSL(e) & 1) * SL + ep((e) / 2) * 2048 + ((e)&1) * 1024)

        u32x4 ov;
        bf16x8 qf[2];
        auto OPS = [&](auto kc) __attribute__((always_inline)) {
          constexpr int k = decltype(kc)::value, q = k / 10, r = k % 10, c = k - 60;
          (void)ov, (void)acc, (void)xw, (void)lane, (void)xq, (void)x5;
          if constexpr (k < 60) {
            if constexpr (r < 4) asm volatile("v_cvt_pk_bf16_f32 %0, %1, %2" : "=v"(ov[r]) : "v"(acc[r / 2][q][(r & 1) * 2]), "v"(acc[r / 2][q][(r & 1) * 2 + 1]));
            else if constexpr (r < 8) asm volatile("v_min_u32 %0, -2, %0" : "+v"(ov[r - 4]));
            else if constexpr (r == 9) asm volatile("global_store_dwordx4 %0, %1, %2 offset:%3 sc1" ::"v"(lane * 16), "v"(ov), "s"(xw), "n"(q * 1024 - 3072) : "memory");
          } else if constexpr (c >= 40 && c < 48) {
            if constexpr (c & 1) asm volatile("v_and_b32 %0, 0xffff0000, %1" : "=v"(x5[(c >> 2) & 1][c & 3]) : "v"(xq[5][(c >> 1) & 3]));
            else asm volatile("v_lshlrev_b32 %0, 16, %1" : "=v"(x5[(c >> 2) & 1][c & 3]) : "v"(xq[5][(c >> 1) & 3]));
          } else if constexpr (c < 40) {
            if constexpr (c & 1) asm volatile("v_and_b32 %0, 0xffff0000, %1" : "=v"(acc[(c >> 2) & 1][c >> 3][c & 3]) : "v"(xq[c >> 3][(c >> 1) & 3]));
            else asm volatile("v_lshlrev_b32 %0, 16, %1" : "=v"(acc[(c >> 2) & 1][c >> 3][c & 3]) : "v"(xq[c >> 3][(c >> 1) & 3]));
          }
        };
#define IC(v) std::integral_constant<int, (v)>{}
#define FILL(g)                                                                              \
  do {                                                                                       \
    if constexpr (m >= 0) {                                                                  \
      OPS(IC(m / 2 * 5 + (m & 1) * 3 + (g)));                                                \
      if constexpr ((g) && !(m & 1)) OPS(IC(m / 2 * 5 + 2));                                 \
    }                                                                                        \
  } while (0)
        auto BP = [&](auto nn) __attribute__((always_inline)) {
          constexpr int n = decltype(nn)::value, s = es(n / 2), p = n < E3 ? ep(n / 2) : 5 + n - E3, i = n & 1, e = n + 6, m = n - E0 - 30;
          (void)ga, (void)qf, (void)idt, (void)rka, (void)rkb, (void)gaa, (void)xq, (void)ov, (void)xw, (void)x, (void)pv, (void)xr, (void)ufl;
          (void)acc, (void)X, (void)Y, (void)yb, (void)ye, (void)xra, (void)xo0, (void)xt, (void)ws, (void)lane, (void)mt, (void)nt, (void)cc, (void)lds0, (void)wid, (void)g4, (void)perm;
          (void)OPS;
          TSCAP();
          if constexpr (n == E0 + 55) TS(10);
          if constexpr (n == UF_AT) {
            TS(45);
            WAIT_V(n > HX0 ? (n - HX0 + HXS - 1) / HXS : 0);
            if (lane == 0) ST4(ufl + (mt * 4 + wid) * 32, 1u);
            TS(46);
          }
          if constexpr (n == 37) TS(48);
          if constexpr (n == 37 || n == E0 + 5 || n == E0 + 17 || n == B46) BARRIER();
          if constexpr (n == 37) TS(4);
          if constexpr (n == E0 + 25) { TS(3); SKEWPT(); }
          if constexpr (p == 0 && !i && n != 22) WAIT_V(n == 0 ? 15 : s < 14 ? 8 : 4 * (15 - s));
          if constexpr (!i) WAIT_L(e < NE ? 4 : NE - 2 - n);
          if constexpr (n < 22) MFMA0(acc[i][p], X[0][0][i], Y[n & 6]);
          else if constexpr (n < E3) MFMAI(acc[i][p], X[s & 3][0][i], Y[n & 6]);
          else MFMA(acc[0][p], qf[0], Y[n & 7]);
          if constexpr (e < E3) YRD(e);
          else if constexpr (e < NE) DSR(Y[e & 7], rkb, (e - E3) * 256);
          FILL(0);
          if constexpr (n < E3 && (s == 1 || s == 15)) MFMA(acc[i][p], X[s & 3][1][i], Y[(n & 6) + 1]);
          else if constexpr (n < E3) MFMAI(acc[i][p], X[s & 3][1][i], Y[(n & 6) + 1]);
          else MFMA(acc[1][p], qf[1], Y[n & 7]);
          if constexpr (n >= HX0 && n < HX0 + 12 * HXS && (n - HX0) % HXS == 0) {
            constexpr int k = (n - HX0) / HXS;
            static_assert(k < 8 || n >= 22, "slot still in use");
            XLD(XS(4 + k / 2)[k & 1], WP(4 + k / 2), k / 2, k & 1);
          }
          if constexpr (n >= 272 && n < 294 && n % 4 == 0) DSR(xq[(n - 272) / 4], xra, (n - 272) / 4 * 4096);
          if constexpr (n >= E0 && n < E0 + 4) XLD(X[3][(n - E0) / 2][i], WP(31), 30 + (n - E0) / 2, i);
          FILL(1);
          if constexpr (n == B46 || n == B46 + 1) DSR(qf[n - B46], rka, (n - B46) * 256);
          if constexpr (n == B46 + 2 || n == B46 + 3) DSR(ga[n - B46 - 2], gaa, (n - B46 - 2) * 16);
          if constexpr (n == B46 + 4 || n == B46 + 5) DSR(idt[n - B46 - 4], yb[1], IDT - 4 * SL + (n - B46 - 4) * 1024);
          if constexpr (n >= TP_XL && n < TP_XL + 4) LDXS(pv[TP_XL + 5 - n], lane * 16, xr, (n - TP_XL) * 1024 - 3072);
          if constexpr (n == TP_X2 || n == TP_X2 + 1) LDXS(pv[TP_X2 + 1 - n], lane * 16, xr, (n - TP_X2) * 1024 + 1024);
        };
        do {
          STEP2(0, xpb, xpa);
          if (__builtin_expect(u == 17, 0)) {
            TS(57);
            TSC1(40);
            TSC0();
            f32x4 c0, c1;
            const char* up = Ug + (mt * 2 + (wid >> 1)) * SL + (wid & 1) * 1024 + 4096;
            const unsigned ul = lds0 + (2 + (wid >> 1)) * SL + (wid & 1) * 1024 + lane * 16;
            u32x4 uv;
            DSR(c0, lds0 + SCA + wid * 128 + g4 * 32, 0);
            DSR(c1, lds0 + SCA + wid * 128 + g4 * 32, 16);
#define UMUL(j, k) asm volatile("v_mul_f32 %0, %0, %1" : "+v"(acc[(k) >> 2][j][(k)&3]) : "v"(((k) >> 2 ? c1 : c0)[(k)&3]))
#define UCVT(j, k) asm volatile("v_cvt_pk_bf16_f32 %0, %1, %2" : "=v"(uv[k]) : "v"(acc[(k) >> 1][j][((k)&1) * 2]), "v"(acc[(k) >> 1][j][((k)&1) * 2 + 1]))
            auto UEP = [&](auto jc, auto rc) __attribute__((always_inline)) {
              constexpr int j = decltype(jc)::value, r = decltype(rc)::value;
              (void)acc, (void)c0, (void)c1, (void)uv, (void)lane, (void)ul, (void)up;
              if constexpr (r < 4) { UMUL(j, 2 * r); UMUL(j, 2 * r + 1); }
              else if constexpr (r < 6) { UCVT(j, 2 * r - 8); UCVT(j, 2 * r - 7); }
              else asm volatile("global_store_dwordx4 %0, %2, %3 offset:%4 sc1\n\tds_write_b128 %1, %2 offset:%5"
                                ::"v"(lane * 16), "v"(ul), "v"(uv), "s"(up + (j >> 2) * 8192), "n"((j & 3) * 2048 - 4096), "n"(j * 2048) : "memory");
            };
#define UGAP(G) do { constexpr int j_ = (G) < 82 ? ((G)-18) / 7 : 9; if constexpr ((G) >= 18 && (G) != 81) UEP(IC(j_), IC((G)-ug(j_))); } while (0)
            sfor<44>([&](auto nn) __attribute__((always_inline)) {
              constexpr int n = decltype(nn)::value, s = as(n / 2), p = ap(n / 2), i = n & 1, e = n + 6;
              (void)acc, (void)X, (void)Y, (void)ye, (void)yo, (void)xo0, (void)xpa, (void)UEP;
              if constexpr (n == 0 || n == 6) WAIT_V(8);
              if constexpr (!i) WAIT_L(e < 44 ? 4 : 42 - n);
              MFMAI(acc[i][p], X[s][0][i], Y[(n + 4) & 6]);
              if constexpr (e < 44) DSR(Y[(e + 4) & 7], i ? yo : ye, (as(e / 2) & 1) * SL + ap(e / 2) * 2048);
              UGAP(2 * n);
              if constexpr (s == 2) MFMAI(acc[i][p], X[s][1][i], Y[((n + 4) & 6) + 1]);
              else MFMA(acc[i][p], X[s][1][i], Y[((n + 4) & 6) + 1]);
              if constexpr (n < 4) XLD(X[1][n / 2][i], xpa, 2 + n / 2, i);
              UGAP(2 * n + 1);
            });
            TSC1(41);
            TS(0);
            SKEWPT();
            UEP(IC(9), IC(6));
            asm volatile("s_nop 1");
            sfor<7>([&](auto rc) __attribute__((always_inline)) { UEP(IC(10), rc); });
            asm volatile("s_nop 1" ::"v"(uv));
            ye = lds0 + lane * 16;
            yo = ye + 1024;
            so = 4 * SL;
            a0 = ye + 4 * SL;
            a1 = yo + 4 * SL;
            yb[0] = ye + 2 * SL;
            yb[1] = a0;
            asm volatile("" : "+v"(yb[0]), "+v"(yb[1]), "+v"(ye), "+v"(yo));
            LBAR();
            TS(44);
            sfor<6>([&](auto ee) __attribute__((always_inline)) { (void)Y, (void)yb, (void)ye; YRD(decltype(ee)::value); });
            sfor<44>(BP);
            u = 19;
            xpa = WP(8);
          }
          STEP2(2, xpa, xpb);
        } while (u != 24);
        TS(2);
        sfor<NE - 264>([&](auto nn) __attribute__((always_inline)) { BP(std::integral_constant<int, 264 + decltype(nn)::value>{}); });
        __builtin_amdgcn_s_setprio(0);
        TSC1(59);
        TS(13);
        SKEWPT();
      }
    } else if (wid < 6) {
      const int hw = wid - 4;
      __builtin_amdgcn_s_setprio(2);
      unsigned voy[11];
#pragma unroll
      for (int i = 0; i < 11; ++i) voy[i] = (unsigned)((PROW(i) + hw * 8 + (lane >> 3)) * 4096 + ((lane & 7) ^ (lane >> 3)) * 16);
      const unsigned lbase = lds0 + hw * 1024;
      if (zd) voy[5] = (lane & 7) * 16;
      auto stage = [&](int n, int slot) {
        const char* yp = Yx + (((n + qx * 4) & 31) << 7);
        const unsigned l = lbase + slot * SL;
#pragma unroll
        for (int i = 0; i < 11; ++i) GLDS(i == 5 && zd ? lbase + ZSC : l + i * 2048, voy[i], yp);
      };
      asm volatile("s_waitcnt vmcnt(%3)" ::"v"(voy[0]), "v"(voy[10]), "v"(tag0), "n"(0) : "memory");
      BARRIER();
      if (P_L) __builtin_amdgcn_s_sleep(P_L);
#pragma unroll 1
      for (int s = 0; s <= A_LD; ++s) stage(s, s);
      WAIT_V(11 * A_LD);
      TS(38);
      BARRIER();
      int slot = A_LD + 1;
#pragma unroll 1
      for (int k = 1; k <= 31 - A_LD; ++k) {
        WAIT_V(11 * (A_LD - 1));
        BARRIER();
        if (hw == 1) TSD(k);
        stage(k + A_LD, slot);
        slot = slot == NS - 1 ? 0 : slot + 1;
        if (k == CPF_K && hw == 1 && lane < CPF_N) PFL(cpf);
      }
      if (A_LD > 2) { WAIT_V(22); BARRIER(); }
      WAIT_V(0); BARRIER();
      const unsigned* ufp = ufl + (KT(min(lane >> 2, 6) + 1) * 4 + (lane & 3)) * 32;
      unsigned miss = ~0u;
      const char* y1 = Ug + KT(1) * 2 * SL + hw * 11264;
      asm volatile("" : "+s"(y1), "+v"(ufp));
      BARRIER();
      TS(47);
      if (U_D) __builtin_amdgcn_s_sleep(U_D);
      auto ULDF = [&](unsigned em0) __attribute__((always_inline)) {
        unsigned f;
        const u64 em = miss & em0;
        asm volatile("s_mov_b64 exec, %2\n\tglobal_load_dword %0, %1, off sc1\n\ts_mov_b64 exec, -1\n\ts_waitcnt vmcnt(0)" : "=&v"(f) : "v"(ufp), "s"(em) : "memory");
        miss &= ~(unsigned)em | (unsigned)__builtin_amdgcn_ballot_w64(f == SENT);
      };
#define UPOLL(need, em0) UNTIL((miss & (need)) == 0, U_R, ULDF(em0))
#define G1(o) "global_load_lds_dwordx4 %0, %1 offset:" #o " sc1\n\t"
#define ULD(yp, l)                                                                                                                          \
  do {                                                                                                                                      \
    asm volatile("s_mov_b32 m0, %2\n\ts_nop 0\n\t" G1(0) G1(1024) G1(2048) G1(3072)::"v"(lane * 16), "s"(yp), "s"(l) : "memory");            \
    asm volatile("s_mov_b32 m0, %2\n\ts_nop 0\n\t" G1(0) G1(1024) G1(2048) G1(3072)::"v"(lane * 16 + 4096), "s"(yp), "s"((l) + 4096) : "memory"); \
    asm volatile("s_mov_b32 m0, %2\n\ts_nop 0\n\t" G1(0) G1(1024) G1(2048)::"v"(lane * 16 + 8192), "s"(yp), "s"((l) + 8192) : "memory");     \
  } while (0)
      {
        SPIN((miss & 3u) == 0, U_R, ULDF(15u));
        TS(48);
        ULD(y1, lds0 + 4 * SL + hw * 11264);
        UPOLL(15u, 15u);
        unsigned f;
        asm volatile("global_load_dword %0, %1, off sc1" : "=v"(f) : "v"(ufp) : "memory");
        ULD(y1 + SL, lds0 + hw * 11264);
        asm volatile("s_waitcnt vmcnt(11)" : "+v"(f)::"memory");
        BARRIER();
        miss = (unsigned)__builtin_amdgcn_ballot_w64(f == SENT);
      }
      slot = 1;
#pragma unroll 1
      for (int n = 4; n < 16; ++n) {
        UPOLL(3u << (2 * n - 4), ~0u);
        const char* yp = Ug + (KT(n >> 1) * 2 + (n & 1)) * SL + hw * 11264;
        unsigned l = lds0 + slot * SL + hw * 11264;
        slot = slot == NS - 1 ? 0 : slot + 1;
        if (n == CPF_B && hw == 1 && lane < CPF_N) PFL(cpf);
        asm volatile("" : "+s"(yp), "+s"(l));
        if (n >= 6) {
          WAIT_V(22);
          BARRIER();
          if (hw == 1) TSD(n + 44);
        }
        ULD(yp, l);
      }
      WAIT_V(22);
      BARRIER();
      WAIT_V(0);
      BARRIER();
      BARRIER();
      BARRIER();
      if (b < 56 && tid == 256) ST4(reinterpret_cast<unsigned*>(ws + WS_WAK) + b * 32, 1u);
      TSE();
      return;
    } else {
      const int aw = wid - 6, rp = min(mt * 2 + aw, 10);
      unsigned xfo = aw ? WS_XF2 + b * 8 : WS_XFL + b * 128;
      asm volatile("" : "+v"(xfo));
      __builtin_amdgcn_s_setprio(3);
      PBAR(0);
      asm volatile("s_mov_b64 exec, 1\n\tglobal_store_dwordx2 %0, %1, %2 sc1\n\ts_mov_b64 exec, -1" ::"v"(xfo), "v"(tag), "s"(ws) : "memory");
      __builtin_amdgcn_s_setprio(0);
      if (zd) {
#pragma unroll
        for (int s = 0; s < NS; ++s) *reinterpret_cast<u32x4*>(shm + s * SL + 10240 + aw * 1024 + lane * 16) = u32x4{0u, 0u, 0u, 0u};
      }
      LBAR();
      float rs[4] = {};
      const unsigned ra = lds0 + rp * 2048 + lane * 16;
      unsigned so = 0;
#define RSUM()                                                                                                                        \
  do {                                                                                                                                \
    u32x4 R_[2];                                                                                                                      \
    asm volatile("ds_read_b128 %0, %2\n\tds_read_b128 %1, %2 offset:1024\n\ts_waitcnt lgkmcnt(0)" : "=&v"(R_[0]), "=&v"(R_[1]) : "v"(ra + so) : "memory"); \
    _Pragma("unroll") for (int j = 0; j < 8; ++j) {                                                                                   \
      asm volatile("v_add_f32 %0, %0, %1" : "+v"(rs[(j >> 2) * 2]) : "v"(R_[j >> 2][j & 3] << 16));                                   \
      asm volatile("v_add_f32 %0, %0, %1" : "+v"(rs[(j >> 2) * 2 + 1]) : "v"(R_[j >> 2][j & 3] & 0xffff0000u));                       \
    }                                                                                                                                 \
    so = so == (NS - 1) * SL ? 0 : so + SL;                                                                                           \
  } while (0)
      {
#pragma unroll 1
        for (int bi = 0; bi < F_AT; ++bi) {
          RSUM();
          BARRIER();
        }
        u32x4 f;
#define FLD() asm volatile("global_load_dwordx4 %0, %1, %2 sc1" : "=v"(f) : "v"(lane * 16), "s"(ws + WS_XF2 + aw * 1024) : "memory")
        FLD();
#pragma unroll 1
        for (int bi = F_AT; bi < 4; ++bi) {
          RSUM();
          BARRIER();
        }
        POLL { PDBG(it);
          asm volatile("s_waitcnt vmcnt(0)" : "+v"(f)::"memory");
          if (__all(mk64(f[1], f[0]) == tag && mk64(f[3], f[2]) == tag)) break;
          __builtin_amdgcn_s_sleep(1);
          FLD();
        }
        asm volatile("s_waitcnt vmcnt(0)" : "+v"(f)::"memory");
      }
      const unsigned xo = (unsigned)(((qx * 512 + aw * 4 + g4) * D + mt * 128) * 2 + ((l16 ^ (aw * 4 + g4)) << 4));
#pragma unroll 1
      for (int bi = 4; bi < H_AT; ++bi) {
        RSUM();
        const int i = bi - X_AT;
        if (i >= 0 && i < 12) GLDS(lds0 + XR + i * 2048 + aw * 1024, xo, x + (PROW(5 + i / 2) + (i & 1) * 8) * D);
        BARRIER();
      }
      {
        u64 f;
        const unsigned* fp = hfl + lane * 2;
        asm volatile("global_load_dwordx2 %0, %1, off sc1" : "=v"(f) : "v"(fp) : "memory");
        RSUM();
        BARRIER();
        RSUM();
        BARRIER();
        POLL { PDBG(it);
          asm volatile("s_waitcnt vmcnt(0)" : "+v"(f)::"memory");
          if (__all(f == tag)) break;
          __builtin_amdgcn_s_sleep(1);
          asm volatile("global_load_dwordx2 %0, %1, off sc1" : "=v"(f) : "v"(fp) : "memory");
        }
        asm volatile("s_waitcnt vmcnt(0)" : "+v"(f)::"memory");
        unsigned c;
        const unsigned* cp = modv + 1024 + mt * 128 + aw * 64 + lane;
        asm volatile("global_load_dword %0, %1, off sc1" : "=v"(c) : "v"(cp) : "memory");
        RSUM();
        BARRIER();
        RSUM();
        BARRIER();
        POLL { PDBG(it);
          asm volatile("s_waitcnt vmcnt(0)" : "+v"(c)::"memory");
          if (__all(c != SENT)) break;
          __builtin_amdgcn_s_sleep(1);
          asm volatile("global_load_dword %0, %1, off sc1" : "=v"(c) : "v"(cp) : "memory");
        }
        asm volatile("s_waitcnt vmcnt(0)" : "+v"(c)::"memory");
        *reinterpret_cast<unsigned*>(shm + SCA + (aw * 64 + lane) * 4) = c ^ (cc ? 0x80000000u : 0u);
      }
#pragma unroll 1
      for (int bi = H_AT + 4; bi < 32; ++bi) {
        RSUM();
        if (bi < 30) BARRIER();
      }
      unsigned* rsb = reinterpret_cast<unsigned*>(ws + WS_RS) + qx * 512;
      {
        const float v0 = dpp_add<0x141>(dpp_add<0x4E>(dpp_add<0xB1>(rs[0] + rs[1])));
        const float v1 = dpp_add<0x141>(dpp_add<0x4E>(dpp_add<0xB1>(rs[2] + rs[3])));
        if ((lane & 7) < 2 && mt * 2 + aw < 11 && !(nt == 2 && rp == 5)) ST4(rsb + cc * S + PROW(rp) + (lane & 1) * 8 + (lane >> 3), nsent((lane & 1) ? v1 : v0));
      }
      BARRIER();
#pragma unroll 1
      for (int k = 34; k <= (aw ? B_VAT : 46); ++k) BARRIER();
      u32x2 m[6];
      const unsigned lr = min(lane, 47);
      const unsigned o0 = aw ? (unsigned)(WS_Q - WS_MOD) + mt * 512 + lane * 8 : 8192u + mt * 512 + lane * 8;
      const unsigned o1 = aw ? (unsigned)(WS_RS - WS_MOD) + (qx * 512 + PROW(5 + (lr >> 3)) + (lr & 7) * 2) * 4 : o0 + 8192;
      const unsigned o2 = aw ? o1 + 8192 : o0 + 16384, o3 = aw ? 8192u + mt * 512 + lane * 8 : 28672u + min(lane, 23) * 8;
#define VLD()                                                                                                                     \
  asm volatile("global_load_dwordx2 %0, %6, %10 sc1\n\tglobal_load_dwordx2 %1, %6, %11 sc1\n\tglobal_load_dwordx2 %2, %7, %10 sc1\n\t" \
               "global_load_dwordx2 %3, %7, %11 sc1\n\tglobal_load_dwordx2 %4, %8, %10 sc1\n\tglobal_load_dwordx2 %5, %9, %10 sc1"    \
               : "=&v"(m[0]), "=&v"(m[1]), "=&v"(m[2]), "=&v"(m[3]), "=&v"(m[4]), "=&v"(m[5])                                     \
               : "v"(o0), "v"(o1), "v"(o2), "v"(o3), "s"(modv), "s"(modv + 1024)                                                  \
               : "memory")
      if (!aw) __builtin_amdgcn_s_sleep(V_D);
      VLD();
      BARRIER();
      if (aw) {
#pragma unroll
        for (int k = B_VAT + 2; k <= 45; ++k) BARRIER();
      }
      TS(30);
      POLL { PDBG(it);
        asm volatile("s_waitcnt vmcnt(0)" : W6(m)::"memory");
        if (__all(max(max(max(max2(m[0]), max2(m[1])), max(max2(m[2]), aw ? 0u : max2(m[3]))), max(max2(m[4]), max2(m[5]))) != SENT)) break;
        __builtin_amdgcn_s_sleep(2);
        VLD();
      }
      asm volatile("s_waitcnt vmcnt(0)" : W6(m)::"memory");
      if (aw == 0) {
        *reinterpret_cast<unsigned*>(shm + TV + lane * 4) = hi2(m[2]);
        *reinterpret_cast<unsigned*>(shm + TV + 256 + lane * 4) = hi2(m[1]);
        *reinterpret_cast<unsigned*>(shm + TV + 512 + lane * 4) = hi2(m[3]);
        const float p0 = __uint_as_float(m[4][0]), p1 = __uint_as_float(m[4][1]);
        const unsigned hh = pack2(p0, p1);
        *reinterpret_cast<unsigned*>(shm + SCW + lane * 4) = hh;
        *reinterpret_cast<unsigned*>(shm + SCW + 256 + lane * 4) = pack2(p0 - bflo(hh), p1 - bfhi(hh));
        *reinterpret_cast<u32x2*>(shm + SCW + 512 + lane * 8) = u32x2{0x3f803f80u, 0x3f803f80u};
        const float u = __uint_as_float(m[5][0]) + __uint_as_float(m[5][1]), u2 = dpp_add<0xB1>(u), u8 = dpp_add<0x141>(dpp_add<0x4E>(u2));
        if (lane < 17 && !(lane & 1)) *reinterpret_cast<float*>(shm + TSUM + lane * 2) = lane < 16 ? u2 : u8;
      } else {
        BARRIER();
#pragma unroll
        for (int j = 0; j < 2; ++j) {
          const float r0 = __uint_as_float(m[0][j]), r1 = __uint_as_float(m[1][j]);
          const unsigned a0 = pack2(r0, r0), a1 = pack2(r1, r1);
          *reinterpret_cast<u32x4*>(shm + RKA + ((lane >> 4) * 32 + ((lane >> 1) & 1) * 16 + ((lane >> 2) & 3) * 4 + (lane & 1) * 2 + j) * 16) =
              u32x4{a0, pack2(r0 - bflo(a0), r0 - bflo(a0)), a1, pack2(r1 - bflo(a1), r1 - bflo(a1))};
          const float s0 = __uint_as_float(m[2][j]), s1 = -__uint_as_float(m[4][j]);
          const unsigned b0 = pack2(s0, s0 - rbf(s0)), b1 = pack2(s1, s1 - rbf(s1));
          *reinterpret_cast<u32x4*>(shm + RKB + (lr * 2 + j) * 16) = u32x4{b0, b0, b1, b1};
        }
        *reinterpret_cast<u32x2*>(shm + RKZ + lane * 8) = u32x2{0u, 0u};
        const unsigned one = g4 == (l16 >> 2) ? ((lane & 1) ? 0x3f800000u : 0x3f80u) : 0u, e0 = (lane & 2) ? 0u : one, e1 = (lane & 2) ? one : 0u;
        *reinterpret_cast<u32x4*>(shm + IDT + lane * 16) = u32x4{e0, e1, 0u, 0u};
        *reinterpret_cast<u32x4*>(shm + IDT + 1024 + lane * 16) = u32x4{0u, 0u, e0, e1};
        *reinterpret_cast<u32x2*>(shm + TGA + lane * 8) = m[5];
        LBAR();
      }
      TS(17);
    }
    const int row0 = rg * 16 + l16, col1 = mt * 128 + g4 * 8;
    u32x4 xq[4], sa4[4];
    if (wid < 4) {
      auto X1A = [&](auto kc) __attribute__((always_inline)) {
        constexpr int k = decltype(kc)::value;
        (void)acc, (void)idt, (void)pv;
        MFMA(acc[0][5 + k], idt[0], pv[k]);
        MFMA(acc[1][5 + k], idt[1], pv[k]);
      };
      auto X1B = [&](auto kc) __attribute__((always_inline)) {
        constexpr int j = decltype(kc)::value;
        (void)acc, (void)ga, (void)x5, (void)xw1;
        asm volatile("" : "+v"(acc[0][5 + j]), "+v"(acc[1][5 + j]));
        const u32x4 v = pk8((j < 5 ? acc[0][j % 5] : x5[0]) + ga[0] * acc[0][5 + j], (j < 5 ? acc[1][j % 5] : x5[1]) + ga[1] * acc[1][5 + j]);
        asm volatile("ds_write_b128 %0, %1 offset:%2" ::"v"(xw1), "v"(v), "n"(j * 4096) : "memory");
      };
      asm volatile("s_waitcnt vmcnt(2)" : "+v"(pv[2]), "+v"(pv[3]), "+v"(pv[4]), "+v"(pv[5])::"memory");
      UNTIL(__all(max(max(max4(pv[2]), max4(pv[3])), max(max4(pv[4]), max4(pv[5]))) != SENT), X_R,
            { WAIT_V(0); XCHLD(); asm volatile("s_waitcnt vmcnt(0)" : W6(pv)::"memory"); });
      X1A(IC(5)); X1A(IC(4)); X1A(IC(3)); X1B(IC(5)); X1A(IC(2)); X1B(IC(4)); X1B(IC(3));
      asm volatile("s_waitcnt vmcnt(0)" : "+v"(pv[0]), "+v"(pv[1])::"memory");
#define XCHL2()                                                                                                                             \
  asm volatile("global_load_dwordx4 %0, %2, %3 offset:2048 sc1\n\tglobal_load_dwordx4 %1, %2, %3 offset:1024 sc1\n\ts_waitcnt vmcnt(0)" \
               : "=&v"(pv[0]), "=&v"(pv[1]) : "v"(lane * 16), "s"(xr) : "memory")
      UNTIL(__all(max(max4(pv[0]), max4(pv[1])) != SENT), X_R, XCHL2());
      TS(27);
      X1A(IC(1)); X1A(IC(0)); X1B(IC(2)); X1B(IC(1)); X1B(IC(0));
    }
    {
      TS(6);
      LBAR();
#pragma unroll
      for (int q = 0; q < 4; ++q) {
        DSR(xq[q], xa, q * 1024);
        DSR(sa4[q], saa, q * 64);
      }
      f32x2 cj;
      float sw, ce;
      asm volatile("ds_read_b32 %2, %5\n\tds_read_b64 %0, %3\n\tds_read_b32 %1, %4\n\ts_waitcnt lgkmcnt(0)"
                   : "=&v"(cj), "=&v"(sw), "=&v"(ce), "+v"(cja), "+v"(swa), "+v"(cea), "+v"(xq[0]), "+v"(xq[1]), "+v"(xq[2]), "+v"(xq[3]), "+v"(sa4[0]),
                     "+v"(sa4[1]), "+v"(sa4[2]), "+v"(sa4[3])::"memory");
      if (!act) {
        TSE();
        return;
      }
      f32x4 s4 = {0.f, 0.f, 0.f, 0.f}, gr = {0.f, 0.f, 0.f, 0.f};
#pragma unroll
      for (int q = 0; q < 4; ++q) {
        gr = MM(xq[q], xq[q], gr);
        s4 = MM(sa4[q], xq[q], s4);
      }
      const u32x4 gd = __builtin_bit_cast(u32x4, gr) & dk;
      const float m = s4[2] * (1.0f / 128);
      float q2 = ar4(__uint_as_float((gd[0] | gd[1]) | (gd[2] | gd[3]))) - s4[2] * m, lg = (s4[0] + s4[1]) - m * ce;
      if (__builtin_expect(__any(!(s4[2] * m <= 256.0f * q2)), 0)) {
        f32x4 a = {0.f, 0.f, 0.f, 0.f}, l = {0.f, 0.f, 0.f, 0.f};
#pragma unroll
        for (int q = 0; q < 4; ++q) {
          const u32x4 ch = *reinterpret_cast<const u32x4*>(shm + SCW + g4 * 16 + q * 64), cl = *reinterpret_cast<const u32x4*>(shm + SCW + 256 + g4 * 16 + q * 64);
          const f32x4 d0 = lo4(xq[q]) - m, d1 = hi4(xq[q]) - m;
          a += d0 * d0 + d1 * d1;
          l += d0 * (lo4(ch) + lo4(cl)) + d1 * (hi4(ch) + hi4(cl));
        }
        q2 = ar4((a[0] + a[1]) + (a[2] + a[3]));
        lg = ar4((l[0] + l[1]) + (l[2] + l[3]));
      }
      const u32x4 own = {nsent(m), nsent(q2), nsent(lg), 0u};
      asm volatile("s_mov_b64 exec, 0xffff\n\tglobal_store_dwordx4 %0, %1, %2 sc1\n\ts_mov_b64 exec, -1\n\ts_nop 1" ::"v"(sto), "v"(own), "s"(stp) : "memory");
      TS(7);
      SKEWPT();
#define STOK() __all(max(max(max(sr[0][0], sr[0][1]), sr[0][2]), max(max(sr[1][0], sr[1][1]), sr[1][2])) != SENT)
      const bool pub = mt == (rg & 7);
      u64 pm = (unsigned)__builtin_amdgcn_readfirstlane(pub);
      unsigned klo = 0xffffu - (unsigned)row0, kof = WS_KEY + rg * 128;
      asm volatile("" : "+s"(pm), "+v"(klo), "+v"(kof));
      if (pub) __builtin_amdgcn_s_setprio(2);
      if (OPT_SD) __builtin_amdgcn_s_sleep(OPT_SD);
      if (!pub) __builtin_amdgcn_s_sleep(NPD);
      STLD();
      UNTIL(STOK(), 1, { STLD(); asm volatile(".Lcpf_e2:"); });
      TS(9);
      const f32x2 mk = {__uint_as_float(sr[0][0]), __uint_as_float(sr[1][0])};
      const f32x2 qk = {__uint_as_float(sr[0][1]), __uint_as_float(sr[1][1])};
      const f32x2 lk = {__uint_as_float(sr[0][2]), __uint_as_float(sr[1][2])};
      const float mean = ar4(mk[0] + mk[1]) * (1.0f / 8);
      const f32x2 dm = mk - mean;
      const f32x2 a = qk + 128.0f * (dm * dm), l = lk + dm * cj;
      const float al = swap32_add(a[0] + a[1], l[0] + l[1]), al2 = swap16_add(al, al);
      const auto alx = __builtin_amdgcn_permlane32_swap(__float_as_uint(al2), __float_as_uint(al2), false, false);
      const float rstd = __builtin_amdgcn_rsqf(__uint_as_float(alx[0]) * (1.0f / D) + 1e-5f);
      const float rd = rstd * __uint_as_float(alx[1]) + sw;
      const unsigned rb = f2bf(rd);
      const unsigned ord = rb ^ ((rb & 0x8000u) ? 0xffffu : 0x8000u);
      const unsigned key = (ord << 16) | klo;
      unsigned t1 = key, t2 = 0u;
      TOP2(0xB1); TOP2(0x4E); TOP2(0x141); TOP2(0x140);
      asm volatile("s_mov_b64 exec, %3\n\tglobal_store_dwordx2 %0, %1, %2 sc1\n\ts_mov_b64 exec, -1" ::"v"(kof), "v"(u32x2{~t1, ~t2}), "s"(ws), "s"(pm) : "memory");
      TS(1);
      SKEWPT();
      if (key >= t2) {
        const int sl = rg * 2 + (key == t2);
#pragma unroll
        for (int q = 0; q < 4; ++q) ST16(sl * 2048 + col1 * 2, nsent4(xq[q]), ws + WS_CAND, q * 64);
        if (g4 == 0 && pub) ST16(sl * 16, (u32x4{nsent(mean), nsent(rstd), 0u, 0u}), ws + WS_HDR, 0);
      }
      TS(8);
      if (OUT_D) __builtin_amdgcn_s_sleep(OUT_D);
#pragma unroll
      for (int q = 0; q < 4; ++q) {
        const u32x4 cq = *reinterpret_cast<const u32x4*>(shm + TV + q * 64 + g4 * 16), sbq = *reinterpret_cast<const u32x4*>(shm + TV + 256 + q * 64 + g4 * 16);
        const u32x4 gbq = *reinterpret_cast<const u32x4*>(shm + TV + 512 + q * 64 + g4 * 16);
        const f32x4 x0 = lo4(xq[q]), x1 = hi4(xq[q]);
        const f32x4 h0 = ((x0 - mean) * rstd) * lo4(cq) + lo4(sbq), h1 = ((x1 - mean) * rstd) * hi4(cq) + hi4(sbq);
        ST16((row0 * D + col1) * 2, pk8(x0 + lo4(gbq) * h0, x1 + hi4(gbq) * h1), out, q * 64);
      }
      WAIT_V(0);
      if (lane == 0) ST4(reinterpret_cast<unsigned*>(ws + WS_DON) + (rg * 8 + mt) * 2, 1u);
      TS(16);
    }
    TSE();
    return;
  }
  {
    const int hid = b - 192, hi = j8 - 24;
    constexpr int RNG = 65536, SSI = MISC, GP = MISC + 2048, SSA = MISC + 3072, HS = MISC + 5120, SX1 = MISC + 9216, ACT = MISC + 13312,
                  HFLG = MISC + 13568, RR = MISC + 13824;
    unsigned* flg = reinterpret_cast<unsigned*>(shm + HFLG);
    const int ec = xcd * 7 + hi, hd = hi == 7 ? 7 : (hi + 7 - ((3 + xcd) & 7) % 7) % 7;
    const bool pon = wid < ((PF_SH >> (4 * hd)) & 7);
    const char* pfh = PFG((int)((PF_BS >> (8 * hd)) & 31) + wid, 8 - qx);
#define HPF(g) do { if (pon) PFL(pfh + ((((g) + qx) & 7) << 9)); } while (0)
    WHLD(4);
    const int cj = hd == 1 ? 34 + xcd : hd * 8 - 16 + xcd, co = hd == 1 && xcd < 6 ? xcd : cj / 7, ce = hd == 1 && xcd < 6 ? 3 : cj % 7 + (cj % 7 >= 3);
    const int cb0 = hd % 7 == 0 ? (hd ? 24 + xcd : 16 + ((xcd + 7) & 7)) : (co < 2 ? co : co + 2) * 8 + ce;
    const unsigned om = (unsigned)((wid * 16 + (lane >> 3)) * 12288 + (lane & 7) * 16);
    const unsigned og = (unsigned)((wid * 8 + (lane >> 3)) * 7168 + (lane & 7) * 16);
    const unsigned od = (unsigned)((wid * 8 + (lane >> 3)) * 2048 + (lane & 7) * 16);
    const char* pm = reinterpret_cast<const char*>(wmod) + cb0 * 128;
    const char* pg = reinterpret_cast<const char*>(wg) + ec * 128;
    const char* pu = reinterpret_cast<const char*>(wu) + ec * 128;
    const char* pd = reinterpret_cast<const char*>(wd) + (size_t)ec * 131072;
    const unsigned ow = (unsigned)(((wid & 1) * 16 + (lane >> 3)) * 12288 + (wid >> 1) * 128 + (lane & 7) * 16);
    const char* pw = reinterpret_cast<const char*>(wmod) + 8192 + xcd * 512;
#define RQM(r)                                                                                   \
  do {                                                                                           \
    const char* p_ = pm + (size_t)(r)*1572864;                                                   \
    GLDSP(lds0 + RNG + ((r)&3) * 16384 + wid * 2048, om, p_, WPOL);                              \
    GLDSP(lds0 + RNG + ((r)&3) * 16384 + wid * 2048 + 1024, om, p_ + 98304, WPOL);               \
  } while (0)
#define RQE(r)                                                                                                                \
  do {                                                                                                                        \
    GLDSP(lds0 + RNG + ((r)&3) * 16384 + wid * 1024, (r) < 16 ? og : od, (r) < 16 ? pg + (r)*458752 : pd + ((r)-16) * 256, WPOL);   \
    GLDSP(lds0 + RNG + ((r)&3) * 16384 + 8192 + wid * 1024, (r) < 16 ? og : od, (r) < 16 ? pu + (r)*458752 : pd + ((r)-16) * 256 + 128, WPOL); \
  } while (0)
    PBAR(4);
    XFLAG();
    const u32x4 sv = {SENT, SENT, SENT, SENT};
    auto PUBS = [&](int cb, unsigned v, unsigned p, unsigned r) __attribute__((always_inline)) {
      const int vi = cb >> 4, col = (cb & 15) * 64 + lane;
      ST4(modv + cb * 64 + lane, v);
      if (vi == 4) ST4(modv + 6144 + col, p);
      if (vi == 4 ? (lane & 31) == 0 : vi == 3 && lane == 0) ST4(modv + 7168 + (vi == 4 ? col >> 5 : 32 + (cb & 15)), r);
    };
    if (wid == 4 || (hi == 7 && wid < 4)) PUBS(wid == 4 ? cb0 : 64 + xcd * 4 + wid, SENT, SENT, SENT);
    if (tid >= 480) ST4(reinterpret_cast<unsigned*>(ws + WS_Q) + hsl * 32 + tid - 480, SENT);
    unsigned cv, wv;
    asm volatile("global_load_dword %0, %2, %3\n\tglobal_load_dword %1, %2, %4" : "=&v"(cv), "=&v"(wv) : "v"(tid * 4), "s"(cvec), "s"(wr_) : "memory");
    if (hi < 7) {
      if (PF_N < 3) HPF(2);
      HPF(3);
      const char* ap = ws + WS_ARM + (size_t)(ec * 8 + wid) * 3328;
#pragma unroll
      for (int j = 0; j < 4; ++j) ST16(lane * 16, sv, ap, j < 3 ? j * 1024 : 2304);
      HPF(4);
      WAIT_V(4);
    } else {
      WAIT_V(0);
    }
    asm volatile("; AUDIT_OK %0 %1" : "+v"(cv), "+v"(wv)::"memory");
    *reinterpret_cast<unsigned*>(shm + SSI + tid * 4) = pack2(silu_bf(bflo(cv)), silu_bf(bfhi(cv)));
    *reinterpret_cast<unsigned*>(shm + SX1 + tid * 4) = wv;
    LBAR();
    TS(50);
    const unsigned ha = (unsigned)(((l16 >> 2) * 8 + (lane & 3)) * 2048 + wid * 256);
#define HFR(j) *reinterpret_cast<const u32x4*>(shm + ha + ((j)&1) * 8192 + ((j) >> 2) * 128 + (((((j) >> 1) & 1) * 4 + g4) ^ (lane & 7)) * 16)
    HPF(5);
#pragma unroll
    for (int j = 0; j < 8; ++j) ST16(lane * 16, HFR(j), ws + WS_WH + (size_t)hsl * 65536 + wid * 8192 + 4096, j * 1024 - 4096);
    HPF(6);
    RQM(0);
    RQM(1);
    RQM(2);
    const unsigned tm = lds0 + RNG + ((wid >> 2) * 64 + g4 * 8 + ((lane >> 2) & 3)) * 128 + (wid & 3) * 32 + (lane & 3) * 8;
    f32x4 ma = {0.f, 0.f, 0.f, 0.f};
#define GVR(i)                                                                                                                               \
  do {                                                                                                                                       \
    _Pragma("unroll") for (int f = 0; f < 2; ++f)                                                                                            \
        ma = MM(trd(tm + ((i)&3) * 16384 + f * 4096), *reinterpret_cast<const u32x4*>(shm + SSI + ((i)*128 + (wid >> 2) * 64 + f * 32 + g4 * 8) * 2), ma); \
    if ((i) == 7 && l16 == 0) *reinterpret_cast<f32x4*>(shm + GP + wid * 64 + g4 * 16) = ma;                                                 \
  } while (0)
    const float* gp = reinterpret_cast<const float*>(shm + GP);
    auto PUB = [&](int cb, float s) __attribute__((always_inline)) {
      const int vi = cb >> 4;
      const float v = rbf(((vi == 1 || vi == 4) ? 1.0f : 0.0f) + rbf(s));
      const float p = v * bf2f(*reinterpret_cast<const u16*>(shm + SX1 + ((cb & 15) * 64 + lane) * 2));
      const float r16 = row_sum(p), r32 = swap16_add(r16, r16);
      PUBS(cb, nsent(v), nsent(p), nsent(vi == 4 ? r32 : swap32_add(r32, r32)));
    };
    {
      sfor<8>([&](auto ii) __attribute__((always_inline)) {
        constexpr int i = decltype(ii)::value;
        (void)ow, (void)pw, (void)om, (void)pm, (void)og, (void)od, (void)pg, (void)pu, (void)pd, (void)lds0, (void)wid, (void)pfl, (void)Yx, (void)l16, (void)g4, (void)xcd, (void)hfl, (void)lane, (void)hid;
        TSCAP();
        WAIT_V(4);
        LBAR();
        if (wid == 2) TS(i);
        if constexpr (i == 0) { if (tid == 0) ST4(reinterpret_cast<u64*>(hfl + hid * 2), tag); }
        if constexpr (i == 0) HPF(7);
        if constexpr (i < 5) {
          RQM(i + 3);
        } else {
          const bool e = hid < 56;
          const char* p_ = pw + (i - 5) * 393216;
          GLDSP(lds0 + RNG + (i - 5) * 16384 + wid * (e ? 1024 : 2048), e ? og : ow, e ? pg + (i - 5) * 458752 : p_, WPOL);
          GLDSP(lds0 + RNG + (i - 5) * 16384 + wid * (e ? 1024 : 2048) + (e ? 8192 : 1024), e ? og : ow, e ? pu + (i - 5) * 458752 : p_ + 98304, WPOL);
        }
        GVR(i);
      });
    }
    TS(51);
#define PUB0() do { if (wid == 0) PUB(cb0, gp[lane] + gp[64 + lane]); } while (0)
    u64 sf, hf;
#define SFLD()                                                                                                                     \
  asm volatile("s_mov_b64 exec, 3\n\tglobal_load_dwordx2 %0, %2, %3 sc1\n\ts_mov_b64 exec, 0xff\n\tglobal_load_dwordx2 %1, %5, %4 sc1\n\ts_mov_b64 exec, -1" \
               : "=&v"(sf), "=&v"(hf) : "v"(lane * 128), "s"(ws + WS_SFL + wid * 256), "s"(ws + WS_HFL + wid * 64), "v"(lane * 8) : "memory")
    auto SAW = [&]() __attribute__((always_inline)) {
      POLL { PDBG(it);
        if (((__builtin_amdgcn_ballot_w64(sf != tag) & 3u) | (__builtin_amdgcn_ballot_w64(hf != tag) & 0xffu)) == 0) break;
        __builtin_amdgcn_s_sleep(4);
        SFLD();
        asm volatile("s_waitcnt vmcnt(0)" : "+v"(sf), "+v"(hf)::"memory");
      }
    };
    u32x2 s2;
#define SALD() asm volatile("global_load_dwordx2 %0, %1, %2 sc1" : "=v"(s2) : "v"(tid * 8), "s"(modv) : "memory")
    auto QMM = [&]() __attribute__((always_inline)) {
      *reinterpret_cast<unsigned*>(shm + SSA + tid * 4) = hi2(s2);
      f32x4 qa[2] = {};
#pragma unroll
      for (int j = 0; j < 8; ++j) qa[j & 1] = MM(HFR(j), *reinterpret_cast<const u32x4*>(shm + SSA + wid * 256 + (j >> 1) * 64 + g4 * 16), qa[j & 1]);
      if (l16 < 2) *reinterpret_cast<f32x4*>(shm + HS + wid * 128 + g4 * 32 + l16 * 16) = l16 ? qa[1] : qa[0];
    };
    auto QPUB = [&]() __attribute__((always_inline)) {
      if (tid >= 480) {
        float s = 0.f;
#pragma unroll
        for (int w = 0; w < 8; ++w) s += *reinterpret_cast<const float*>(shm + HS + w * 128 + (tid - 480) * 4);
        ST4(reinterpret_cast<unsigned*>(ws + WS_Q) + hsl * 32 + tid - 480, nsent(s));
      }
    };
    u32x8 GU[16], DN[8];
    if (hid < 56) {
      const unsigned tg = lds0 + RNG + (g4 * 8 + ((lane >> 2) & 3)) * 128 + ((lane >> 1) & 1) * 8192 + wid * 16 + (lane & 1) * 8;
      const unsigned td = lds0 + RNG + (g4 * 8 + ((lane >> 2) & 3)) * 128 + (wid >> 2) * 8192 + (wid & 3) * 32 + (lane & 3) * 8;
      sfor<24>([&](auto rr) __attribute__((always_inline)) {
        constexpr int r = decltype(rr)::value;
        (void)td, (void)tg, (void)GU, (void)DN, (void)og, (void)od, (void)pg, (void)pu, (void)pd, (void)lds0, (void)wid, (void)pfl, (void)Yx, (void)l16, (void)g4, (void)xcd;
        (void)s2, (void)modv, (void)tid;
        (void)hid, (void)lane, (void)sf, (void)hf;
        (void)sv, (void)hi, (void)ws;
#define EX(r) (((r) < 0 ? 0 : (r) < 2 ? 4 : (r) == 2 ? 2 : 0) + ((r) == Q_AT - 6 ? 2 : 0) + ((r) == Q_AT - 3))
        WAIT_V(r > 21 ? 2 * (23 - r) : 4 + EX(r - 2) + EX(r - 1));
        LBAR();
        TSCAP();
        if (wid == 3) TS(r);
        if constexpr (r == 0) PUB0();
        if constexpr (r == 1) {
          if (cb0 < 16 && wid == 0) {
            WAIT_V(2 + EX(0));
            if (lane == 0) ST4(reinterpret_cast<u64*>(ws + WS_SFL) + cb0 * 16, tag);
          }
        }
        if constexpr (r == Q_AT - 6) SFLD();
        if constexpr (r == Q_AT - 3) {
          asm volatile("" : "+v"(sf), "+v"(hf));
          SAW();
          SALD();
        }
        if constexpr (r < 3) {
          const char* ap = ws + WS_XCH + xcd * 589824 + (hi * 8 + wid) * 10240 + r * 4096;
#pragma unroll
          for (int j = 0; j < (r < 2 ? 4 : 2); ++j) ST16(lane * 16, sv, ap, j * 1024);
          if (r == 2 && wid < 3) ST16(lane * 16, sv, ws + WS_XCH + xcd * 589824 + min(560 + hi * 3 + wid, 575) * 1024, 0);
        }
        if constexpr (r < 21) RQE(r + 3);
        if constexpr (r < 16) {
          GU[r] = trd2(tg + (r & 3) * 16384);
          asm volatile("" : "+v"(GU[r]));
        } else {
          DN[r - 16] = trd2(td + (r & 3) * 16384);
          asm volatile("" : "+v"(DN[r - 16]));
        }
        if constexpr (r == Q_AT) {
          asm volatile("" : "+v"(s2));
          QMM();
        }
        if constexpr (r == Q_AT + 1) QPUB();
      });
      TS(52);
    } else {
      const unsigned tw = lds0 + RNG + (wid >> 1) * 4096 + (g4 * 8 + ((lane >> 2) & 3)) * 128 + (wid & 1) * 64 + (lane & 3) * 8;
      f32x4 mb[2] = {};
      auto WR = [&](int r0, int r1) __attribute__((always_inline)) {
#pragma unroll 1
        for (int r = r0; r < r1; ++r) {
          WAIT_V(4);
          LBAR();
          if (wid == 2 && r < 22) TSD(r + 8);
          const char* p_ = pw + min(r + 3, 31) * 393216;
          GLDSP(lds0 + RNG + ((r + 3) & 3) * 16384 + wid * 2048, ow, p_, WPOL);
          GLDSP(lds0 + RNG + ((r + 3) & 3) * 16384 + wid * 2048 + 1024, ow, p_ + 98304, WPOL);
          const u32x4 sf = *reinterpret_cast<const u32x4*>(shm + SSI + (r * 32 + g4 * 8) * 2);
          mb[0] = MM(trd(tw + (r & 3) * 16384), sf, mb[0]);
          mb[1] = MM(trd(tw + (r & 3) * 16384 + 32), sf, mb[1]);
        }
      };
      WR(0, 1);
      PUB0();
      WR(1, MQ_AT - 6);
      SFLD();
      WR(MQ_AT - 6, MQ_AT - 3);
      asm volatile("; AUDIT_OK %0 %1" : "+v"(sf), "+v"(hf));
      SAW();
      SALD();
      WR(MQ_AT - 3, MQ_AT);
      asm volatile("; AUDIT_OK %0" : "+v"(s2));
      QMM();
      WR(MQ_AT, MQ_AT + 1);
      QPUB();
      WR(MQ_AT + 1, 32);
      if (l16 < 2) *reinterpret_cast<f32x4*>(shm + GP + wid * 128 + l16 * 64 + g4 * 16) = l16 ? mb[1] : mb[0];
      LBAR();
      if (wid < 4) PUB(64 + xcd * 4 + wid, gp[tid]);
      TS(52);
      WAIT_V(0);
      TSE();
      return;
    }
    bool early = false;
    if (tid == 0) {
      POLL { PDBG(it);
        unsigned g;
        asm volatile("global_load_dword %0, %1, %2 sc1\n\ts_waitcnt vmcnt(0)" : "=&v"(g) : "v"(0u), "s"(ws + WS_WAK + hid * 128) : "memory");
        if (g != SENT) break;
        early = true;
        __builtin_amdgcn_s_sleep(OPT_KS);
      }
    }
    BARRIER();
    TS(21);
    const int tok = hid & 1, col = (hid & 16) * 32 + (lane & 3) * 128 + (hid & 14) * 8 + (lane >> 2);
    u32x4 c4, s4;
    unsigned gbu;
    POLL { PDBG(it);
      asm volatile("global_load_dwordx4 %0, %3, %5 sc1\n\tglobal_load_dwordx4 %1, %3, %6 sc1\n\tglobal_load_dword %2, %4, %7 sc1\n\ts_waitcnt vmcnt(0)"
                   : "=&v"(c4), "=&v"(s4), "=&v"(gbu) : "v"((tid & 255) * 16), "v"(col * 4), "s"(modv + 4096), "s"(modv + 3072), "s"(modv + 5120) : "memory");
      if (__all(max(max(max4(c4), max4(s4)), gbu) != SENT)) break;
      __builtin_amdgcn_s_sleep(8);
    }
    const f32x4 cb2 = __builtin_bit_cast(f32x4, c4), sb2 = __builtin_bit_cast(f32x4, s4);
    unsigned hra = lds0 + HS + g4 * 64 + (lane & 4) * 8 + (lane & 1) * 16, hwa = lds0 + HS + ((tid & 255) >> 2) * 64 + (wid >> 2) * 32 + (tid & 3) * 4;
    unsigned pto = (g4 & 1) * 4096 + ((g4 >> 1) * 8 + wid) * 256 + l16 * 16, six = (lane & 1) ? 0xEEEEu : 0x4444u;
    unsigned awa = lds0 + ACT + (wid >> 1) * 64 + g4 * 32 + (lane & 2) * 8 + (wid & 1) * 8 + (lane & 4) + (lane & 1) * 2;
    f32x4 d = {0.f, 0.f, 0.f, 0.f};
    unsigned sla = lds0 + HFLG + 8 + (wid >> 2) * 4;
    asm volatile("" : "+v"(sla), "+v"(hra), "+v"(hwa), "+v"(pto), "+v"(awa), "+v"(six), "+v"(d));
    const float gb = __uint_as_float(gbu);
    if (wid == 0) {
      u32x2 g0 = {SENT, SENT}, g1 = {SENT, SENT};
      const char* gp = ws + WS_KEY + lane * 128;
      if (__any(early)) __builtin_amdgcn_s_sleep(OPT_KD);
      TS(20);
      u64 m0 = ~0ull, m1 = ~0ull;
      int left = PCAP;
      do {
        asm volatile("s_sleep 1\n\ts_mov_b64 exec, %4\n\tglobal_load_dwordx2 %0, %2, off sc1\n\ts_mov_b64 exec, %5\n\tglobal_load_dwordx2 %1, %3, off sc1\n\t"
                     "s_mov_b64 exec, -1\n\ts_waitcnt vmcnt(0)"
                     : "+v"(g0), "+v"(g1) : "v"(gp), "v"(gp + 8192), "s"(m0), "s"(m1) : "memory");
        m0 = __builtin_amdgcn_ballot_w64(max2(g0) == SENT);
        m1 = __builtin_amdgcn_ballot_w64(max2(g1) == SENT);
        left = (m0 | m1) ? left - 1 : 0;
        asm volatile("" : "+s"(left));
        PDBG(PCAP - left);
      } while (left);
      g0 = ~g0;
      g1 = ~g1;
      const unsigned a1 = max(g0[0], g1[0]), a2 = max(min(g0[0], g1[0]), max(g0[1], g1[1]));
      const unsigned k1 = wave_max_u(a1);
      const unsigned k2 = wave_max_u(a1 == k1 ? a2 : a1);
      const int rg1 = key_row(k1) >> 4, rg2 = key_row(k2) >> 4;
      asm volatile("ds_write_b128 %0, %1" ::"v"(lds0 + HFLG), "v"(u32x4{k1, k2, rg1 * 32u, rg2 * 32u + (rg2 == rg1) * 16u}) : "memory");
      TS(22);
    }
    LBAR();
    {
      unsigned sl;
      asm volatile("ds_read_b32 %0, %1\n\ts_waitcnt lgkmcnt(0)" : "=&v"(sl) : "v"(sla) : "memory");
      u32x2 v, hd;
#define CLD()                                                                                                          \
  asm volatile("global_load_dwordx2 %0, %2, %4 sc1\n\tglobal_load_dwordx2 %1, %3, %5 sc1\n\ts_waitcnt vmcnt(0)"        \
               : "=&v"(v), "=&v"(hd) : "v"(sl * 128 + (tid & 255) * 8), "v"(sl), "s"(ws + WS_CAND), "s"(ws + WS_HDR) : "memory")
      CLD();
      UNTIL(__all(max(max2(v), max2(hd)) != SENT), 1, { CLD(); asm volatile(".Lcpf_e1:"); });
      TS(23);
      const float mean = __uint_as_float(hd[0]), rstd = __uint_as_float(hd[1]);
      const f32x4 h = ((f32x4{bflo(v[0]), bfhi(v[0]), bflo(v[1]), bfhi(v[1])} - mean) * rstd) * cb2 + sb2;
      asm volatile("ds_write2_b32 %0, %1, %2 offset1:4" ::"v"(hwa), "v"(pack2(h[0], h[1])), "v"(pack2(h[2], h[3])) : "memory");
      *reinterpret_cast<u32x2*>(shm + SX1 + tid * 8) = v;
    }
    SKEWPT();
    TS(26);
    LBAR();
    TS(27);
    f32x4 dn[8];
    {
      u32x4 hb[8];
      sfor<23>([&](auto ff) __attribute__((always_inline)) {
        constexpr int f = decltype(ff)::value, c = f - 7;
        (void)d, (void)hb, (void)GU, (void)hra, (void)six;
        if constexpr (c >= 0) {
          WAIT_L(f <= 16 ? 6 : 22 - f);
          SMF(d, hb[c & 7], GU[c], six);
        }
        if constexpr (f < 16) DSR(hb[f & 7], hra, f * 256);
      });
      asm volatile("s_nop 7" : "+v"(d));
      TS(29);
      const float dv = rbf(d[0] + d[1]), uv = dpp_f<0x128>(dv);
      const unsigned av = f2bf(rbf(dv * __builtin_amdgcn_rcpf(1.0f + __expf(-dv))) * uv);
      asm volatile("s_mov_b64 exec, 0xff00ff\n\tds_write_b16 %0, %1\n\ts_mov_b64 exec, -1" ::"v"(awa), "v"(av) : "memory");
    }
#pragma unroll
    for (int c = 0; c < 8; ++c) asm volatile("v_mov_b64 %0, 0" : "=v"(dn[c].lo));
    asm volatile("" : W8(dn));
    TS(24);
    LBAR();
    {
      u32x4 a0;
      asm volatile("ds_read_b128 %0, %1 offset:%2\n\ts_waitcnt lgkmcnt(0)" : "=&v"(a0) : "v"(hra), "n"(ACT - HS) : "memory");
#pragma unroll
      for (int c = 0; c < 8; ++c) SMF(dn[(c >> 1) + (c & 1) * 4], a0, DN[(c >> 1) + (c & 1) * 4], six);
      asm volatile("s_nop 7" : W8(dn));
      u32x4 o;
#pragma unroll
      for (int i = 0; i < 4; ++i) o[i] = nsent((g4 & 2) ? dn[4 + i][0] + dn[4 + i][1] : dn[i][0] + dn[i][1]);
      ST16(pto, o, ws + WS_PART + ec * 8192, 0);
    }
    TS(25);
    SKEWPT();
    if (hid >= 32) {
      if (hi == 6 && wid == 1 && lane < 35) {
        char* fp = ws + (lane < 32 ? WS_XFL + (xcd * 32 + lane) * 128 : lane < 34 ? WS_SFL + (xcd * 2 + lane - 32) * 128 : WS_EPO);
        if (lane < 34 || xcd == 0) ST4(reinterpret_cast<u64*>(fp), lane < 34 ? ~tag : tag);
      }
      if (hid == 40 && tid < 320) ST4(reinterpret_cast<u64*>(ws + WS_HFL) + tid, ~tag);
      TSE();
      return;
    }
    const unsigned kk = flg[tok];
    const int row = key_row(kk);
    float x1 = bf2f(*reinterpret_cast<const u16*>(shm + SX1 + tok * 2048 + col * 2));
    float h2 = bf2f(*reinterpret_cast<const u16*>(shm + HS + (col >> 4) * 64 + tok * 32 + (col & 2) * 8 + (col & 12) + (col & 1) * 2));
    float rsg = rbf(1.0f / (1.0f + __expf(-key_val(kk))));
    unsigned oo3 = (unsigned)(row * D + col) * 2, rra = lds0 + RR + lane * 32, rwa = lds0 + RR + ((l16 * 4 + g4) * 8 + wid) * 4;
    asm volatile("" : "+v"(rwa), "+v"(rsg), "+v"(x1), "+v"(h2), "+v"(oo3), "+v"(rra));
    if (wid == 7) {
      POLL { PDBG(it);
        unsigned g;
        asm volatile("global_load_dword %0, %1, %2 sc1\n\ts_waitcnt vmcnt(0)" : "=&v"(g) : "v"(((row >> 4) * 8 + (col >> 7)) * 8), "s"(ws + WS_DON) : "memory");
        if (__all(g != SENT)) break;
        __builtin_amdgcn_s_sleep(2);
      }
      asm volatile("s_barrier\n\ts_endpgm");
    }
    {
      const unsigned po = (unsigned)((wid * 8 + g4) * 8192 + tok * 4096 + (hid >> 1) * 256 + l16 * 16);
      u32x4 v[2];
      if (OPT_RD) __builtin_amdgcn_s_sleep(OPT_RD);
#define PLD()                                                                                                          \
  asm volatile("global_load_dwordx4 %0, %2, %3 sc1\n\tglobal_load_dwordx4 %1, %2, %4 sc1\n\ts_waitcnt vmcnt(0)"        \
               : "=&v"(v[0]), "=&v"(v[1]) : "v"(po), "s"(ws + WS_PART), "s"(ws + WS_PART + 32768) : "memory")
      SPIN(__all(max(max4(v[0]), max4(v[1])) != SENT), OPT_RR, PLD());
      TS(28);
      const f32x4 t = __builtin_bit_cast(f32x4, v[0]) + __builtin_bit_cast(f32x4, v[1]);
      asm volatile("ds_write_b32 %0, %1" ::"v"(rwa), "v"(swap16_add(swap32_add(t[0], t[2]), swap32_add(t[1], t[3]))) : "memory");
    }
    LBAR();
    TS(5);
    if (wid == 0) {
      f32x4 r0, r1;
      asm volatile("ds_read_b128 %0, %2\n\tds_read_b128 %1, %2 offset:16\n\ts_waitcnt lgkmcnt(0)" : "=&v"(r0), "=&v"(r1) : "v"(rra) : "memory");
      const float sum = ((r0[0] + r0[1]) + (r0[2] + r0[3])) + ((r1[0] + r1[1]) + r1[2]);
      asm volatile("global_store_short %0, %1, %2" ::"v"(oo3), "v"(f2bf(x1 + gb * rbf(h2 + rbf(rbf(sum) * rsg)))), "s"(out) : "memory");
    }
    TS(15);
    TSE();
  }
}

extern "C" void kernel_launch(void* const* d_in, const int* in_sizes, int n_in, void* d_out, int out_size, void* d_ws, size_t ws_size,
                              hipStream_t stream) {
  const u16* x = (const u16*)d_in[0];
  const u16* wsr = (const u16*)d_in[1];
  const u16* wsi = (const u16*)d_in[2];
  const u16* whr = (const u16*)d_in[3];
  const u16* whi = (const u16*)d_in[4];
  const u16* w_r = (const u16*)d_in[6];
  const u16* w_gate = (const u16*)d_in[7];
  const u16* w_up = (const u16*)d_in[8];
  const u16* w_down = (const u16*)d_in[9];
  const u16* c = (const u16*)d_in[10];
  const u16* w_mod = (const u16*)d_in[11];
  u16* out = (u16*)d_out;
  k_main<<<256, 512, 0, stream>>>(x, (char*)d_ws, wsr, wsi, c, w_mod, whr, whi, w_r, w_gate, w_up, w_down, out);
}
```
